```python
import torch

def _softmax(x, axis=-1):
    return torch.softmax(x, dim=axis)
def _gelu(x):
    return torch.nn.functional.gelu(x, approximate="tanh")
def _silu(x):
    return torch.nn.functional.silu(x)
def _sigmoid(x):
    return torch.sigmoid(x)
def _layernorm(x, axis=-1, eps=1e-5):
    m = x.mean(dim=axis, keepdim=True)
    v = x.var(dim=axis, keepdim=True, unbiased=False)
    return (x - m) / torch.sqrt(v + eps)
def _scatter_add(tgt, idx, src, axis=0):
    return tgt.clone().index_add_(axis, idx, src)
def _segment_sum(src, seg, n):
    out = torch.zeros((n,) + src.shape[1:], dtype=src.dtype)
    return out.index_add_(0, seg, src)
def _pad(x, pw):
    flat = tuple(v for pair in reversed(pw) for v in pair)
    return torch.nn.functional.pad(x, flat)

kernel_name = 'interleave_layers__softmax_full_mha__glu_gated_reglu__prompt'

def setup_inputs():

    _w = (lambda a: (lambda t: t.to(torch.bfloat16) if t.is_floating_point() else t)(torch.as_tensor(a)))
    def _A_setup():
        B, S, D = 1, 2048, 512
        return {
            "x": _w((lambda *s: torch.randn(*s, dtype=torch.bfloat16) * 0.05)(B, S, D)),
            "wqkv": _w((lambda *s: torch.randn(*s, dtype=torch.bfloat16) * 0.05)(D, 3 * D)),
            "wo": _w((lambda *s: torch.randn(*s, dtype=torch.bfloat16) * 0.05)(D, D)),
        }
    _A_in = _A_setup()

    def _B_setup():
        B, S, D, F = 1, 2048, 512, 1792
        return {
            "x": _w((lambda *s: torch.randn(*s, dtype=torch.bfloat16) * 0.05)(B, S, D)),
            "W_gate": _w((lambda *s: torch.randn(*s, dtype=torch.bfloat16) * 0.4)(D, F) * (2.0 / D) ** 0.5),
            "W_up": _w((lambda *s: torch.randn(*s, dtype=torch.bfloat16) * 0.4)(D, F) * (2.0 / D) ** 0.5),
            "W_down": _w((lambda *s: torch.randn(*s, dtype=torch.bfloat16) * 0.4)(F, D) * (1.0 / F) ** 0.5),
        }
    _B_in = _B_setup()

    return {
        "A_x": _A_in["x"],
        "A_wqkv": _A_in["wqkv"],
        "A_wo": _A_in["wo"],
        "B_x": _B_in["x"],
        "B_W_gate": _B_in["W_gate"],
        "B_W_up": _B_in["W_up"],
        "B_W_down": _B_in["W_down"],
    }

def reference(A_x, A_wqkv, A_wo, B_x, B_W_gate, B_W_up, B_W_down):
    def _A_ref(x, wqkv, wo):
        B, S, D = x.shape
        H = 32; Dh = D // H
        qkv = (lambda x, *d: x.permute(*d))((x @ wqkv).reshape(B, S, 3, H, Dh), 2, 0, 3, 1, 4)
        q, k, v = qkv[0], qkv[1], qkv[2]
        scale = Dh ** -0.5
        scores = torch.einsum("bhqd,bhkd->bhqk", q, k) * scale
        p = _softmax(scores, axis=-1)
        o = torch.einsum("bhqk,bhkd->bhqd", p, v)
        return (lambda x, *d: x.permute(*d))(o, 0, 2, 1, 3).reshape(B, S, D) @ wo

    def _B_ref(x, W_gate, W_up, W_down):
        gate = torch.einsum("bsd,df->bsf", x, W_gate)
        up = torch.einsum("bsd,df->bsf", x, W_up)
        gate = gate * (lambda x, dt: x.to(dt))(gate > 0, gate.dtype)
        h = gate * up
        return torch.einsum("bsf,fd->bsd", h, W_down)

    x = A_x
    x = x + _A_ref(_layernorm(x, axis=-1), A_wqkv, A_wo)
    x = x + _B_ref(_layernorm(x, axis=-1), B_W_gate, B_W_up, B_W_down)
    x = x + _A_ref(_layernorm(x, axis=-1), A_wqkv, A_wo)
    x = x + _B_ref(_layernorm(x, axis=-1), B_W_gate, B_W_up, B_W_down)
    return x
```

```cpp
#include <hip/hip_runtime.h>
#include <hip/hip_bf16.h>
#include <stdint.h>

typedef short bf16x8 __attribute__((ext_vector_type(8)));
typedef float f32x16 __attribute__((ext_vector_type(16)));
typedef float f32x4 __attribute__((ext_vector_type(4)));
typedef float f32x2 __attribute__((ext_vector_type(2)));
typedef unsigned u32x4 __attribute__((ext_vector_type(4)));
typedef unsigned u32x2 __attribute__((ext_vector_type(2)));
typedef __bf16 bf16v2 __attribute__((ext_vector_type(2)));
typedef unsigned short u16;

#define DI __device__ __forceinline__
#define MFMA(a, b, c) __builtin_amdgcn_mfma_f32_32x32x16_bf16((a), (b), (c), 0, 0, 0)
#define LDSP(T) __attribute__((address_space(3))) T*

constexpr int S_ = 2048, D_ = 512, H_ = 32, F_ = 1792;
constexpr int NTT = S_ / 32;
constexpr int KSD = D_ / 16;
constexpr int KSF = F_ / 16;
constexpr float QSCALE = 0.25f * 1.4426950408889634f;
constexpr float LN_EPS = 1e-5f;

constexpr size_t KB = 1024;
constexpr size_t OFF_WQKV = 0;
constexpr size_t OFF_WO = OFF_WQKV + 1536 * KB;
constexpr size_t OFF_WGU = OFF_WO + 512 * KB;
constexpr size_t OFF_WD = OFF_WGU + 3584 * KB;
constexpr size_t OFF_CSQ = OFF_WD + 1792 * KB;
constexpr size_t OFF_CSG = OFF_CSQ + 8 * KB;
constexpr size_t OFF_X0 = OFF_CSG + 16 * KB;
constexpr size_t OFF_X2 = OFF_X0 + 2048 * KB;
constexpr size_t OFF_X3 = OFF_X2 + 2048 * KB;
constexpr size_t OFF_ST0 = OFF_X3 + 2048 * KB;
constexpr size_t OFF_ST2 = OFF_ST0 + 256 * KB;
constexpr size_t OFF_ST3 = OFF_ST2 + 256 * KB;
constexpr size_t OFF_Q = OFF_ST3 + 256 * KB;
constexpr size_t OFF_K = OFF_Q + 2048 * KB;
constexpr size_t OFF_V = OFF_K + 2048 * KB;
constexpr size_t OFF_O = OFF_V + 2048 * KB;
constexpr size_t OFF_H = OFF_O + 2048 * KB;
constexpr size_t WS_END = OFF_H + 7168 * KB;

DI int kperm(int hh, int j) { return 8 * (j >> 2) + 4 * hh + (j & 3); }
DI float bf2f(u16 v) { return __uint_as_float((unsigned)v << 16); }
DI unsigned pk2(float lo, float hi) {
  f32x2 v = {lo, hi};
  bf16v2 r = __builtin_convertvector(v, bf16v2);
  return __builtin_bit_cast(unsigned, r);
}
DI float lo16(unsigned u) { return __uint_as_float(u << 16); }
DI float hi16(unsigned u) { return __uint_as_float(u & 0xffff0000u); }
DI float xhalf_add(float v) {
  auto r = __builtin_amdgcn_permlane32_swap(__float_as_uint(v), __float_as_uint(v), false, false);
  return __uint_as_float(r[0]) + __uint_as_float(r[1]);
}
DI float xhalf_max(float v) {
  auto r = __builtin_amdgcn_permlane32_swap(__float_as_uint(v), __float_as_uint(v), false, false);
  return fmaxf(__uint_as_float(r[0]), __uint_as_float(r[1]));
}

template <int IMM>
DI void gld16(bf16x8& d, unsigned voff, const char* sbase) {
  asm volatile("global_load_dwordx4 %0, %1, %2 offset:%3" : "=v"(d) : "v"(voff), "s"(sbase), "i"(IMM) : "memory");
}
template <int IMM>
DI void glds16(unsigned voff, const char* sbase, unsigned lds) {
  unsigned keep;
  asm volatile("s_mov_b32 %0, m0\n\ts_mov_b32 m0, %3\n\ts_nop 0\n\tglobal_load_lds_dwordx4 %1, %2 offset:%4\n\ts_mov_b32 m0, %0"
               : "=&s"(keep) : "v"(voff), "s"(sbase), "s"(lds), "i"(IMM) : "memory");
}

__global__ __launch_bounds__(256) void prep_kernel(const u16* __restrict__ x, const u16* __restrict__ wqkv, const u16* __restrict__ wo,
                                                   const u16* __restrict__ wg, const u16* __restrict__ wu, const u16* __restrict__ wd,
                                                   char* __restrict__ ws) {
  __shared__ float red[4][32];
  const int b = blockIdx.x, tid = threadIdx.x, w = tid >> 6, lane = tid & 63, r = lane & 31, hh = lane >> 5;
  if (b < 232) {
    const u16* src; int ld; char* dst; float* cs = nullptr; int kind, s0 = 0, nb = 0, ii = 0, nt_ = 0;
    if (b < 48) { kind = 0; nb = b / 3; ii = b % 3; src = wqkv + ii * 512 + nb * 32 + r; ld = 1536; dst = ws + OFF_WQKV; cs = (float*)(ws + OFF_CSQ) + b * 32; }
    else if (b < 64) { kind = 1; nt_ = b - 48; src = wo + nt_ * 32 + r; ld = 512; dst = ws + OFF_WO; }
    else if (b < 176) { kind = 2; int ft = b - 64; nb = ft / 7; ii = ft % 7; src = (r < 16 ? wg : wu) + ft * 16 + (r & 15); ld = 1792; dst = ws + OFF_WGU; cs = (float*)(ws + OFF_CSG) + ft * 32; }
    else { kind = 3; int q = b - 176;   int f0 = q * 32; nt_ = 0; s0 = f0; src = wd + r; ld = 512; dst = ws + OFF_WD; }
    float sum = 0.f;
#pragma unroll 2
    for (int t = 0; t < 8; ++t) {
      int s = w * 8 + t; const u16* sp = src; size_t fidx;
      if (kind == 0) fidx = (size_t)((nb * 8 + (s >> 2)) * 3 + ii) * 4 + (s & 3);
      else if (kind == 1) fidx = (size_t)nt_ * 32 + s;
      else if (kind == 2) fidx = (size_t)((nb * 8 + (s >> 2)) * 7 + ii) * 4 + (s & 3);
      else { int f = s0 + s; int nt = f / KSF; s = f % KSF; sp = src + nt * 32; fidx = f; }
      u16 v[8];
#pragma unroll
      for (int j = 0; j < 8; ++j) v[j] = sp[(size_t)(16 * s + kperm(hh, j)) * ld];
      u32x4 o;
#pragma unroll
      for (int j = 0; j < 4; ++j) { o[j] = (unsigned)v[2 * j] | ((unsigned)v[2 * j + 1] << 16); sum += bf2f(v[2 * j]) + bf2f(v[2 * j + 1]); }
      *(u32x4*)(dst + fidx * 1024 + lane * 16) = o;
    }
    sum = xhalf_add(sum);
    if (lane < 32) red[w][r] = sum;
    __syncthreads();
    if (cs && tid < 32) cs[tid] = (red[0][tid] + red[1][tid]) + (red[2][tid] + red[3][tid]);
  } else {
    const int tt = b - 232;
    const u16* row = x + (size_t)(tt * 32 + r) * D_;
#pragma unroll
    for (int t = 0; t < 4; ++t) {
      const int nblk = w * 4 + t; float s1 = 0.f, s2 = 0.f;
#pragma unroll
      for (int e = 0; e < 2; ++e) {
        const int s = nblk * 2 + e;
        u32x2 a = *(const u32x2*)(row + 16 * s + 4 * hh), c = *(const u32x2*)(row + 16 * s + 8 + 4 * hh);
        u32x4 o = {a[0], a[1], c[0], c[1]};
#pragma unroll
        for (int j = 0; j < 4; ++j) { float p = lo16(o[j]), q = hi16(o[j]); s1 += p + q; s2 += p * p + q * q; }
        *(u32x4*)(ws + OFF_X0 + ((size_t)tt * KSD + s) * 1024 + lane * 16) = o;
      }
      s1 = xhalf_add(s1); s2 = xhalf_add(s2);
      if (lane < 32) { f32x2 st = {s1, s2}; *(f32x2*)(ws + OFF_ST0 + ((size_t)nblk * S_ + tt * 32 + r) * 8) = st; }
    }
  }
}

template <int NT, int NSLOT, int MODE>
__global__ __launch_bounds__(256, 1) void lngemm_kernel(const char* __restrict__ xt, const char* __restrict__ stats, const char* __restrict__ wt,
                                                        const float* __restrict__ cs, char* __restrict__ out0, char* __restrict__ out1,
                                                        char* __restrict__ out2) {
  constexpr int CH = NT * 4096;
  __shared__ __attribute__((aligned(16))) char smem[NSLOT * CH + 1024];
  const int tid = threadIdx.x, lane = tid & 63, r = lane & 31, hh = lane >> 5;
  const int w = __builtin_amdgcn_readfirstlane(tid >> 6);
  const int xcd = blockIdx.x & 7, idx = blockIdx.x >> 3;
  int tb, nb;
  if (MODE == 0) { tb = (xcd & 3) * 4 + (idx & 3); nb = (xcd >> 2) * 8 + (idx >> 2); }
  else { tb = (xcd & 1) * 8 + (idx & 7); nb = (xcd >> 1) * 4 + (idx >> 3); }
  const int tt = tb * 4 + w;
  const unsigned voff = lane * 16;
  const unsigned lds0 = (unsigned)(uintptr_t)smem;
  const char* xb = xt + (size_t)tt * KSD * 1024;
  const char* wb = wt + (size_t)nb * 8 * CH + w * NT * 1024;
  const unsigned ldw = lds0 + w * NT * 1024;

  f32x2 stp[8];
#pragma unroll
  for (int i = 0; i < 8; ++i) stp[i] = *(const f32x2*)(stats + ((size_t)(hh * 8 + i) * S_ + tt * 32 + r) * 8);

  bf16x8 xf[32];
#define ISSUE(c)                                                                                             \
  do {                                                                                                       \
    const char* wsrc_ = wb + (c) * CH; const unsigned wdst_ = ldw + ((c) % NSLOT) * CH;                      \
    glds16<0>(voff, wsrc_, wdst_); glds16<1024>(voff, wsrc_, wdst_); glds16<2048>(voff, wsrc_, wdst_);       \
    if (NT > 3) { glds16<3072>(voff, wsrc_, wdst_); glds16<0>(voff, wsrc_ + 4096, wdst_ + 4096);             \
      glds16<1024>(voff, wsrc_ + 4096, wdst_ + 4096); glds16<2048>(voff, wsrc_ + 4096, wdst_ + 4096); }      \
    const char* xsrc_ = xb + (c) * 4096;                                                                     \
    gld16<0>(xf[4 * (c)], voff, xsrc_); gld16<1024>(xf[4 * (c) + 1], voff, xsrc_);                           \
    gld16<2048>(xf[4 * (c) + 2], voff, xsrc_); gld16<3072>(xf[4 * (c) + 3], voff, xsrc_);                    \
  } while (0)
  asm volatile("s_nop 4" ::: "memory");
#pragma unroll
  for (int c = 0; c < NSLOT; ++c) ISSUE(c);

  f32x16 acc[NT];
#pragma unroll
  for (int i = 0; i < NT; ++i) acc[i] = f32x16{};
  constexpr int PER = NT + 4;
  const LDSP(const char) lp = (LDSP(const char))smem + lane * 16;
#pragma unroll
  for (int c = 0; c < 8; ++c) {
    const int last = (c + NSLOT - 2 > NSLOT - 1 ? c + NSLOT - 2 : NSLOT - 1) > 7 ? 7 : (c + NSLOT - 2 > NSLOT - 1 ? c + NSLOT - 2 : NSLOT - 1);
    const int n = (last - c) * PER;
    asm volatile("s_waitcnt vmcnt(%4) lgkmcnt(0)\n\ts_barrier"
                 : "+v"(xf[4 * c]), "+v"(xf[4 * c + 1]), "+v"(xf[4 * c + 2]), "+v"(xf[4 * c + 3]) : "i"(n) : "memory");
    if (c >= 1 && c + NSLOT - 1 <= 7) ISSUE(c + NSLOT - 1);
#pragma unroll
    for (int u = 0; u < 4; ++u) {
#pragma unroll
      for (int i = 0; i < NT; ++i) {
        const bf16x8 wf = *(const LDSP(bf16x8))(lp + (c % NSLOT) * CH + (i * 4 + u) * 1024);
        if (MODE == 0 && i == 2) acc[i] = MFMA(xf[4 * c + u], wf, acc[i]);
        else acc[i] = MFMA(wf, xf[4 * c + u], acc[i]);
      }
    }
  }
#undef ISSUE
  float s1 = 0.f, s2 = 0.f;
#pragma unroll
  for (int i = 0; i < 8; ++i) { s1 += stp[i][0]; s2 += stp[i][1]; }
  s1 = xhalf_add(s1); s2 = xhalf_add(s2);
  const float mean = s1 * (1.f / D_);
  const float var = fmaxf(s2 * (1.f / D_) - mean * mean, 0.f);
  const float rstd = rsqrtf(var + LN_EPS);
  const float* csb = cs + (size_t)nb * NT * 32;
  if (MODE == 0) {
#pragma unroll
    for (int i = 0; i < 2; ++i) {
      const float sc = i == 0 ? rstd * QSCALE : rstd;
      float v[16];
#pragma unroll
      for (int g = 0; g < 4; ++g) {
        const f32x4 c4 = *(const f32x4*)(csb + i * 32 + 8 * g + 4 * hh);
#pragma unroll
        for (int j = 0; j < 4; ++j) v[4 * g + j] = (acc[i][4 * g + j] - mean * c4[j]) * sc;
      }
      char* o = (i == 0 ? out0 : out1) + ((size_t)(2 * nb) * NTT + tt) * 1024 + lane * 16;
      u32x4 a = {pk2(v[0], v[1]), pk2(v[2], v[3]), pk2(v[4], v[5]), pk2(v[6], v[7])};
      u32x4 b = {pk2(v[8], v[9]), pk2(v[10], v[11]), pk2(v[12], v[13]), pk2(v[14], v[15])};
      *(u32x4*)o = a;
      *(u32x4*)(o + NTT * 1024) = b;
    }
    LDSP(float) sx = (LDSP(float))(smem + NSLOT * CH) + w * 64;
    if (hh == 0) sx[r] = rstd; else sx[32 + r] = mean * rstd;
    asm volatile("s_waitcnt lgkmcnt(0)" ::: "memory");
    const float cv = csb[64 + r];
    float v[16];
#pragma unroll
    for (int g = 0; g < 4; ++g) {
      const f32x4 a4 = *(const LDSP(f32x4))(sx + 8 * g + 4 * hh), b4 = *(const LDSP(f32x4))(sx + 32 + 8 * g + 4 * hh);
#pragma unroll
      for (int j = 0; j < 4; ++j) v[4 * g + j] = acc[2][4 * g + j] * a4[j] - b4[j] * cv;
    }
    char* o = out2 + ((size_t)(2 * nb + (r >> 4)) * NTT + tt) * 1024 + hh * 256 + (r & 15) * 16;
    u32x4 a = {pk2(v[0], v[1]), pk2(v[2], v[3]), pk2(v[4], v[5]), pk2(v[6], v[7])};
    u32x4 b = {pk2(v[8], v[9]), pk2(v[10], v[11]), pk2(v[12], v[13]), pk2(v[14], v[15])};
    *(u32x4*)o = a;
    *(u32x4*)(o + 512) = b;
  } else {
#pragma unroll
    for (int i = 0; i < NT; ++i) {
      float v[16];
#pragma unroll
      for (int g = 0; g < 4; ++g) {
        const f32x4 c4 = *(const f32x4*)(csb + i * 32 + 8 * g + 4 * hh);
#pragma unroll
        for (int j = 0; j < 4; ++j) v[4 * g + j] = (acc[i][4 * g + j] - mean * c4[j]) * rstd;
      }
      float h[8];
#pragma unroll
      for (int j = 0; j < 8; ++j) h[j] = (v[j] > 0.f ? v[j] : 0.f) * v[8 + j];
      u32x4 a = {pk2(h[0], h[1]), pk2(h[2], h[3]), pk2(h[4], h[5]), pk2(h[6], h[7])};
      *(u32x4*)(out0 + ((size_t)tt * KSF + nb * NT + i) * 1024 + lane * 16) = a;
    }
  }
}

__global__ __launch_bounds__(512, 2) void attn_kernel(const char* __restrict__ qf, const char* __restrict__ kf, const char* __restrict__ vf,
                                                      char* __restrict__ of) {
  constexpr int LK = 0, LV = 65536, LO = 131072;
  __shared__ __attribute__((aligned(16))) char smem[LO + 4096];
  const int tid = threadIdx.x, lane = tid & 63, r = lane & 31, hh = lane >> 5;
  const int w = __builtin_amdgcn_readfirstlane(tid >> 6);
  const int xcd = blockIdx.x & 7, idx = blockIdx.x >> 3;
  const int head = xcd * 4 + (idx & 3), qb = idx >> 2;
  const int tt = qb * 8 + w;
  const unsigned voff = lane * 16;
  const unsigned lds0 = (unsigned)(uintptr_t)smem;
  const char* ks = kf + (size_t)head * 65536 + w * 1024;
  const char* vs = vf + (size_t)head * 65536 + w * 1024;
  asm volatile("s_nop 4" ::: "memory");
  bf16x8 q;
  gld16<0>(q, voff, qf + ((size_t)head * NTT + tt) * 1024);
#pragma unroll
  for (int c = 0; c < 8; ++c) {
    glds16<0>(voff, ks + c * 8192, lds0 + LK + c * 8192 + w * 1024);
    glds16<0>(voff, vs + c * 8192, lds0 + LV + c * 8192 + w * 1024);
  }
  { u32x2 ones = {0x3f803f80u, 0x3f803f80u}; *(LDSP(u32x2))((LDSP(char))smem + LO + tid * 8) = ones; }
  asm volatile("s_waitcnt vmcnt(14) lgkmcnt(0)\n\ts_barrier" : "+v"(q) :: "memory");

  const LDSP(const char) kp0 = (LDSP(const char))smem + LK + lane * 16;
  const LDSP(const char) vp0 = (LDSP(const char))smem + (r < 16 ? LV : LO) + hh * 256 + (r & 15) * 16;
  const int vstep = r < 16 ? 4096 : 0;

  f32x16 negm;
  {
    const bf16x8 k0 = *(const LDSP(bf16x8))kp0;
    f32x16 s = MFMA(k0, q, f32x16{});
    float m = fmaxf(fmaxf(s[0], s[1]), s[2]);
#pragma unroll
    for (int i = 3; i < 15; i += 2) m = fmaxf(fmaxf(m, s[i]), s[i + 1]);
    m = fmaxf(m, s[15]);
    m = xhalf_max(m);
#pragma unroll
    for (int i = 0; i < 16; ++i) negm[i] = -m;
  }
  f32x16 o = f32x16{};
  const LDSP(const char) kp = kp0; const LDSP(const char) vp = vp0;
  LDSP(const char) kq = kp; LDSP(const char) vq = vp;
  for (int g = 0; g < 16; ++g) {
    if (g == 2) asm volatile("s_waitcnt vmcnt(0) lgkmcnt(0)\n\ts_barrier" ::: "memory");
#pragma unroll
    for (int t = 0; t < 4; ++t) {
      const bf16x8 k = *(const LDSP(bf16x8))(kq + t * 1024);
      const bf16x8 v0 = *(const LDSP(bf16x8))(vq + t * 1024), v1 = *(const LDSP(bf16x8))(vq + t * 1024 + 512);
      f32x16 s = MFMA(k, q, negm);
#pragma unroll
      for (int i = 0; i < 16; ++i) s[i] = __builtin_amdgcn_exp2f(s[i]);
      u32x4 p0 = {pk2(s[0], s[1]), pk2(s[2], s[3]), pk2(s[4], s[5]), pk2(s[6], s[7])};
      u32x4 p1 = {pk2(s[8], s[9]), pk2(s[10], s[11]), pk2(s[12], s[13]), pk2(s[14], s[15])};
      o = MFMA(v0, __builtin_bit_cast(bf16x8, p0), o);
      o = MFMA(v1, __builtin_bit_cast(bf16x8, p1), o);
    }
    kq += 4096; vq += vstep;
  }
  bool bad = !(o[8] < 1.8446744e19f);
#pragma unroll
  for (int i = 0; i < 8; ++i) bad = bad || !(fabsf(o[i]) < 3.0e38f);
  if (__builtin_expect(__any(bad), 0)) {
    float m = -INFINITY;
    o = f32x16{};
    kq = kp; vq = vp;
    for (int g = 0; g < 16; ++g) {
#pragma unroll 1
      for (int t = 0; t < 4; ++t) {
        const bf16x8 k = *(const LDSP(bf16x8))(kq + t * 1024);
        const bf16x8 v0 = *(const LDSP(bf16x8))(vq + t * 1024), v1 = *(const LDSP(bf16x8))(vq + t * 1024 + 512);
        f32x16 s = MFMA(k, q, f32x16{});
        float tm = s[0];
#pragma unroll
        for (int i = 1; i < 16; ++i) tm = fmaxf(tm, s[i]);
        tm = xhalf_max(tm);
        const float mn = fmaxf(m, tm);
        const float alpha = __builtin_amdgcn_exp2f(m - mn);
        m = mn;
#pragma unroll
        for (int i = 0; i < 16; ++i) { o[i] *= alpha; s[i] = __builtin_amdgcn_exp2f(s[i] - mn); }
        u32x4 p0 = {pk2(s[0], s[1]), pk2(s[2], s[3]), pk2(s[4], s[5]), pk2(s[6], s[7])};
        u32x4 p1 = {pk2(s[8], s[9]), pk2(s[10], s[11]), pk2(s[12], s[13]), pk2(s[14], s[15])};
        o = MFMA(v0, __builtin_bit_cast(bf16x8, p0), o);
        o = MFMA(v1, __builtin_bit_cast(bf16x8, p1), o);
      }
      kq += 4096; vq += vstep;
    }
  }
  const float inv = 1.0f / o[8];
  u32x4 a = {pk2(o[0] * inv, o[1] * inv), pk2(o[2] * inv, o[3] * inv), pk2(o[4] * inv, o[5] * inv), pk2(o[6] * inv, o[7] * inv)};
  *(u32x4*)(of + ((size_t)tt * KSD + head) * 1024 + lane * 16) = a;
}

template <int KS, int WIN, bool FINAL>
__global__ __launch_bounds__(256, 1) void resgemm_kernel(const char* __restrict__ xt, const char* __restrict__ wt, const char* __restrict__ res,
                                                         char* __restrict__ out, char* __restrict__ stats) {
  __shared__ __attribute__((aligned(16))) char smem[65536];
  constexpr int KW = KS / 4;
  const int tid = threadIdx.x, lane = tid & 63, r = lane & 31, hh = lane >> 5;
  const int w = __builtin_amdgcn_readfirstlane(tid >> 6);
  const int xcd = blockIdx.x & 7, idx = blockIdx.x >> 3;
  const int tb = xcd * 4 + (idx & 3), nb = idx >> 2;
  const char* xp = xt + ((size_t)(tb * 2) * KS + w * KW) * 1024 + lane * 16;
  const char* wp = wt + ((size_t)(nb * 2) * KS + w * KW) * 1024 + lane * 16;
  const int ont = nb * 2 + (w >> 1), ott = tb * 2 + (w & 1);
  const u32x4 r0 = *(const u32x4*)(res + ((size_t)ott * KSD + 2 * ont) * 1024 + lane * 16);
  const u32x4 r1 = *(const u32x4*)(res + ((size_t)ott * KSD + 2 * ont + 1) * 1024 + lane * 16);
  bf16x8 x0[WIN], x1[WIN], w0[WIN], w1[WIN];
#pragma unroll
  for (int s = 0; s < WIN; ++s) {
    w0[s] = *(const bf16x8*)(wp + s * 1024); x0[s] = *(const bf16x8*)(xp + s * 1024);
    w1[s] = *(const bf16x8*)(wp + (KS + s) * 1024); x1[s] = *(const bf16x8*)(xp + (KS + s) * 1024);
  }
  f32x16 acc[4];
#pragma unroll
  for (int i = 0; i < 4; ++i) acc[i] = f32x16{};
#pragma unroll
  for (int s = 0; s < KW; ++s) {
    const int b = s % WIN;
    acc[0] = MFMA(w0[b], x0[b], acc[0]);
    acc[1] = MFMA(w0[b], x1[b], acc[1]);
    acc[2] = MFMA(w1[b], x0[b], acc[2]);
    acc[3] = MFMA(w1[b], x1[b], acc[3]);
    if (s + WIN < KW) {
      const int sn = s + WIN;
      w0[b] = *(const bf16x8*)(wp + sn * 1024); x0[b] = *(const bf16x8*)(xp + sn * 1024);
      w1[b] = *(const bf16x8*)(wp + (KS + sn) * 1024); x1[b] = *(const bf16x8*)(xp + (KS + sn) * 1024);
    }
  }
  LDSP(char) sp = (LDSP(char))smem + lane * 16;
#pragma unroll
  for (int ow = 0; ow < 4; ++ow)
#pragma unroll
    for (int g = 0; g < 4; ++g) {
      f32x4 v = {acc[ow][4 * g], acc[ow][4 * g + 1], acc[ow][4 * g + 2], acc[ow][4 * g + 3]};
      *(LDSP(f32x4))(sp + ((ow * 4 + w) * 4 + g) * 1024) = v;
    }
  __syncthreads();
  float v[16];
#pragma unroll
  for (int g = 0; g < 4; ++g) {
    f32x4 a = *(const LDSP(f32x4))(sp + ((w * 4 + 0) * 4 + g) * 1024), b = *(const LDSP(f32x4))(sp + ((w * 4 + 1) * 4 + g) * 1024);
    f32x4 c = *(const LDSP(f32x4))(sp + ((w * 4 + 2) * 4 + g) * 1024), d = *(const LDSP(f32x4))(sp + ((w * 4 + 3) * 4 + g) * 1024);
#pragma unroll
    for (int j = 0; j < 4; ++j) v[4 * g + j] = (a[j] + b[j]) + (c[j] + d[j]);
  }
  u32x4 p0, p1;
#pragma unroll
  for (int j = 0; j < 4; ++j) {
    p0[j] = pk2(v[2 * j] + lo16(r0[j]), v[2 * j + 1] + hi16(r0[j]));
    p1[j] = pk2(v[8 + 2 * j] + lo16(r1[j]), v[8 + 2 * j + 1] + hi16(r1[j]));
  }
  if (!FINAL) {
    *(u32x4*)(out + ((size_t)ott * KSD + 2 * ont) * 1024 + lane * 16) = p0;
    *(u32x4*)(out + ((size_t)ott * KSD + 2 * ont + 1) * 1024 + lane * 16) = p1;
    float s1 = 0.f, s2 = 0.f;
#pragma unroll
    for (int j = 0; j < 4; ++j) {
      float a = lo16(p0[j]), b = hi16(p0[j]), c = lo16(p1[j]), d = hi16(p1[j]);
      s1 += (a + b) + (c + d); s2 += (a * a + b * b) + (c * c + d * d);
    }
    s1 = xhalf_add(s1); s2 = xhalf_add(s2);
    if (lane < 32) { f32x2 st = {s1, s2}; *(f32x2*)(stats + ((size_t)ont * S_ + ott * 32 + r) * 8) = st; }
  } else {
    auto s0 = __builtin_amdgcn_permlane32_swap(p0[0], p0[2], false, false);
    auto s1 = __builtin_amdgcn_permlane32_swap(p0[1], p0[3], false, false);
    auto s2 = __builtin_amdgcn_permlane32_swap(p1[0], p1[2], false, false);
    auto s3 = __builtin_amdgcn_permlane32_swap(p1[1], p1[3], false, false);
    u32x4 a = {s0[0], s1[0], s0[1], s1[1]}, b = {s2[0], s3[0], s2[1], s3[1]};
    char* o = out + ((size_t)(ott * 32 + r) * D_ + ont * 32) * 2 + hh * 16;
    *(u32x4*)o = a;
    *(u32x4*)(o + 32) = b;
  }
}

extern "C" void kernel_launch(void* const* d_in, const int* in_sizes, int n_in, void* d_out, int out_size, void* d_ws, size_t ws_size,
                              hipStream_t stream) {
  const u16* x = (const u16*)d_in[0];
  const u16* wqkv = (const u16*)d_in[1];
  const u16* wo = (const u16*)d_in[2];
  const u16* wg = (const u16*)d_in[4];
  const u16* wu = (const u16*)d_in[5];
  const u16* wd = (const u16*)d_in[6];
  char* ws = (char*)d_ws;
  prep_kernel<<<296, 256, 0, stream>>>(x, wqkv, wo, wg, wu, wd, ws);
  for (int layer = 0; layer < 2; ++layer) {
    const char* xin = ws + (layer == 0 ? OFF_X0 : OFF_X3);
    const char* stin = ws + (layer == 0 ? OFF_ST0 : OFF_ST3);
    lngemm_kernel<3, 8, 0><<<256, 256, 0, stream>>>(xin, stin, ws + OFF_WQKV, (const float*)(ws + OFF_CSQ), ws + OFF_Q, ws + OFF_K, ws + OFF_V);
    attn_kernel<<<256, 512, 0, stream>>>(ws + OFF_Q, ws + OFF_K, ws + OFF_V, ws + OFF_O);
    resgemm_kernel<32, 8, false><<<256, 256, 0, stream>>>(ws + OFF_O, ws + OFF_WO, xin, ws + OFF_X2, ws + OFF_ST2);
    lngemm_kernel<7, 5, 1><<<256, 256, 0, stream>>>(ws + OFF_X2, ws + OFF_ST2, ws + OFF_WGU, (const float*)(ws + OFF_CSG), ws + OFF_H, nullptr, nullptr);
    if (layer == 0)
      resgemm_kernel<112, 14, false><<<256, 256, 0, stream>>>(ws + OFF_H, ws + OFF_WD, ws + OFF_X2, ws + OFF_X3, ws + OFF_ST3);
    else
      resgemm_kernel<112, 14, true><<<256, 256, 0, stream>>>(ws + OFF_H, ws + OFF_WD, ws + OFF_X2, (char*)d_out, nullptr);
  }
}
```

```cpp
#include <hip/hip_runtime.h>
#include <hip/hip_bf16.h>
#include <stdint.h>

typedef short bf16x8 __attribute__((ext_vector_type(8)));
typedef float f32x16 __attribute__((ext_vector_type(16)));
typedef float f32x4 __attribute__((ext_vector_type(4)));
typedef float f32x2 __attribute__((ext_vector_type(2)));
typedef unsigned u32x4 __attribute__((ext_vector_type(4)));
typedef unsigned u32x2 __attribute__((ext_vector_type(2)));
typedef __bf16 bf16v2 __attribute__((ext_vector_type(2)));
typedef unsigned short u16;

#define STAMP(i)
#define STAMP_DECL
#define STAMP_STORE()
#define DIAG_ARG
#define DIAG_PASS(k)
#define DI __device__ __forceinline__
#define MFMA(a, b, c) __builtin_amdgcn_mfma_f32_32x32x16_bf16((a), (b), (c), 0, 0, 0)
#define LDSP(T) __attribute__((address_space(3))) T*

constexpr int S_ = 2048, D_ = 512, H_ = 32, F_ = 1792;
constexpr int NTT = S_ / 32;
constexpr int KSD = D_ / 16;
constexpr int KSF = F_ / 16;
constexpr float QSCALE = 0.25f * 1.4426950408889634f;
constexpr float LN_EPS = 1e-5f;

constexpr size_t KB = 1024;
constexpr size_t OFF_WO = 0;
constexpr size_t OFF_WGU = OFF_WO + 512 * KB;
constexpr size_t OFF_WD = OFF_WGU + 3584 * KB;
constexpr size_t OFF_CSQ = OFF_WD + 1792 * KB;
constexpr size_t OFF_CSG = OFF_CSQ + 8 * KB;
constexpr size_t OFF_CSP = OFF_CSG + 16 * KB;
constexpr size_t OFF_X2 = OFF_CSP + 256 * KB;
constexpr size_t OFF_X3 = OFF_X2 + 2048 * KB;
constexpr size_t OFF_ST2 = OFF_X3 + 2048 * KB;
constexpr size_t OFF_ST3 = OFF_ST2 + 256 * KB;
constexpr size_t OFF_Q = OFF_ST3 + 256 * KB;
constexpr size_t OFF_K = OFF_Q + 2048 * KB;
constexpr size_t OFF_V = OFF_K + 2048 * KB;
constexpr size_t OFF_O = OFF_V + 2048 * KB;
constexpr size_t OFF_H = OFF_O + 2048 * KB;
constexpr size_t WS_END = OFF_H + 7168 * KB;

template <int I> struct IC { static constexpr int value = I; };
template <int B, int E, class F> DI void sfor(F&& f) { if constexpr (B < E) { f(IC<B>{}); sfor<B + 1, E>(f); } }
DI int kperm(int hh, int j) { return 8 * (j >> 2) + 4 * hh + (j & 3); }
DI float bf2f(u16 v) { return __uint_as_float((unsigned)v << 16); }
DI unsigned pk2(float lo, float hi) {
  f32x2 v = {lo, hi};
  bf16v2 r = __builtin_convertvector(v, bf16v2);
  return __builtin_bit_cast(unsigned, r);
}
DI float lo16(unsigned u) { return __uint_as_float(u << 16); }
DI float hi16(unsigned u) { return __uint_as_float(u & 0xffff0000u); }
DI float xhalf_add(float v) {
  auto r = __builtin_amdgcn_permlane32_swap(__float_as_uint(v), __float_as_uint(v), false, false);
  return __uint_as_float(r[0]) + __uint_as_float(r[1]);
}
DI float xhalf_max(float v) {
  auto r = __builtin_amdgcn_permlane32_swap(__float_as_uint(v), __float_as_uint(v), false, false);
  return fmaxf(__uint_as_float(r[0]), __uint_as_float(r[1]));
}

#define QLOOK 3
#define STF 1
DI void st16(void* p, u32x4 v) {
  asm volatile("global_store_dwordx4 %0, %1, off sc1\n\ts_nop 1" :: "v"(p), "v"(v) : "memory");
}
template <int IMM>
DI void gld16(bf16x8& d, unsigned voff, const char* sbase) {
  asm volatile("global_load_dwordx4 %0, %1, %2 offset:%3" : "=v"(d) : "v"(voff), "s"(sbase), "i"(IMM) : "memory");
}
template <int IMM>
DI void glds16(unsigned voff, const char* sbase, unsigned lds) {
  unsigned keep;
  asm volatile("s_mov_b32 %0, m0\n\ts_mov_b32 m0, %3\n\ts_nop 0\n\tglobal_load_lds_dwordx4 %1, %2 offset:%4\n\ts_mov_b32 m0, %0"
               : "=&s"(keep) : "v"(voff), "s"(sbase), "s"(lds), "i"(IMM) : "memory");
}

#define SBAR() __builtin_amdgcn_sched_barrier(0)
#define PIN(x) asm volatile("" : "+v"(x))
typedef short s16x4 __attribute__((ext_vector_type(4)));
DI s16x4 trread(LDSP(const char) p) { return __builtin_amdgcn_ds_read_tr16_b64_v4i16((LDSP(s16x4))p); }
DI bf16x8 trfrag(LDSP(const char) p, int off2) { const s16x4 lo = trread(p), hi = trread(p + off2); return __builtin_shufflevector(lo, hi, 0, 1, 2, 3, 4, 5, 6, 7); }
DI float dot2(unsigned a, unsigned b, float c) { return __builtin_amdgcn_fdot2_f32_bf16(__builtin_bit_cast(bf16v2, a), __builtin_bit_cast(bf16v2, b), c, false); }
constexpr unsigned ONE2 = 0x3f803f80u;

template <bool FIRST, int LOOK>
__global__ __launch_bounds__(512, 2) void qkv_kernel(const char* __restrict__ xt, const char* __restrict__ stats, const char* __restrict__ wn,
                                                     float* __restrict__ cs, char* __restrict__ outq, char* __restrict__ outk,
                                                     char* __restrict__ outv DIAG_ARG) {
  STAMP_DECL; STAMP(0);
  constexpr int NSLOT = 5, WCH = 12288, CH = WCH + 16384;
  constexpr int LCS = NSLOT * CH;
  constexpr int LST = LCS + 2048;
  __shared__ __attribute__((aligned(16))) char smem[LST + 2048];
  const int tid = threadIdx.x, lane = tid & 63, r = lane & 31, hh = lane >> 5;
  const int w = __builtin_amdgcn_readfirstlane(tid >> 6);
  const int xcd = blockIdx.x & 7, idx = blockIdx.x >> 3;
  const int tb = (xcd & 3) * 4 + (idx & 3), nb = (xcd >> 2) * 8 + (idx >> 2);
  const unsigned lds0 = (unsigned)(uintptr_t)smem;
  const int j = w & 3, tt = tb * 4 + j;
  const int xoff = FIRST ? WCH + j * 4096 + r * 128 : WCH + j * 4096 + lane * 16;
  const int xsw = (r >> 1) & 7;
  const int woff = ((FIRST ? 8 * hh : 4 * hh) + ((lane & 15) >> 2)) * 64 + ((lane >> 4) & 1) * 32 + (lane & 3) * 8;
  constexpr int WHI = FIRST ? 256 : 512;
  LDSP(const char) lp = (LDSP(const char))smem;
#define XFRAG(slot, u) (*(LDSP(const bf16x8))(lp + (slot) * CH + xoff + (FIRST ? (((2 * (u) + hh) ^ xsw) * 16) : (u) * 1024)))
#define WFRAG(slot, seg, u) trfrag(lp + (slot) * CH + (seg) * 4096 + (u) * 1024 + woff, WHI)
  if (w >= 4) {
    const unsigned wv = (lane >> 2) * 3072 + (lane & 3) * 16;
    const char* wb = wn + (size_t)j * 16 * 3072 + nb * 64;
    const unsigned ldw = lds0 + j * 1024, ldx = lds0 + WCH + j * 4096;
    const unsigned xv0 = FIRST ? (lane >> 3) * 1024 + (((lane & 7) ^ (lane >> 4)) * 16) : lane * 16;
    const unsigned xv1 = FIRST ? (lane >> 3) * 1024 + (((lane & 7) ^ ((lane >> 4) + 4)) * 16) : lane * 16;
    const char* xb = xt + (size_t)tt * 32768;
    constexpr int XCS = FIRST ? 128 : 4096, XIS = FIRST ? 8192 : 1024;
#define ISSUE(c)                                                                                             \
  do {                                                                                                       \
    const char* ws_ = wb + (size_t)(c) * 64 * 3072; const unsigned wd_ = ldw + ((c) % NSLOT) * CH;           \
    const char* xs_ = xb + (c) * XCS; const unsigned xd_ = ldx + ((c) % NSLOT) * CH;                         \
    glds16<0>(xv0, xs_, xd_); glds16<0>(wv, ws_, wd_);                                                       \
    glds16<0>(xv1, xs_ + XIS, xd_ + 1024); glds16<0>(wv, ws_ + 1024, wd_ + 4096);                            \
    glds16<0>(xv0, xs_ + 2 * XIS, xd_ + 2048); glds16<0>(wv, ws_ + 2048, wd_ + 8192);                        \
    glds16<0>(xv1, xs_ + 3 * XIS, xd_ + 3072);                                                               \
  } while (0)
    asm volatile("s_nop 4" ::: "memory");
#pragma unroll
    for (int c = 0; c < LOOK; ++c) ISSUE(c);
    float s1 = 0.f, s2 = 0.f, c0 = 0.f, c1 = 0.f, c2 = 0.f;
#pragma unroll
    for (int c = 0; c < 8; ++c) {
      const int last = c + LOOK - 1 > 7 ? 7 : c + LOOK - 1;
      asm volatile("s_waitcnt vmcnt(%0) lgkmcnt(0)\n\ts_barrier" :: "i"((last - c) * 7) : "memory");
      if (c + LOOK <= 7) ISSUE(c + LOOK);
      if (FIRST) {
#pragma unroll
        for (int u = 0; u < 4; ++u) {
          const u32x4 x = __builtin_bit_cast(u32x4, XFRAG(c % NSLOT, u));
#pragma unroll
          for (int e = 0; e < 4; ++e) { s1 = dot2(x[e], ONE2, s1); s2 = dot2(x[e], x[e], s2); }
        }
        LDSP(const char) wq = lp + j * 1024;
        const u32x4 a = __builtin_bit_cast(u32x4, trfrag(wq + (c % NSLOT) * CH + woff, WHI));
        const u32x4 b = __builtin_bit_cast(u32x4, trfrag(wq + (c % NSLOT) * CH + 4096 + woff, WHI));
        const u32x4 d = __builtin_bit_cast(u32x4, trfrag(wq + (c % NSLOT) * CH + 8192 + woff, WHI));
#pragma unroll
        for (int e = 0; e < 4; ++e) { c0 = dot2(a[e], ONE2, c0); c1 = dot2(b[e], ONE2, c1); c2 = dot2(d[e], ONE2, c2); }
      }
    }
#undef ISSUE
    if (FIRST) {
      s1 = xhalf_add(s1); s2 = xhalf_add(s2); c0 = xhalf_add(c0); c1 = xhalf_add(c1); c2 = xhalf_add(c2);
      const float mean = s1 * (1.f / D_);
      const float rstd = rsqrtf(fmaxf(s2 * (1.f / D_) - mean * mean, 0.f) + LN_EPS);
      LDSP(float) sx = (LDSP(float))((LDSP(char))smem + LST) + j * 128;
      LDSP(float) cx = (LDSP(float))((LDSP(char))smem + LCS) + j * 96;
      if (hh == 0) { sx[r] = rstd; sx[64 + r] = mean; cx[r] = c0; cx[32 + r] = c1; } else { sx[32 + r] = mean * rstd; cx[64 + r] = c2; }
      asm volatile("s_waitcnt lgkmcnt(0)\n\ts_barrier" ::: "memory");
    }
    return;
  }
  LDSP(float) sx = (LDSP(float))((LDSP(char))smem + LST) + w * 128;
  LDSP(float) cx = (LDSP(float))((LDSP(char))smem + LCS);
  float mean, rstd;
  if (!FIRST) {
    f32x2 stp[8];
#pragma unroll
    for (int i = 0; i < 8; ++i) stp[i] = *(const f32x2*)(stats + ((size_t)(hh * 8 + i) * S_ + tt * 32 + r) * 8);
    if (tid < 96) cx[tid] = cs[nb * 96 + tid];
    float s1 = 0.f, s2 = 0.f;
#pragma unroll
    for (int i = 0; i < 8; ++i) { s1 += stp[i][0]; s2 += stp[i][1]; }
    s1 = xhalf_add(s1); s2 = xhalf_add(s2);
    mean = s1 * (1.f / D_);
    rstd = rsqrtf(fmaxf(s2 * (1.f / D_) - mean * mean, 0.f) + LN_EPS);
    if (hh == 0) sx[r] = rstd; else sx[32 + r] = mean * rstd;
  }
  f32x16 acc[3];
#pragma unroll
  for (int i = 0; i < 3; ++i) acc[i] = f32x16{};
  bf16x8 wf[2][3], xf[2];
  STAMP(1);
#pragma unroll
  for (int s = 0; s <= 32; ++s) {
    const int c = s >> 2, u = s & 3, cur = s & 1, prv = cur ^ 1;
    if (s < 32 && u == 0) {
      asm volatile("s_waitcnt lgkmcnt(0)\n\ts_barrier" ::: "memory");
      if (c == 0) STAMP(2);
      if (c == 4) STAMP(3);
    }
    SBAR();
    if (s < 32) { xf[cur] = XFRAG(c % NSLOT, u); wf[cur][0] = WFRAG(c % NSLOT, 0, u); }
    SBAR();
    if (s > 0) acc[0] = MFMA(wf[prv][0], xf[prv], acc[0]);
    SBAR();
    if (s < 32) { wf[cur][1] = WFRAG(c % NSLOT, 1, u); wf[cur][2] = WFRAG(c % NSLOT, 2, u); }
    SBAR();
    if (s > 0) { acc[1] = MFMA(wf[prv][1], xf[prv], acc[1]); acc[2] = MFMA(xf[prv], wf[prv][2], acc[2]); }
    SBAR();
  }
#undef XFRAG
#undef WFRAG
  STAMP(4);
  if (FIRST) {
    asm volatile("s_barrier" ::: "memory");
    rstd = sx[r]; mean = sx[64 + r];
    if (tb == 0 && tid < 96) cs[nb * 96 + tid] = (cx[tid] + cx[96 + tid]) + (cx[192 + tid] + cx[288 + tid]);
  }
#define CS4(o) (FIRST ? (*(LDSP(const f32x4))(cx + (o)) + *(LDSP(const f32x4))(cx + 96 + (o))) + (*(LDSP(const f32x4))(cx + 192 + (o)) + *(LDSP(const f32x4))(cx + 288 + (o))) \
                      : *(LDSP(const f32x4))(cx + (o)))
#pragma unroll
  for (int i = 0; i < 2; ++i) {
    const float sc = i == 0 ? rstd * QSCALE : rstd;
    float v[16];
#pragma unroll
    for (int g = 0; g < 4; ++g) {
      const f32x4 c4 = CS4(i * 32 + 8 * g + 4 * hh);
#pragma unroll
      for (int e = 0; e < 4; ++e) v[4 * g + e] = (acc[i][4 * g + e] - mean * c4[e]) * sc;
    }
    char* o = (i == 0 ? outq : outk) + ((size_t)(2 * nb) * NTT + tt) * 1024 + lane * 16;
    u32x4 a = {pk2(v[0], v[1]), pk2(v[2], v[3]), pk2(v[4], v[5]), pk2(v[6], v[7])};
    u32x4 b = {pk2(v[8], v[9]), pk2(v[10], v[11]), pk2(v[12], v[13]), pk2(v[14], v[15])};
    st16(o, a);
    st16(o + NTT * 1024, b);
  }
#undef CS4
  {
    const float cv = FIRST ? (cx[64 + r] + cx[160 + r]) + (cx[256 + r] + cx[352 + r]) : cx[64 + r];
    float v[16];
#pragma unroll
    for (int g = 0; g < 4; ++g) {
      const f32x4 a4 = *(LDSP(const f32x4))(sx + 8 * g + 4 * hh), b4 = *(LDSP(const f32x4))(sx + 32 + 8 * g + 4 * hh);
#pragma unroll
      for (int e = 0; e < 4; ++e) v[4 * g + e] = acc[2][4 * g + e] * a4[e] - b4[e] * cv;
    }
    char* o = outv + ((size_t)(2 * nb + (r >> 4)) * NTT + tt) * 1024 + hh * 256 + (r & 15) * 16;
    u32x4 a = {pk2(v[0], v[1]), pk2(v[2], v[3]), pk2(v[4], v[5]), pk2(v[6], v[7])};
    u32x4 b = {pk2(v[8], v[9]), pk2(v[10], v[11]), pk2(v[12], v[13]), pk2(v[14], v[15])};
    st16(o, a);
    st16(o + 512, b);
  }
  STAMP(5); STAMP_STORE();
}

DI float x16_add(float v) {
  auto r = __builtin_amdgcn_permlane16_swap(__float_as_uint(v), __float_as_uint(v), false, false);
  return __uint_as_float(r[0]) + __uint_as_float(r[1]);
}
__global__ __launch_bounds__(512, 2) void gu_kernel(const char* __restrict__ xt, const char* __restrict__ stats, const char* __restrict__ wt,
                                                    const float* __restrict__ cs, char* __restrict__ outh DIAG_ARG) {
  STAMP_DECL; STAMP(0);
  constexpr int NSLOT = 3, LOOK = 2, WCH = 28672, CH = WCH + 16384;
  __shared__ __attribute__((aligned(16))) char smem[NSLOT * CH];
  const int tid = threadIdx.x, lane = tid & 63;
  const int w = __builtin_amdgcn_readfirstlane(tid >> 6);
  const int xcd = blockIdx.x & 7, idx = blockIdx.x >> 3;
  const int tb = (xcd & 1) * 8 + (idx & 7), nb = (xcd >> 1) * 4 + (idx >> 3);
  const unsigned voff = lane * 16;
  const unsigned lds0 = (unsigned)(uintptr_t)smem;
  if (w >= 4) {
    const int j = w - 4;
    const char* xb = xt + (size_t)(tb * 4 + j) * 32768;
    const char* wb = wt + (size_t)nb * 8 * WCH + j * 7168;
    const unsigned ldw = lds0 + j * 7168, ldx = lds0 + WCH + j * 4096;
#define ISSUE(c)                                                                                             \
  do {                                                                                                       \
    const char* ws_ = wb + (c) * WCH; const unsigned wd_ = ldw + ((c) % NSLOT) * CH;                         \
    const char* xs_ = xb + (c) * 4096; const unsigned xd_ = ldx + ((c) % NSLOT) * CH;                        \
    glds16<0>(voff, xs_, xd_); glds16<0>(voff, ws_, wd_);                                                    \
    glds16<1024>(voff, xs_, xd_); glds16<1024>(voff, ws_, wd_);                                              \
    glds16<2048>(voff, xs_, xd_); glds16<2048>(voff, ws_, wd_);                                              \
    glds16<3072>(voff, xs_, xd_); glds16<3072>(voff, ws_, wd_);                                              \
    glds16<0>(voff, ws_ + 4096, wd_ + 4096); glds16<1024>(voff, ws_ + 4096, wd_ + 4096);                     \
    glds16<2048>(voff, ws_ + 4096, wd_ + 4096);                                                              \
  } while (0)
    asm volatile("s_nop 4" ::: "memory");
#pragma unroll
    for (int c = 0; c < LOOK; ++c) ISSUE(c);
#pragma unroll
    for (int c = 0; c < 8; ++c) {
      const int last = c + LOOK - 1 > 7 ? 7 : c + LOOK - 1;
      asm volatile("s_waitcnt vmcnt(%0)\n\ts_barrier" :: "i"((last - c) * 11) : "memory");
      if (c + LOOK <= 7) ISSUE(c + LOOK);
    }
#undef ISSUE
    return;
  }
  const int tt = tb * 4 + w;
  const int g4 = lane >> 4, c16 = lane & 15;
  f32x4 ac[2][14];
  float rs0, rs1;
  {
    f32x2 sa[4], sb[4];
#pragma unroll
    for (int i = 0; i < 4; ++i) {
      sa[i] = *(const f32x2*)(stats + ((size_t)(g4 * 4 + i) * S_ + tt * 32 + c16) * 8);
      sb[i] = *(const f32x2*)(stats + ((size_t)(g4 * 4 + i) * S_ + tt * 32 + 16 + c16) * 8);
    }
    f32x4 c4[14];
#pragma unroll
    for (int i = 0; i < 14; ++i) c4[i] = *(const f32x4*)(cs + nb * 224 + i * 16 + 4 * g4);
    float a1 = (sa[0][0] + sa[1][0]) + (sa[2][0] + sa[3][0]), a2 = (sa[0][1] + sa[1][1]) + (sa[2][1] + sa[3][1]);
    float b1 = (sb[0][0] + sb[1][0]) + (sb[2][0] + sb[3][0]), b2 = (sb[0][1] + sb[1][1]) + (sb[2][1] + sb[3][1]);
    a1 = xhalf_add(x16_add(a1)); a2 = xhalf_add(x16_add(a2)); b1 = xhalf_add(x16_add(b1)); b2 = xhalf_add(x16_add(b2));
    const float m0 = a1 * (1.f / D_), m1 = b1 * (1.f / D_);
    rs0 = 1.f / (fmaxf(a2 * (1.f / D_) - m0 * m0, 0.f) + LN_EPS);
    rs1 = 1.f / (fmaxf(b2 * (1.f / D_) - m1 * m1, 0.f) + LN_EPS);
#pragma unroll
    for (int i = 0; i < 14; ++i) { ac[0][i] = c4[i] * -m0; ac[1][i] = c4[i] * -m1; }
  }
  LDSP(const char) lpw = (LDSP(const char))smem + lane * 16;
  LDSP(const char) lpx = (LDSP(const char))smem + WCH + w * 4096 + (g4 >> 1) * 1024 + ((g4 & 1) * 32 + c16) * 16;
  bf16x8 wf[2][7], xf[2][2];
  STAMP(1);
#pragma unroll
  for (int q = 0; q <= 32; ++q) {
    const int s = q >> 1, hf = q & 1, c = q >> 2, u = s & 1, cur = q & 1, prv = cur ^ 1, ps = (q - 1) >> 1;
    if (q < 32 && (q & 3) == 0) {
      asm volatile("s_waitcnt lgkmcnt(0)\n\ts_barrier" ::: "memory");
      if (c == 0) STAMP(2);
      if (c == 4) STAMP(3);
    }
    SBAR();
    if (q < 32 && hf == 0) { xf[s & 1][0] = *(LDSP(const bf16x8))(lpx + (c % NSLOT) * CH + u * 2048); xf[s & 1][1] = *(LDSP(const bf16x8))(lpx + (c % NSLOT) * CH + u * 2048 + 256); }
#pragma unroll
    for (int i = 0; i < 7; ++i) {
      if (q < 32) {
        if (2 * i < 7) wf[cur][2 * i] = *(LDSP(const bf16x8))(lpw + (c % NSLOT) * CH + ((7 * hf + 2 * i) * 2 + u) * 1024);
        if (2 * i + 1 < 7) wf[cur][2 * i + 1] = *(LDSP(const bf16x8))(lpw + (c % NSLOT) * CH + ((7 * hf + 2 * i + 1) * 2 + u) * 1024);
      }
      SBAR();
      if (q > 0) {
        ac[0][7 * prv + i] = __builtin_amdgcn_mfma_f32_16x16x32_bf16(wf[prv][i], xf[ps & 1][0], ac[0][7 * prv + i], 0, 0, 0);
        ac[1][7 * prv + i] = __builtin_amdgcn_mfma_f32_16x16x32_bf16(wf[prv][i], xf[ps & 1][1], ac[1][7 * prv + i], 0, 0, 0);
      }
      SBAR();
    }
  }
  STAMP(4);
  char* ob = outh + ((size_t)tt * KSF + nb * 7) * 1024 + (((g4 & 1) * 32) + (g4 >> 1) * 16 + c16) * 16;
#pragma unroll
  for (int i = 0; i < 7; ++i) {
    float h0[4], h1[4];
#pragma unroll
    for (int e = 0; e < 4; ++e) {
      h0[e] = fmaxf(ac[0][2 * i][e], 0.f) * (ac[0][2 * i + 1][e] * rs0);
      h1[e] = fmaxf(ac[1][2 * i][e], 0.f) * (ac[1][2 * i + 1][e] * rs1);
    }
    auto e0 = __builtin_amdgcn_permlane32_swap(pk2(h0[0], h0[1]), pk2(h1[0], h1[1]), false, false);
    auto e1 = __builtin_amdgcn_permlane32_swap(pk2(h0[2], h0[3]), pk2(h1[2], h1[3]), false, false);
    u32x4 a = {e0[0], e1[0], e0[1], e1[1]};
    st16(ob + i * 1024, a);
  }
  STAMP(5); STAMP_STORE();
}

template <bool PREP>
__global__ __launch_bounds__(1024, 4) void attn_kernel(const char* __restrict__ qf, const char* __restrict__ kf, const char* __restrict__ vf,
                                                       char* __restrict__ of, const u16* __restrict__ wo, const u16* __restrict__ wg,
                                                       const u16* __restrict__ wu, const u16* __restrict__ wd, char* __restrict__ ws DIAG_ARG) {
  STAMP_DECL; STAMP(0);
  constexpr int LK = 0, LV = 65536, LO = 131072, LX = LO + 4096;
  __shared__ __attribute__((aligned(16))) char smem[LX + 24576];
  const int tid = threadIdx.x, lane = tid & 63, r = lane & 31, hh = lane >> 5;
  const int w = __builtin_amdgcn_readfirstlane(tid >> 6);
  const int wq = w & 7, half = w >> 3;
  const int xcd = blockIdx.x & 7, idx = blockIdx.x >> 3;
  const int head = xcd * 4 + (idx & 3), qb = idx >> 2;
  const int tt = qb * 8 + wq;
  const unsigned voff = lane * 16;
  const unsigned lds0 = (unsigned)(uintptr_t)smem;
  const char* ks = kf + (size_t)head * 65536 + half * 32768 + wq * 1024;
  const char* vs = vf + (size_t)head * 65536 + half * 32768 + wq * 1024;
  const unsigned kd = lds0 + LK + half * 32768 + wq * 1024, vd = lds0 + LV + half * 32768 + wq * 1024;
  asm volatile("s_nop 4" ::: "memory");
  bf16x8 q;
  gld16<0>(q, voff, qf + ((size_t)head * NTT + tt) * 1024);
  glds16<0>(voff, ks, kd);
  glds16<0>(voff, vs, vd);
  { const unsigned one = ((tid >> 2) & 11) == 0 ? 0x3f803f80u : 0u;
    *(LDSP(unsigned))((LDSP(char))smem + LO + tid * 4) = one; }
  STAMP(1);
  asm volatile("s_waitcnt vmcnt(0) lgkmcnt(0)\n\ts_barrier" : "+v"(q) :: "memory");
  STAMP(2);
#pragma unroll
  for (int c = 1; c < 4; ++c) {
    glds16<0>(voff, ks + c * 8192, kd + c * 8192);
    glds16<0>(voff, vs + c * 8192, vd + c * 8192);
  }

  LDSP(const char) kp = (LDSP(const char))smem + LK + lane * 16;
  LDSP(const char) vp = (LDSP(const char))smem + (r < 16 ? LV : LO) + hh * 256 + (r & 15) * 16;
  const int vstep = r < 16 ? 4096 : 0;

  f32x16 negm, sA, sB;
  bf16x8 kA, kB;
  LDSP(const char) kq = kp + half * 32768; LDSP(const char) vq = vp + half * 8 * vstep;
  {
    const bf16x8 k0 = *(LDSP(const bf16x8))kp;
    const bf16x8 k1 = *(LDSP(const bf16x8))kq;
    kA = *(LDSP(const bf16x8))(kq + 1024);
    f32x16 s = MFMA(k0, q, f32x16{});
    float m = fmaxf(fmaxf(s[0], s[1]), s[2]);
#pragma unroll
    for (int i = 3; i < 15; i += 2) m = fmaxf(fmaxf(m, s[i]), s[i + 1]);
    m = fmaxf(m, s[15]);
    m = xhalf_max(m);
#pragma unroll
    for (int i = 0; i < 16; ++i) negm[i] = -m;
    PIN(negm);
    sA = MFMA(k1, q, negm);
  }
  f32x16 o = f32x16{};
#define EX(S, i) S[i] = __builtin_amdgcn_exp2f(S[i])
#define STEP(CUR, NXT, KC, KN, t)                                                                     \
  do {                                                                                                \
    const bf16x8 v0 = *(LDSP(const bf16x8))(vq + (t) * 1024), v1 = *(LDSP(const bf16x8))(vq + (t) * 1024 + 512); \
    KN = *(LDSP(const bf16x8))(kq + ((t) + 2) * 1024);                                                \
    SBAR();                                                                                           \
    EX(CUR, 0); EX(CUR, 1); EX(CUR, 2); EX(CUR, 3); PIN(CUR); SBAR();                                 \
    NXT = MFMA(KC, q, negm); SBAR();                                                                  \
    EX(CUR, 4); EX(CUR, 5); EX(CUR, 6); EX(CUR, 7);                                                   \
    u32x4 p0 = {pk2(CUR[0], CUR[1]), pk2(CUR[2], CUR[3]), pk2(CUR[4], CUR[5]), pk2(CUR[6], CUR[7])};  \
    PIN(p0); EX(CUR, 8); EX(CUR, 9); PIN(CUR); SBAR();                                                \
    o = MFMA(v0, __builtin_bit_cast(bf16x8, p0), o); SBAR();                                          \
    EX(CUR, 10); EX(CUR, 11); EX(CUR, 12); EX(CUR, 13); EX(CUR, 14); EX(CUR, 15);                     \
    u32x4 p1 = {pk2(CUR[8], CUR[9]), pk2(CUR[10], CUR[11]), pk2(CUR[12], CUR[13]), pk2(CUR[14], CUR[15])}; \
    PIN(p1); SBAR();                                                                                  \
    o = MFMA(v1, __builtin_bit_cast(bf16x8, p1), o); SBAR();                                          \
  } while (0)
#define GROUP() do { STEP(sA, sB, kA, kB, 0); STEP(sB, sA, kB, kA, 1); STEP(sA, sB, kA, kB, 2); STEP(sB, sA, kB, kA, 3); kq += 4096; vq += vstep; } while (0)
  __builtin_amdgcn_s_setprio(3);
  GROUP();
  asm volatile("s_waitcnt vmcnt(0) lgkmcnt(0)\n\ts_barrier" ::: "memory");
  const int job = w * 256 + blockIdx.x;
  u16 pa[8], pb[8];
  if (PREP && job < 2944) {
    const u16 *sa, *sb; int ld;
    if (job < 1792) {
      const int ft = job >> 4, S = job & 15, g4 = lane >> 4;
      const int o = (32 * S + 16 * (g4 >> 1) + 4 * (g4 & 1)) * F_ + ft * 16 + (lane & 15);
      sa = wg + o; sb = wu + o; ld = F_;
    } else {
      const bool isO = job < 2048;
      const int f = (isO ? job - 1792 : job - 2048) * 2, ks = isO ? KSD : KSF;
      const int nt = f / ks, s0 = f - nt * ks;
      sa = (isO ? wo : wd) + (16 * s0 + 4 * hh) * D_ + nt * 32 + r; sb = sa + 16 * D_; ld = D_;
    }
#pragma unroll
    for (int e = 0; e < 8; ++e) { pa[e] = sa[(8 * (e >> 2) + (e & 3)) * ld]; pb[e] = sb[(8 * (e >> 2) + (e & 3)) * ld]; }
  }
  asm volatile("" ::: "memory");
  for (int g = 1; g < 3; ++g) GROUP();
  __builtin_amdgcn_s_setprio(2);
  for (int g = 3; g < 5; ++g) GROUP();
  if (PREP && job < 2944) {
    u32x4 a, b;
#pragma unroll
    for (int e = 0; e < 4; ++e) { a[e] = (unsigned)pa[2 * e] | ((unsigned)pa[2 * e + 1] << 16); b[e] = (unsigned)pb[2 * e] | ((unsigned)pb[2 * e + 1] << 16); }
    if (job < 1792) {
      const int ft = job >> 4, S = job & 15, nb = ft / 7, ii = ft - nb * 7;
      char* d = ws + OFF_WGU + ((size_t)((nb * 8 + (S >> 1)) * 14 + 2 * ii) * 2 + (S & 1)) * 1024 + lane * 16;
      st16(d, a); st16(d + 2048, b);
      float ca = 0.f, cb = 0.f;
#pragma unroll
      for (int e = 0; e < 4; ++e) { ca = dot2(a[e], ONE2, ca); cb = dot2(b[e], ONE2, cb); }
      ca = xhalf_add(x16_add(ca)); cb = xhalf_add(x16_add(cb));
      if (lane < 32) ((float*)(ws + OFF_CSP))[S * 3584 + ft * 32 + lane] = lane < 16 ? ca : cb;
    } else {
      char* d = ws + (job < 2048 ? OFF_WO + (size_t)(job - 1792) * 2048 : OFF_WD + (size_t)(job - 2048) * 2048) + lane * 16;
      st16(d, a); st16(d + 1024, b);
    }
  }
  __builtin_amdgcn_s_setprio(1);
  for (int g = 5; g < 7; ++g) GROUP();
  __builtin_amdgcn_s_setprio(0);
  GROUP();
#undef GROUP
#undef STEP
  STAMP(4);
  LDSP(char) xp = (LDSP(char))smem + LX + wq * 3072 + lane * 16;
  if (half == 1) {
    f32x4 a = {o[0], o[1], o[2], o[3]}, b = {o[4], o[5], o[6], o[7]};
    *(LDSP(f32x4))xp = a; *(LDSP(f32x4))(xp + 1024) = b; *(LDSP(float))(xp + 2048) = o[8];
  }
  asm volatile("s_waitcnt lgkmcnt(0)\n\ts_barrier" ::: "memory");
  if (half == 1) return;
  {
    const f32x4 a = *(LDSP(const f32x4))xp, b = *(LDSP(const f32x4))(xp + 1024);
    const float l = *(LDSP(const float))(xp + 2048);
#pragma unroll
    for (int i = 0; i < 4; ++i) { o[i] += a[i]; o[4 + i] += b[i]; }
    o[8] += l;
  }
  bool bad = !(o[8] < 1.8446744e19f);
#pragma unroll
  for (int i = 0; i < 8; ++i) bad = bad || !(fabsf(o[i]) < 3.0e38f);
  if (__builtin_expect(__any(bad), 0)) {
    float m = -INFINITY;
    o = f32x16{};
    kq = kp; vq = vp;
    for (int g = 0; g < 16; ++g) {
#pragma unroll 1
      for (int t = 0; t < 4; ++t) {
        const bf16x8 k = *(LDSP(const bf16x8))(kq + t * 1024);
        const bf16x8 v0 = *(LDSP(const bf16x8))(vq + t * 1024), v1 = *(LDSP(const bf16x8))(vq + t * 1024 + 512);
        f32x16 s = MFMA(k, q, f32x16{});
        float tm = s[0];
#pragma unroll
        for (int i = 1; i < 16; ++i) tm = fmaxf(tm, s[i]);
        tm = xhalf_max(tm);
        const float mn = fmaxf(m, tm);
        const float alpha = __builtin_amdgcn_exp2f(m - mn);
        m = mn;
#pragma unroll
        for (int i = 0; i < 16; ++i) { o[i] *= alpha; s[i] = __builtin_amdgcn_exp2f(s[i] - mn); }
        u32x4 p0 = {pk2(s[0], s[1]), pk2(s[2], s[3]), pk2(s[4], s[5]), pk2(s[6], s[7])};
        u32x4 p1 = {pk2(s[8], s[9]), pk2(s[10], s[11]), pk2(s[12], s[13]), pk2(s[14], s[15])};
        o = MFMA(v0, __builtin_bit_cast(bf16x8, p0), o);
        o = MFMA(v1, __builtin_bit_cast(bf16x8, p1), o);
      }
      kq += 4096; vq += vstep;
    }
  }
  const float inv = 1.0f / o[8];
  u32x4 a = {pk2(o[0] * inv, o[1] * inv), pk2(o[2] * inv, o[3] * inv), pk2(o[4] * inv, o[5] * inv), pk2(o[6] * inv, o[7] * inv)};
  st16(of + ((size_t)tt * KSD + head) * 1024 + lane * 16, a);
  STAMP(5); STAMP_STORE();
}

template <int KS, int WIN, bool FINAL, bool FIRST>
__global__ __launch_bounds__(256, 1) void resgemm_kernel(const char* __restrict__ xt, const char* __restrict__ wt, const char* __restrict__ res,
                                                         char* __restrict__ out, char* __restrict__ stats, const float* __restrict__ csp,
                                                         float* __restrict__ cs DIAG_ARG) {
  STAMP_DECL; STAMP(0);
  __shared__ __attribute__((aligned(16))) char smem[49152];
  constexpr int KW = KS / 4;
  const int tid = threadIdx.x, lane = tid & 63, r = lane & 31, hh = lane >> 5;
  const int w = __builtin_amdgcn_readfirstlane(tid >> 6);
  const int xcd = blockIdx.x & 7, idx = blockIdx.x >> 3;
  const int tb = xcd * 4 + (idx & 3), nb = idx >> 2;
  const unsigned voff = lane * 16;
  const int ont = nb * 2 + (w >> 1), ott = tb * 2 + (w & 1);
  const char* xa = xt + ((size_t)ott * KS + w * KW) * 1024;
  const char* xb = xt + ((size_t)(ott ^ 1) * KS + w * KW) * 1024;
  const char* wa = wt + ((size_t)ont * KS + w * KW) * 1024;
  const char* wb = wt + ((size_t)(ont ^ 1) * KS + w * KW) * 1024;
  bf16x8 r0, r1;
  u32x2 rn[4];
  float cp[16];
  if (FIRST) {
    const char* rp = res + (size_t)(ott * 32 + r) * 1024 + ont * 64 + hh * 8;
#pragma unroll
    for (int g = 0; g < 4; ++g) rn[g] = *(const u32x2*)(rp + g * 16);
    if (blockIdx.x < 14) {
#pragma unroll
      for (int i = 0; i < 16; ++i) cp[i] = csp[i * 3584 + blockIdx.x * 256 + tid];
    }
  }
  asm volatile("s_nop 4" ::: "memory");
  bf16x8 x0[WIN], x1[WIN], w0[WIN], w1[WIN];
#define LD(s) do { gld16<((s) & 3) * 1024>(w0[(s) % WIN], voff, wa + ((s) >> 2) * 4096); gld16<((s) & 3) * 1024>(x0[(s) % WIN], voff, xa + ((s) >> 2) * 4096); \
                   gld16<((s) & 3) * 1024>(w1[(s) % WIN], voff, wb + ((s) >> 2) * 4096); gld16<((s) & 3) * 1024>(x1[(s) % WIN], voff, xb + ((s) >> 2) * 4096); } while (0)
  sfor<0, WIN>([&](auto ic) { constexpr int s = decltype(ic)::value; LD(s); });
  f32x16 acc[4];
#pragma unroll
  for (int i = 0; i < 4; ++i) acc[i] = f32x16{};
  sfor<0, KW>([&](auto ic) {
    constexpr int s = decltype(ic)::value;
    constexpr int b = s % WIN;
    constexpr int ahead = (s + WIN - 1 < KW - 1 ? s + WIN - 1 : KW - 1) - s;
    constexpr int extra = (!FIRST && s + WIN > KW) ? 2 : 0;
    asm volatile("s_waitcnt vmcnt(%4)" : "+v"(w0[b]), "+v"(x0[b]), "+v"(w1[b]), "+v"(x1[b]) : "i"(ahead * 4 + extra) : "memory");
    if (s == 0) STAMP(2);
    acc[0] = MFMA(w0[b], x0[b], acc[0]);
    acc[1] = MFMA(w0[b], x1[b], acc[1]);
    acc[2] = MFMA(w1[b], x0[b], acc[2]);
    acc[3] = MFMA(w1[b], x1[b], acc[3]);
    SBAR();
    if constexpr (s + WIN < KW) LD(s + WIN);
    if constexpr (!FIRST && s + WIN == KW) { gld16<0>(r0, voff, res + ((size_t)ott * KSD + 2 * ont) * 1024); gld16<1024>(r1, voff, res + ((size_t)ott * KSD + 2 * ont) * 1024); }
    SBAR();
  });
#undef LD
  STAMP(4);
  if (FIRST && blockIdx.x < 14) {
#pragma unroll
    for (int d = 8; d >= 1; d >>= 1)
#pragma unroll
      for (int i = 0; i < d; ++i) cp[i] += cp[i + d];
    cs[blockIdx.x * 256 + tid] = cp[0];
  }
  LDSP(char) sp = (LDSP(char))smem + lane * 16;
#pragma unroll
  for (int i = 1; i < 4; ++i)
#pragma unroll
    for (int g = 0; g < 4; ++g) {
      f32x4 v = {acc[i][4 * g], acc[i][4 * g + 1], acc[i][4 * g + 2], acc[i][4 * g + 3]};
      *(LDSP(f32x4))(sp + (((w ^ i) * 3 + (i - 1)) * 4 + g) * 1024) = v;
    }
  if (FIRST) {
    asm volatile("s_waitcnt lgkmcnt(0)\n\ts_barrier" ::: "memory");
    u32x4 a = {rn[0][0], rn[0][1], rn[1][0], rn[1][1]}, b = {rn[2][0], rn[2][1], rn[3][0], rn[3][1]};
    r0 = __builtin_bit_cast(bf16x8, a); r1 = __builtin_bit_cast(bf16x8, b);
  } else {
    asm volatile("s_waitcnt vmcnt(0) lgkmcnt(0)\n\ts_barrier" : "+v"(r0), "+v"(r1) :: "memory");
  }
  float v[16];
#pragma unroll
  for (int g = 0; g < 4; ++g) {
    f32x4 a = *(LDSP(const f32x4))(sp + ((w * 3 + 0) * 4 + g) * 1024), b = *(LDSP(const f32x4))(sp + ((w * 3 + 1) * 4 + g) * 1024);
    f32x4 c = *(LDSP(const f32x4))(sp + ((w * 3 + 2) * 4 + g) * 1024);
#pragma unroll
    for (int j = 0; j < 4; ++j) v[4 * g + j] = (a[j] + b[j]) + (c[j] + acc[0][4 * g + j]);
  }
  const u32x4 q0 = __builtin_bit_cast(u32x4, r0), q1 = __builtin_bit_cast(u32x4, r1);
  u32x4 p0, p1;
#pragma unroll
  for (int j = 0; j < 4; ++j) {
    p0[j] = pk2(v[2 * j] + lo16(q0[j]), v[2 * j + 1] + hi16(q0[j]));
    p1[j] = pk2(v[8 + 2 * j] + lo16(q1[j]), v[8 + 2 * j + 1] + hi16(q1[j]));
  }
  if (!FINAL) {
    st16(out + ((size_t)ott * KSD + 2 * ont) * 1024 + lane * 16, p0);
    st16(out + ((size_t)ott * KSD + 2 * ont + 1) * 1024 + lane * 16, p1);
    float s1 = 0.f, s2 = 0.f;
#pragma unroll
    for (int j = 0; j < 4; ++j) {
      float a = lo16(p0[j]), b = hi16(p0[j]), c = lo16(p1[j]), d = hi16(p1[j]);
      s1 += (a + b) + (c + d); s2 += (a * a + b * b) + (c * c + d * d);
    }
    s1 = xhalf_add(s1); s2 = xhalf_add(s2);
    if (lane < 32) { f32x2 st = {s1, s2}; *(f32x2*)(stats + ((size_t)ont * S_ + ott * 32 + r) * 8) = st; }
  } else {
    auto s0 = __builtin_amdgcn_permlane32_swap(p0[0], p0[2], false, false);
    auto s1 = __builtin_amdgcn_permlane32_swap(p0[1], p0[3], false, false);
    auto s2 = __builtin_amdgcn_permlane32_swap(p1[0], p1[2], false, false);
    auto s3 = __builtin_amdgcn_permlane32_swap(p1[1], p1[3], false, false);
    u32x4 a = {s0[0], s1[0], s0[1], s1[1]}, b = {s2[0], s3[0], s2[1], s3[1]};
    char* o = out + ((size_t)(ott * 32 + r) * D_ + ont * 32) * 2 + hh * 16;
    st16(o, a);
    st16(o + 32, b);
  }
  STAMP(5); STAMP_STORE();
}

extern "C" void kernel_launch(void* const* d_in, const int* in_sizes, int n_in, void* d_out, int out_size, void* d_ws, size_t ws_size,
                              hipStream_t stream) {
  const u16* x = (const u16*)d_in[0];
  const u16* wqkv = (const u16*)d_in[1];
  const u16* wo = (const u16*)d_in[2];
  const u16* wg = (const u16*)d_in[4];
  const u16* wu = (const u16*)d_in[5];
  const u16* wd = (const u16*)d_in[6];
  char* ws = (char*)d_ws;
  float* csq = (float*)(ws + OFF_CSQ);
  float* csg = (float*)(ws + OFF_CSG);
  const float* csp = (const float*)(ws + OFF_CSP);
  qkv_kernel<true, QLOOK><<<256, 512, 0, stream>>>((const char*)x, nullptr, (const char*)wqkv, csq, ws + OFF_Q, ws + OFF_K, ws + OFF_V DIAG_PASS(0));
  attn_kernel<true><<<256, 1024, 0, stream>>>(ws + OFF_Q, ws + OFF_K, ws + OFF_V, ws + OFF_O, wo, wg, wu, wd, ws DIAG_PASS(1));
  resgemm_kernel<32, 6, false, true><<<256, 256, 0, stream>>>(ws + OFF_O, ws + OFF_WO, (const char*)x, ws + OFF_X2, ws + OFF_ST2, csp, csg DIAG_PASS(2));
  gu_kernel<<<256, 512, 0, stream>>>(ws + OFF_X2, ws + OFF_ST2, ws + OFF_WGU, csg, ws + OFF_H DIAG_PASS(3));
  resgemm_kernel<112, 6, false, false><<<256, 256, 0, stream>>>(ws + OFF_H, ws + OFF_WD, ws + OFF_X2, ws + OFF_X3, ws + OFF_ST3, nullptr, nullptr DIAG_PASS(4));
  qkv_kernel<false, QLOOK><<<256, 512, 0, stream>>>(ws + OFF_X3, ws + OFF_ST3, (const char*)wqkv, csq, ws + OFF_Q, ws + OFF_K, ws + OFF_V DIAG_PASS(5));
  attn_kernel<false><<<256, 1024, 0, stream>>>(ws + OFF_Q, ws + OFF_K, ws + OFF_V, ws + OFF_O, wo, wg, wu, wd, ws DIAG_PASS(6));
  resgemm_kernel<32, 6, false, false><<<256, 256, 0, stream>>>(ws + OFF_O, ws + OFF_WO, ws + OFF_X3, ws + OFF_X2, ws + OFF_ST2, nullptr, nullptr DIAG_PASS(7));
  gu_kernel<<<256, 512, 0, stream>>>(ws + OFF_X2, ws + OFF_ST2, ws + OFF_WGU, csg, ws + OFF_H DIAG_PASS(8));
  resgemm_kernel<112, 6, true, false><<<256, 256, 0, stream>>>(ws + OFF_H, ws + OFF_WD, ws + OFF_X2, (char*)d_out, nullptr, nullptr, nullptr DIAG_PASS(9));
}
```

```cpp
#include <hip/hip_runtime.h>
#include <hip/hip_bf16.h>
#include <stdint.h>

typedef short bf16x8 __attribute__((ext_vector_type(8)));
typedef float f32x16 __attribute__((ext_vector_type(16)));
typedef float f32x4 __attribute__((ext_vector_type(4)));
typedef float f32x2 __attribute__((ext_vector_type(2)));
typedef unsigned u32x4 __attribute__((ext_vector_type(4)));
typedef unsigned u32x2 __attribute__((ext_vector_type(2)));
typedef __bf16 bf16v2 __attribute__((ext_vector_type(2)));
typedef unsigned short u16;

#define STAMP(i)
#define STAMP_DECL
#define STAMP_STORE()
#define DIAG_ARG
#define DIAG_PASS(k)
#define DI __device__ __forceinline__
#define MFMA(a, b, c) __builtin_amdgcn_mfma_f32_32x32x16_bf16((a), (b), (c), 0, 0, 0)
#define LDSP(T) __attribute__((address_space(3))) T*

constexpr int S_ = 2048, D_ = 512, H_ = 32, F_ = 1792;
constexpr int NTT = S_ / 32;
constexpr int KSD = D_ / 16;
constexpr int KSF = F_ / 16;
constexpr float QSCALE = 0.25f * 1.4426950408889634f;
constexpr float LN_EPS = 1e-5f;

constexpr size_t KB = 1024;
constexpr size_t OFF_WO = 0;
constexpr size_t OFF_WGU = OFF_WO + 512 * KB;
constexpr size_t OFF_WD = OFF_WGU + 3584 * KB;
constexpr size_t OFF_WQ = OFF_WD + 1792 * KB;
constexpr size_t OFF_CSQ = OFF_WQ + 1536 * KB;
constexpr size_t OFF_CSG = OFF_CSQ + 8 * KB;
constexpr size_t OFF_CSP = OFF_CSG + 16 * KB;
constexpr size_t OFF_ST2 = OFF_CSP + 232 * KB;
constexpr size_t OFF_ST3 = OFF_ST2 + 256 * KB;
constexpr size_t OFF_X2 = OFF_ST3 + 256 * KB;
constexpr size_t OFF_X3 = OFF_X2 + 2048 * KB;
constexpr size_t OFF_Q = OFF_X3 + 2048 * KB;
constexpr size_t OFF_K = OFF_Q + 2048 * KB;
constexpr size_t OFF_V = OFF_K + 2048 * KB;
constexpr size_t OFF_O = OFF_V + 2048 * KB;
constexpr size_t OFF_H = OFF_Q;

DI unsigned pk2(float lo, float hi) {
  f32x2 v = {lo, hi};
  bf16v2 r = __builtin_convertvector(v, bf16v2);
  return __builtin_bit_cast(unsigned, r);
}
DI float lo16(unsigned u) { return __uint_as_float(u << 16); }
DI float hi16(unsigned u) { return __uint_as_float(u & 0xffff0000u); }
DI float xhalf_add(float v) {
  auto r = __builtin_amdgcn_permlane32_swap(__float_as_uint(v), __float_as_uint(v), false, false);
  return __uint_as_float(r[0]) + __uint_as_float(r[1]);
}
DI float xhalf_max(float v) {
  auto r = __builtin_amdgcn_permlane32_swap(__float_as_uint(v), __float_as_uint(v), false, false);
  return fmaxf(__uint_as_float(r[0]), __uint_as_float(r[1]));
}
DI float x16_add(float v) {
  auto r = __builtin_amdgcn_permlane16_swap(__float_as_uint(v), __float_as_uint(v), false, false);
  return __uint_as_float(r[0]) + __uint_as_float(r[1]);
}
DI void st16(void* p, u32x4 v) { asm volatile("global_store_dwordx4 %0, %1, off sc1\n\ts_nop 1" :: "v"(p), "v"(v) : "memory"); }
template <int IMM>
DI void gld16(bf16x8& d, unsigned voff, const char* sbase) {
  asm volatile("global_load_dwordx4 %0, %1, %2 offset:%3" : "=v"(d) : "v"(voff), "s"(sbase), "i"(IMM) : "memory");
}
DI void glds1(unsigned voff, const char* sbase, unsigned lds) {
  unsigned keep;
  asm volatile("s_mov_b32 %0, m0\n\ts_mov_b32 m0, %3\n\ts_nop 0\n\tglobal_load_lds_dwordx4 %1, %2\n\ts_mov_b32 m0, %0"
               : "=&s"(keep) : "v"(voff), "s"(sbase), "s"(lds) : "memory");
}
DI void glds3(unsigned voff, const char* sbase, unsigned lds) {
  unsigned keep;
  asm volatile("s_mov_b32 %0, m0\n\ts_mov_b32 m0, %3\n\ts_nop 0\n\tglobal_load_lds_dwordx4 %1, %2\n\tglobal_load_lds_dwordx4 %1, %2 offset:1024\n\t"
               "global_load_lds_dwordx4 %1, %2 offset:2048\n\ts_mov_b32 m0, %0"
               : "=&s"(keep) : "v"(voff), "s"(sbase), "s"(lds) : "memory");
}
DI void glds4(unsigned voff, const char* sbase, unsigned lds) {
  unsigned keep;
  asm volatile("s_mov_b32 %0, m0\n\ts_mov_b32 m0, %3\n\ts_nop 0\n\tglobal_load_lds_dwordx4 %1, %2\n\tglobal_load_lds_dwordx4 %1, %2 offset:1024\n\t"
               "global_load_lds_dwordx4 %1, %2 offset:2048\n\tglobal_load_lds_dwordx4 %1, %2 offset:3072\n\ts_mov_b32 m0, %0"
               : "=&s"(keep) : "v"(voff), "s"(sbase), "s"(lds) : "memory");
}
#define SBAR() __builtin_amdgcn_sched_barrier(0)
#define PIN(x) asm volatile("" : "+v"(x))
#define WAITBAR_V(n) asm volatile("s_waitcnt vmcnt(%0) lgkmcnt(0)\n\ts_barrier" :: "i"(n) : "memory")
#define WAITBAR_L() asm volatile("s_waitcnt lgkmcnt(0)\n\ts_barrier" ::: "memory")
typedef short s16x4 __attribute__((ext_vector_type(4)));
DI s16x4 trread(LDSP(const char) p) { return __builtin_amdgcn_ds_read_tr16_b64_v4i16((LDSP(s16x4))p); }
DI bf16x8 trfrag(LDSP(const char) p, int off2) { const s16x4 lo = trread(p), hi = trread(p + off2); return __builtin_shufflevector(lo, hi, 0, 1, 2, 3, 4, 5, 6, 7); }
DI bf16x8 ldfrag(LDSP(const char) p) { return *(LDSP(const bf16x8))p; }
DI float dot2(unsigned a, unsigned b, float c) { return __builtin_amdgcn_fdot2_f32_bf16(__builtin_bit_cast(bf16v2, a), __builtin_bit_cast(bf16v2, b), c, false); }
constexpr unsigned ONE2 = 0x3f803f80u;

template <int LOOK, int PER> DI void ring_wait(int i) {
  if (i <= 8 - LOOK) WAITBAR_V((LOOK - 1) * PER);
  else if (LOOK > 3 && i == 5) WAITBAR_V(2 * PER);
  else if (LOOK > 2 && i == 6) WAITBAR_V(PER);
  else WAITBAR_V(0);
}

DI void qkv_epilogue(const f32x16 (&acc)[3], LDSP(const float) cx, LDSP(const float) sx, int lane, int tt, int nb, char* ws) {
  const int r = lane & 31, hh = lane >> 5;
  const float rstd = sx[r], mr = sx[32 + r];
#pragma unroll
  for (int i = 0; i < 2; ++i) {
    const float sc = i == 0 ? rstd * QSCALE : rstd, ms = i == 0 ? mr * QSCALE : mr;
    float v[16];
#pragma unroll
    for (int g = 0; g < 4; ++g) {
      const f32x4 c4 = *(LDSP(const f32x4))(cx + i * 32 + 8 * g + 4 * hh);
#pragma unroll
      for (int e = 0; e < 4; ++e) v[4 * g + e] = acc[i][4 * g + e] * sc - ms * c4[e];
    }
    char* o = ws + (i == 0 ? OFF_Q : OFF_K) + ((size_t)(2 * nb) * NTT + tt) * 1024 + lane * 16;
    u32x4 a = {pk2(v[0], v[1]), pk2(v[2], v[3]), pk2(v[4], v[5]), pk2(v[6], v[7])};
    u32x4 b = {pk2(v[8], v[9]), pk2(v[10], v[11]), pk2(v[12], v[13]), pk2(v[14], v[15])};
    st16(o, a);
    st16(o + NTT * 1024, b);
  }
  const float cv = cx[64 + r];
  float v[16];
#pragma unroll
  for (int g = 0; g < 4; ++g) {
    const f32x4 a4 = *(LDSP(const f32x4))(sx + 8 * g + 4 * hh), b4 = *(LDSP(const f32x4))(sx + 32 + 8 * g + 4 * hh);
#pragma unroll
    for (int e = 0; e < 4; ++e) v[4 * g + e] = acc[2][4 * g + e] * a4[e] - b4[e] * cv;
  }
  char* o = ws + OFF_V + ((size_t)(2 * nb + (r >> 4)) * NTT + tt) * 1024 + hh * 256 + (r & 15) * 16;
  u32x4 a = {pk2(v[0], v[1]), pk2(v[2], v[3]), pk2(v[4], v[5]), pk2(v[6], v[7])};
  u32x4 b = {pk2(v[8], v[9]), pk2(v[10], v[11]), pk2(v[12], v[13]), pk2(v[14], v[15])};
  st16(o, a);
  st16(o + 512, b);
}

extern "C" __global__ __launch_bounds__(512, 2) void k_qkv1(const char* __restrict__ x, const char* __restrict__ wn, char* __restrict__ ws DIAG_ARG) {
  STAMP_DECL; STAMP(0);
  constexpr int NSLOT = 4, LOOK = 3, WCH = 24576, CH = 32768;
  constexpr int LCS = NSLOT * CH;
  constexpr int LST = LCS + 1024;
  __shared__ __attribute__((aligned(16))) char smem[LST + 512];
  const int tid = threadIdx.x, lane = tid & 63, r = lane & 31, hh = lane >> 5;
  const int w = __builtin_amdgcn_readfirstlane(tid >> 6);
  const int xcd = blockIdx.x & 7, idx = blockIdx.x >> 3;
  const int tb = (xcd & 3) * 8 + (idx & 7), nq = (xcd >> 2) * 4 + (idx >> 3);
  const unsigned lds0 = (unsigned)(uintptr_t)smem;
  const int q4 = (lane & 15) >> 2;
  const int wbase = (8 * hh + q4) * 128 + ((lane >> 4) & 1) * 32 + (lane & 3) * 8;
  const int xsw = (r >> 1) & 7;
  LDSP(const char) lp = (LDSP(const char))smem;
  if (w >= 4) {
    const int j = w - 4;
    const int rot = ((idx & 7) + 2 * (idx >> 3)) & 7;
    const unsigned wv = (lane >> 3) * 3072 + (((lane & 7) ^ (((lane >> 4) & 1) << 2)) * 16);
    const unsigned xv0 = (lane >> 3) * 1024 + (((lane & 7) ^ (lane >> 4)) * 16);
    const unsigned xv1 = (lane >> 3) * 1024 + (((lane & 7) ^ ((lane >> 4) + 4)) * 16);
    const char* wb = wn + (size_t)j * 16 * 3072 + nq * 128;
    const char* xb = x + (size_t)(tb * 64 + j * 16) * 1024;
    float a0 = 0.f, a1 = 0.f, a2 = 0.f, a3 = 0.f;
    asm volatile("s_nop 4" ::: "memory");
#pragma unroll 1
    for (int c = 0; c < 8 + LOOK; ++c) {
      const int i = c - LOOK;
      if (i >= 0) ring_wait<LOOK, 8>(i);
      if (c < 8) {
        const int kc = (c + rot) & 7;
        const char* ws_ = wb + (size_t)kc * 64 * 3072; const char* xs_ = xb + kc * 128;
        const unsigned d = lds0 + (c & 3) * CH + j * 2048;
        glds1(xv0, xs_, d + WCH); glds1(wv, ws_, d); glds1(wv, ws_ + 1024, d + 8192); glds1(wv, ws_ + 2048, d + 16384);
        glds1(xv1, xs_ + 8192, d + WCH + 1024); glds1(wv, ws_ + 8 * 3072, d + 1024); glds1(wv, ws_ + 8 * 3072 + 1024, d + 8192 + 1024);
        glds1(wv, ws_ + 8 * 3072 + 2048, d + 16384 + 1024);
      }
      if (i >= 0) {
        LDSP(const char) sl = lp + (i & 3) * CH;
        if (j < 3) {
          LDSP(const char) p0 = sl + j * 8192 + wbase + (q4 >> 1) * 64;
          LDSP(const char) p1 = sl + j * 8192 + wbase + ((q4 >> 1) ^ 1) * 64;
#pragma unroll
          for (int u = 0; u < 4; ++u) {
            const u32x4 a = __builtin_bit_cast(u32x4, trfrag(p0 + u * 2048, 512)), b = __builtin_bit_cast(u32x4, trfrag(p1 + u * 2048, 512));
            a0 = dot2(a[0], ONE2, a0); a1 = dot2(b[0], ONE2, a1); a2 = dot2(a[1], ONE2, a2); a3 = dot2(b[1], ONE2, a3);
            a0 = dot2(a[2], ONE2, a0); a1 = dot2(b[2], ONE2, a1); a2 = dot2(a[3], ONE2, a2); a3 = dot2(b[3], ONE2, a3);
          }
        } else {
#pragma unroll
          for (int u = 0; u < 4; ++u) {
            LDSP(const char) p = sl + WCH + r * 128 + (((2 * u + hh) ^ xsw) * 16);
            const u32x4 a = __builtin_bit_cast(u32x4, ldfrag(p)), b = __builtin_bit_cast(u32x4, ldfrag(p + 4096));
#pragma unroll
            for (int e = 0; e < 4; ++e) { a0 = dot2(a[e], ONE2, a0); a1 = dot2(a[e], a[e], a1); a2 = dot2(b[e], ONE2, a2); a3 = dot2(b[e], b[e], a3); }
          }
        }
      }
    }
    if (j < 3) {
      const float c0 = xhalf_add(a0 + a2), c1 = xhalf_add(a1 + a3);
      const float cv = hh ? c1 : c0;
      ((LDSP(float))((LDSP(char))smem + LCS))[hh * 96 + j * 32 + r] = cv;
      if (tb == 0) ((float*)(ws + OFF_CSQ))[(nq * 2 + hh) * 96 + j * 32 + r] = cv;
    } else {
      a0 = xhalf_add(a0); a1 = xhalf_add(a1); a2 = xhalf_add(a2); a3 = xhalf_add(a3);
      const float mean = (hh ? a2 : a0) * (1.f / D_);
      const float rstd = rsqrtf(fmaxf((hh ? a3 : a1) * (1.f / D_) - mean * mean, 0.f) + LN_EPS);
      LDSP(float) sx = (LDSP(float))((LDSP(char))smem + LST) + hh * 64;
      sx[r] = rstd; sx[32 + r] = mean * rstd;
    }
    WAITBAR_L();
    return;
  }
  const int ta = w & 1, hp = w >> 1;
  const int woff = wbase + ((hp ^ (q4 >> 1)) * 64);
  const int xoff = WCH + ta * 4096 + r * 128;
  f32x16 acc[3];
#pragma unroll
  for (int i = 0; i < 3; ++i) acc[i] = f32x16{};
  bf16x8 wf[2][3], xf[2];
#define RD0(B, sl, u) do { xf[B] = ldfrag(sl + xoff + (((2 * (u) + hh) ^ xsw) * 16)); wf[B][0] = trfrag(sl + woff + (u) * 2048, 512); } while (0)
#define RD1(B, sl, u) do { wf[B][1] = trfrag(sl + woff + 8192 + (u) * 2048, 512); wf[B][2] = trfrag(sl + woff + 16384 + (u) * 2048, 512); } while (0)
#define KSTEP(B, sl, u) do { SBAR(); RD0((B) ^ 1, sl, u); SBAR(); acc[0] = MFMA(wf[B][0], xf[B], acc[0]); SBAR(); RD1((B) ^ 1, sl, u); SBAR(); \
                             acc[1] = MFMA(wf[B][1], xf[B], acc[1]); acc[2] = MFMA(xf[B], wf[B][2], acc[2]); SBAR(); } while (0)
  STAMP(1);
  WAITBAR_L();
  STAMP(2);
  RD0(0, lp, 0); RD1(0, lp, 0);
#pragma unroll 1
  for (int c = 0; c < 8; ++c) {
    LDSP(const char) sl = lp + (c & 3) * CH;
    KSTEP(0, sl, 1); KSTEP(1, sl, 2); KSTEP(0, sl, 3);
    WAITBAR_L();
    LDSP(const char) sn = lp + ((c + 1) & 3) * CH;
    KSTEP(1, sn, 0);
  }
#undef RD0
#undef RD1
#undef KSTEP
  STAMP(4);
  qkv_epilogue(acc, (LDSP(const float))(lp + LCS) + hp * 96, (LDSP(const float))(lp + LST) + ta * 64, lane, tb * 2 + ta, nq * 2 + hp, ws);
  STAMP(5); STAMP_STORE();
}

extern "C" __global__ __launch_bounds__(512, 2) void k_qkv2(char* __restrict__ ws DIAG_ARG) {
  STAMP_DECL; STAMP(0);
  constexpr int NSLOT = 4, LOOK = 3, WCH = 12288, CH = WCH + 16384;
  constexpr int LCS = NSLOT * CH;
  __shared__ __attribute__((aligned(16))) char smem[LCS + 4 * 768];
  const int tid = threadIdx.x, lane = tid & 63, r = lane & 31, hh = lane >> 5;
  const int w = __builtin_amdgcn_readfirstlane(tid >> 6);
  const int xcd = blockIdx.x & 7, idx = blockIdx.x >> 3;
  const int tb = (xcd & 3) * 4 + (idx & 3), nb = (xcd >> 2) * 8 + (idx >> 2);
  const unsigned voff = lane * 16;
  const unsigned lds0 = (unsigned)(uintptr_t)smem;
  const int tt = tb * 4 + (w & 3);
  if (w >= 4) {
    const int j = w - 4;
    const char* xb = ws + OFF_X3 + (size_t)tt * 32768;
    const char* wb = ws + OFF_WQ + (size_t)nb * 8 * WCH + j * 3072;
    asm volatile("s_nop 4" ::: "memory");
#pragma unroll 1
    for (int c = 0; c < 8 + LOOK; ++c) {
      if (c >= LOOK) ring_wait<LOOK, 7>(c - LOOK);
      if (c < 8) {
        const unsigned d = lds0 + (c & 3) * CH;
        glds4(voff, xb + c * 4096, d + WCH + j * 4096);
        glds3(voff, wb + c * WCH, d + j * 3072);
      }
    }
    return;
  }
  LDSP(float) cx = (LDSP(float))((LDSP(char))smem + LCS) + w * 192;
  {
    const f32x4* sp = (const f32x4*)(ws + OFF_ST3 + (size_t)(tt * 32 + r) * 128 + hh * 64);
    const f32x4 p0 = sp[0], p1 = sp[1], p2 = sp[2], p3 = sp[3];
    const float* cs = (const float*)(ws + OFF_CSQ) + nb * 96;
    const float c0 = cs[lane], c1 = cs[64 + r];
    const f32x4 t = (p0 + p1) + (p2 + p3);
    const float s1 = xhalf_add(t[0] + t[2]), s2 = xhalf_add(t[1] + t[3]);
    const float mean = s1 * (1.f / D_);
    const float rstd = rsqrtf(fmaxf(s2 * (1.f / D_) - mean * mean, 0.f) + LN_EPS);
    cx[lane] = c0; cx[64 + lane] = c1;
    cx[128 + lane] = hh ? mean * rstd : rstd;
  }
  LDSP(const char) lp = (LDSP(const char))smem + lane * 16;
  const int xoff = WCH + w * 4096;
  f32x16 acc[3];
#pragma unroll
  for (int i = 0; i < 3; ++i) acc[i] = f32x16{};
  bf16x8 wf[2][3], xf[2];
#define RD0(B, sl, u) do { xf[B] = ldfrag(sl + xoff + (u) * 1024); wf[B][0] = ldfrag(sl + (u) * 1024); } while (0)
#define RD1(B, sl, u) do { wf[B][1] = ldfrag(sl + 4096 + (u) * 1024); wf[B][2] = ldfrag(sl + 8192 + (u) * 1024); } while (0)
#define KSTEP(B, sl, u) do { SBAR(); RD0((B) ^ 1, sl, u); SBAR(); acc[0] = MFMA(wf[B][0], xf[B], acc[0]); SBAR(); RD1((B) ^ 1, sl, u); SBAR(); \
                             acc[1] = MFMA(wf[B][1], xf[B], acc[1]); acc[2] = MFMA(xf[B], wf[B][2], acc[2]); SBAR(); } while (0)
  STAMP(1);
  WAITBAR_L();
  STAMP(2);
  RD0(0, lp, 0); RD1(0, lp, 0);
#pragma unroll 1
  for (int c = 0; c < 8; ++c) {
    LDSP(const char) sl = lp + (c & 3) * CH;
    KSTEP(0, sl, 1); KSTEP(1, sl, 2); KSTEP(0, sl, 3);
    WAITBAR_L();
    LDSP(const char) sn = lp + ((c + 1) & 3) * CH;
    KSTEP(1, sn, 0);
  }
#undef RD0
#undef RD1
#undef KSTEP
  STAMP(4);
  qkv_epilogue(acc, cx, cx + 128, lane, tt, nb, ws);
  STAMP(5); STAMP_STORE();
}

extern "C" __global__ __launch_bounds__(512, 2) void k_gu(char* __restrict__ ws DIAG_ARG) {
  STAMP_DECL; STAMP(0);
  constexpr int NSLOT = 3, LOOK = 2, WCH = 28672, CH = WCH + 16384, LCS = NSLOT * CH;
  __shared__ __attribute__((aligned(16))) char smem[LCS + 4096];
  const int tid = threadIdx.x, lane = tid & 63;
  const int w = __builtin_amdgcn_readfirstlane(tid >> 6);
  const int xcd = blockIdx.x & 7, idx = blockIdx.x >> 3;
  const int tb = (xcd & 1) * 8 + (idx & 7), nb = (xcd >> 1) * 4 + (idx >> 3);
  const unsigned voff = lane * 16;
  const unsigned lds0 = (unsigned)(uintptr_t)smem;
  const int tt = tb * 4 + (w & 3);
  if (w >= 4) {
    const int j = w - 4;
    const char* xb = ws + OFF_X2 + (size_t)tt * 32768;
    const char* wb = ws + OFF_WGU + (size_t)nb * 8 * WCH + j * 7168;
    unsigned d = lds0;
    asm volatile("s_nop 4" ::: "memory");
#pragma unroll 1
    for (int c = 0; c < 8 + LOOK; ++c) {
      if (c >= LOOK) ring_wait<LOOK, 11>(c - LOOK);
      if (c < 8) {
        glds4(voff, xb + c * 4096, d + WCH + j * 4096);
        glds4(voff, wb + c * WCH, d + j * 7168);
        glds3(voff, wb + c * WCH + 4096, d + j * 7168 + 4096);
        d = d == lds0 + 2 * CH ? lds0 : d + CH;
      }
    }
    return;
  }
  const int g4 = lane >> 4, c16 = lane & 15;
  f32x4 ac[2][14];
  float rs0, rs1;
  {
    const f32x4* sp = (const f32x4*)(ws + OFF_ST2 + (size_t)(tt * 32 + c16) * 128 + g4 * 32);
    const f32x4 a0 = sp[0], a1 = sp[1], b0 = sp[128], b1 = sp[129];
    const f32x4 cv = *(const f32x4*)(ws + OFF_CSG + (size_t)nb * 896 + (lane < 56 ? lane : 0) * 16);
    LDSP(char) cl = (LDSP(char))smem + LCS + w * 1024;
    *(LDSP(f32x4))(cl + lane * 16) = cv;
    const f32x4 ta = a0 + a1, tb_ = b0 + b1;
    const float m0 = xhalf_add(x16_add(ta[0] + ta[2])) * (1.f / D_), q0 = xhalf_add(x16_add(ta[1] + ta[3])) * (1.f / D_);
    const float m1 = xhalf_add(x16_add(tb_[0] + tb_[2])) * (1.f / D_), q1 = xhalf_add(x16_add(tb_[1] + tb_[3])) * (1.f / D_);
    rs0 = 1.f / (fmaxf(q0 - m0 * m0, 0.f) + LN_EPS);
    rs1 = 1.f / (fmaxf(q1 - m1 * m1, 0.f) + LN_EPS);
#pragma unroll
    for (int i = 0; i < 14; ++i) { const f32x4 c4 = *(LDSP(const f32x4))(cl + i * 64 + g4 * 16); ac[0][i] = c4 * -m0; ac[1][i] = c4 * -m1; }
  }
  LDSP(const char) lw = (LDSP(const char))smem + lane * 16;
  LDSP(const char) lx = (LDSP(const char))smem + WCH + w * 4096 + (g4 >> 1) * 1024 + ((g4 & 1) * 32 + c16) * 16;
  bf16x8 wf[2][7], xf[2][2];
#define RDX(u, so) do { xf[u][0] = ldfrag(lx + (so) + (u) * 2048); xf[u][1] = ldfrag(lx + (so) + (u) * 2048 + 256); } while (0)
#define RDW(hf, u, so, i) wf[hf][i] = ldfrag(lw + (so) + ((7 * (hf) + (i)) * 2 + (u)) * 1024)
#define MM(B, XP, i) do { ac[0][7 * (B) + (i)] = __builtin_amdgcn_mfma_f32_16x16x32_bf16(wf[B][i], xf[XP][0], ac[0][7 * (B) + (i)], 0, 0, 0); \
                          ac[1][7 * (B) + (i)] = __builtin_amdgcn_mfma_f32_16x16x32_bf16(wf[B][i], xf[XP][1], ac[1][7 * (B) + (i)], 0, 0, 0); } while (0)
#define GSTEP(B, XP, u, so) do { SBAR(); if ((B) == 1) RDX(u, so); RDW((B) ^ 1, u, so, 0); RDW((B) ^ 1, u, so, 1); SBAR(); MM(B, XP, 0); SBAR(); \
    RDW((B) ^ 1, u, so, 2); RDW((B) ^ 1, u, so, 3); SBAR(); MM(B, XP, 1); SBAR(); RDW((B) ^ 1, u, so, 4); RDW((B) ^ 1, u, so, 5); SBAR(); MM(B, XP, 2); SBAR(); \
    RDW((B) ^ 1, u, so, 6); SBAR(); MM(B, XP, 3); MM(B, XP, 4); MM(B, XP, 5); MM(B, XP, 6); SBAR(); } while (0)
  STAMP(1);
  WAITBAR_L();
  STAMP(2);
  RDX(0, 0);
#pragma unroll
  for (int i = 0; i < 7; ++i) RDW(0, 0, 0, i);
  int so = 0;
#pragma unroll 1
  for (int c = 0; c < 8; ++c) {
    GSTEP(0, 0, 0, so); GSTEP(1, 0, 1, so); GSTEP(0, 1, 1, so);
    WAITBAR_L();
    so = so == 2 * CH ? 0 : so + CH;
    GSTEP(1, 1, 0, so);
  }
#undef RDX
#undef RDW
#undef MM
#undef GSTEP
  STAMP(4);
  char* ob = ws + OFF_H + ((size_t)tt * KSF + nb * 7) * 1024 + (((g4 & 1) * 32) + (g4 >> 1) * 16 + c16) * 16;
#pragma unroll
  for (int i = 0; i < 7; ++i) {
    float h0[4], h1[4];
#pragma unroll
    for (int e = 0; e < 4; ++e) {
      h0[e] = fmaxf(ac[0][2 * i][e], 0.f) * (ac[0][2 * i + 1][e] * rs0);
      h1[e] = fmaxf(ac[1][2 * i][e], 0.f) * (ac[1][2 * i + 1][e] * rs1);
    }
    auto e0 = __builtin_amdgcn_permlane32_swap(pk2(h0[0], h0[1]), pk2(h1[0], h1[1]), false, false);
    auto e1 = __builtin_amdgcn_permlane32_swap(pk2(h0[2], h0[3]), pk2(h1[2], h1[3]), false, false);
    u32x4 a = {e0[0], e1[0], e0[1], e1[1]};
    st16(ob + i * 1024, a);
  }
  STAMP(5); STAMP_STORE();
}

extern "C" __global__ __launch_bounds__(1024, 4) void k_attn(char* __restrict__ ws, const char* __restrict__ wq, const char* __restrict__ wo,
                                                             const char* __restrict__ wg, const char* __restrict__ wu,
                                                             const char* __restrict__ wd, size_t prep DIAG_ARG) {
  STAMP_DECL; STAMP(0);
  constexpr int LK = 0, LV = 65536, LO = 131072, LX = LO + 4096;
  __shared__ __attribute__((aligned(16))) char smem[LX + 24576];
  const int tid = threadIdx.x, lane = tid & 63, r = lane & 31, hh = lane >> 5;
  const int w = __builtin_amdgcn_readfirstlane(tid >> 6);
  const int wq_ = w & 7, half = w >> 3;
  const int xcd = blockIdx.x & 7, idx = blockIdx.x >> 3;
  const int head = xcd * 4 + (idx & 3), qb = idx >> 2;
  const int tt = qb * 8 + wq_;
  const unsigned voff = lane * 16;
  const unsigned lds0 = (unsigned)(uintptr_t)smem;
  const char* ks = ws + OFF_K + (size_t)head * 65536 + half * 32768;
  const unsigned kd = lds0 + LK + half * 32768;
  asm volatile("s_nop 4" ::: "memory");
  bf16x8 q;
  gld16<0>(q, voff, ws + OFF_Q + ((size_t)head * NTT + tt) * 1024);
  glds1(voff, ks + wq_ * 1024, kd + wq_ * 1024);
  glds1(voff, ks + (OFF_V - OFF_K) + wq_ * 1024, kd + LV + wq_ * 1024);
  { const unsigned one = ((tid >> 2) & 11) == 0 ? ONE2 : 0u;
    *(LDSP(unsigned))((LDSP(char))smem + LO + tid * 4) = one; }
  STAMP(1);
  asm volatile("s_waitcnt vmcnt(0) lgkmcnt(0)\n\ts_barrier" : "+v"(q) :: "memory");
  STAMP(2);
  glds3(voff, ks + 8192 + wq_ * 3072, kd + 8192 + wq_ * 3072);
  glds3(voff, ks + (OFF_V - OFF_K) + 8192 + wq_ * 3072, kd + LV + 8192 + wq_ * 3072);
  const int job = prep ? w * 256 + blockIdx.x : 4096;
  unsigned pa[4] = {}, pb[4] = {}, pc[4] = {}, pe[4] = {};
  char* pd = nullptr; int pd2 = 1024;
  if (job < 3712) {
    const char *sa, *sb; int ld; unsigned lo;
    if (job < 1792) {
      const int ft = job >> 4, S = job & 15, nb = ft / 7, ii = ft - nb * 7;
      ld = F_ * 2; lo = (16 * hh + 4 * ((lane >> 4) & 1)) * (F_ * 2) + (lane & 15) * 2;
      sa = wg + 32 * S * (F_ * 2) + ft * 32; sb = wu + 32 * S * (F_ * 2) + ft * 32;
      pd = ws + OFF_WGU + ((size_t)((nb * 8 + (S >> 1)) * 14 + 2 * ii) * 2 + (S & 1)) * 1024; pd2 = 2048;
    } else {
      const char* src; int nt, s0;
      if (job < 2048) { const int f = (job - 1792) * 2; nt = f >> 5; s0 = f & 31; src = wo; ld = 1024; pd = ws + OFF_WO + (size_t)f * 1024; }
      else if (job < 2944) { const int f = (job - 2048) * 2; nt = f / KSF; s0 = f - nt * KSF; src = wd; ld = 1024; pd = ws + OFF_WD + (size_t)f * 1024; }
      else { const int t = (job - 2944) >> 4, nb = t / 3, ii = t - nb * 3; s0 = ((job - 2944) & 15) * 2; nt = ii * 16 + nb; src = wq; ld = 3072;
             pd = ws + OFF_WQ + (size_t)(((nb * 8 + (s0 >> 2)) * 3 + ii) * 4 + (s0 & 3)) * 1024; }
      lo = 4 * hh * ld + r * 2;
      sa = src + (size_t)16 * s0 * ld + nt * 64; sb = sa + 16 * ld;
    }
    asm volatile("s_nop 4" ::: "memory");
#pragma unroll
    for (int e = 0; e < 4; ++e) {
      const int ro = (8 * (e >> 1) + 2 * (e & 1)) * ld;
      asm volatile("global_load_ushort %0, %1, %2" : "=v"(pa[e]) : "v"(lo), "s"(sa + ro) : "memory");
      asm volatile("global_load_ushort %0, %1, %2" : "=v"(pc[e]) : "v"(lo), "s"(sa + ro + ld) : "memory");
      asm volatile("global_load_ushort %0, %1, %2" : "=v"(pb[e]) : "v"(lo), "s"(sb + ro) : "memory");
      asm volatile("global_load_ushort %0, %1, %2" : "=v"(pe[e]) : "v"(lo), "s"(sb + ro + ld) : "memory");
    }
  }

  LDSP(const char) kp = (LDSP(const char))smem + LK + lane * 16;
  LDSP(const char) vp = (LDSP(const char))smem + (r < 16 ? LV : LO) + hh * 256 + (r & 15) * 16;
  const int vstep = r < 16 ? 4096 : 0;

  f32x16 negm, sA, sB;
  bf16x8 kA, kB;
  LDSP(const char) kq = kp + half * 32768; LDSP(const char) vq = vp + half * 8 * vstep;
  {
    const bf16x8 k0 = ldfrag(kp), k1 = ldfrag(kq);
    kA = ldfrag(kq + 1024);
    f32x16 s = MFMA(k0, q, f32x16{});
    float m = fmaxf(fmaxf(s[0], s[1]), s[2]);
#pragma unroll
    for (int i = 3; i < 15; i += 2) m = fmaxf(fmaxf(m, s[i]), s[i + 1]);
    m = fmaxf(m, s[15]);
    m = xhalf_max(m);
#pragma unroll
    for (int i = 0; i < 16; ++i) negm[i] = -m;
    PIN(negm);
    sA = MFMA(k1, q, negm);
  }
  f32x16 o = f32x16{};
#define EX(S, i) S[i] = __builtin_amdgcn_exp2f(S[i])
#define STEP(CUR, NXT, KC, KN, t)                                                                     \
  do {                                                                                                \
    const bf16x8 v0 = ldfrag(vq + (t) * 1024), v1 = ldfrag(vq + (t) * 1024 + 512);                    \
    KN = ldfrag(kq + ((t) + 2) * 1024);                                                               \
    SBAR();                                                                                           \
    EX(CUR, 0); EX(CUR, 1); EX(CUR, 2); EX(CUR, 3); PIN(CUR); SBAR();                                 \
    NXT = MFMA(KC, q, negm); SBAR();                                                                  \
    EX(CUR, 4); EX(CUR, 5); EX(CUR, 6); EX(CUR, 7);                                                   \
    u32x4 p0 = {pk2(CUR[0], CUR[1]), pk2(CUR[2], CUR[3]), pk2(CUR[4], CUR[5]), pk2(CUR[6], CUR[7])};  \
    PIN(p0); EX(CUR, 8); EX(CUR, 9); PIN(CUR); SBAR();                                                \
    o = MFMA(v0, __builtin_bit_cast(bf16x8, p0), o); SBAR();                                          \
    EX(CUR, 10); EX(CUR, 11); EX(CUR, 12); EX(CUR, 13); EX(CUR, 14); EX(CUR, 15);                     \
    u32x4 p1 = {pk2(CUR[8], CUR[9]), pk2(CUR[10], CUR[11]), pk2(CUR[12], CUR[13]), pk2(CUR[14], CUR[15])}; \
    PIN(p1); SBAR();                                                                                  \
    o = MFMA(v1, __builtin_bit_cast(bf16x8, p1), o); SBAR();                                          \
  } while (0)
  __builtin_amdgcn_s_setprio(3);
#pragma unroll 1
  for (int g = 0; g < 8; ++g) {
    if (g == 1) { if (job < 3712) WAITBAR_V(16); else WAITBAR_V(0); }
    if (g == 3) __builtin_amdgcn_s_setprio(2);
    if (g == 5) {
      __builtin_amdgcn_s_setprio(1);
      if (job < 3712) {
        u32x4 fa, fb;
        asm volatile("s_waitcnt vmcnt(0)\n\tv_lshl_or_b32 %0, %8, 16, %4\n\tv_lshl_or_b32 %1, %9, 16, %5\n\tv_lshl_or_b32 %2, %10, 16, %6\n\tv_lshl_or_b32 %3, %11, 16, %7"
                     : "=&v"(fa[0]), "=&v"(fa[1]), "=&v"(fa[2]), "=&v"(fa[3])
                     : "v"(pa[0]), "v"(pa[1]), "v"(pa[2]), "v"(pa[3]), "v"(pc[0]), "v"(pc[1]), "v"(pc[2]), "v"(pc[3]) : "memory");
        asm volatile("v_lshl_or_b32 %0, %8, 16, %4\n\tv_lshl_or_b32 %1, %9, 16, %5\n\tv_lshl_or_b32 %2, %10, 16, %6\n\tv_lshl_or_b32 %3, %11, 16, %7"
                     : "=&v"(fb[0]), "=&v"(fb[1]), "=&v"(fb[2]), "=&v"(fb[3])
                     : "v"(pb[0]), "v"(pb[1]), "v"(pb[2]), "v"(pb[3]), "v"(pe[0]), "v"(pe[1]), "v"(pe[2]), "v"(pe[3]) : "memory");
        st16(pd + voff, fa); st16(pd + pd2 + voff, fb);
        if (job < 1792) {
          float ca = 0.f, cb = 0.f;
#pragma unroll
          for (int e = 0; e < 4; ++e) { ca = dot2(fa[e], ONE2, ca); cb = dot2(fb[e], ONE2, cb); }
          ca = xhalf_add(x16_add(ca)); cb = xhalf_add(x16_add(cb));
          if (lane < 32) ((float*)(ws + OFF_CSP))[(job & 15) * 3584 + (job >> 4) * 32 + lane] = lane < 16 ? ca : cb;
        }
      }
    }
    if (g == 7) __builtin_amdgcn_s_setprio(0);
    STEP(sA, sB, kA, kB, 0); STEP(sB, sA, kB, kA, 1); STEP(sA, sB, kA, kB, 2); STEP(sB, sA, kB, kA, 3);
    kq += 4096; vq += vstep;
  }
#undef STEP
  STAMP(4);
  LDSP(char) xp = (LDSP(char))smem + LX + wq_ * 3072 + lane * 16;
  if (half == 1) {
    f32x4 a = {o[0], o[1], o[2], o[3]}, b = {o[4], o[5], o[6], o[7]};
    *(LDSP(f32x4))xp = a; *(LDSP(f32x4))(xp + 1024) = b; *(LDSP(float))(xp + 2048) = o[8];
  }
  WAITBAR_L();
  if (half == 1) return;
  {
    const f32x4 a = *(LDSP(const f32x4))xp, b = *(LDSP(const f32x4))(xp + 1024);
    const float l = *(LDSP(const float))(xp + 2048);
#pragma unroll
    for (int i = 0; i < 4; ++i) { o[i] += a[i]; o[4 + i] += b[i]; }
    o[8] += l;
  }
  bool bad = !(o[8] < 1.8446744e19f);
#pragma unroll
  for (int i = 0; i < 8; ++i) bad = bad || !(fabsf(o[i]) < 3.0e38f);
  if (__builtin_expect(__any(bad), 0)) {
    float m = -INFINITY;
    o = f32x16{};
    kq = kp; vq = vp;
#pragma unroll 1
    for (int t = 0; t < 64; ++t) {
      const bf16x8 k = ldfrag(kq), v0 = ldfrag(vq), v1 = ldfrag(vq + 512);
      f32x16 s = MFMA(k, q, f32x16{});
      float tm = s[0];
#pragma unroll
      for (int i = 1; i < 16; ++i) tm = fmaxf(tm, s[i]);
      tm = xhalf_max(tm);
      const float mn = fmaxf(m, tm);
      const float alpha = __builtin_amdgcn_exp2f(m - mn);
      m = mn;
#pragma unroll
      for (int i = 0; i < 16; ++i) { o[i] *= alpha; s[i] = __builtin_amdgcn_exp2f(s[i] - mn); }
      u32x4 p0 = {pk2(s[0], s[1]), pk2(s[2], s[3]), pk2(s[4], s[5]), pk2(s[6], s[7])};
      u32x4 p1 = {pk2(s[8], s[9]), pk2(s[10], s[11]), pk2(s[12], s[13]), pk2(s[14], s[15])};
      o = MFMA(v0, __builtin_bit_cast(bf16x8, p0), o);
      o = MFMA(v1, __builtin_bit_cast(bf16x8, p1), o);
      kq += 1024; vq += vstep >> 2;
    }
  }
  const float inv = 1.0f / o[8];
  u32x4 a = {pk2(o[0] * inv, o[1] * inv), pk2(o[2] * inv, o[3] * inv), pk2(o[4] * inv, o[5] * inv), pk2(o[6] * inv, o[7] * inv)};
  st16(ws + OFF_O + ((size_t)tt * KSD + head) * 1024 + lane * 16, a);
  STAMP(5); STAMP_STORE();
}

extern "C" __global__ __launch_bounds__(256, 2) void k_res(const char* __restrict__ xt, const char* __restrict__ wt, const char* __restrict__ res,
                                                           char* __restrict__ out, char* __restrict__ stats, char* __restrict__ ws, int ks,
                                                           int mode DIAG_ARG) {
  STAMP_DECL; STAMP(0);
  __shared__ __attribute__((aligned(16))) char smem[49152];
  const int tid = threadIdx.x, lane = tid & 63, r = lane & 31, hh = lane >> 5;
  const int w = __builtin_amdgcn_readfirstlane(tid >> 6);
  const int xcd = blockIdx.x & 7, idx = blockIdx.x >> 3;
  const int tb = xcd * 4 + (idx & 3), nb = idx >> 2;
  const unsigned voff = lane * 16;
  const int kw = ks >> 2;
  const int ont = nb * 2 + (w >> 1), ott = tb * 2 + (w & 1);
  const char* xa = xt + ((size_t)ott * ks + w * kw) * 1024;
  const char* xb = xt + ((size_t)(ott ^ 1) * ks + w * kw) * 1024;
  const char* wa = wt + ((size_t)ont * ks + w * kw) * 1024;
  const char* wb = wt + ((size_t)(ont ^ 1) * ks + w * kw) * 1024;
  asm volatile("s_nop 4" ::: "memory");
  bf16x8 x0[4], x1[4], w0[4], w1[4];
#define LD(b) do { gld16<(b) * 1024>(w0[b], voff, wa); gld16<(b) * 1024>(x0[b], voff, xa); gld16<(b) * 1024>(w1[b], voff, wb); gld16<(b) * 1024>(x1[b], voff, xb); } while (0)
#define MM(b, n) do { asm volatile("s_waitcnt vmcnt(%4)" : "+v"(w0[b]), "+v"(x0[b]), "+v"(w1[b]), "+v"(x1[b]) : "i"(n) : "memory"); \
    acc[0] = MFMA(w0[b], x0[b], acc[0]); acc[1] = MFMA(w0[b], x1[b], acc[1]); acc[2] = MFMA(w1[b], x0[b], acc[2]); acc[3] = MFMA(w1[b], x1[b], acc[3]); SBAR(); } while (0)
  LD(0); LD(1); LD(2); LD(3);
  f32x16 acc[4];
#pragma unroll
  for (int i = 0; i < 4; ++i) acc[i] = f32x16{};
  const bool nat = mode & 1;
  unsigned ro = nat ? r * 1024 + hh * 8 : lane * 16;
  const char* rb = res + (size_t)ott * 32768 + (nat ? ont * 64 : ont * 2048);
  const int d1 = nat ? 16 : 8, d2 = nat ? 32 : 1024;
  float cp[16];
  const bool csjob = nat && blockIdx.x < 14;
  if (csjob) {
    const float* csp = (const float*)(ws + OFF_CSP) + blockIdx.x * 256 + tid;
#pragma unroll
    for (int i = 0; i < 16; ++i) cp[i] = csp[i * 3584];
  }
  PIN(ro);
  int it = (kw >> 2) - 1;
#pragma unroll 1
  do {
    xa += 4096; xb += 4096; wa += 4096; wb += 4096;
    MM(0, 12); LD(0); SBAR(); MM(1, 12); LD(1); SBAR(); MM(2, 12); LD(2); SBAR(); MM(3, 12); LD(3); SBAR();
  } while (--it > 0);
  u32x2 rn[4];
  asm volatile("global_load_dwordx2 %0, %1, %2" : "=v"(rn[0]) : "v"(ro), "s"(rb) : "memory");
  asm volatile("global_load_dwordx2 %0, %1, %2" : "=v"(rn[1]) : "v"(ro), "s"(rb + d1) : "memory");
  asm volatile("global_load_dwordx2 %0, %1, %2" : "=v"(rn[2]) : "v"(ro), "s"(rb + d2) : "memory");
  asm volatile("global_load_dwordx2 %0, %1, %2" : "=v"(rn[3]) : "v"(ro), "s"(rb + d2 + d1) : "memory");
  MM(0, 16); MM(1, 12); MM(2, 8); MM(3, 4);
#undef LD
#undef MM
  STAMP(4);
  LDSP(char) sp = (LDSP(char))smem + lane * 16;
#pragma unroll
  for (int i = 1; i < 4; ++i)
#pragma unroll
    for (int g = 0; g < 4; ++g) {
      f32x4 v = {acc[i][4 * g], acc[i][4 * g + 1], acc[i][4 * g + 2], acc[i][4 * g + 3]};
      *(LDSP(f32x4))(sp + (((w ^ i) * 3 + (i - 1)) * 4 + g) * 1024) = v;
    }
  asm volatile("s_waitcnt vmcnt(0) lgkmcnt(0)\n\ts_barrier" : "+v"(rn[0]), "+v"(rn[1]), "+v"(rn[2]), "+v"(rn[3]) :: "memory");
  u32x4 p0, p1;
#pragma unroll
  for (int g = 0; g < 4; ++g) {
    const f32x4 a = *(LDSP(const f32x4))(sp + ((w * 3 + 0) * 4 + g) * 1024), b = *(LDSP(const f32x4))(sp + ((w * 3 + 1) * 4 + g) * 1024);
    const f32x4 c = *(LDSP(const f32x4))(sp + ((w * 3 + 2) * 4 + g) * 1024);
    float v[4];
#pragma unroll
    for (int e = 0; e < 4; ++e) v[e] = (a[e] + b[e]) + (c[e] + acc[0][4 * g + e]);
    const unsigned u0 = pk2(v[0] + lo16(rn[g][0]), v[1] + hi16(rn[g][0])), u1 = pk2(v[2] + lo16(rn[g][1]), v[3] + hi16(rn[g][1]));
    if (g < 2) { p0[2 * g] = u0; p0[2 * g + 1] = u1; } else { p1[2 * g - 4] = u0; p1[2 * g - 3] = u1; }
  }
  if (!(mode & 2)) {
    char* o = out + ((size_t)ott * KSD + 2 * ont) * 1024 + lane * 16;
    st16(o, p0);
    st16(o + 1024, p1);
    float s1 = 0.f, s2 = 0.f;
#pragma unroll
    for (int e = 0; e < 4; ++e) { s1 = dot2(p0[e], ONE2, s1); s2 = dot2(p0[e], p0[e], s2); s1 = dot2(p1[e], ONE2, s1); s2 = dot2(p1[e], p1[e], s2); }
    s1 = xhalf_add(s1); s2 = xhalf_add(s2);
    if (lane < 32) { f32x2 st = {s1, s2}; *(f32x2*)(stats + ((size_t)(ott * 32 + r) * 16 + ont) * 8) = st; }
    if (csjob) {
#pragma unroll
      for (int d = 8; d >= 1; d >>= 1)
#pragma unroll
        for (int i = 0; i < d; ++i) cp[i] += cp[i + d];
      ((float*)(ws + OFF_CSG))[blockIdx.x * 256 + tid] = cp[0];
    }
  } else {
    auto s0 = __builtin_amdgcn_permlane32_swap(p0[0], p0[2], false, false);
    auto s1 = __builtin_amdgcn_permlane32_swap(p0[1], p0[3], false, false);
    auto s2 = __builtin_amdgcn_permlane32_swap(p1[0], p1[2], false, false);
    auto s3 = __builtin_amdgcn_permlane32_swap(p1[1], p1[3], false, false);
    u32x4 a = {s0[0], s1[0], s0[1], s1[1]}, b = {s2[0], s3[0], s2[1], s3[1]};
    char* o = out + ((size_t)(ott * 32 + r) * D_ + ont * 32) * 2 + hh * 16;
    st16(o, a);
    st16(o + 32, b);
  }
  STAMP(5); STAMP_STORE();
}

extern "C" void kernel_launch(void* const* d_in, const int* in_sizes, int n_in, void* d_out, int out_size, void* d_ws, size_t ws_size,
                              hipStream_t stream) {
  const char* x = (const char*)d_in[0];
  const char* wqkv = (const char*)d_in[1];
  const char* wo = (const char*)d_in[2];
  const char* wg = (const char*)d_in[4];
  const char* wu = (const char*)d_in[5];
  const char* wd = (const char*)d_in[6];
  char* ws = (char*)d_ws;
  k_qkv1<<<256, 512, 0, stream>>>(x, wqkv, ws DIAG_PASS(0));
  k_attn<<<256, 1024, 0, stream>>>(ws, wqkv, wo, wg, wu, wd, (size_t)1 DIAG_PASS(1));
  k_res<<<256, 256, 0, stream>>>(ws + OFF_O, ws + OFF_WO, x, ws + OFF_X2, ws + OFF_ST2, ws, KSD, 1 DIAG_PASS(2));
  k_gu<<<256, 512, 0, stream>>>(ws DIAG_PASS(3));
  k_res<<<256, 256, 0, stream>>>(ws + OFF_H, ws + OFF_WD, ws + OFF_X2, ws + OFF_X3, ws + OFF_ST3, ws, KSF, 0 DIAG_PASS(4));
  k_qkv2<<<256, 512, 0, stream>>>(ws DIAG_PASS(5));
  k_attn<<<256, 1024, 0, stream>>>(ws, wqkv, wo, wg, wu, wd, (size_t)0 DIAG_PASS(6));
  k_res<<<256, 256, 0, stream>>>(ws + OFF_O, ws + OFF_WO, ws + OFF_X3, ws + OFF_X2, ws + OFF_ST2, ws, KSD, 0 DIAG_PASS(7));
  k_gu<<<256, 512, 0, stream>>>(ws DIAG_PASS(8));
  k_res<<<256, 256, 0, stream>>>(ws + OFF_H, ws + OFF_WD, ws + OFF_X2, (char*)d_out, ws + OFF_ST3, ws, KSF, 2 DIAG_PASS(9));
}
```

```cpp
#include <hip/hip_runtime.h>
#include <hip/hip_bf16.h>
#include <stdint.h>

typedef short bf16x8 __attribute__((ext_vector_type(8)));
typedef float f32x16 __attribute__((ext_vector_type(16)));
typedef float f32x4 __attribute__((ext_vector_type(4)));
typedef float f32x2 __attribute__((ext_vector_type(2)));
typedef unsigned u32x4 __attribute__((ext_vector_type(4)));
typedef unsigned u32x2 __attribute__((ext_vector_type(2)));
typedef __bf16 bf16v2 __attribute__((ext_vector_type(2)));
typedef unsigned short u16;

#define STAMP(i)
#define STAMP_DECL
#define STAMP_STORE()
#define DIAG_ARG
#define DIAG_PASS(k)
#define DI __device__ __forceinline__
#define MFMA(a, b, c) __builtin_amdgcn_mfma_f32_32x32x16_bf16((a), (b), (c), 0, 0, 0)
#define LDSP(T) __attribute__((address_space(3))) T*

constexpr int S_ = 2048, D_ = 512, H_ = 32, F_ = 1792;
constexpr int NTT = S_ / 32;
constexpr int KSD = D_ / 16;
constexpr int KSF = F_ / 16;
constexpr float QSCALE = 0.25f * 1.4426950408889634f;
constexpr float LN_EPS = 1e-5f;

constexpr size_t KB = 1024;
constexpr size_t OFF_WO = 0;
constexpr size_t OFF_WGU = OFF_WO + 512 * KB;
constexpr size_t OFF_WD = OFF_WGU + 3584 * KB;
constexpr size_t OFF_WQ = OFF_WD + 1792 * KB;
constexpr size_t OFF_CSG = OFF_WQ + 1536 * KB;
constexpr size_t OFF_CSP = OFF_CSG + 24 * KB;
constexpr size_t OFF_ST2 = OFF_CSP + 320 * KB;
constexpr size_t OFF_ST3 = OFF_ST2 + 256 * KB;
constexpr size_t OFF_X2 = OFF_ST3 + 256 * KB;
constexpr size_t OFF_X3 = OFF_X2 + 2048 * KB;
constexpr size_t OFF_Q = OFF_X3 + 2048 * KB;
constexpr size_t OFF_K = OFF_Q + 2048 * KB;
constexpr size_t OFF_V = OFF_K + 2048 * KB;
constexpr size_t OFF_O = OFF_V + 2048 * KB;
constexpr size_t OFF_H = OFF_O + 2048 * KB;

DI unsigned pk2(float lo, float hi) {
  f32x2 v = {lo, hi};
  bf16v2 r = __builtin_convertvector(v, bf16v2);
  return __builtin_bit_cast(unsigned, r);
}
DI float lo16(unsigned u) { return __uint_as_float(u << 16); }
DI float hi16(unsigned u) { return __uint_as_float(u & 0xffff0000u); }
DI float xhalf_add(float v) {
  auto r = __builtin_amdgcn_permlane32_swap(__float_as_uint(v), __float_as_uint(v), false, false);
  return __uint_as_float(r[0]) + __uint_as_float(r[1]);
}
DI float xhalf_max(float v) {
  auto r = __builtin_amdgcn_permlane32_swap(__float_as_uint(v), __float_as_uint(v), false, false);
  return fmaxf(__uint_as_float(r[0]), __uint_as_float(r[1]));
}
DI float x16_add(float v) {
  auto r = __builtin_amdgcn_permlane16_swap(__float_as_uint(v), __float_as_uint(v), false, false);
  return __uint_as_float(r[0]) + __uint_as_float(r[1]);
}
DI void st16(void* p, u32x4 v) { asm volatile("global_store_dwordx4 %0, %1, off sc1\n\ts_nop 1" :: "v"(p), "v"(v) : "memory"); }
template <int IMM>
DI void gld16(bf16x8& d, unsigned voff, const char* sbase) {
  asm volatile("global_load_dwordx4 %0, %1, %2 offset:%3" : "=v"(d) : "v"(voff), "s"(sbase), "i"(IMM) : "memory");
}
DI void glds1(unsigned voff, const char* sbase, unsigned lds) {
  unsigned keep;
  asm volatile("s_mov_b32 %0, m0\n\ts_mov_b32 m0, %3\n\ts_nop 0\n\tglobal_load_lds_dwordx4 %1, %2\n\ts_mov_b32 m0, %0"
               : "=&s"(keep) : "v"(voff), "s"(sbase), "s"(lds) : "memory");
}
DI void glds3(unsigned voff, const char* sbase, unsigned lds) {
  unsigned keep;
  asm volatile("s_mov_b32 %0, m0\n\ts_mov_b32 m0, %3\n\ts_nop 0\n\tglobal_load_lds_dwordx4 %1, %2\n\tglobal_load_lds_dwordx4 %1, %2 offset:1024\n\t"
               "global_load_lds_dwordx4 %1, %2 offset:2048\n\ts_mov_b32 m0, %0"
               : "=&s"(keep) : "v"(voff), "s"(sbase), "s"(lds) : "memory");
}
DI void glds4(unsigned voff, const char* sbase, unsigned lds) {
  unsigned keep;
  asm volatile("s_mov_b32 %0, m0\n\ts_mov_b32 m0, %3\n\ts_nop 0\n\tglobal_load_lds_dwordx4 %1, %2\n\tglobal_load_lds_dwordx4 %1, %2 offset:1024\n\t"
               "global_load_lds_dwordx4 %1, %2 offset:2048\n\tglobal_load_lds_dwordx4 %1, %2 offset:3072\n\ts_mov_b32 m0, %0"
               : "=&s"(keep) : "v"(voff), "s"(sbase), "s"(lds) : "memory");
}
#define SBAR() __builtin_amdgcn_sched_barrier(0)
#define PIN(x) asm volatile("" : "+v"(x))
#define WAITBAR_V(n) asm volatile("s_waitcnt vmcnt(%0) lgkmcnt(0)\n\ts_barrier" :: "i"(n) : "memory")
#define WAITBAR_L() do { asm volatile("" ::: "memory"); __builtin_amdgcn_s_waitcnt(0xc07f); __builtin_amdgcn_s_barrier(); asm volatile("" ::: "memory"); } while (0)
typedef short s16x4 __attribute__((ext_vector_type(4)));
DI s16x4 trread(LDSP(const char) p) { return __builtin_amdgcn_ds_read_tr16_b64_v4i16((LDSP(s16x4))p); }
DI bf16x8 trfrag(LDSP(const char) p, int off2) { const s16x4 lo = trread(p), hi = trread(p + off2); return __builtin_shufflevector(lo, hi, 0, 1, 2, 3, 4, 5, 6, 7); }
DI bf16x8 ldfrag(LDSP(const char) p) { return *(LDSP(const bf16x8))p; }
DI float dot2(unsigned a, unsigned b, float c) { return __builtin_amdgcn_fdot2_f32_bf16(__builtin_bit_cast(bf16v2, a), __builtin_bit_cast(bf16v2, b), c, false); }
constexpr unsigned ONE2 = 0x3f803f80u;

template <int LOOK, int PER> DI void ring_wait(int i) {
  if (i <= 8 - LOOK) WAITBAR_V((LOOK - 1) * PER);
  else if (LOOK > 3 && i == 5) WAITBAR_V(2 * PER);
  else if (LOOK > 2 && i == 6) WAITBAR_V(PER);
  else WAITBAR_V(0);
}

DI void qk_store(const float (&v)[16], char* o) {
  u32x4 a = {pk2(v[0], v[1]), pk2(v[2], v[3]), pk2(v[4], v[5]), pk2(v[6], v[7])};
  u32x4 b = {pk2(v[8], v[9]), pk2(v[10], v[11]), pk2(v[12], v[13]), pk2(v[14], v[15])};
  st16(o, a);
  st16(o + NTT * 1024, b);
}
DI void v_store(const float (&v)[16], char* o) {
  u32x4 a = {pk2(v[0], v[1]), pk2(v[2], v[3]), pk2(v[4], v[5]), pk2(v[6], v[7])};
  u32x4 b = {pk2(v[8], v[9]), pk2(v[10], v[11]), pk2(v[12], v[13]), pk2(v[14], v[15])};
  st16(o, a);
  st16(o + 512, b);
}
DI float row16_sum(float v) {
  v += __builtin_bit_cast(float, __builtin_amdgcn_update_dpp(0, __builtin_bit_cast(int, v), 0x128, 0xf, 0xf, false));
  v += __builtin_bit_cast(float, __builtin_amdgcn_update_dpp(0, __builtin_bit_cast(int, v), 0x124, 0xf, 0xf, false));
  v += __builtin_bit_cast(float, __builtin_amdgcn_update_dpp(0, __builtin_bit_cast(int, v), 0x122, 0xf, 0xf, false));
  v += __builtin_bit_cast(float, __builtin_amdgcn_update_dpp(0, __builtin_bit_cast(int, v), 0x121, 0xf, 0xf, false));
  return v;
}
#define KSTEP(B, u) do { SBAR(); RX((B) ^ 1, u); RW((B) ^ 1, 0, u); SBAR(); acc[0] = MFMA(wf[B][0], xf[B], acc[0]); SBAR(); RW((B) ^ 1, 1, u); RW((B) ^ 1, 2, u); SBAR(); \
                         acc[1] = MFMA(wf[B][1], xf[B], acc[1]); acc[2] = MFMA(xf[B], wf[B][2], acc[2]); SBAR(); } while (0)
#define QCHUNK() do { KSTEP(1, 0); KSTEP(0, 1); KSTEP(1, 2); KSTEP(0, 3); } while (0)
#define QINIT() \
  f32x16 acc[3]; bf16x8 wf[2][3], xf[2]; \
  _Pragma("unroll") for (int i = 0; i < 3; ++i) { acc[i] = f32x16{}; wf[1][i] = bf16x8{}; } \
  xf[1] = bf16x8{}
#define QFINISH() do { acc[0] = MFMA(wf[1][0], xf[1], acc[0]); acc[1] = MFMA(wf[1][1], xf[1], acc[1]); acc[2] = MFMA(xf[1], wf[1][2], acc[2]); } while (0)

extern "C" __global__ __launch_bounds__(768, 3) void k_qkv1(const char* __restrict__ x, const char* __restrict__ wn, char* __restrict__ ws DIAG_ARG) {
  STAMP_DECL; STAMP(0);
  constexpr int LOOK = 3, CH = 24576, LXN = 4 * CH;
  __shared__ __attribute__((aligned(16))) char smem[LXN + 65536];
  const int tid = threadIdx.x, lane = tid & 63, r = lane & 31, hh = lane >> 5;
  const int w = __builtin_amdgcn_readfirstlane(tid >> 6);
  const int xcd = blockIdx.x & 7, idx = blockIdx.x >> 3;
  const int tb = xcd * 4 + (idx & 3), nq = idx >> 2;
  const unsigned lds0 = (unsigned)(uintptr_t)smem;
  const int rot = 2 * (idx & 3);
  if (w >= 8) {
    const int j = w - 8;
    const unsigned wv = (lane >> 3) * 3072 + (((lane & 7) ^ (((lane >> 4) & 1) << 2)) * 16);
    const char* wb = wn + (size_t)j * 16 * 3072 + nq * 128;
    asm volatile("s_sleep 2\n\ts_nop 4" ::: "memory");
#pragma unroll 1
    for (int c = 0; c < 8 + LOOK; ++c) {
      if (c >= LOOK) ring_wait<LOOK, 6>(c - LOOK);
      if (c < 8) {
        const char* ws_ = wb + (size_t)((c + rot) & 7) * 64 * 3072;
        const unsigned d = lds0 + (c & 3) * CH + j * 2048;
        glds1(wv, ws_, d); glds1(wv, ws_ + 1024, d + 8192); glds1(wv, ws_ + 2048, d + 16384);
        glds1(wv, ws_ + 8 * 3072, d + 1024); glds1(wv, ws_ + 8 * 3072 + 1024, d + 8192 + 1024); glds1(wv, ws_ + 8 * 3072 + 2048, d + 16384 + 1024);
      }
    }
    return;
  }
  {
    const int n = w;
    const u32x4* xr = (const u32x4*)(x + (size_t)(tb * 64 + n * 8) * 1024) + lane;
    u32x4 v[8];
#pragma unroll
    for (int i = 0; i < 8; ++i) v[i] = xr[i * 64];
    float s[8], q[8];
#pragma unroll
    for (int i = 0; i < 8; ++i) {
      s[i] = 0.f; q[i] = 0.f;
#pragma unroll
      for (int e = 0; e < 4; ++e) { s[i] = dot2(v[i][e], ONE2, s[i]); q[i] = dot2(v[i][e], v[i][e], q[i]); }
    }
    float s4[4], q4[4], s2[2], q2[2];
#pragma unroll
    for (int i = 0; i < 4; ++i) {
      const int lo = (i & 1) + 4 * (i >> 1);
      auto a = __builtin_amdgcn_permlane32_swap(__float_as_uint(s[lo]), __float_as_uint(s[lo + 2]), false, false);
      auto b = __builtin_amdgcn_permlane32_swap(__float_as_uint(q[lo]), __float_as_uint(q[lo + 2]), false, false);
      s4[i] = __uint_as_float(a[0]) + __uint_as_float(a[1]); q4[i] = __uint_as_float(b[0]) + __uint_as_float(b[1]);
    }
#pragma unroll
    for (int i = 0; i < 2; ++i) {
      auto a = __builtin_amdgcn_permlane16_swap(__float_as_uint(s4[2 * i]), __float_as_uint(s4[2 * i + 1]), false, false);
      auto b = __builtin_amdgcn_permlane16_swap(__float_as_uint(q4[2 * i]), __float_as_uint(q4[2 * i + 1]), false, false);
      s2[i] = row16_sum(__uint_as_float(a[0]) + __uint_as_float(a[1])); q2[i] = row16_sum(__uint_as_float(b[0]) + __uint_as_float(b[1]));
    }
    float ra[2], rb[2];
#pragma unroll
    for (int i = 0; i < 2; ++i) {
      const float mean = s2[i] * (1.f / D_);
      ra[i] = rsqrtf(fmaxf(q2[i] * (1.f / D_) - mean * mean, 0.f) + LN_EPS);
      rb[i] = -mean * ra[i];
    }
    LDSP(char) xd = (LDSP(char))smem + LXN + (lane >> 3) * 8192 + n * 1024;
#pragma unroll
    for (int i = 0; i < 8; ++i) {
      const float a = __builtin_bit_cast(float, __builtin_amdgcn_readlane(__builtin_bit_cast(int, ra[i >> 2]), (i & 3) * 16));
      const float b = __builtin_bit_cast(float, __builtin_amdgcn_readlane(__builtin_bit_cast(int, rb[i >> 2]), (i & 3) * 16));
      u32x4 o;
#pragma unroll
      for (int e = 0; e < 4; ++e) o[e] = pk2(lo16(v[i][e]) * a + b, hi16(v[i][e]) * a + b);
      *(LDSP(u32x4))(xd + i * 128 + (((lane & 7) ^ ((4 * n + (i >> 1)) & 7)) * 16)) = o;
    }
  }
  STAMP(1);
  WAITBAR_L();
  if (w >= 4) return;
  STAMP(2);
  const int ta = w & 1, hp = w >> 1, q4 = (lane & 15) >> 2, xsw = (r >> 1) & 7;
  LDSP(const char) lw = (LDSP(const char))smem + (8 * hh + q4) * 128 + ((hp ^ (q4 >> 1)) * 64) + ((lane >> 4) & 1) * 32 + (lane & 3) * 8;
  LDSP(const char) lx = (LDSP(const char))smem + LXN + ta * 4096 + r * 128;
  int xo[4];
#pragma unroll
  for (int u = 0; u < 4; ++u) xo[u] = ((2 * u + hh) ^ xsw) * 16;
  QINIT();
#define RX(B, u) xf[B] = ldfrag(sx + xo[u])
#define RW(B, i, u) wf[B][i] = trfrag(sw + (i) * 8192 + (u) * 2048, 512)
#pragma unroll 1
  for (int c = 0; c < 8; ++c) {
    LDSP(const char) sw = lw + (c & 3) * CH;
    LDSP(const char) sx = lx + ((c + rot) & 7) * 8192;
    QCHUNK();
    WAITBAR_L();
  }
  QFINISH();
#undef RX
#undef RW
  STAMP(4);
  const int tt = tb * 2 + ta, nb = nq * 2 + hp;
  float v[16];
#pragma unroll
  for (int e = 0; e < 16; ++e) v[e] = acc[0][e] * QSCALE;
  qk_store(v, ws + OFF_Q + ((size_t)(2 * nb) * NTT + tt) * 1024 + lane * 16);
#pragma unroll
  for (int e = 0; e < 16; ++e) v[e] = acc[1][e];
  qk_store(v, ws + OFF_K + ((size_t)(2 * nb) * NTT + tt) * 1024 + lane * 16);
#pragma unroll
  for (int e = 0; e < 16; ++e) v[e] = acc[2][e];
  v_store(v, ws + OFF_V + ((size_t)(2 * nb + (r >> 4)) * NTT + tt) * 1024 + hh * 256 + (r & 15) * 16);
  STAMP(5); STAMP_STORE();
}

DI void stat_issue(f32x4 (&p)[4], const char* stats, int tt, unsigned voff) {
  const char* sb = stats + (size_t)tt * 4096;
  asm volatile("global_load_dwordx4 %0, %4, %5\n\tglobal_load_dwordx4 %1, %4, %5 offset:1024\n\tglobal_load_dwordx4 %2, %4, %5 offset:2048\n\t"
               "global_load_dwordx4 %3, %4, %5 offset:3072" : "=&v"(p[0]), "=&v"(p[1]), "=&v"(p[2]), "=&v"(p[3]) : "v"(voff), "s"(sb) : "memory");
}
DI void stat_finish(f32x4 (&p)[4], float& mean, float& var) {
  asm volatile("" : "+v"(p[0]), "+v"(p[1]), "+v"(p[2]), "+v"(p[3]));
  const f32x4 t = (p[0] + p[1]) + (p[2] + p[3]);
  auto a = __builtin_amdgcn_permlane16_swap(__float_as_uint(t[0]), __float_as_uint(t[2]), false, false);
  auto b = __builtin_amdgcn_permlane16_swap(__float_as_uint(t[1]), __float_as_uint(t[3]), false, false);
  mean = xhalf_add(__uint_as_float(a[0]) + __uint_as_float(a[1])) * (1.f / D_);
  var = fmaxf(xhalf_add(__uint_as_float(b[0]) + __uint_as_float(b[1])) * (1.f / D_) - mean * mean, 0.f) + LN_EPS;
}

extern "C" __global__ __launch_bounds__(512, 2) void k_qkv2(char* __restrict__ ws DIAG_ARG) {
  STAMP_DECL; STAMP(0);
  constexpr int NSLOT = 4, LOOK = 3, WCH = 12288, CH = WCH + 16384;
  constexpr int LCS = NSLOT * CH, LST = LCS + 1024;
  __shared__ __attribute__((aligned(16))) char smem[LST + 1024];
  const int tid = threadIdx.x, lane = tid & 63, r = lane & 31, hh = lane >> 5;
  const int w = __builtin_amdgcn_readfirstlane(tid >> 6);
  const int xcd = blockIdx.x & 7, idx = blockIdx.x >> 3;
  const int tb = (xcd & 3) * 4 + (idx & 3), nb = (xcd >> 2) * 8 + (idx >> 2);
  const unsigned voff = lane * 16;
  const unsigned lds0 = (unsigned)(uintptr_t)smem;
  const int tt = tb * 4 + (w & 3);
  if (w >= 4) {
    const int j = w - 4;
    const char* xb = ws + OFF_X3 + (size_t)tt * 32768;
    const char* wb = ws + OFF_WQ + (size_t)nb * 8 * WCH + j * 3072;
    f32x4 sp[4];
    asm volatile("s_nop 4" ::: "memory");
#define ISSUE(c) do { const unsigned d = lds0 + ((c) & 3) * CH; glds4(voff, xb + (c) * 4096, d + WCH + j * 4096); glds3(voff, wb + (c) * WCH, d + j * 3072); } while (0)
    ISSUE(0);
    stat_issue(sp, ws + OFF_ST3, tt, voff);
    glds1(voff, ws + OFF_CSG + (3584 + nb * 96) * 4, lds0 + LCS);
    ISSUE(1); ISSUE(2);
    WAITBAR_V(19);
    ISSUE(3);
    WAITBAR_V(14);
#pragma unroll 1
    for (int c = 4; c < 8; ++c) { ISSUE(c); WAITBAR_V(14); }
    { float mean, var; stat_finish(sp, mean, var); const float rs = rsqrtf(var);
      ((LDSP(float))((LDSP(char))smem + LST))[j * 64 + hh * 32 + (lane & 15) * 2 + ((lane >> 4) & 1)] = hh ? mean * rs : rs; }
    WAITBAR_V(7);
    WAITBAR_V(0);
#undef ISSUE
    return;
  }
  LDSP(const char) lp = (LDSP(const char))smem + lane * 16;
  const int xoff = WCH + w * 4096;
  QINIT();
#define RX(B, u) xf[B] = ldfrag(sl + xoff + (u) * 1024)
#define RW(B, i, u) wf[B][i] = ldfrag(sl + (i) * 4096 + (u) * 1024)
  STAMP(1);
  WAITBAR_L();
  STAMP(2);
#pragma unroll 1
  for (int c = 0; c < 8; ++c) {
    LDSP(const char) sl = lp + (c & 3) * CH;
    QCHUNK();
    WAITBAR_L();
  }
  LDSP(const float) cx = (LDSP(const float))((LDSP(char))smem + LCS);
  LDSP(const float) sx = (LDSP(const float))((LDSP(char))smem + LST) + w * 64;
  f32x4 cq[8], ra[4], rb[4];
  const float rstd = sx[r], mr = sx[32 + r], cv = cx[64 + r];
#pragma unroll
  for (int g = 0; g < 8; ++g) cq[g] = *(LDSP(const f32x4))(cx + 8 * g + 4 * hh);
#pragma unroll
  for (int g = 0; g < 4; ++g) { ra[g] = *(LDSP(const f32x4))(sx + 8 * g + 4 * hh); rb[g] = *(LDSP(const f32x4))(sx + 32 + 8 * g + 4 * hh); }
  SBAR();
  QFINISH();
  SBAR();
#undef RX
#undef RW
  STAMP(4);
  float v[16];
  const float rq = rstd * QSCALE, mq = mr * QSCALE;
#pragma unroll
  for (int e = 0; e < 16; ++e) v[e] = acc[0][e] * rq - mq * cq[e >> 2][e & 3];
  qk_store(v, ws + OFF_Q + ((size_t)(2 * nb) * NTT + tt) * 1024 + lane * 16);
#pragma unroll
  for (int e = 0; e < 16; ++e) v[e] = acc[1][e] * rstd - mr * cq[4 + (e >> 2)][e & 3];
  qk_store(v, ws + OFF_K + ((size_t)(2 * nb) * NTT + tt) * 1024 + lane * 16);
#pragma unroll
  for (int e = 0; e < 16; ++e) v[e] = acc[2][e] * ra[e >> 2][e & 3] - rb[e >> 2][e & 3] * cv;
  v_store(v, ws + OFF_V + ((size_t)(2 * nb + (r >> 4)) * NTT + tt) * 1024 + hh * 256 + (r & 15) * 16);
  STAMP(5); STAMP_STORE();
}

extern "C" __global__ __launch_bounds__(512, 2) void k_gu(char* __restrict__ ws DIAG_ARG) {
  STAMP_DECL; STAMP(0);
  constexpr int NSLOT = 3, LOOK = 2, WCH = 28672, CH = WCH + 16384, LCS = NSLOT * CH, LST = LCS + 1024;
  __shared__ __attribute__((aligned(16))) char smem[LST + 1024];
  const int tid = threadIdx.x, lane = tid & 63;
  const int w = __builtin_amdgcn_readfirstlane(tid >> 6);
  const int xcd = blockIdx.x & 7, idx = blockIdx.x >> 3;
  const int tb = (xcd & 1) * 8 + (idx & 7), nb = (xcd >> 1) * 4 + (idx >> 3);
  const unsigned voff = lane * 16;
  const unsigned lds0 = (unsigned)(uintptr_t)smem;
  const int tt = tb * 4 + (w & 3);
  if (w >= 4) {
    const int j = w - 4;
    const char* xb = ws + OFF_X2 + (size_t)tt * 32768;
    const char* wb = ws + OFF_WGU + (size_t)nb * 8 * WCH + j * 7168;
    f32x4 sp[4];
    asm volatile("s_nop 4" ::: "memory");
#define ISSUE(c, d) do { glds4(voff, xb + (c) * 4096, (d) + WCH + j * 4096); glds4(voff, wb + (c) * WCH, (d) + j * 7168); \
                         glds3(voff, wb + (c) * WCH + 4096, (d) + j * 7168 + 4096); } while (0)
    ISSUE(0, lds0);
    stat_issue(sp, ws + OFF_ST2, tt, voff);
    glds1(voff, ws + OFF_CSG + nb * 896, lds0 + LCS);
    ISSUE(1, lds0 + CH);
    WAITBAR_V(16);
    ISSUE(2, lds0 + 2 * CH);
    WAITBAR_V(11);
    unsigned d = lds0;
#pragma unroll 1
    for (int c = 3; c < 8; ++c) { ISSUE(c, d); d = d == lds0 + 2 * CH ? lds0 : d + CH; WAITBAR_V(11); }
    { float mean, var; stat_finish(sp, mean, var);
      ((LDSP(float))((LDSP(char))smem + LST))[j * 64 + (lane >> 5) * 32 + (lane & 15) * 2 + ((lane >> 4) & 1)] = (lane >> 5) ? mean : 1.f / var; }
    WAITBAR_V(0);
#undef ISSUE
    return;
  }
  const int g4 = lane >> 4, c16 = lane & 15;
  f32x4 ac[2][14];
#pragma unroll
  for (int i = 0; i < 14; ++i) { ac[0][i] = f32x4{}; ac[1][i] = f32x4{}; }
  LDSP(const char) lw = (LDSP(const char))smem + lane * 16;
  LDSP(const char) lx = (LDSP(const char))smem + WCH + w * 4096 + (g4 >> 1) * 1024 + ((g4 & 1) * 32 + c16) * 16;
  bf16x8 wf[2][7], xf[2][2];
  xf[1][0] = bf16x8{}; xf[1][1] = bf16x8{};
#pragma unroll
  for (int i = 0; i < 7; ++i) wf[1][i] = bf16x8{};
#define RDX(u, so, a) xf[u][a] = ldfrag(lx + (so) + (u) * 2048 + (a) * 256)
#define RDW(hf, u, so, i) wf[hf][i] = ldfrag(lw + (so) + ((7 * (hf) + (i)) * 2 + (u)) * 1024)
#define M1(B, XP, i, a) do { SBAR(); ac[a][7 * (B) + (i)] = __builtin_amdgcn_mfma_f32_16x16x32_bf16(wf[B][i], xf[XP][a], ac[a][7 * (B) + (i)], 0, 0, 0); SBAR(); } while (0)
#define MM(B, XP, i) do { M1(B, XP, i, 0); M1(B, XP, i, 1); } while (0)
#define GSTEP(B, XP, u, so) do { \
    M1(B, XP, 0, 0); RDW((B) ^ 1, u, so, 0); M1(B, XP, 0, 1); if ((B) == 1) RDX(u, so, 0); M1(B, XP, 1, 0); if ((B) == 1) RDX(u, so, 1); M1(B, XP, 1, 1); RDW((B) ^ 1, u, so, 1); \
    M1(B, XP, 2, 0); RDW((B) ^ 1, u, so, 2); M1(B, XP, 2, 1); RDW((B) ^ 1, u, so, 3); M1(B, XP, 3, 0); RDW((B) ^ 1, u, so, 4); M1(B, XP, 3, 1); RDW((B) ^ 1, u, so, 5); \
    M1(B, XP, 4, 0); RDW((B) ^ 1, u, so, 6); M1(B, XP, 4, 1); MM(B, XP, 5); MM(B, XP, 6); } while (0)
  STAMP(1);
  WAITBAR_L();
  STAMP(2);
  int so = 0;
#pragma unroll 1
  for (int c = 0; c < 8; ++c) {
    GSTEP(1, 1, 0, so); GSTEP(0, 0, 0, so); GSTEP(1, 0, 1, so); GSTEP(0, 1, 1, so);
    WAITBAR_L();
    so = so == 2 * CH ? 0 : so + CH;
  }
  LDSP(const float) sx = (LDSP(const float))((LDSP(char))smem + LST) + w * 64 + c16;
  const float rs0 = sx[0], rs1 = sx[16], m0 = sx[32], m1 = sx[48];
  f32x4 c4[14];
#pragma unroll
  for (int i = 0; i < 14; ++i) c4[i] = *(LDSP(const f32x4))((LDSP(const char))smem + LCS + i * 64 + g4 * 16);
  MM(1, 1, 0); MM(1, 1, 1); MM(1, 1, 2); MM(1, 1, 3); MM(1, 1, 4); MM(1, 1, 5); MM(1, 1, 6);
#undef RDX
#undef RDW
#undef M1
#undef MM
#undef GSTEP
  STAMP(4);
  char* ob = ws + OFF_H + ((size_t)tt * KSF + nb * 7) * 1024 + (((g4 & 1) * 32) + (g4 >> 1) * 16 + c16) * 16;
#pragma unroll
  for (int i = 0; i < 7; ++i) {
    const f32x4 ga = ac[0][2 * i] - c4[2 * i] * m0, ua = (ac[0][2 * i + 1] - c4[2 * i + 1] * m0) * rs0;
    const f32x4 gb = ac[1][2 * i] - c4[2 * i] * m1, ub = (ac[1][2 * i + 1] - c4[2 * i + 1] * m1) * rs1;
    float h0[4], h1[4];
#pragma unroll
    for (int e = 0; e < 4; ++e) { h0[e] = fmaxf(ga[e], 0.f) * ua[e]; h1[e] = fmaxf(gb[e], 0.f) * ub[e]; }
    auto e0 = __builtin_amdgcn_permlane32_swap(pk2(h0[0], h0[1]), pk2(h1[0], h1[1]), false, false);
    auto e1 = __builtin_amdgcn_permlane32_swap(pk2(h0[2], h0[3]), pk2(h1[2], h1[3]), false, false);
    u32x4 a = {e0[0], e1[0], e0[1], e1[1]};
    st16(ob + i * 1024, a);
  }
  STAMP(5); STAMP_STORE();
}

extern "C" __global__ __launch_bounds__(1024, 4) void k_attn(char* __restrict__ ws, const char* __restrict__ wq, const char* __restrict__ wo,
                                                             const char* __restrict__ wg, const char* __restrict__ wu,
                                                             const char* __restrict__ wd, size_t prep DIAG_ARG) {
  STAMP_DECL; STAMP(0);
  constexpr int LK = 0, LV = 65536, LO = 131072, LX = LO + 4096;
  __shared__ __attribute__((aligned(16))) char smem[LX + 24576];
  const int tid = threadIdx.x, lane = tid & 63, r = lane & 31, hh = lane >> 5;
  const int w = __builtin_amdgcn_readfirstlane(tid >> 6);
  const int wq_ = w & 7, half = w >> 3;
  const int xcd = blockIdx.x & 7, idx = blockIdx.x >> 3;
  const int head = xcd * 4 + (idx & 3), qb = idx >> 2;
  const int tt = qb * 8 + wq_;
  const unsigned voff = lane * 16;
  const unsigned lds0 = (unsigned)(uintptr_t)smem;
  const char* ks = ws + OFF_K + (size_t)head * 65536 + half * 32768;
  const unsigned kd = lds0 + LK + half * 32768;
  asm volatile("s_nop 4" ::: "memory");
  bf16x8 q;
  gld16<0>(q, voff, ws + OFF_Q + ((size_t)head * NTT + tt) * 1024);
  glds1(voff, ks + wq_ * 1024, kd + wq_ * 1024);
  glds1(voff, ks + (OFF_V - OFF_K) + wq_ * 1024, kd + LV + wq_ * 1024);
  { const unsigned one = ((tid >> 2) & 11) == 0 ? ONE2 : 0u;
    *(LDSP(unsigned))((LDSP(char))smem + LO + tid * 4) = one; }
  STAMP(1);
  asm volatile("s_waitcnt vmcnt(0) lgkmcnt(0)\n\ts_barrier" : "+v"(q) :: "memory");
  STAMP(2);
  glds3(voff, ks + 8192 + wq_ * 3072, kd + 8192 + wq_ * 3072);
  glds3(voff, ks + (OFF_V - OFF_K) + 8192 + wq_ * 3072, kd + LV + 8192 + wq_ * 3072);
  LDSP(const char) kp = (LDSP(const char))smem + LK + lane * 16;
  LDSP(const char) vp = (LDSP(const char))smem + (r < 16 ? LV : LO) + hh * 256 + (r & 15) * 16;
  const int vstep = r < 16 ? 4096 : 0;

  f32x16 negm, sA, sB;
  bf16x8 kA, kB;
  LDSP(const char) kq = kp + half * 32768; LDSP(const char) vq = vp + half * 8 * vstep;
  {
    const bf16x8 k0 = ldfrag(kp), k1 = ldfrag(kq);
    kA = ldfrag(kq + 1024);
    f32x16 s = MFMA(k0, q, f32x16{});
    float m = fmaxf(fmaxf(s[0], s[1]), s[2]);
#pragma unroll
    for (int i = 3; i < 15; i += 2) m = fmaxf(fmaxf(m, s[i]), s[i + 1]);
    m = fmaxf(m, s[15]);
    m = xhalf_max(m);
#pragma unroll
    for (int i = 0; i < 16; ++i) negm[i] = -m;
    PIN(negm);
    sA = MFMA(k1, q, negm);
  }
  f32x16 o = f32x16{};
#define EX(S, i) S[i] = __builtin_amdgcn_exp2f(S[i])
#define STEP(CUR, NXT, KC, KN, t)                                                                     \
  do {                                                                                                \
    const bf16x8 v0 = ldfrag(vq + (t) * 1024), v1 = ldfrag(vq + (t) * 1024 + 512);                    \
    KN = ldfrag(kq + ((t) + 2) * 1024);                                                               \
    SBAR();                                                                                           \
    EX(CUR, 0); EX(CUR, 1); EX(CUR, 2); EX(CUR, 3); PIN(CUR); SBAR();                                 \
    NXT = MFMA(KC, q, negm); SBAR();                                                                  \
    EX(CUR, 4); EX(CUR, 5); EX(CUR, 6); EX(CUR, 7);                                                   \
    u32x4 p0 = {pk2(CUR[0], CUR[1]), pk2(CUR[2], CUR[3]), pk2(CUR[4], CUR[5]), pk2(CUR[6], CUR[7])};  \
    PIN(p0); EX(CUR, 8); EX(CUR, 9); PIN(CUR); SBAR();                                                \
    o = MFMA(v0, __builtin_bit_cast(bf16x8, p0), o); SBAR();                                          \
    EX(CUR, 10); EX(CUR, 11); EX(CUR, 12); EX(CUR, 13); EX(CUR, 14); EX(CUR, 15);                     \
    u32x4 p1 = {pk2(CUR[8], CUR[9]), pk2(CUR[10], CUR[11]), pk2(CUR[12], CUR[13]), pk2(CUR[14], CUR[15])}; \
    PIN(p1); SBAR();                                                                                  \
    o = MFMA(v1, __builtin_bit_cast(bf16x8, p1), o); SBAR();                                          \
  } while (0)
  __builtin_amdgcn_s_setprio(3);
  STEP(sA, sB, kA, kB, 0); STEP(sB, sA, kB, kA, 1); STEP(sA, sB, kA, kB, 2); STEP(sB, sA, kB, kA, 3);
  kq += 4096; vq += vstep;
  WAITBAR_V(0);
  const int job = prep ? w * 256 + blockIdx.x : 4096;
  unsigned pa[4] = {}, pb[4] = {}, pc[4] = {}, pe[4] = {};
  char* pd = nullptr; int pd2 = 1024;
  if (job < 3712) {
    const char *sa, *sb; int ld; unsigned lo;
    if (job < 1792) {
      const int ft = job >> 4, S = job & 15, nb = ft / 7, ii = ft - nb * 7;
      ld = F_ * 2; lo = (16 * hh + 4 * ((lane >> 4) & 1)) * (F_ * 2) + (lane & 15) * 2;
      sa = wg + 32 * S * (F_ * 2) + ft * 32; sb = wu + 32 * S * (F_ * 2) + ft * 32;
      pd = ws + OFF_WGU + ((size_t)((nb * 8 + (S >> 1)) * 14 + 2 * ii) * 2 + (S & 1)) * 1024; pd2 = 2048;
    } else {
      const char* src; int nt, s0;
      if (job < 2048) { const int f = (job - 1792) * 2; nt = f >> 5; s0 = f & 31; src = wo; ld = 1024; pd = ws + OFF_WO + (size_t)f * 1024; }
      else if (job < 2944) { const int f = (job - 2048) * 2; nt = f / KSF; s0 = f - nt * KSF; src = wd; ld = 1024; pd = ws + OFF_WD + (size_t)f * 1024; }
      else { const int t = (job - 2944) >> 4, nb = t / 3, ii = t - nb * 3; s0 = ((job - 2944) & 15) * 2; nt = ii * 16 + nb; src = wq; ld = 3072;
             pd = ws + OFF_WQ + (size_t)(((nb * 8 + (s0 >> 2)) * 3 + ii) * 4 + (s0 & 3)) * 1024; }
      lo = 4 * hh * ld + r * 2;
      sa = src + (size_t)16 * s0 * ld + nt * 64; sb = sa + 16 * ld;
    }
    asm volatile("s_nop 4" ::: "memory");
#pragma unroll
    for (int e = 0; e < 4; ++e) {
      const int ro = (8 * (e >> 1) + 2 * (e & 1)) * ld;
      asm volatile("global_load_ushort %0, %1, %2" : "=v"(pa[e]) : "v"(lo), "s"(sa + ro) : "memory");
      asm volatile("global_load_ushort %0, %1, %2" : "=v"(pc[e]) : "v"(lo), "s"(sa + ro + ld) : "memory");
      asm volatile("global_load_ushort %0, %1, %2" : "=v"(pb[e]) : "v"(lo), "s"(sb + ro) : "memory");
      asm volatile("global_load_ushort %0, %1, %2" : "=v"(pe[e]) : "v"(lo), "s"(sb + ro + ld) : "memory");
    }
  }

#pragma unroll 1
  for (int g = 1; g < 8; ++g) {
    if (g == 3) __builtin_amdgcn_s_setprio(2);
    if (g == 5) {
      __builtin_amdgcn_s_setprio(1);
      if (job < 3712) {
        u32x4 fa, fb;
        asm volatile("s_waitcnt vmcnt(0)\n\tv_lshl_or_b32 %0, %8, 16, %4\n\tv_lshl_or_b32 %1, %9, 16, %5\n\tv_lshl_or_b32 %2, %10, 16, %6\n\tv_lshl_or_b32 %3, %11, 16, %7"
                     : "=&v"(fa[0]), "=&v"(fa[1]), "=&v"(fa[2]), "=&v"(fa[3])
                     : "v"(pa[0]), "v"(pa[1]), "v"(pa[2]), "v"(pa[3]), "v"(pc[0]), "v"(pc[1]), "v"(pc[2]), "v"(pc[3]) : "memory");
        asm volatile("v_lshl_or_b32 %0, %8, 16, %4\n\tv_lshl_or_b32 %1, %9, 16, %5\n\tv_lshl_or_b32 %2, %10, 16, %6\n\tv_lshl_or_b32 %3, %11, 16, %7"
                     : "=&v"(fb[0]), "=&v"(fb[1]), "=&v"(fb[2]), "=&v"(fb[3])
                     : "v"(pb[0]), "v"(pb[1]), "v"(pb[2]), "v"(pb[3]), "v"(pe[0]), "v"(pe[1]), "v"(pe[2]), "v"(pe[3]) : "memory");
        st16(pd + voff, fa); st16(pd + pd2 + voff, fb);
        if (job < 1792 || job >= 2944) {
          float ca = 0.f, cb = 0.f;
#pragma unroll
          for (int e = 0; e < 4; ++e) { ca = dot2(fa[e], ONE2, ca); cb = dot2(fb[e], ONE2, cb); }
          if (job < 1792) { ca = xhalf_add(x16_add(ca)); cb = xhalf_add(x16_add(cb)); ca = lane < 16 ? ca : cb; }
          else ca = xhalf_add(ca + cb);
          if (lane < 32) ((float*)(ws + OFF_CSP))[(job & 15) * 5120 + ((job < 1792 ? job : job - 2944 + 1792) >> 4) * 32 + lane] = ca;
        }
      }
    }
    if (g == 7) __builtin_amdgcn_s_setprio(0);
    STEP(sA, sB, kA, kB, 0); STEP(sB, sA, kB, kA, 1); STEP(sA, sB, kA, kB, 2); STEP(sB, sA, kB, kA, 3);
    kq += 4096; vq += vstep;
  }
#undef STEP
  STAMP(4);
  LDSP(char) xp = (LDSP(char))smem + LX + wq_ * 3072 + lane * 16;
  if (half == 1) {
    f32x4 a = {o[0], o[1], o[2], o[3]}, b = {o[4], o[5], o[6], o[7]};
    *(LDSP(f32x4))xp = a; *(LDSP(f32x4))(xp + 1024) = b; *(LDSP(float))(xp + 2048) = o[8];
  }
  WAITBAR_L();
  if (half == 1) return;
  {
    const f32x4 a = *(LDSP(const f32x4))xp, b = *(LDSP(const f32x4))(xp + 1024);
    const float l = *(LDSP(const float))(xp + 2048);
#pragma unroll
    for (int i = 0; i < 4; ++i) { o[i] += a[i]; o[4 + i] += b[i]; }
    o[8] += l;
  }
  bool bad = !(o[8] < 1.8446744e19f);
#pragma unroll
  for (int i = 0; i < 8; ++i) bad = bad || !(fabsf(o[i]) < 3.0e38f);
  if (__builtin_expect(__any(bad), 0)) {
    float m = -INFINITY;
    o = f32x16{};
    kq = kp; vq = vp;
#pragma unroll 1
    for (int t = 0; t < 64; ++t) {
      const bf16x8 k = ldfrag(kq), v0 = ldfrag(vq), v1 = ldfrag(vq + 512);
      f32x16 s = MFMA(k, q, f32x16{});
      float tm = s[0];
#pragma unroll
      for (int i = 1; i < 16; ++i) tm = fmaxf(tm, s[i]);
      tm = xhalf_max(tm);
      const float mn = fmaxf(m, tm);
      const float alpha = __builtin_amdgcn_exp2f(m - mn);
      m = mn;
#pragma unroll
      for (int i = 0; i < 16; ++i) { o[i] *= alpha; s[i] = __builtin_amdgcn_exp2f(s[i] - mn); }
      u32x4 p0 = {pk2(s[0], s[1]), pk2(s[2], s[3]), pk2(s[4], s[5]), pk2(s[6], s[7])};
      u32x4 p1 = {pk2(s[8], s[9]), pk2(s[10], s[11]), pk2(s[12], s[13]), pk2(s[14], s[15])};
      o = MFMA(v0, __builtin_bit_cast(bf16x8, p0), o);
      o = MFMA(v1, __builtin_bit_cast(bf16x8, p1), o);
      kq += 1024; vq += vstep >> 2;
    }
  }
  const float inv = 1.0f / o[8];
  u32x4 a = {pk2(o[0] * inv, o[1] * inv), pk2(o[2] * inv, o[3] * inv), pk2(o[4] * inv, o[5] * inv), pk2(o[6] * inv, o[7] * inv)};
  st16(ws + OFF_O + ((size_t)tt * KSD + head) * 1024 + lane * 16, a);
  STAMP(5); STAMP_STORE();
}

extern "C" __global__ __launch_bounds__(256, 2) void k_res(const char* __restrict__ xt, const char* __restrict__ wt, const char* __restrict__ res,
                                                           char* __restrict__ out, char* __restrict__ stats, char* __restrict__ ws, int ks,
                                                           int mode DIAG_ARG) {
  STAMP_DECL; STAMP(0);
  __shared__ __attribute__((aligned(16))) char smem[49152];
  const int tid = threadIdx.x, lane = tid & 63, r = lane & 31, hh = lane >> 5;
  const int w = __builtin_amdgcn_readfirstlane(tid >> 6);
  const int xcd = blockIdx.x & 7, idx = blockIdx.x >> 3;
  const int tb = xcd * 4 + (idx & 3), nb = idx >> 2;
  const unsigned voff = lane * 16;
  const int kw = ks >> 2;
  const int ont = nb * 2 + (w >> 1), ott = tb * 2 + (w & 1);
  const char* xa = xt + ((size_t)ott * ks + w * kw) * 1024;
  const char* xb = xt + ((size_t)(ott ^ 1) * ks + w * kw) * 1024;
  const char* wa = wt + ((size_t)ont * ks + w * kw) * 1024;
  const char* wb = wt + ((size_t)(ont ^ 1) * ks + w * kw) * 1024;
  asm volatile("s_nop 4" ::: "memory");
  bf16x8 x0[4], x1[4], w0[4], w1[4];
#define LD(b) do { gld16<(b) * 1024>(w0[b], voff, wa); gld16<(b) * 1024>(x0[b], voff, xa); gld16<(b) * 1024>(w1[b], voff, wb); gld16<(b) * 1024>(x1[b], voff, xb); } while (0)
#define MM(b, n) do { asm volatile("s_waitcnt vmcnt(%4)" : "+v"(w0[b]), "+v"(x0[b]), "+v"(w1[b]), "+v"(x1[b]) : "i"(n) : "memory"); \
    acc[0] = MFMA(w0[b], x0[b], acc[0]); acc[1] = MFMA(w0[b], x1[b], acc[1]); acc[2] = MFMA(w1[b], x0[b], acc[2]); acc[3] = MFMA(w1[b], x1[b], acc[3]); SBAR(); } while (0)
  LD(0); LD(1); LD(2); LD(3);
  f32x16 acc[4];
#pragma unroll
  for (int i = 0; i < 4; ++i) acc[i] = f32x16{};
  const bool nat = mode & 1;
  unsigned ro = nat ? r * 1024 + hh * 8 : lane * 16;
  const char* rb = res + (size_t)ott * 32768 + (nat ? ont * 64 : ont * 2048);
  const int d1 = nat ? 16 : 8, d2 = nat ? 32 : 1024;
  float cp[16];
  const bool csjob = nat && blockIdx.x < 20;
  if (csjob) {
    const float* csp = (const float*)(ws + OFF_CSP) + blockIdx.x * 256 + tid;
#pragma unroll
    for (int i = 0; i < 16; ++i) cp[i] = csp[i * 5120];
  }
  PIN(ro);
  int it = (kw >> 2) - 1;
#pragma unroll 1
  do {
    xa += 4096; xb += 4096; wa += 4096; wb += 4096;
    MM(0, 12); LD(0); SBAR(); MM(1, 12); LD(1); SBAR(); MM(2, 12); LD(2); SBAR(); MM(3, 12); LD(3); SBAR();
  } while (--it > 0);
  u32x2 rn[4];
  asm volatile("global_load_dwordx2 %0, %1, %2" : "=v"(rn[0]) : "v"(ro), "s"(rb) : "memory");
  asm volatile("global_load_dwordx2 %0, %1, %2" : "=v"(rn[1]) : "v"(ro), "s"(rb + d1) : "memory");
  asm volatile("global_load_dwordx2 %0, %1, %2" : "=v"(rn[2]) : "v"(ro), "s"(rb + d2) : "memory");
  asm volatile("global_load_dwordx2 %0, %1, %2" : "=v"(rn[3]) : "v"(ro), "s"(rb + d2 + d1) : "memory");
  MM(0, 16); MM(1, 12); MM(2, 8); MM(3, 4);
#undef LD
#undef MM
  STAMP(4);
  LDSP(char) sp = (LDSP(char))smem + lane * 16;
#pragma unroll
  for (int i = 1; i < 4; ++i)
#pragma unroll
    for (int g = 0; g < 4; ++g) {
      f32x4 v = {acc[i][4 * g], acc[i][4 * g + 1], acc[i][4 * g + 2], acc[i][4 * g + 3]};
      *(LDSP(f32x4))(sp + (((w ^ i) * 3 + (i - 1)) * 4 + g) * 1024) = v;
    }
  asm volatile("s_waitcnt vmcnt(0) lgkmcnt(0)\n\ts_barrier" : "+v"(rn[0]), "+v"(rn[1]), "+v"(rn[2]), "+v"(rn[3]) :: "memory");
  u32x4 p0, p1;
#pragma unroll
  for (int g = 0; g < 4; ++g) {
    const f32x4 a = *(LDSP(const f32x4))(sp + ((w * 3 + 0) * 4 + g) * 1024), b = *(LDSP(const f32x4))(sp + ((w * 3 + 1) * 4 + g) * 1024);
    const f32x4 c = *(LDSP(const f32x4))(sp + ((w * 3 + 2) * 4 + g) * 1024);
    float v[4];
#pragma unroll
    for (int e = 0; e < 4; ++e) v[e] = (a[e] + b[e]) + (c[e] + acc[0][4 * g + e]);
    const unsigned u0 = pk2(v[0] + lo16(rn[g][0]), v[1] + hi16(rn[g][0])), u1 = pk2(v[2] + lo16(rn[g][1]), v[3] + hi16(rn[g][1]));
    if (g < 2) { p0[2 * g] = u0; p0[2 * g + 1] = u1; } else { p1[2 * g - 4] = u0; p1[2 * g - 3] = u1; }
  }
  if (!(mode & 2)) {
    char* o = out + ((size_t)ott * KSD + 2 * ont) * 1024 + lane * 16;
    st16(o, p0);
    st16(o + 1024, p1);
    float s1 = 0.f, s2 = 0.f;
#pragma unroll
    for (int e = 0; e < 4; ++e) { s1 = dot2(p0[e], ONE2, s1); s2 = dot2(p0[e], p0[e], s2); s1 = dot2(p1[e], ONE2, s1); s2 = dot2(p1[e], p1[e], s2); }
    s1 = xhalf_add(s1); s2 = xhalf_add(s2);
    if (lane < 32) { f32x2 st = {s1, s2}; *(f32x2*)(stats + (size_t)ott * 4096 + ont * 256 + r * 8) = st; }
    if (csjob) {
#pragma unroll
      for (int d = 8; d >= 1; d >>= 1)
#pragma unroll
        for (int i = 0; i < d; ++i) cp[i] += cp[i + d];
      ((float*)(ws + OFF_CSG))[blockIdx.x * 256 + tid] = cp[0];
    }
  } else {
    auto s0 = __builtin_amdgcn_permlane32_swap(p0[0], p0[2], false, false);
    auto s1 = __builtin_amdgcn_permlane32_swap(p0[1], p0[3], false, false);
    auto s2 = __builtin_amdgcn_permlane32_swap(p1[0], p1[2], false, false);
    auto s3 = __builtin_amdgcn_permlane32_swap(p1[1], p1[3], false, false);
    u32x4 a = {s0[0], s1[0], s0[1], s1[1]}, b = {s2[0], s3[0], s2[1], s3[1]};
    char* o = out + ((size_t)(ott * 32 + r) * D_ + ont * 32) * 2 + hh * 16;
    st16(o, a);
    st16(o + 32, b);
  }
  STAMP(5); STAMP_STORE();
}

extern "C" void kernel_launch(void* const* d_in, const int* in_sizes, int n_in, void* d_out, int out_size, void* d_ws, size_t ws_size,
                              hipStream_t stream) {
  const char* x = (const char*)d_in[0];
  const char* wqkv = (const char*)d_in[1];
  const char* wo = (const char*)d_in[2];
  const char* wg = (const char*)d_in[4];
  const char* wu = (const char*)d_in[5];
  const char* wd = (const char*)d_in[6];
  char* ws = (char*)d_ws;
  k_qkv1<<<256, 768, 0, stream>>>(x, wqkv, ws DIAG_PASS(0));
  k_attn<<<256, 1024, 0, stream>>>(ws, wqkv, wo, wg, wu, wd, (size_t)1 DIAG_PASS(1));
  k_res<<<256, 256, 0, stream>>>(ws + OFF_O, ws + OFF_WO, x, ws + OFF_X2, ws + OFF_ST2, ws, KSD, 1 DIAG_PASS(2));
  k_gu<<<256, 512, 0, stream>>>(ws DIAG_PASS(3));
  k_res<<<256, 256, 0, stream>>>(ws + OFF_H, ws + OFF_WD, ws + OFF_X2, ws + OFF_X3, ws + OFF_ST3, ws, KSF, 0 DIAG_PASS(4));
  k_qkv2<<<256, 512, 0, stream>>>(ws DIAG_PASS(5));
  k_attn<<<256, 1024, 0, stream>>>(ws, wqkv, wo, wg, wu, wd, (size_t)0 DIAG_PASS(6));
  k_res<<<256, 256, 0, stream>>>(ws + OFF_O, ws + OFF_WO, ws + OFF_X3, ws + OFF_X2, ws + OFF_ST2, ws, KSD, 0 DIAG_PASS(7));
  k_gu<<<256, 512, 0, stream>>>(ws DIAG_PASS(8));
  k_res<<<256, 256, 0, stream>>>(ws + OFF_H, ws + OFF_WD, ws + OFF_X2, (char*)d_out, ws + OFF_ST3, ws, KSF, 2 DIAG_PASS(9));
}
```

```cpp
#include <hip/hip_runtime.h>
#include <hip/hip_bf16.h>
#include <stdint.h>

typedef short bf16x8 __attribute__((ext_vector_type(8)));
typedef float f32x16 __attribute__((ext_vector_type(16)));
typedef float f32x4 __attribute__((ext_vector_type(4)));
typedef float f32x2 __attribute__((ext_vector_type(2)));
typedef unsigned u32x4 __attribute__((ext_vector_type(4)));
typedef unsigned u32x2 __attribute__((ext_vector_type(2)));
typedef __bf16 bf16v2 __attribute__((ext_vector_type(2)));
typedef unsigned short u16;

#define STAMP(i)
#define STAMP_DECL
#define PSTAMP(i)
#define STAMP_STORE()
#define DIAG_ARG
#define DIAG_PASS(k)
#define DI __device__ __forceinline__
#define MFMA(a, b, c) __builtin_amdgcn_mfma_f32_32x32x16_bf16((a), (b), (c), 0, 0, 0)
#define LDSP(T) __attribute__((address_space(3))) T*

constexpr int S_ = 2048, D_ = 512, H_ = 32, F_ = 1792;
constexpr int NTT = S_ / 32;
constexpr int KSD = D_ / 16;
constexpr int KSF = F_ / 16;
constexpr float QSCALE = 0.25f * 1.4426950408889634f;
constexpr float LN_EPS = 1e-5f;

constexpr size_t KB = 1024;
constexpr size_t OFF_CNT = 0;
constexpr size_t OFF_WO = 16 * KB;
constexpr size_t OFF_WGU = OFF_WO + 512 * KB;
constexpr size_t OFF_WD = OFF_WGU + 3584 * KB;
constexpr size_t OFF_WQ = OFF_WD + 1792 * KB;
constexpr size_t OFF_CSP = OFF_WQ + 1536 * KB;
constexpr size_t OFF_ST3 = OFF_CSP + 320 * KB;
constexpr size_t OFF_X3 = OFF_ST3 + 256 * KB;
constexpr size_t OFF_L = OFF_X3 + 2048 * KB;
constexpr size_t OFF_Q = OFF_L;
constexpr size_t OFF_K = OFF_Q + 2048 * KB;
constexpr size_t OFF_V = OFF_K + 2048 * KB;
constexpr size_t OFF_O = OFF_V + 2048 * KB;
constexpr size_t OFF_X2 = OFF_O + 2048 * KB;
constexpr size_t OFF_ST2 = OFF_X2 + 2048 * KB;
constexpr size_t OFF_H = OFF_ST2 + 256 * KB;
constexpr size_t LSTRIDE = OFF_H + 7168 * KB - OFF_L;
constexpr int C_O = 0, C_X2 = 16, C_H = 48, C_X3 = 80, C_KV = 96, C_PREP = 112, C_N = 128;

DI unsigned pk2(float lo, float hi) {
  f32x2 v = {lo, hi};
  bf16v2 r = __builtin_convertvector(v, bf16v2);
  return __builtin_bit_cast(unsigned, r);
}
DI float lo16(unsigned u) { return __uint_as_float(u << 16); }
DI float hi16(unsigned u) { return __uint_as_float(u & 0xffff0000u); }
DI float xhalf_add(float v) {
  auto r = __builtin_amdgcn_permlane32_swap(__float_as_uint(v), __float_as_uint(v), false, false);
  return __uint_as_float(r[0]) + __uint_as_float(r[1]);
}
DI float xhalf_max(float v) {
  auto r = __builtin_amdgcn_permlane32_swap(__float_as_uint(v), __float_as_uint(v), false, false);
  return fmaxf(__uint_as_float(r[0]), __uint_as_float(r[1]));
}
DI float x16_add(float v) {
  auto r = __builtin_amdgcn_permlane16_swap(__float_as_uint(v), __float_as_uint(v), false, false);
  return __uint_as_float(r[0]) + __uint_as_float(r[1]);
}
DI void st16(void* p, u32x4 v) { asm volatile("global_store_dwordx4 %0, %1, off sc1\n\ts_nop 1" :: "v"(p), "v"(v) : "memory"); }
template <int IMM>
DI void st16s(unsigned voff, const char* sbase, u32x4 v) {
  asm volatile("global_store_dwordx4 %0, %1, %2 offset:%3 sc1\n\ts_nop 1" :: "v"(voff), "v"(v), "s"(sbase), "i"(IMM) : "memory");
}
template <int IMM>
DI void gld16(bf16x8& d, unsigned voff, const char* sbase) {
  asm volatile("global_load_dwordx4 %0, %1, %2 offset:%3 sc1" : "=v"(d) : "v"(voff), "s"(sbase), "i"(IMM) : "memory");
}
#define GLDS_HEAD "s_mov_b32 %0, m0\n\ts_mov_b32 m0, %3\n\ts_nop 0\n\t"
#define GLDS_OP(o) "global_load_lds_dwordx4 %1, %2 offset:" #o " sc1\n\t"
#define GLDS_TAIL "s_mov_b32 m0, %0" : "=&s"(keep) : "v"(voff), "s"(sbase), "s"(lds) : "memory"
DI void glds1(unsigned voff, const char* sbase, unsigned lds) { unsigned keep; asm volatile(GLDS_HEAD GLDS_OP(0) GLDS_TAIL); }
DI void glds2(unsigned voff, const char* sbase, unsigned lds) { unsigned keep; asm volatile(GLDS_HEAD GLDS_OP(0) GLDS_OP(1024) GLDS_TAIL); }
DI void glds3(unsigned voff, const char* sbase, unsigned lds) { unsigned keep; asm volatile(GLDS_HEAD GLDS_OP(0) GLDS_OP(1024) GLDS_OP(2048) GLDS_TAIL); }
DI void glds4(unsigned voff, const char* sbase, unsigned lds) { unsigned keep; asm volatile(GLDS_HEAD GLDS_OP(0) GLDS_OP(1024) GLDS_OP(2048) GLDS_OP(3072) GLDS_TAIL); }
#define SBAR() __builtin_amdgcn_sched_barrier(0)
#define PIN(x) asm volatile("" : "+v"(x))
#define WAITV(n) asm volatile("s_waitcnt vmcnt(%0)" :: "i"(n) : "memory")
#define WAITBAR_V(n) asm volatile("s_waitcnt vmcnt(%0) lgkmcnt(0)\n\ts_barrier" :: "i"(n) : "memory")
#define WAITBAR_L() do { asm volatile("" ::: "memory"); __builtin_amdgcn_s_waitcnt(0xc07f); __builtin_amdgcn_s_barrier(); asm volatile("" ::: "memory"); } while (0)
typedef short s16x4 __attribute__((ext_vector_type(4)));
DI s16x4 trread(LDSP(const char) p) { return __builtin_amdgcn_ds_read_tr16_b64_v4i16((LDSP(s16x4))p); }
DI bf16x8 trfrag(LDSP(const char) p, int off2) { const s16x4 lo = trread(p), hi = trread(p + off2); return __builtin_shufflevector(lo, hi, 0, 1, 2, 3, 4, 5, 6, 7); }
DI bf16x8 ldfrag(LDSP(const char) p) { return *(LDSP(const bf16x8))p; }
DI float dot2(unsigned a, unsigned b, float c) { return __builtin_amdgcn_fdot2_f32_bf16(__builtin_bit_cast(bf16v2, a), __builtin_bit_cast(bf16v2, b), c, false); }
constexpr unsigned ONE2 = 0x3f803f80u;
#define BC8(x) __builtin_bit_cast(bf16x8, x)

template <int LOOK, int PER> DI void ring_wait(int i) {
  if (i <= 8 - LOOK) WAITBAR_V((LOOK - 1) * PER);
  else if (LOOK > 3 && i == 5) WAITBAR_V(2 * PER);
  else if (LOOK > 2 && i == 6) WAITBAR_V(PER);
  else WAITBAR_V(0);
}

DI void qk_store(const float (&v)[16], char* o) {
  u32x4 a = {pk2(v[0], v[1]), pk2(v[2], v[3]), pk2(v[4], v[5]), pk2(v[6], v[7])};
  u32x4 b = {pk2(v[8], v[9]), pk2(v[10], v[11]), pk2(v[12], v[13]), pk2(v[14], v[15])};
  st16(o, a);
  st16(o + NTT * 1024, b);
}
DI void v_store(const float (&v)[16], char* o) {
  u32x4 a = {pk2(v[0], v[1]), pk2(v[2], v[3]), pk2(v[4], v[5]), pk2(v[6], v[7])};
  u32x4 b = {pk2(v[8], v[9]), pk2(v[10], v[11]), pk2(v[12], v[13]), pk2(v[14], v[15])};
  st16(o, a);
  st16(o + 512, b);
}
DI float row16_sum(float v) {
  v += __builtin_bit_cast(float, __builtin_amdgcn_update_dpp(0, __builtin_bit_cast(int, v), 0x128, 0xf, 0xf, false));
  v += __builtin_bit_cast(float, __builtin_amdgcn_update_dpp(0, __builtin_bit_cast(int, v), 0x124, 0xf, 0xf, false));
  v += __builtin_bit_cast(float, __builtin_amdgcn_update_dpp(0, __builtin_bit_cast(int, v), 0x122, 0xf, 0xf, false));
  v += __builtin_bit_cast(float, __builtin_amdgcn_update_dpp(0, __builtin_bit_cast(int, v), 0x121, 0xf, 0xf, false));
  return v;
}
#define KSTEP(B, u) do { SBAR(); RX((B) ^ 1, u); RW((B) ^ 1, 0, u); SBAR(); acc[0] = MFMA(wf[B][0], xf[B], acc[0]); SBAR(); RW((B) ^ 1, 1, u); RW((B) ^ 1, 2, u); SBAR(); \
                         acc[1] = MFMA(wf[B][1], xf[B], acc[1]); acc[2] = MFMA(xf[B], wf[B][2], acc[2]); SBAR(); } while (0)
#define QCHUNK() do { KSTEP(1, 0); KSTEP(0, 1); KSTEP(1, 2); KSTEP(0, 3); } while (0)
#define QINIT() \
  f32x16 acc[3]; bf16x8 wf[2][3], xf[2]; \
  _Pragma("unroll") for (int i = 0; i < 3; ++i) { acc[i] = f32x16{}; wf[1][i] = bf16x8{}; } \
  xf[1] = bf16x8{}
#define QFINISH() do { acc[0] = MFMA(wf[1][0], xf[1], acc[0]); acc[1] = MFMA(wf[1][1], xf[1], acc[1]); acc[2] = MFMA(xf[1], wf[1][2], acc[2]); } while (0)

extern "C" __global__ __launch_bounds__(768, 3) void k_qkv1(const char* __restrict__ x, const char* __restrict__ wn, char* __restrict__ ws DIAG_ARG) {
  STAMP_DECL; STAMP(0);
  constexpr int LOOK = 3, CH = 24576, LXN = 4 * CH;
  __shared__ __attribute__((aligned(16))) char smem[LXN + 65536];
  const int tid = threadIdx.x, lane = tid & 63, r = lane & 31, hh = lane >> 5;
  const int w = __builtin_amdgcn_readfirstlane(tid >> 6);
  const int xcd = blockIdx.x & 7, idx = blockIdx.x >> 3;
  const int tb = xcd * 4 + (idx & 3), nq = idx >> 2;
  const unsigned lds0 = (unsigned)(uintptr_t)smem;
  const int rot = 2 * (idx & 3);
  if (w >= 8) {
    const int j = w - 8;
    const unsigned wv = (lane >> 3) * 3072 + (((lane & 7) ^ (((lane >> 4) & 1) << 2)) * 16);
    const char* wb = wn + (size_t)j * 16 * 3072 + nq * 128;
    if (blockIdx.x == 0 && lane < 32) *(unsigned*)(ws + OFF_CNT + (j * 32 + lane) * 128) = 0u;
    asm volatile("s_sleep 2\n\ts_nop 4" ::: "memory");
#pragma unroll 1
    for (int c = 0; c < 8 + LOOK; ++c) {
      if (c >= LOOK) ring_wait<LOOK, 6>(c - LOOK);
      if (c < 8) {
        const char* ws_ = wb + (size_t)((c + rot) & 7) * 64 * 3072;
        const unsigned d = lds0 + (c & 3) * CH + j * 2048;
        glds1(wv, ws_, d); glds1(wv, ws_ + 1024, d + 8192); glds1(wv, ws_ + 2048, d + 16384);
        glds1(wv, ws_ + 8 * 3072, d + 1024); glds1(wv, ws_ + 8 * 3072 + 1024, d + 8192 + 1024); glds1(wv, ws_ + 8 * 3072 + 2048, d + 16384 + 1024);
      }
    }
    return;
  }
  {
    const int n = w;
    const u32x4* xr = (const u32x4*)(x + (size_t)(tb * 64 + n * 8) * 1024) + lane;
    u32x4 v[8];
#pragma unroll
    for (int i = 0; i < 8; ++i) v[i] = xr[i * 64];
    float s[8], q[8];
#pragma unroll
    for (int i = 0; i < 8; ++i) {
      s[i] = 0.f; q[i] = 0.f;
#pragma unroll
      for (int e = 0; e < 4; ++e) { s[i] = dot2(v[i][e], ONE2, s[i]); q[i] = dot2(v[i][e], v[i][e], q[i]); }
    }
    float s4[4], q4[4], s2[2], q2[2];
#pragma unroll
    for (int i = 0; i < 4; ++i) {
      const int lo = (i & 1) + 4 * (i >> 1);
      auto a = __builtin_amdgcn_permlane32_swap(__float_as_uint(s[lo]), __float_as_uint(s[lo + 2]), false, false);
      auto b = __builtin_amdgcn_permlane32_swap(__float_as_uint(q[lo]), __float_as_uint(q[lo + 2]), false, false);
      s4[i] = __uint_as_float(a[0]) + __uint_as_float(a[1]); q4[i] = __uint_as_float(b[0]) + __uint_as_float(b[1]);
    }
#pragma unroll
    for (int i = 0; i < 2; ++i) {
      auto a = __builtin_amdgcn_permlane16_swap(__float_as_uint(s4[2 * i]), __float_as_uint(s4[2 * i + 1]), false, false);
      auto b = __builtin_amdgcn_permlane16_swap(__float_as_uint(q4[2 * i]), __float_as_uint(q4[2 * i + 1]), false, false);
      s2[i] = row16_sum(__uint_as_float(a[0]) + __uint_as_float(a[1])); q2[i] = row16_sum(__uint_as_float(b[0]) + __uint_as_float(b[1]));
    }
    float ra[2], rb[2];
#pragma unroll
    for (int i = 0; i < 2; ++i) {
      const float mean = s2[i] * (1.f / D_);
      ra[i] = rsqrtf(fmaxf(q2[i] * (1.f / D_) - mean * mean, 0.f) + LN_EPS);
      rb[i] = -mean * ra[i];
    }
    LDSP(char) xd = (LDSP(char))smem + LXN + (lane >> 3) * 8192 + n * 1024;
#pragma unroll
    for (int i = 0; i < 8; ++i) {
      const float a = __builtin_bit_cast(float, __builtin_amdgcn_readlane(__builtin_bit_cast(int, ra[i >> 2]), (i & 3) * 16));
      const float b = __builtin_bit_cast(float, __builtin_amdgcn_readlane(__builtin_bit_cast(int, rb[i >> 2]), (i & 3) * 16));
      u32x4 o;
#pragma unroll
      for (int e = 0; e < 4; ++e) o[e] = pk2(lo16(v[i][e]) * a + b, hi16(v[i][e]) * a + b);
      *(LDSP(u32x4))(xd + i * 128 + (((lane & 7) ^ ((4 * n + (i >> 1)) & 7)) * 16)) = o;
    }
  }
  STAMP(1);
  WAITBAR_L();
  if (w >= 4) return;
  STAMP(2);
  const int ta = w & 1, hp = w >> 1, q4 = (lane & 15) >> 2, xsw = (r >> 1) & 7;
  LDSP(const char) lw = (LDSP(const char))smem + (8 * hh + q4) * 128 + ((hp ^ (q4 >> 1)) * 64) + ((lane >> 4) & 1) * 32 + (lane & 3) * 8;
  LDSP(const char) lx = (LDSP(const char))smem + LXN + ta * 4096 + r * 128;
  int xo[4];
#pragma unroll
  for (int u = 0; u < 4; ++u) xo[u] = ((2 * u + hh) ^ xsw) * 16;
  QINIT();
#define RX(B, u) xf[B] = ldfrag(sx + xo[u])
#define RW(B, i, u) wf[B][i] = trfrag(sw + (i) * 8192 + (u) * 2048, 512)
#pragma unroll 1
  for (int c = 0; c < 8; ++c) {
    LDSP(const char) sw = lw + (c & 3) * CH;
    LDSP(const char) sx = lx + ((c + rot) & 7) * 8192;
    QCHUNK();
    WAITBAR_L();
  }
  QFINISH();
#undef RX
#undef RW
  STAMP(4);
  const int tt = tb * 2 + ta, nb = nq * 2 + hp;
  float v[16];
#pragma unroll
  for (int e = 0; e < 16; ++e) v[e] = acc[0][e] * QSCALE;
  qk_store(v, ws + OFF_Q + ((size_t)(2 * nb) * NTT + tt) * 1024 + lane * 16);
#pragma unroll
  for (int e = 0; e < 16; ++e) v[e] = acc[1][e];
  qk_store(v, ws + OFF_K + ((size_t)(2 * nb) * NTT + tt) * 1024 + lane * 16);
#pragma unroll
  for (int e = 0; e < 16; ++e) v[e] = acc[2][e];
  v_store(v, ws + OFF_V + ((size_t)(2 * nb + (r >> 4)) * NTT + tt) * 1024 + hh * 256 + (r & 15) * 16);
  STAMP(5); STAMP_STORE();
}

DI void poll(const char* cnt, unsigned voff, unsigned target) {
#pragma unroll 1
  for (unsigned spins = 0; spins < 200000u; ++spins) {
    unsigned v;
    asm volatile("global_load_dword %0, %1, %2 sc1\n\ts_waitcnt vmcnt(0)" : "=v"(v) : "v"(voff), "s"(cnt) : "memory");
    if (__all(v >= target)) break;
    __builtin_amdgcn_s_sleep(1);
  }
}
DI void poll_b(const char* cnt, unsigned target, LDSP(unsigned) go, unsigned seq, int j) {
  if (j == 0) {
    poll(cnt, 0u, target);
    __hip_atomic_store(go, seq, __ATOMIC_RELAXED, __HIP_MEMORY_SCOPE_WORKGROUP);
  } else {
#pragma unroll 1
    for (unsigned spins = 0; spins < 4000000u; ++spins) {
      if (__builtin_amdgcn_readfirstlane(__hip_atomic_load(go, __ATOMIC_RELAXED, __HIP_MEMORY_SCOPE_WORKGROUP)) >= seq) break;
      __builtin_amdgcn_s_sleep(1);
    }
    WAITV(0);
  }
  asm volatile("" ::: "memory");
}
DI void arrive4(LDSP(unsigned) lw, char* cnt, int lane) {
  WAITV(0);
  if (lane == 0) {
    const unsigned old = __hip_atomic_fetch_add(lw, 1u, __ATOMIC_RELAXED, __HIP_MEMORY_SCOPE_WORKGROUP);
    if ((old & 3u) == 3u) asm volatile("global_atomic_add %0, %1, %2" :: "v"(0u), "v"(1u), "s"(cnt) : "memory");
  }
  asm volatile("" ::: "memory");
}
DI void stat_issue(f32x4 (&p)[4], const char* sb, unsigned voff) {
  asm volatile("global_load_dwordx4 %0, %4, %5 sc1\n\tglobal_load_dwordx4 %1, %4, %5 offset:1024 sc1\n\tglobal_load_dwordx4 %2, %4, %5 offset:2048 sc1\n\t"
               "global_load_dwordx4 %3, %4, %5 offset:3072 sc1" : "=&v"(p[0]), "=&v"(p[1]), "=&v"(p[2]), "=&v"(p[3]) : "v"(voff), "s"(sb) : "memory");
}
DI void stat_finish(f32x4 (&p)[4], float& mean, float& var) {
  asm volatile("" : "+v"(p[0]), "+v"(p[1]), "+v"(p[2]), "+v"(p[3]));
  const f32x4 t = (p[0] + p[1]) + (p[2] + p[3]);
  auto a = __builtin_amdgcn_permlane16_swap(__float_as_uint(t[0]), __float_as_uint(t[2]), false, false);
  auto b = __builtin_amdgcn_permlane16_swap(__float_as_uint(t[1]), __float_as_uint(t[3]), false, false);
  mean = xhalf_add(__uint_as_float(a[0]) + __uint_as_float(a[1])) * (1.f / D_);
  var = fmaxf(xhalf_add(__uint_as_float(b[0]) + __uint_as_float(b[1])) * (1.f / D_) - mean * mean, 0.f) + LN_EPS;
}
DI void colsum_part(const char* src, unsigned voff, LDSP(char) dst) {
  f32x4 p[4];
  asm volatile("global_load_dwordx4 %0, %4, %5 sc1\n\tglobal_load_dwordx4 %1, %4, %6 sc1\n\tglobal_load_dwordx4 %2, %4, %7 sc1\n\t"
               "global_load_dwordx4 %3, %4, %8 sc1\n\ts_waitcnt vmcnt(0)" : "=&v"(p[0]), "=&v"(p[1]), "=&v"(p[2]), "=&v"(p[3])
               : "v"(voff), "s"(src), "s"(src + 20480), "s"(src + 40960), "s"(src + 61440) : "memory");
  *(LDSP(f32x4))dst = (p[0] + p[1]) + (p[2] + p[3]);
}

#define RES_FINISH(WR, RD) do { \
    _Pragma("unroll") for (int i = 1; i < 4; ++i) _Pragma("unroll") for (int g = 0; g < 4; ++g) { \
      const f32x4 v_ = {acc[i][4 * g], acc[i][4 * g + 1], acc[i][4 * g + 2], acc[i][4 * g + 3]}; \
      *(LDSP(f32x4))(lb + (WR) + g * 1024) = v_; } \
    WAITBAR_L(); \
    _Pragma("unroll") for (int g = 0; g < 4; ++g) { \
      f32x4 t_ = {acc[0][4 * g], acc[0][4 * g + 1], acc[0][4 * g + 2], acc[0][4 * g + 3]}; \
      f32x4 u_[3]; \
      _Pragma("unroll") for (int i = 1; i < 4; ++i) u_[i - 1] = *(LDSP(const f32x4))(lb + (RD) + g * 1024); \
      t_ = (u_[0] + u_[1]) + (u_[2] + t_); \
      res[2 * g] = pk2(t_[0] + lo16(res[2 * g]), t_[1] + hi16(res[2 * g])); \
      res[2 * g + 1] = pk2(t_[2] + lo16(res[2 * g + 1]), t_[3] + hi16(res[2 * g + 1])); } } while (0)
DI void res_store_tiled(const unsigned (&res)[8], char* o, char* st, int lane) {
  const u32x4 p0 = {res[0], res[1], res[2], res[3]}, p1 = {res[4], res[5], res[6], res[7]};
  st16(o, p0);
  st16(o + 1024, p1);
  float s1 = 0.f, s2 = 0.f;
#pragma unroll
  for (int e = 0; e < 8; ++e) { s1 = dot2(res[e], ONE2, s1); s2 = dot2(res[e], res[e], s2); }
  s1 = xhalf_add(s1); s2 = xhalf_add(s2);
  if (lane < 32) { const f32x2 v = {s1, s2}; asm volatile("global_store_dwordx2 %0, %1, off sc1" :: "v"(st), "v"(v) : "memory"); }
}

extern "C" __global__ __launch_bounds__(512, 2) void k_main(char* __restrict__ ws_, const char* __restrict__ x, const char* __restrict__ wq,
                                                            const char* __restrict__ wo, const char* __restrict__ wg, const char* __restrict__ wu,
                                                            const char* __restrict__ wd, char* __restrict__ out DIAG_ARG) {
  constexpr int L_K = 0, L_V = 65536, L_ONE = 131072, L_CMB = 133120;
  constexpr int GW = 28672, GCH = GW + 16384;
  constexpr int L_DW = 24576;
  constexpr int QW = 12288, L_QX = 49152;
  constexpr int L_CS = 157696, L_ST = L_CS + 4096, L_CNT = L_ST + 1024, L_DBG = L_CNT + 64;
  __shared__ __attribute__((aligned(16))) char smem[163840];
  const int w = __builtin_amdgcn_readfirstlane(threadIdx.x >> 6);
  const unsigned lds0 = (unsigned)(uintptr_t)smem;
#define FRESH() \
  int tid = threadIdx.x; PIN(tid); int bid = blockIdx.x, j = w & 3; char* ws = ws_; asm volatile("" : "+s"(bid), "+s"(j), "+s"(ws)); \
  const int lane = tid & 63, r = lane & 31, hh = lane >> 5, xcd = bid & 7, idx = bid >> 3, grp = xcd * 2 + (idx >> 4), m = idx & 15; \
  const unsigned voff = lane * 16; LDSP(char) lb = (LDSP(char))smem + lane * 16; char* cnt = ws + OFF_CNT; char* wl = ws + layer * LSTRIDE; \
  LDSP(unsigned) lcnt = (LDSP(unsigned))((LDSP(char))smem + L_CNT) + (w >> 2); \
  const int ont = (m >> 1) * 2 + (j >> 1), ott = grp * 4 + (m & 1) * 2 + (j & 1); \
  const int gtb = (xcd & 1) * 8 + (idx & 7), gnb = (xcd >> 1) * 4 + (idx >> 3), qtb = (xcd & 3) * 4 + (idx & 3), qnb = (xcd >> 2) * 8 + (idx >> 2); \
  (void)r; (void)hh; (void)voff; (void)lb; (void)cnt; (void)wl; (void)lcnt; (void)ont; (void)ott; (void)gtb; (void)gnb; (void)qtb; (void)qnb
  unsigned res[8];
  if (threadIdx.x < 3) ((LDSP(unsigned))((LDSP(char))smem + L_CNT))[threadIdx.x] = 0u;
  if (w >= 4) {
    const int layer = 0;
    FRESH();
    const char* rp = x + (size_t)(ott * 32 + r) * 1024 + ont * 64 + hh * 8;
#pragma unroll
    for (int g = 0; g < 4; ++g) { const u32x2 t = *(const u32x2*)(rp + g * 16); res[2 * g] = t[0]; res[2 * g + 1] = t[1]; }
  } else {
#pragma unroll
    for (int g = 0; g < 8; ++g) res[g] = 0u;
  }
#pragma unroll 1
  for (int layer = 0; layer < 2; ++layer) {
    {
      FRESH();
      const int head = xcd * 4 + (idx & 3), qb = idx >> 2, half = w >> 2, tt0 = qb * 8 + 2 * j;
      PSTAMP(0);
      if (layer) { if (w == 4) poll(cnt + (C_KV + (head >> 1)) * 128, 0u, 16u); WAITBAR_L(); }
      PSTAMP(1);
      const char* ks = wl + OFF_K + (size_t)head * 65536 + half * 32768;
      const unsigned kd = lds0 + L_K + half * 32768;
      bf16x8 q0, q1;
      gld16<0>(q0, voff, wl + OFF_Q + ((size_t)head * NTT + tt0) * 1024);
      gld16<1024>(q1, voff, wl + OFF_Q + ((size_t)head * NTT + tt0) * 1024);
      glds2(voff, ks + j * 2048, kd + j * 2048);
      glds2(voff, ks + (OFF_V - OFF_K) + j * 2048, kd + L_V + j * 2048);
      *(LDSP(unsigned))((LDSP(char))smem + L_ONE + tid * 4) = ((tid >> 2) & 11) == 0 ? ONE2 : 0u;
      asm volatile("s_waitcnt vmcnt(0) lgkmcnt(0)\n\ts_barrier" : "+v"(q0), "+v"(q1) :: "memory");
      PSTAMP(2);
      glds3(voff, ks + 8192 + j * 6144, kd + 8192 + j * 6144);
      glds3(voff, ks + (OFF_V - OFF_K) + 8192 + j * 6144, kd + L_V + 8192 + j * 6144);
      glds3(voff, ks + 11264 + j * 6144, kd + 11264 + j * 6144);
      glds3(voff, ks + (OFF_V - OFF_K) + 11264 + j * 6144, kd + L_V + 11264 + j * 6144);
      LDSP(const char) kp = lb + L_K;
      LDSP(const char) vp0 = (LDSP(const char))smem + (r < 16 ? L_V : L_ONE) + hh * 256 + (r & 15) * 16;
      const int vstep = r < 16 ? 2048 : 0;
      LDSP(const char) kq = kp + half * 32768; LDSP(const char) vq = vp0 + half * 16 * vstep;
      f32x16 n0, n1, sA, sB, o0 = f32x16{}, o1 = f32x16{};
      bf16x8 kA, kB, vp = bf16x8{};
      u32x4 p1 = {0u, 0u, 0u, 0u};
      {
        const bf16x8 k0 = ldfrag(kp);
        kA = ldfrag(kq);
        const f32x16 s = MFMA(k0, q0, f32x16{}), t = MFMA(k0, q1, f32x16{});
        float a = fmaxf(fmaxf(s[0], s[1]), s[2]), b = fmaxf(fmaxf(t[0], t[1]), t[2]);
#pragma unroll
        for (int i = 3; i < 15; i += 2) { a = fmaxf(fmaxf(a, s[i]), s[i + 1]); b = fmaxf(fmaxf(b, t[i]), t[i + 1]); }
        a = xhalf_max(fmaxf(a, s[15])); b = xhalf_max(fmaxf(b, t[15]));
#pragma unroll
        for (int i = 0; i < 16; ++i) { n0[i] = -a; n1[i] = -b; }
        PIN(n0); PIN(n1);
        sA = MFMA(kA, q0, n0);
      }
#define EX(S, i) S[i] = __builtin_amdgcn_exp2f(S[i])
#define STEPN(CUR, NXT, KN_, QN, NN, O, VA, OP, VP, READS) do { \
      EX(CUR, 0); EX(CUR, 1); PIN(CUR); SBAR(); OP = MFMA(VP, BC8(p1), OP); SBAR(); READS; SBAR(); \
      EX(CUR, 2); EX(CUR, 3); EX(CUR, 4); EX(CUR, 5); PIN(CUR); SBAR(); NXT = MFMA(KN_, QN, NN); SBAR(); \
      u32x4 p0; EX(CUR, 6); EX(CUR, 7); p0[0] = pk2(CUR[0], CUR[1]); p0[1] = pk2(CUR[2], CUR[3]); PIN(CUR); SBAR(); \
      EX(CUR, 8); EX(CUR, 9); p0[2] = pk2(CUR[4], CUR[5]); p0[3] = pk2(CUR[6], CUR[7]); PIN(p0); PIN(CUR); SBAR(); \
      EX(CUR, 10); EX(CUR, 11); PIN(CUR); SBAR(); O = MFMA(VA, BC8(p0), O); SBAR(); \
      EX(CUR, 12); EX(CUR, 13); EX(CUR, 14); EX(CUR, 15); \
      p1[0] = pk2(CUR[8], CUR[9]); p1[1] = pk2(CUR[10], CUR[11]); p1[2] = pk2(CUR[12], CUR[13]); p1[3] = pk2(CUR[14], CUR[15]); PIN(p1); SBAR(); } while (0)
#define TILE(t, KA, KB) do { bf16x8 va, vb; \
      STEPN(sA, sB, KA, q1, n1, o0, va, o1, vp, (va = ldfrag(vq + (t) * 1024), vb = ldfrag(vq + (t) * 1024 + 512), KB = ldfrag(kq + ((t) + 1) * 1024))); \
      STEPN(sB, sA, KB, q0, n0, o1, va, o0, vb, (vp = vb)); } while (0)
      __builtin_amdgcn_s_setprio(3);
#pragma unroll 1
      for (int g = 0; g < 3; ++g) { TILE(0, kA, kB); TILE(1, kB, kA); kq += 2048; vq += vstep; }
      WAITBAR_V(0);
#pragma unroll 1
      for (int ph = 0; ph < 4; ++ph) {
        if (ph == 1) __builtin_amdgcn_s_setprio(2);
        if (ph == 2) __builtin_amdgcn_s_setprio(1);
        if (ph == 3) __builtin_amdgcn_s_setprio(0);
        if (layer == 0) {
          const int q4 = (lane & 15) >> 2, b16 = (lane >> 4) & 1;
          const int scr = L_CMB + (half ? j * 5120 : 20480 + j * 2048);
          const int jo = ((ph - 1) * 8 + w) * 256 + bid;
          if ((ph == 1 || ph == 2) && jo < 3712) {
            const bool gu = jo < 1792;
            LDSP(const char) sp = (LDSP(const char))smem + scr + (lane & 3) * 8 +
                                  (gu ? (16 * hh + 4 * b16 + q4) * 32 : (4 * hh + q4) * 64 + b16 * 32);
            const int ho = gu ? 256 : 512;
            const char* pd;
            if (gu) { const int ft = jo >> 4, S = jo & 15, nb = ft / 7, ii = ft - nb * 7; pd = ws + OFF_WGU + ((size_t)((nb * 8 + (S >> 1)) * 14 + 2 * ii) * 2 + (S & 1)) * 1024; }
            else if (jo < 2048) pd = ws + OFF_WO + (size_t)(jo - 1792) * 2048;
            else if (jo < 2944) pd = ws + OFF_WD + (size_t)(jo - 2048) * 2048;
            else { const int t = (jo - 2944) >> 4, nb = t / 3, ii = t - nb * 3, s0 = (jo & 15) * 2; pd = ws + OFF_WQ + (size_t)(((nb * 8 + (s0 >> 2)) * 3 + ii) * 4 + (s0 & 3)) * 1024; }
            WAITV(0);
            const u32x4 fa = __builtin_bit_cast(u32x4, trfrag(sp, ho)), fb = __builtin_bit_cast(u32x4, trfrag(sp + 1024, ho));
            st16s<0>(voff, pd, fa); st16s<0>(voff, pd + (gu ? 2048 : 1024), fb);
            if (gu || jo >= 2944) {
              float ca = (lo16(fa[0]) + hi16(fa[0])) + (lo16(fa[1]) + hi16(fa[1])) + (lo16(fa[2]) + hi16(fa[2])) + (lo16(fa[3]) + hi16(fa[3]));
              float cb = (lo16(fb[0]) + hi16(fb[0])) + (lo16(fb[1]) + hi16(fb[1])) + (lo16(fb[2]) + hi16(fb[2])) + (lo16(fb[3]) + hi16(fb[3]));
              if (gu) { ca = xhalf_add(x16_add(ca)); cb = xhalf_add(x16_add(cb)); ca = lane < 16 ? ca : cb; }
              else ca = xhalf_add(ca + cb);
              const char* cp = ws + OFF_CSP + ((jo & 15) * 5120 + ((gu ? jo : jo - 2944 + 1792) >> 4) * 32) * 4;
              if (lane < 32) asm volatile("global_store_dword %0, %1, %2 sc1" :: "v"(lane * 4), "v"(ca), "s"(cp) : "memory");
            }
          }
          const int jn = jo + 2048;
          if (ph < 2 && jn < 3712) {
            const char *sa, *sb; unsigned gv;
            if (jn < 1792) {
              const int o = 32 * (jn & 15) * (F_ * 2) + (jn >> 4) * 32;
              gv = (lane >> 1) * (F_ * 2) + (lane & 1) * 16; sa = wg + o; sb = wu + o;
            } else {
              const char* src; int nt, s0, ld = 1024;
              if (jn < 2048) { const int f = (jn - 1792) * 2; nt = f >> 5; s0 = f & 31; src = wo; }
              else if (jn < 2944) { const int f = (jn - 2048) * 2; nt = f / KSF; s0 = f - nt * KSF; src = wd; }
              else { const int t = (jn - 2944) >> 4, nb = t / 3; s0 = (jn & 15) * 2; nt = (t - nb * 3) * 16 + nb; src = wq; ld = 3072; }
              gv = (lane >> 2) * ld + (lane & 3) * 16; sa = src + (size_t)16 * s0 * ld + nt * 64; sb = sa + 16 * ld;
            }
            __builtin_amdgcn_s_waitcnt(0xc07f);
            glds1(gv, sa, lds0 + scr); glds1(gv, sb, lds0 + scr + 1024);
          }
          if (ph == 3) arrive4(lcnt, cnt + (C_PREP + xcd) * 128, lane);
        }
        int n = ph == 0 ? 3 : ph == 3 ? 2 : 4;
#pragma unroll 1
        do { TILE(0, kA, kB); TILE(1, kB, kA); kq += 2048; vq += vstep; } while (--n > 0);
      }
      o1 = MFMA(vp, BC8(p1), o1);
#undef TILE
#undef STEPN
      PSTAMP(3);
      LDSP(char) xp = lb + L_CMB + j * 5120;
      if (half == 1) {
        const f32x4 a = {o0[0], o0[1], o0[2], o0[3]}, b = {o0[4], o0[5], o0[6], o0[7]}, c = {o1[0], o1[1], o1[2], o1[3]}, d = {o1[4], o1[5], o1[6], o1[7]};
        const f32x2 l = {o0[8], o1[8]};
        *(LDSP(f32x4))xp = a; *(LDSP(f32x4))(xp + 1024) = b; *(LDSP(f32x4))(xp + 2048) = c; *(LDSP(f32x4))(xp + 3072) = d; *(LDSP(f32x2))(xp + 4096) = l;
      }
      WAITBAR_L();
      if (half == 0) {
        PSTAMP(11);
        const f32x4 a = *(LDSP(const f32x4))xp, b = *(LDSP(const f32x4))(xp + 1024), c = *(LDSP(const f32x4))(xp + 2048), d = *(LDSP(const f32x4))(xp + 3072);
        const f32x2 l = *(LDSP(const f32x2))(xp + 4096);
#pragma unroll
        for (int i = 0; i < 4; ++i) { o0[i] += a[i]; o0[4 + i] += b[i]; o1[i] += c[i]; o1[4 + i] += d[i]; }
        o0[8] += l[0]; o1[8] += l[1];
        float chk = (o0[8] + o1[8]) * 5.4210109e-20f;
#pragma unroll
        for (int i = 0; i < 8; ++i) chk = __builtin_fmaf(o0[i], 0.f, __builtin_fmaf(o1[i], 0.f, chk));
        const bool bad = !(chk < 1.f);
        if (__builtin_expect(__any(bad), 0)) {
#pragma unroll 1
          for (int z = 0; z < 2; ++z) {
            const bf16x8 q = z ? q1 : q0;
            float mx = -INFINITY;
            f32x16 o = f32x16{};
            kq = kp; vq = vp0;
#pragma unroll 1
            for (int t = 0; t < 64; ++t) {
              const bf16x8 k = ldfrag(kq), v0 = ldfrag(vq), v1 = ldfrag(vq + 512);
              f32x16 s = MFMA(k, q, f32x16{});
              float tm = s[0];
#pragma unroll
              for (int i = 1; i < 16; ++i) tm = fmaxf(tm, s[i]);
              tm = xhalf_max(tm);
              const float mn = fmaxf(mx, tm);
              const float alpha = __builtin_amdgcn_exp2f(mx - mn);
              mx = mn;
#pragma unroll
              for (int i = 0; i < 16; ++i) { o[i] *= alpha; s[i] = __builtin_amdgcn_exp2f(s[i] - mn); }
              const u32x4 pa = {pk2(s[0], s[1]), pk2(s[2], s[3]), pk2(s[4], s[5]), pk2(s[6], s[7])};
              const u32x4 pb = {pk2(s[8], s[9]), pk2(s[10], s[11]), pk2(s[12], s[13]), pk2(s[14], s[15])};
              o = MFMA(v0, BC8(pa), o);
              o = MFMA(v1, BC8(pb), o);
              kq += 1024; vq += vstep >> 1;
            }
            if (z) o1 = o; else o0 = o;
          }
        }
        const float i0 = __builtin_amdgcn_rcpf(o0[8]), i1 = __builtin_amdgcn_rcpf(o1[8]);
        const u32x4 a0 = {pk2(o0[0] * i0, o0[1] * i0), pk2(o0[2] * i0, o0[3] * i0), pk2(o0[4] * i0, o0[5] * i0), pk2(o0[6] * i0, o0[7] * i0)};
        const u32x4 a1 = {pk2(o1[0] * i1, o1[1] * i1), pk2(o1[2] * i1, o1[3] * i1), pk2(o1[4] * i1, o1[5] * i1), pk2(o1[6] * i1, o1[7] * i1)};
        char* op = wl + OFF_O + ((size_t)tt0 * KSD + head) * 1024 + voff;
        st16(op, a0); st16(op + 32768, a1);
        PSTAMP(12);
        arrive4(lcnt, cnt + (C_O + layer * 8 + qb) * 128, lane);
      }
    }
    PSTAMP(4);
    if (layer == 0) poll(ws_ + OFF_CNT + C_PREP * 128, (threadIdx.x & 7) * 128, 64u);
    if (w < 4) {
      WAITBAR_L();
      {
        FRESH();
        const char* wb = ws + OFF_WGU + (size_t)gnb * 8 * GW + j * 7168;
#pragma unroll 1
        for (int c = 0; c < 3; ++c) { glds4(voff, wb + c * GW, lds0 + c * GCH + j * 7168); glds3(voff, wb + c * GW + 4096, lds0 + c * GCH + j * 7168 + 4096); }
        colsum_part(ws + OFF_CSP + (size_t)j * 81920 + gnb * 896, voff, lb + L_CS + j * 1024);
      }
      PSTAMP(14);
      WAITBAR_L();
      PSTAMP(15);
      {
        FRESH();
        const int g4 = lane >> 4, c16 = lane & 15;
        f32x4 ac[2][14];
#pragma unroll
        for (int i = 0; i < 14; ++i) { ac[0][i] = f32x4{}; ac[1][i] = f32x4{}; }
        LDSP(const char) lx = (LDSP(const char))smem + GW + j * 4096 + (g4 >> 1) * 1024 + ((g4 & 1) * 32 + c16) * 16;
        bf16x8 wf[2][7], xf[2][2];
        xf[1][0] = bf16x8{}; xf[1][1] = bf16x8{};
#pragma unroll
        for (int i = 0; i < 7; ++i) wf[1][i] = bf16x8{};
#define RDX(u, so, a) xf[u][a] = ldfrag(lx + (so) + (u) * 2048 + (a) * 256)
#define RDW(hf, u, so, i) wf[hf][i] = ldfrag(lb + (so) + ((7 * (hf) + (i)) * 2 + (u)) * 1024)
#define M1(B, XP, i, a) do { SBAR(); ac[a][7 * (B) + (i)] = __builtin_amdgcn_mfma_f32_16x16x32_bf16(wf[B][i], xf[XP][a], ac[a][7 * (B) + (i)], 0, 0, 0); SBAR(); } while (0)
#define MM(B, XP, i) do { M1(B, XP, i, 0); M1(B, XP, i, 1); } while (0)
#define GSTEP(B, XP, u, so) do { \
    M1(B, XP, 0, 0); RDW((B) ^ 1, u, so, 0); M1(B, XP, 0, 1); if ((B) == 1) RDX(u, so, 0); M1(B, XP, 1, 0); if ((B) == 1) RDX(u, so, 1); M1(B, XP, 1, 1); RDW((B) ^ 1, u, so, 1); \
    M1(B, XP, 2, 0); RDW((B) ^ 1, u, so, 2); M1(B, XP, 2, 1); RDW((B) ^ 1, u, so, 3); M1(B, XP, 3, 0); RDW((B) ^ 1, u, so, 4); M1(B, XP, 3, 1); RDW((B) ^ 1, u, so, 5); \
    M1(B, XP, 4, 0); RDW((B) ^ 1, u, so, 6); M1(B, XP, 4, 1); MM(B, XP, 5); MM(B, XP, 6); } while (0)
        WAITBAR_L();
        PSTAMP(5);
        int so = 0;
#pragma unroll 1
        for (int c = 0; c < 8; ++c) {
          GSTEP(1, 1, 0, so); GSTEP(0, 0, 0, so); GSTEP(1, 0, 1, so); GSTEP(0, 1, 1, so);
          WAITBAR_L();
          so = so == 2 * GCH ? 0 : so + GCH;
        }
        LDSP(const float) sx = (LDSP(const float))((LDSP(char))smem + L_ST) + j * 64 + c16;
        const float rs0 = sx[0], rs1 = sx[16], m0 = -sx[32], m1 = -sx[48];
        f32x4 c4[14];
#pragma unroll
        for (int i = 0; i < 14; ++i) c4[i] = *(LDSP(const f32x4))((LDSP(const char))smem + L_CS + i * 64 + g4 * 16);
        const char* ob = wl + OFF_H + ((size_t)(gtb * 4 + j) * KSF + gnb * 7) * 1024;
        unsigned oo = (((g4 & 1) * 32) + (g4 >> 1) * 16 + c16) * 16;
        asm volatile("" : "+s"(ob)); PIN(oo);
        MM(1, 1, 0); MM(1, 1, 1); MM(1, 1, 2); MM(1, 1, 3); MM(1, 1, 4); MM(1, 1, 5); MM(1, 1, 6);
#undef RDX
#undef RDW
#undef M1
#undef MM
#undef GSTEP
        PSTAMP(6);
        const f32x4 vm0 = {m0, m0, m0, m0}, vm1 = {m1, m1, m1, m1};
#define HOUT(i) do { \
          const f32x4 ga = __builtin_elementwise_fma(c4[2 * (i)], vm0, ac[0][2 * (i)]), ua = __builtin_elementwise_fma(c4[2 * (i) + 1], vm0, ac[0][2 * (i) + 1]) * rs0; \
          const f32x4 gb = __builtin_elementwise_fma(c4[2 * (i)], vm1, ac[1][2 * (i)]), ub = __builtin_elementwise_fma(c4[2 * (i) + 1], vm1, ac[1][2 * (i) + 1]) * rs1; \
          const f32x4 h0 = __builtin_elementwise_max(ga, f32x4{}) * ua, h1 = __builtin_elementwise_max(gb, f32x4{}) * ub; \
          auto e0 = __builtin_amdgcn_permlane32_swap(pk2(h0[0], h0[1]), pk2(h1[0], h1[1]), false, false); \
          auto e1 = __builtin_amdgcn_permlane32_swap(pk2(h0[2], h0[3]), pk2(h1[2], h1[3]), false, false); \
          const u32x4 a_ = {e0[0], e1[0], e0[1], e1[1]}; \
          st16s<((i) & 3) * 1024>(oo, ob + ((i) >> 2) * 4096, a_); } while (0)
        HOUT(0); HOUT(1); HOUT(2); HOUT(3); HOUT(4); HOUT(5); HOUT(6);
#undef HOUT
        PSTAMP(13);
        __builtin_amdgcn_s_barrier();
        arrive4(lcnt, cnt + (C_H + layer * 16 + gtb) * 128, lane);
        PSTAMP(7);
      }
      if (layer == 0) {
        FRESH();
        const char* wb = ws + OFF_WQ + (size_t)qnb * 8 * QW + j * 3072;
        glds3(voff, wb, lds0 + j * 3072); glds3(voff, wb + QW, lds0 + QW + j * 3072);
      }
      WAITBAR_L();
      if (layer == 0) {
        FRESH();
        __builtin_amdgcn_s_barrier();
        colsum_part(ws + OFF_CSP + (size_t)j * 81920 + (3584 + qnb * 96) * 4, voff, lb + L_CS + j * 1024);
        QINIT();
#define RX(B, u) xf[B] = ldfrag(sxp + (u) * 1024)
#define RW(B, i, u) wf[B][i] = ldfrag(swp + (i) * 4096 + (u) * 1024)
        WAITBAR_L();
        PSTAMP(8);
#pragma unroll 1
        for (int c = 0; c < 8; ++c) {
          LDSP(const char) swp = lb + (c & 3) * QW;
          LDSP(const char) sxp = lb + L_QX + (c & 3) * 16384 + j * 4096;
          QCHUNK();
          WAITBAR_L();
        }
        LDSP(const float) cx = (LDSP(const float))((LDSP(char))smem + L_CS);
        LDSP(const float) sx = (LDSP(const float))((LDSP(char))smem + L_ST) + j * 64;
        f32x4 cq[8], ra[4], rb[4];
        const float rstd = sx[r], mr = sx[32 + r], cv = cx[64 + r];
#pragma unroll
        for (int g = 0; g < 8; ++g) cq[g] = *(LDSP(const f32x4))(cx + 8 * g + 4 * hh);
#pragma unroll
        for (int g = 0; g < 4; ++g) { ra[g] = *(LDSP(const f32x4))(sx + 8 * g + 4 * hh); rb[g] = *(LDSP(const f32x4))(sx + 32 + 8 * g + 4 * hh); }
        SBAR();
        QFINISH();
        SBAR();
#undef RX
#undef RW
        PSTAMP(9);
        char* wn = ws + LSTRIDE;
        const int tt = qtb * 4 + j;
        float v[16];
        const float rq = rstd * QSCALE, mq = mr * QSCALE;
#pragma unroll
        for (int e = 0; e < 16; ++e) v[e] = acc[0][e] * rq - mq * cq[e >> 2][e & 3];
        qk_store(v, wn + OFF_Q + ((size_t)(2 * qnb) * NTT + tt) * 1024 + voff);
#pragma unroll
        for (int e = 0; e < 16; ++e) v[e] = acc[1][e] * rstd - mr * cq[4 + (e >> 2)][e & 3];
        qk_store(v, wn + OFF_K + ((size_t)(2 * qnb) * NTT + tt) * 1024 + voff);
#pragma unroll
        for (int e = 0; e < 16; ++e) v[e] = acc[2][e] * ra[e >> 2][e & 3] - rb[e >> 2][e & 3] * cv;
        v_store(v, wn + OFF_V + ((size_t)(2 * qnb + (r >> 4)) * NTT + tt) * 1024 + hh * 256 + (r & 15) * 16);
        arrive4(lcnt, cnt + (C_KV + qnb) * 128, lane);
        PSTAMP(10);
      }
    } else {
      {
        FRESH();
        bf16x8 w0[8], w1[8], x0[8], x1[8];
        const char* wa = ws + OFF_WO + ((size_t)ont * KSD + j * 8) * 1024;
        const char* wb = ws + OFF_WO + ((size_t)(ont ^ 1) * KSD + j * 8) * 1024;
#define LD4(D, o, base) do { gld16<0>(D[o], voff, base); gld16<1024>(D[(o) + 1], voff, base); gld16<2048>(D[(o) + 2], voff, base); gld16<3072>(D[(o) + 3], voff, base); } while (0)
        LD4(w0, 0, wa); LD4(w0, 4, wa + 4096); LD4(w1, 0, wb); LD4(w1, 4, wb + 4096);
        const char* xa = wl + OFF_O + ((size_t)ott * KSD + j * 8) * 1024;
        const char* xb = wl + OFF_O + ((size_t)(ott ^ 1) * KSD + j * 8) * 1024;
        asm volatile("" : "+s"(xa), "+s"(xb));
        __builtin_amdgcn_s_barrier();
        poll_b(cnt + (C_O + layer * 8 + xcd) * 128, 32u, lcnt + 1, layer * 4 + 1, j);
        PSTAMP(5);
        #define LD2(s) do { gld16<((s) & 3) * 1024>(x0[s], voff, xa + ((s) >> 2) * 4096); gld16<((s) & 3) * 1024>(x1[s], voff, xb + ((s) >> 2) * 4096); } while (0)
        LD2(0); LD2(1); LD2(2); LD2(3); LD2(4); LD2(5); LD2(6); LD2(7);
#undef LD2
        f32x16 acc[4];
#pragma unroll
        for (int i = 0; i < 4; ++i) acc[i] = f32x16{};
#define MM4(W0, W1, X0, X1) do { acc[0] = MFMA(W0, X0, acc[0]); acc[1] = MFMA(W0, X1, acc[1]); acc[2] = MFMA(W1, X0, acc[2]); acc[3] = MFMA(W1, X1, acc[3]); } while (0)
#define PIN8(n, X, Y, o) asm volatile("s_waitcnt vmcnt(%8)" : "+v"(X[o]), "+v"(X[(o) + 1]), "+v"(X[(o) + 2]), "+v"(X[(o) + 3]), "+v"(Y[o]), "+v"(Y[(o) + 1]), "+v"(Y[(o) + 2]), "+v"(Y[(o) + 3]) : "i"(n) : "memory")
        PIN8(16, w0, w1, 0); PIN8(16, w0, w1, 4);
#define WX(b, n) asm volatile("s_waitcnt vmcnt(%2)" : "+v"(x0[b]), "+v"(x1[b]) : "i"(n) : "memory")
#define ST(s) do { WX(s, 14 - 2 * (s)); MM4(w0[s], w1[s], x0[s], x1[s]); SBAR(); } while (0)
        ST(0); ST(1); ST(2); ST(3); ST(4); ST(5); ST(6); ST(7);
#undef ST
#undef WX
        PSTAMP(6);
        RES_FINISH((i - 1) * GCH + GW + (j ^ i) * 4096, (i - 1) * GCH + GW + j * 4096);
        PSTAMP(13);
        res_store_tiled(res, wl + OFF_X2 + ((size_t)ott * KSD + 2 * ont) * 1024 + voff, wl + OFF_ST2 + (size_t)ott * 4096 + ont * 256 + r * 8, lane);
        PSTAMP(14);
        arrive4(lcnt, cnt + (C_X2 + layer * 16 + grp) * 128, lane);
        PSTAMP(7);
      }
      {
        FRESH();
        { LDSP(char) cp = (LDSP(char))smem + L_CS + j * 256 + lane * 4;
          *(LDSP(float))cp = (*(LDSP(float))cp + *(LDSP(float))(cp + 1024)) + (*(LDSP(float))(cp + 2048) + *(LDSP(float))(cp + 3072)); }
        const int tt = gtb * 4 + j;
        const char* xb = wl + OFF_X2 + (size_t)tt * 32768;
        const char* wb = ws + OFF_WGU + (size_t)gnb * 8 * GW + j * 7168;
        f32x4 sp[4];
        asm volatile("" : "+s"(xb), "+s"(wb));
        poll_b(cnt + (C_X2 + layer * 16 + gtb) * 128, 16u, lcnt + 1, layer * 4 + 2, j);
        PSTAMP(8);
        __builtin_amdgcn_s_waitcnt(0xc07f);
        glds4(voff, xb, lds0 + GW + j * 4096);
        glds4(voff, xb + 4096, lds0 + GCH + GW + j * 4096);
        glds4(voff, xb + 8192, lds0 + 2 * GCH + GW + j * 4096);
        stat_issue(sp, wl + OFF_ST2 + (size_t)tt * 4096, voff);
        WAITBAR_V(12);
        WAITBAR_V(8);
        unsigned d = lds0;
#pragma unroll 1
        for (int c = 3; c < 8; ++c) {
          glds4(voff, xb + c * 4096, d + GW + j * 4096); glds4(voff, wb + c * GW, d + j * 7168); glds3(voff, wb + c * GW + 4096, d + j * 7168 + 4096);
          d = d == lds0 + 2 * GCH ? lds0 : d + GCH;
          WAITBAR_V(11);
        }
        { float mean, var; stat_finish(sp, mean, var);
          ((LDSP(float))((LDSP(char))smem + L_ST))[j * 64 + hh * 32 + (lane & 15) * 2 + ((lane >> 4) & 1)] = hh ? mean : 1.f / var; }
      }
      {
        FRESH();
        bf16x8 w0[12], w1[12], x0[8], x1[8];
        const char* wa = ws + OFF_WD + ((size_t)ont * KSF + j * 28) * 1024;
        const char* wb = ws + OFF_WD + ((size_t)(ont ^ 1) * KSF + j * 28) * 1024;
        LD4(w0, 0, wa); LD4(w1, 0, wb); LD4(w0, 4, wa + 4096); LD4(w1, 4, wb + 4096); LD4(w0, 8, wa + 8192); LD4(w1, 8, wb + 8192);
        WAITBAR_V(24);
        PIN8(0, w0, w1, 0); PIN8(0, w0, w1, 4); PIN8(0, w0, w1, 8);
        __builtin_amdgcn_s_barrier();
        __builtin_amdgcn_s_barrier();
        const unsigned dw = lds0 + L_DW + j * 32768;
#pragma unroll 1
        for (int c = 0; c < 4; ++c) { glds4(voff, wa + 12288 + c * 4096, dw + c * 8192); glds4(voff, wb + 12288 + c * 4096, dw + c * 8192 + 4096); }
        const char* xa = wl + OFF_H + ((size_t)ott * KSF + j * 28) * 1024;
        const char* xb = wl + OFF_H + ((size_t)(ott ^ 1) * KSF + j * 28) * 1024;
        asm volatile("" : "+s"(xa), "+s"(xb));
        poll_b(cnt + (C_H + layer * 16 + grp) * 128, 16u, lcnt + 1, layer * 4 + 3, j);
        PSTAMP(9);
#define LDX(b, blk) do { gld16<((b) & 3) * 1024>(x0[b], voff, xa + (blk) * 4096); gld16<((b) & 3) * 1024>(x1[b], voff, xb + (blk) * 4096); } while (0)
#define WX(b, n) asm volatile("s_waitcnt vmcnt(%2)" : "+v"(x0[b]), "+v"(x1[b]) : "i"(n) : "memory")
        LDX(0, 0); LDX(1, 0); LDX(2, 0); LDX(3, 0); LDX(4, 1); LDX(5, 1); LDX(6, 1); LDX(7, 1);
        f32x16 acc[4];
#pragma unroll
        for (int i = 0; i < 4; ++i) acc[i] = f32x16{};
        LDSP(const char) lw = lb + L_DW + j * 32768;
#define BLK(o, h, nb, n0) do { _Pragma("unroll") for (int s = 0; s < 1; ++s) {} \
          WX((h) + 0, n0); MM4(w0[(o) + 0], w1[(o) + 0], x0[(h) + 0], x1[(h) + 0]); SBAR(); if ((nb) >= 0) LDX((h) + 0, nb); SBAR(); \
          WX((h) + 1, (nb) >= 0 ? n0 : n0 - 2); MM4(w0[(o) + 1], w1[(o) + 1], x0[(h) + 1], x1[(h) + 1]); SBAR(); if ((nb) >= 0) LDX((h) + 1, nb); SBAR(); \
          WX((h) + 2, (nb) >= 0 ? n0 : n0 - 4); MM4(w0[(o) + 2], w1[(o) + 2], x0[(h) + 2], x1[(h) + 2]); SBAR(); if ((nb) >= 0) LDX((h) + 2, nb); SBAR(); \
          WX((h) + 3, (nb) >= 0 ? n0 : n0 - 6); MM4(w0[(o) + 3], w1[(o) + 3], x0[(h) + 3], x1[(h) + 3]); SBAR(); if ((nb) >= 0) LDX((h) + 3, nb); SBAR(); } while (0)
#define RDL(o, c) do { _Pragma("unroll") for (int s = 0; s < 4; ++s) { w0[(o) + s] = ldfrag(lw + (c) * 8192 + s * 1024); w1[(o) + s] = ldfrag(lw + (c) * 8192 + 4096 + s * 1024); } SBAR(); } while (0)
        BLK(0, 0, 2, 14); RDL(0, 0);
        BLK(4, 4, 3, 14); RDL(4, 1);
        BLK(8, 0, 4, 14); RDL(8, 2);
        BLK(0, 4, 5, 14); RDL(0, 3);
        BLK(4, 0, 6, 14);
        BLK(8, 4, -1, 14);
        BLK(0, 0, -1, 6);
#undef RDL
#undef BLK
#undef LDX
#undef WX
        PSTAMP(10);
        RES_FINISH(L_DW + j * 32768 + (i - 1) * 4096, L_DW + (j ^ i) * 32768 + (i - 1) * 4096);
        if (layer == 0) {
          res_store_tiled(res, ws + OFF_X3 + ((size_t)ott * KSD + 2 * ont) * 1024 + voff, ws + OFF_ST3 + (size_t)ott * 4096 + ont * 256 + r * 8, lane);
          arrive4(lcnt, cnt + (C_X3 + grp) * 128, lane);
        } else {
          auto s0 = __builtin_amdgcn_permlane32_swap(res[0], res[2], false, false);
          auto s1 = __builtin_amdgcn_permlane32_swap(res[1], res[3], false, false);
          auto s2 = __builtin_amdgcn_permlane32_swap(res[4], res[6], false, false);
          auto s3 = __builtin_amdgcn_permlane32_swap(res[5], res[7], false, false);
          const u32x4 a = {s0[0], s1[0], s0[1], s1[1]}, b = {s2[0], s3[0], s2[1], s3[1]};
          char* o = out + ((size_t)(ott * 32 + r) * D_ + ont * 32) * 2 + hh * 16;
          st16(o, a);
          st16(o + 32, b);
        }
        PSTAMP(11);
      }
#undef LD4
#undef MM4
#undef PIN8
      if (layer == 0) {
        FRESH();
        const int tt = qtb * 4 + j;
        const char* xb = ws + OFF_X3 + (size_t)tt * 32768;
        const char* wb = ws + OFF_WQ + (size_t)qnb * 8 * QW + j * 3072;
        f32x4 sp[4];
        asm volatile("" : "+s"(xb), "+s"(wb));
        __builtin_amdgcn_s_barrier();
        poll_b(cnt + (C_X3 + qtb) * 128, 16u, lcnt + 1, 4, j);
        PSTAMP(12);
#define QISSUE(c) do { glds4(voff, xb + (c) * 4096, lds0 + L_QX + ((c) & 3) * 16384 + j * 4096); glds3(voff, wb + (c) * QW, lds0 + ((c) & 3) * QW + j * 3072); } while (0)
        glds4(voff, xb, lds0 + L_QX + j * 4096);
        stat_issue(sp, ws + OFF_ST3 + (size_t)tt * 4096, voff);
        glds4(voff, xb + 4096, lds0 + L_QX + 16384 + j * 4096);
        QISSUE(2);
        WAITBAR_V(15);
        QISSUE(3);
        { LDSP(char) cp = (LDSP(char))smem + L_CS + j * 256 + lane * 4;
          *(LDSP(float))cp = (*(LDSP(float))cp + *(LDSP(float))(cp + 1024)) + (*(LDSP(float))(cp + 2048) + *(LDSP(float))(cp + 3072)); }
        WAITBAR_V(14);
#pragma unroll 1
        for (int c = 4; c < 8; ++c) { QISSUE(c); WAITBAR_V(14); }
#undef QISSUE
        { float mean, var; stat_finish(sp, mean, var); const float rs = rsqrtf(var);
          ((LDSP(float))((LDSP(char))smem + L_ST))[j * 64 + hh * 32 + (lane & 15) * 2 + ((lane >> 4) & 1)] = hh ? mean * rs : rs; }
        WAITBAR_V(7);
        WAITBAR_V(0);
        __builtin_amdgcn_s_barrier();
      }
    }
  }
}

extern "C" void kernel_launch(void* const* d_in, const int* in_sizes, int n_in, void* d_out, int out_size, void* d_ws, size_t ws_size,
                              hipStream_t stream) {
  const char* x = (const char*)d_in[0];
  const char* wqkv = (const char*)d_in[1];
  const char* wo = (const char*)d_in[2];
  const char* wg = (const char*)d_in[4];
  const char* wu = (const char*)d_in[5];
  const char* wd = (const char*)d_in[6];
  char* ws = (char*)d_ws;
  k_qkv1<<<256, 768, 0, stream>>>(x, wqkv, ws DIAG_PASS(0));
  k_main<<<256, 512, 0, stream>>>(ws, x, wqkv, wo, wg, wu, wd, (char*)d_out DIAG_PASS(1));
}
```

```cpp
#include <hip/hip_runtime.h>
#include <hip/hip_bf16.h>
#include <stdint.h>

typedef short bf16x8 __attribute__((ext_vector_type(8)));
typedef float f32x16 __attribute__((ext_vector_type(16)));
typedef float f32x4 __attribute__((ext_vector_type(4)));
typedef float f32x2 __attribute__((ext_vector_type(2)));
typedef unsigned u32x4 __attribute__((ext_vector_type(4)));
typedef unsigned u32x2 __attribute__((ext_vector_type(2)));
typedef __bf16 bf16v2 __attribute__((ext_vector_type(2)));
typedef unsigned short u16;

#define STAMP(i)
#define STAMP_DECL
#define PSTAMP(i)
#define STAMP_STORE()
#define DIAG_ARG
#define DIAG_PASS(k)
#define DI __device__ __forceinline__
#define MFMA(a, b, c) __builtin_amdgcn_mfma_f32_32x32x16_bf16((a), (b), (c), 0, 0, 0)
#define LDSP(T) __attribute__((address_space(3))) T*

constexpr int S_ = 2048, D_ = 512, H_ = 32, F_ = 1792;
constexpr int NTT = S_ / 32;
constexpr int KSD = D_ / 16;
constexpr int KSF = F_ / 16;
constexpr float QSCALE = 0.25f * 1.4426950408889634f;
constexpr float LN_EPS = 1e-5f;

constexpr size_t KB = 1024;
constexpr size_t OFF_CNT = 0;
constexpr size_t OFF_WO = 16 * KB;
constexpr size_t OFF_WGU = OFF_WO + 512 * KB;
constexpr size_t OFF_WD = OFF_WGU + 3584 * KB;
constexpr size_t OFF_WQ = OFF_WD + 1792 * KB;
constexpr size_t OFF_CSP = OFF_WQ + 1536 * KB;
constexpr size_t OFF_ST3 = OFF_CSP + 320 * KB;
constexpr size_t OFF_X3 = OFF_ST3 + 256 * KB;
constexpr size_t OFF_L = OFF_X3 + 2048 * KB;
constexpr size_t OFF_Q = OFF_L;
constexpr size_t OFF_K = OFF_Q + 2048 * KB;
constexpr size_t OFF_V = OFF_K + 2048 * KB;
constexpr size_t OFF_O = OFF_V + 2048 * KB;
constexpr size_t OFF_X2 = OFF_O + 2048 * KB;
constexpr size_t OFF_ST2 = OFF_X2 + 2048 * KB;
constexpr size_t OFF_H = OFF_ST2 + 256 * KB;
constexpr size_t LSTRIDE = OFF_H + 7168 * KB - OFF_L;
constexpr int C_O = 0, C_X2 = 16, C_H = 48, C_X3 = 80, C_KV = 96, C_PREP = 112, C_N = 128;

DI unsigned pk2(float lo, float hi) {
  f32x2 v = {lo, hi};
  bf16v2 r = __builtin_convertvector(v, bf16v2);
  return __builtin_bit_cast(unsigned, r);
}
DI float lo16(unsigned u) { return __uint_as_float(u << 16); }
DI float hi16(unsigned u) { return __uint_as_float(u & 0xffff0000u); }
DI float xhalf_add(float v) {
  auto r = __builtin_amdgcn_permlane32_swap(__float_as_uint(v), __float_as_uint(v), false, false);
  return __uint_as_float(r[0]) + __uint_as_float(r[1]);
}
DI float xhalf_max(float v) {
  auto r = __builtin_amdgcn_permlane32_swap(__float_as_uint(v), __float_as_uint(v), false, false);
  return fmaxf(__uint_as_float(r[0]), __uint_as_float(r[1]));
}
DI float x16_add(float v) {
  auto r = __builtin_amdgcn_permlane16_swap(__float_as_uint(v), __float_as_uint(v), false, false);
  return __uint_as_float(r[0]) + __uint_as_float(r[1]);
}
DI void st16(void* p, u32x4 v) { asm volatile("global_store_dwordx4 %0, %1, off sc1\n\ts_nop 1" :: "v"(p), "v"(v) : "memory"); }
template <int IMM>
DI void st16s(unsigned voff, const char* sbase, u32x4 v) {
  asm volatile("global_store_dwordx4 %0, %1, %2 offset:%3 sc1\n\ts_nop 1" :: "v"(voff), "v"(v), "s"(sbase), "i"(IMM) : "memory");
}
template <int IMM>
DI void gld16(bf16x8& d, unsigned voff, const char* sbase) {
  asm volatile("global_load_dwordx4 %0, %1, %2 offset:%3 sc1" : "=v"(d) : "v"(voff), "s"(sbase), "i"(IMM) : "memory");
}
#define GLDS_HEAD "s_mov_b32 %0, m0\n\ts_mov_b32 m0, %3\n\ts_nop 0\n\t"
#define GLDS_OP(o) "global_load_lds_dwordx4 %1, %2 offset:" #o " sc1\n\t"
#define GLDS_TAIL "s_mov_b32 m0, %0" : "=&s"(keep) : "v"(voff), "s"(sbase), "s"(lds) : "memory"
DI void glds1(unsigned voff, const char* sbase, unsigned lds) { unsigned keep; asm volatile(GLDS_HEAD GLDS_OP(0) GLDS_TAIL); }
DI void glds2(unsigned voff, const char* sbase, unsigned lds) { unsigned keep; asm volatile(GLDS_HEAD GLDS_OP(0) GLDS_OP(1024) GLDS_TAIL); }
DI void glds3(unsigned voff, const char* sbase, unsigned lds) { unsigned keep; asm volatile(GLDS_HEAD GLDS_OP(0) GLDS_OP(1024) GLDS_OP(2048) GLDS_TAIL); }
DI void glds4(unsigned voff, const char* sbase, unsigned lds) { unsigned keep; asm volatile(GLDS_HEAD GLDS_OP(0) GLDS_OP(1024) GLDS_OP(2048) GLDS_OP(3072) GLDS_TAIL); }
#define SBAR() __builtin_amdgcn_sched_barrier(0)
#define PIN(x) asm volatile("" : "+v"(x))
#define WAITV(n) asm volatile("s_waitcnt vmcnt(%0)" :: "i"(n) : "memory")
#define WAITBAR_V(n) asm volatile("s_waitcnt vmcnt(%0) lgkmcnt(0)\n\ts_barrier" :: "i"(n) : "memory")
#define WAITBAR_L() do { asm volatile("" ::: "memory"); __builtin_amdgcn_s_waitcnt(0xc07f); __builtin_amdgcn_s_barrier(); asm volatile("" ::: "memory"); } while (0)
typedef short s16x4 __attribute__((ext_vector_type(4)));
DI s16x4 trread(LDSP(const char) p) { return __builtin_amdgcn_ds_read_tr16_b64_v4i16((LDSP(s16x4))p); }
DI bf16x8 trfrag(LDSP(const char) p, int off2) { const s16x4 lo = trread(p), hi = trread(p + off2); return __builtin_shufflevector(lo, hi, 0, 1, 2, 3, 4, 5, 6, 7); }
DI bf16x8 ldfrag(LDSP(const char) p) { return *(LDSP(const bf16x8))p; }
DI float dot2(unsigned a, unsigned b, float c) { return __builtin_amdgcn_fdot2_f32_bf16(__builtin_bit_cast(bf16v2, a), __builtin_bit_cast(bf16v2, b), c, false); }
constexpr unsigned ONE2 = 0x3f803f80u;
#define BC8(x) __builtin_bit_cast(bf16x8, x)

template <int LOOK, int PER> DI void ring_wait(int i) {
  if (i <= 8 - LOOK) WAITBAR_V((LOOK - 1) * PER);
  else if (LOOK > 3 && i == 5) WAITBAR_V(2 * PER);
  else if (LOOK > 2 && i == 6) WAITBAR_V(PER);
  else WAITBAR_V(0);
}

DI void qk_store(const float (&v)[16], char* o) {
  u32x4 a = {pk2(v[0], v[1]), pk2(v[2], v[3]), pk2(v[4], v[5]), pk2(v[6], v[7])};
  u32x4 b = {pk2(v[8], v[9]), pk2(v[10], v[11]), pk2(v[12], v[13]), pk2(v[14], v[15])};
  st16(o, a);
  st16(o + NTT * 1024, b);
}
DI void v_store(const float (&v)[16], char* o) {
  u32x4 a = {pk2(v[0], v[1]), pk2(v[2], v[3]), pk2(v[4], v[5]), pk2(v[6], v[7])};
  u32x4 b = {pk2(v[8], v[9]), pk2(v[10], v[11]), pk2(v[12], v[13]), pk2(v[14], v[15])};
  st16(o, a);
  st16(o + 512, b);
}
DI float row16_sum(float v) {
  v += __builtin_bit_cast(float, __builtin_amdgcn_update_dpp(0, __builtin_bit_cast(int, v), 0x128, 0xf, 0xf, false));
  v += __builtin_bit_cast(float, __builtin_amdgcn_update_dpp(0, __builtin_bit_cast(int, v), 0x124, 0xf, 0xf, false));
  v += __builtin_bit_cast(float, __builtin_amdgcn_update_dpp(0, __builtin_bit_cast(int, v), 0x122, 0xf, 0xf, false));
  v += __builtin_bit_cast(float, __builtin_amdgcn_update_dpp(0, __builtin_bit_cast(int, v), 0x121, 0xf, 0xf, false));
  return v;
}
#define KSTEP(B, u) do { SBAR(); RX((B) ^ 1, u); RW((B) ^ 1, 0, u); SBAR(); acc[0] = MFMA(wf[B][0], xf[B], acc[0]); SBAR(); RW((B) ^ 1, 1, u); RW((B) ^ 1, 2, u); SBAR(); \
                         acc[1] = MFMA(wf[B][1], xf[B], acc[1]); acc[2] = MFMA(xf[B], wf[B][2], acc[2]); SBAR(); } while (0)
#define QCHUNK() do { KSTEP(1, 0); KSTEP(0, 1); KSTEP(1, 2); KSTEP(0, 3); } while (0)
#define QINIT() \
  f32x16 acc[3]; bf16x8 wf[2][3], xf[2]; \
  _Pragma("unroll") for (int i = 0; i < 3; ++i) { acc[i] = f32x16{}; wf[1][i] = bf16x8{}; } \
  xf[1] = bf16x8{}
#define QFINISH() do { acc[0] = MFMA(wf[1][0], xf[1], acc[0]); acc[1] = MFMA(wf[1][1], xf[1], acc[1]); acc[2] = MFMA(xf[1], wf[1][2], acc[2]); } while (0)

extern "C" __global__ __launch_bounds__(768, 3) void k_qkv1(const char* __restrict__ x, const char* __restrict__ wn, char* __restrict__ ws DIAG_ARG) {
  STAMP_DECL; STAMP(0);
  constexpr int LOOK = 3, CH = 24576, LXN = 4 * CH;
  __shared__ __attribute__((aligned(16))) char smem[LXN + 65536];
  const int tid = threadIdx.x, lane = tid & 63, r = lane & 31, hh = lane >> 5;
  const int w = __builtin_amdgcn_readfirstlane(tid >> 6);
  const int xcd = blockIdx.x & 7, idx = blockIdx.x >> 3;
  const int tb = xcd * 4 + (idx & 3), nq = idx >> 2;
  const unsigned lds0 = (unsigned)(uintptr_t)smem;
  const int rot = 2 * (idx & 3);
  if (w >= 8) {
    const int j = w - 8;
    const unsigned wv = (lane >> 3) * 3072 + (((lane & 7) ^ (((lane >> 4) & 1) << 2)) * 16);
    const char* wb = wn + (size_t)j * 16 * 3072 + nq * 128;
    if (blockIdx.x == 0 && lane < 32) *(unsigned*)(ws + OFF_CNT + (j * 32 + lane) * 128) = 0u;
    asm volatile("s_sleep 2\n\ts_nop 4" ::: "memory");
#pragma unroll 1
    for (int c = 0; c < 8 + LOOK; ++c) {
      if (c >= LOOK) ring_wait<LOOK, 6>(c - LOOK);
      if (c < 8) {
        const char* ws_ = wb + (size_t)((c + rot) & 7) * 64 * 3072;
        const unsigned d = lds0 + (c & 3) * CH + j * 2048;
        glds1(wv, ws_, d); glds1(wv, ws_ + 1024, d + 8192); glds1(wv, ws_ + 2048, d + 16384);
        glds1(wv, ws_ + 8 * 3072, d + 1024); glds1(wv, ws_ + 8 * 3072 + 1024, d + 8192 + 1024); glds1(wv, ws_ + 8 * 3072 + 2048, d + 16384 + 1024);
      }
    }
    return;
  }
  {
    const int n = w;
    const u32x4* xr = (const u32x4*)(x + (size_t)(tb * 64 + n * 8) * 1024) + lane;
    u32x4 v[8];
#pragma unroll
    for (int i = 0; i < 8; ++i) v[i] = xr[i * 64];
    float s[8], q[8];
#pragma unroll
    for (int i = 0; i < 8; ++i) {
      s[i] = 0.f; q[i] = 0.f;
#pragma unroll
      for (int e = 0; e < 4; ++e) { s[i] = dot2(v[i][e], ONE2, s[i]); q[i] = dot2(v[i][e], v[i][e], q[i]); }
    }
    float s4[4], q4[4], s2[2], q2[2];
#pragma unroll
    for (int i = 0; i < 4; ++i) {
      const int lo = (i & 1) + 4 * (i >> 1);
      auto a = __builtin_amdgcn_permlane32_swap(__float_as_uint(s[lo]), __float_as_uint(s[lo + 2]), false, false);
      auto b = __builtin_amdgcn_permlane32_swap(__float_as_uint(q[lo]), __float_as_uint(q[lo + 2]), false, false);
      s4[i] = __uint_as_float(a[0]) + __uint_as_float(a[1]); q4[i] = __uint_as_float(b[0]) + __uint_as_float(b[1]);
    }
#pragma unroll
    for (int i = 0; i < 2; ++i) {
      auto a = __builtin_amdgcn_permlane16_swap(__float_as_uint(s4[2 * i]), __float_as_uint(s4[2 * i + 1]), false, false);
      auto b = __builtin_amdgcn_permlane16_swap(__float_as_uint(q4[2 * i]), __float_as_uint(q4[2 * i + 1]), false, false);
      s2[i] = row16_sum(__uint_as_float(a[0]) + __uint_as_float(a[1])); q2[i] = row16_sum(__uint_as_float(b[0]) + __uint_as_float(b[1]));
    }
    float ra[2], rb[2];
#pragma unroll
    for (int i = 0; i < 2; ++i) {
      const float mean = s2[i] * (1.f / D_);
      ra[i] = rsqrtf(fmaxf(q2[i] * (1.f / D_) - mean * mean, 0.f) + LN_EPS);
      rb[i] = -mean * ra[i];
    }
    LDSP(char) xd = (LDSP(char))smem + LXN + (lane >> 3) * 8192 + n * 1024;
#pragma unroll
    for (int i = 0; i < 8; ++i) {
      const float a = __builtin_bit_cast(float, __builtin_amdgcn_readlane(__builtin_bit_cast(int, ra[i >> 2]), (i & 3) * 16));
      const float b = __builtin_bit_cast(float, __builtin_amdgcn_readlane(__builtin_bit_cast(int, rb[i >> 2]), (i & 3) * 16));
      u32x4 o;
#pragma unroll
      for (int e = 0; e < 4; ++e) o[e] = pk2(lo16(v[i][e]) * a + b, hi16(v[i][e]) * a + b);
      *(LDSP(u32x4))(xd + i * 128 + (((lane & 7) ^ ((4 * n + (i >> 1)) & 7)) * 16)) = o;
    }
  }
  STAMP(1);
  WAITBAR_L();
  if (w >= 4) return;
  STAMP(2);
  const int ta = w & 1, hp = w >> 1, q4 = (lane & 15) >> 2, xsw = (r >> 1) & 7;
  LDSP(const char) lw = (LDSP(const char))smem + (8 * hh + q4) * 128 + ((hp ^ (q4 >> 1)) * 64) + ((lane >> 4) & 1) * 32 + (lane & 3) * 8;
  LDSP(const char) lx = (LDSP(const char))smem + LXN + ta * 4096 + r * 128;
  int xo[4];
#pragma unroll
  for (int u = 0; u < 4; ++u) xo[u] = ((2 * u + hh) ^ xsw) * 16;
  QINIT();
#define RX(B, u) xf[B] = ldfrag(sx + xo[u])
#define RW(B, i, u) wf[B][i] = trfrag(sw + (i) * 8192 + (u) * 2048, 512)
#pragma unroll 1
  for (int c = 0; c < 8; ++c) {
    LDSP(const char) sw = lw + (c & 3) * CH;
    LDSP(const char) sx = lx + ((c + rot) & 7) * 8192;
    QCHUNK();
    WAITBAR_L();
  }
  QFINISH();
#undef RX
#undef RW
  STAMP(4);
  const int tt = tb * 2 + ta, nb = nq * 2 + hp;
  float v[16];
#pragma unroll
  for (int e = 0; e < 16; ++e) v[e] = acc[0][e] * QSCALE;
  qk_store(v, ws + OFF_Q + ((size_t)(2 * nb) * NTT + tt) * 1024 + lane * 16);
#pragma unroll
  for (int e = 0; e < 16; ++e) v[e] = acc[1][e];
  qk_store(v, ws + OFF_K + ((size_t)(2 * nb) * NTT + tt) * 1024 + lane * 16);
#pragma unroll
  for (int e = 0; e < 16; ++e) v[e] = acc[2][e];
  v_store(v, ws + OFF_V + ((size_t)(2 * nb + (r >> 4)) * NTT + tt) * 1024 + hh * 256 + (r & 15) * 16);
  STAMP(5); STAMP_STORE();
}

DI void poll(const char* cnt, unsigned voff, unsigned target) {
#pragma unroll 1
  for (unsigned spins = 0; spins < 200000u; ++spins) {
    unsigned v;
    asm volatile("global_load_dword %0, %1, %2 sc1\n\ts_waitcnt vmcnt(0)" : "=v"(v) : "v"(voff), "s"(cnt) : "memory");
    if (__all(v >= target)) break;
    __builtin_amdgcn_s_sleep(1);
  }
}
DI void poll_b(const char* cnt, unsigned target, LDSP(unsigned) go, unsigned seq, int j) {
  if (j == 0) {
    poll(cnt, 0u, target);
    __hip_atomic_store(go, seq, __ATOMIC_RELAXED, __HIP_MEMORY_SCOPE_WORKGROUP);
  } else {
#pragma unroll 1
    for (unsigned spins = 0; spins < 4000000u; ++spins) {
      if (__builtin_amdgcn_readfirstlane(__hip_atomic_load(go, __ATOMIC_RELAXED, __HIP_MEMORY_SCOPE_WORKGROUP)) >= seq) break;
      __builtin_amdgcn_s_sleep(1);
    }
    WAITV(0);
  }
  asm volatile("" ::: "memory");
}
DI void arrive4(LDSP(unsigned) lw, char* cnt, int lane) {
  WAITV(0);
  if (lane == 0) {
    const unsigned old = __hip_atomic_fetch_add(lw, 1u, __ATOMIC_RELAXED, __HIP_MEMORY_SCOPE_WORKGROUP);
    if ((old & 3u) == 3u) asm volatile("global_atomic_add %0, %1, %2" :: "v"(0u), "v"(1u), "s"(cnt) : "memory");
  }
  asm volatile("" ::: "memory");
}
DI void stat_issue(f32x4 (&p)[4], const char* sb, unsigned voff) {
  asm volatile("global_load_dwordx4 %0, %4, %5 sc1\n\tglobal_load_dwordx4 %1, %4, %5 offset:1024 sc1\n\tglobal_load_dwordx4 %2, %4, %5 offset:2048 sc1\n\t"
               "global_load_dwordx4 %3, %4, %5 offset:3072 sc1" : "=&v"(p[0]), "=&v"(p[1]), "=&v"(p[2]), "=&v"(p[3]) : "v"(voff), "s"(sb) : "memory");
}
DI void stat_finish(f32x4 (&p)[4], float& mean, float& var) {
  asm volatile("" : "+v"(p[0]), "+v"(p[1]), "+v"(p[2]), "+v"(p[3]));
  const f32x4 t = (p[0] + p[1]) + (p[2] + p[3]);
  auto a = __builtin_amdgcn_permlane16_swap(__float_as_uint(t[0]), __float_as_uint(t[2]), false, false);
  auto b = __builtin_amdgcn_permlane16_swap(__float_as_uint(t[1]), __float_as_uint(t[3]), false, false);
  mean = xhalf_add(__uint_as_float(a[0]) + __uint_as_float(a[1])) * (1.f / D_);
  var = fmaxf(xhalf_add(__uint_as_float(b[0]) + __uint_as_float(b[1])) * (1.f / D_) - mean * mean, 0.f) + LN_EPS;
}
DI void colsum_part(const char* src, unsigned voff, LDSP(char) dst) {
  f32x4 p[4];
  asm volatile("global_load_dwordx4 %0, %4, %5 sc1\n\tglobal_load_dwordx4 %1, %4, %6 sc1\n\tglobal_load_dwordx4 %2, %4, %7 sc1\n\t"
               "global_load_dwordx4 %3, %4, %8 sc1\n\ts_waitcnt vmcnt(0)" : "=&v"(p[0]), "=&v"(p[1]), "=&v"(p[2]), "=&v"(p[3])
               : "v"(voff), "s"(src), "s"(src + 20480), "s"(src + 40960), "s"(src + 61440) : "memory");
  *(LDSP(f32x4))dst = (p[0] + p[1]) + (p[2] + p[3]);
}

#define RES_FINISH(WR, RD) do { \
    _Pragma("unroll") for (int i = 1; i < 4; ++i) _Pragma("unroll") for (int g = 0; g < 4; ++g) { \
      const f32x4 v_ = {acc[i][4 * g], acc[i][4 * g + 1], acc[i][4 * g + 2], acc[i][4 * g + 3]}; \
      *(LDSP(f32x4))(lb + (WR) + g * 1024) = v_; } \
    asm volatile("s_waitcnt vmcnt(0)" : "+v"(rn[0]), "+v"(rn[1]), "+v"(rn[2]), "+v"(rn[3]) :: "memory"); \
    WAITBAR_L(); \
    _Pragma("unroll") for (int g = 0; g < 4; ++g) { \
      f32x4 t_ = {acc[0][4 * g], acc[0][4 * g + 1], acc[0][4 * g + 2], acc[0][4 * g + 3]}; \
      f32x4 u_[3]; \
      _Pragma("unroll") for (int i = 1; i < 4; ++i) u_[i - 1] = *(LDSP(const f32x4))(lb + (RD) + g * 1024); \
      t_ = (u_[0] + u_[1]) + (u_[2] + t_); \
      res[2 * g] = pk2(t_[0] + lo16(rn[g][0]), t_[1] + hi16(rn[g][0])); \
      res[2 * g + 1] = pk2(t_[2] + lo16(rn[g][1]), t_[3] + hi16(rn[g][1])); } } while (0)
DI void res_load(u32x2 (&rn)[4], unsigned vo, const char* p0, int d1, int d2) {
  asm volatile("global_load_dwordx2 %0, %4, %5 sc1\n\tglobal_load_dwordx2 %1, %4, %6 sc1\n\tglobal_load_dwordx2 %2, %4, %7 sc1\n\tglobal_load_dwordx2 %3, %4, %8 sc1"
               : "=&v"(rn[0]), "=&v"(rn[1]), "=&v"(rn[2]), "=&v"(rn[3]) : "v"(vo), "s"(p0), "s"(p0 + d1), "s"(p0 + d2), "s"(p0 + d2 + d1) : "memory");
}
DI void res_store_tiled(const unsigned (&res)[8], char* o, char* st, int lane) {
  const u32x4 p0 = {res[0], res[1], res[2], res[3]}, p1 = {res[4], res[5], res[6], res[7]};
  st16(o, p0);
  st16(o + 1024, p1);
  float s1 = 0.f, s2 = 0.f;
#pragma unroll
  for (int e = 0; e < 8; ++e) { s1 = dot2(res[e], ONE2, s1); s2 = dot2(res[e], res[e], s2); }
  s1 = xhalf_add(s1); s2 = xhalf_add(s2);
  if (lane < 32) { const f32x2 v = {s1, s2}; asm volatile("global_store_dwordx2 %0, %1, off sc1" :: "v"(st), "v"(v) : "memory"); }
}

DI const char* karg(int i) {
  const __attribute__((address_space(4))) unsigned long long* ka = (const __attribute__((address_space(4))) unsigned long long*)__builtin_amdgcn_kernarg_segment_ptr();
  asm volatile("" : "+s"(ka));
  return (const char*)ka[i];
}
extern "C" __global__ __launch_bounds__(512, 2) void k_main(char* __restrict__ ws_, const char* x_, const char* wq_, const char* wo_, const char* wg_,
                                                            const char* wu_, const char* wd_, char* out_ DIAG_ARG) {
  constexpr int L_K = 0, L_V = 65536, L_ONE = 131072, L_CMB = 133120;
  constexpr int GW = 28672, GCH = GW + 16384;
  constexpr int L_D0 = 2 * GCH, L_D1 = L_D0 + 32768, L_D2 = 0, L_D3 = GCH;
  constexpr int QW = 12288, L_QX = 49152;
  constexpr int L_CS = 157696, L_ST = L_CS + 4096, L_CNT = L_ST + 1024, L_DBG = L_CNT + 64;
  __shared__ __attribute__((aligned(16))) char smem[163840];
  const int w = __builtin_amdgcn_readfirstlane(threadIdx.x >> 6);
  const unsigned lds0 = (unsigned)(uintptr_t)smem;
#define FRESH() \
  int tid = threadIdx.x; PIN(tid); int bid = blockIdx.x, j = w & 3; char* ws = ws_; asm volatile("" : "+s"(bid), "+s"(j), "+s"(ws)); \
  const int lane = tid & 63, r = lane & 31, hh = lane >> 5, xcd = bid & 7, idx = bid >> 3, grp = xcd * 2 + (idx >> 4), m = idx & 15; \
  const unsigned voff = lane * 16; LDSP(char) lb = (LDSP(char))smem + lane * 16; char* cnt = ws + OFF_CNT; char* wl = ws + layer * LSTRIDE; \
  LDSP(unsigned) lcnt = (LDSP(unsigned))((LDSP(char))smem + L_CNT) + (w >> 2); \
  const int ont = (m >> 1) * 2 + (j >> 1), ott = grp * 4 + (m & 1) * 2 + (j & 1); \
  const int gtb = (xcd & 1) * 8 + (idx & 7), gnb = (xcd >> 1) * 4 + (idx >> 3), qtb = (xcd & 3) * 4 + (idx & 3), qnb = (xcd >> 2) * 8 + (idx >> 2); \
  (void)r; (void)hh; (void)voff; (void)lb; (void)cnt; (void)wl; (void)lcnt; (void)ont; (void)ott; (void)gtb; (void)gnb; (void)qtb; (void)qnb
  if (threadIdx.x < 8) ((LDSP(unsigned))((LDSP(char))smem + L_CNT))[threadIdx.x] = 0u;
#pragma unroll 1
  for (int layer = 0; layer < 2; ++layer) {
    {
      FRESH();
      const int head = xcd * 4 + (idx & 3), qb = idx >> 2, half = w >> 2, tt0 = qb * 8 + 2 * j;
      PSTAMP(0);
      if (layer) { if (w == 4) poll(cnt + (C_KV + (head >> 1)) * 128, 0u, 16u); WAITBAR_L(); }
      PSTAMP(1);
      const char* ks = wl + OFF_K + (size_t)head * 65536 + half * 32768;
      const unsigned kd = lds0 + L_K + half * 32768;
      bf16x8 q0, q1;
      gld16<0>(q0, voff, wl + OFF_Q + ((size_t)head * NTT + tt0) * 1024);
      gld16<1024>(q1, voff, wl + OFF_Q + ((size_t)head * NTT + tt0) * 1024);
      glds2(voff, ks + j * 2048, kd + j * 2048);
      glds2(voff, ks + (OFF_V - OFF_K) + j * 2048, kd + L_V + j * 2048);
      *(LDSP(unsigned))((LDSP(char))smem + L_ONE + tid * 4) = ((tid >> 2) & 11) == 0 ? ONE2 : 0u;
      asm volatile("s_waitcnt vmcnt(0) lgkmcnt(0)\n\ts_barrier" : "+v"(q0), "+v"(q1) :: "memory");
      PSTAMP(2);
      LDSP(const char) kp = lb + L_K;
      LDSP(const char) vp0 = (LDSP(const char))smem + (r < 16 ? L_V : L_ONE) + hh * 256 + (r & 15) * 16;
      const int vstep = r < 16 ? 2048 : 0;
      LDSP(const char) kq = kp + half * 32768; LDSP(const char) vq = vp0 + half * 16 * vstep;
      f32x16 n0, n1, sA, sB, o0 = f32x16{}, o1 = f32x16{};
      bf16x8 kA, kB, vp = bf16x8{};
      u32x4 p1 = {0u, 0u, 0u, 0u};
      {
        const bf16x8 k0 = ldfrag(kp);
        kA = ldfrag(kq);
        const f32x16 s = MFMA(k0, q0, f32x16{}), t = MFMA(k0, q1, f32x16{});
        float a = fmaxf(fmaxf(s[0], s[1]), s[2]), b = fmaxf(fmaxf(t[0], t[1]), t[2]);
#pragma unroll
        for (int i = 3; i < 15; i += 2) { a = fmaxf(fmaxf(a, s[i]), s[i + 1]); b = fmaxf(fmaxf(b, t[i]), t[i + 1]); }
        a = xhalf_max(fmaxf(a, s[15])); b = xhalf_max(fmaxf(b, t[15]));
#pragma unroll
        for (int i = 0; i < 16; ++i) { n0[i] = -a; n1[i] = -b; }
        PIN(n0); PIN(n1);
        sA = MFMA(kA, q0, n0);
      }
#define EX(S, i) S[i] = __builtin_amdgcn_exp2f(S[i])
#define STEPN(CUR, NXT, KN_, QN, NN, O, VA, OP, VP, READS) do { \
      EX(CUR, 0); EX(CUR, 1); PIN(CUR); SBAR(); OP = MFMA(VP, BC8(p1), OP); SBAR(); READS; SBAR(); \
      EX(CUR, 2); EX(CUR, 3); EX(CUR, 4); EX(CUR, 5); PIN(CUR); SBAR(); NXT = MFMA(KN_, QN, NN); SBAR(); \
      u32x4 p0; EX(CUR, 6); EX(CUR, 7); p0[0] = pk2(CUR[0], CUR[1]); p0[1] = pk2(CUR[2], CUR[3]); PIN(CUR); SBAR(); \
      EX(CUR, 8); EX(CUR, 9); p0[2] = pk2(CUR[4], CUR[5]); p0[3] = pk2(CUR[6], CUR[7]); PIN(p0); PIN(CUR); SBAR(); \
      EX(CUR, 10); EX(CUR, 11); PIN(CUR); SBAR(); O = MFMA(VA, BC8(p0), O); SBAR(); \
      EX(CUR, 12); EX(CUR, 13); EX(CUR, 14); EX(CUR, 15); \
      p1[0] = pk2(CUR[8], CUR[9]); p1[1] = pk2(CUR[10], CUR[11]); p1[2] = pk2(CUR[12], CUR[13]); p1[3] = pk2(CUR[14], CUR[15]); PIN(p1); SBAR(); } while (0)
#define TILE(t, KA, KB) do { bf16x8 va, vb; \
      STEPN(sA, sB, KA, q1, n1, o0, va, o1, vp, (va = ldfrag(vq + (t) * 1024), vb = ldfrag(vq + (t) * 1024 + 512), KB = ldfrag(kq + ((t) + 1) * 1024))); \
      STEPN(sB, sA, KB, q0, n0, o1, va, o0, vb, (vp = vb)); } while (0)
      __builtin_amdgcn_s_setprio(3);
#pragma unroll 1
      for (int ph = -2; ph < 4; ++ph) {
        if (ph == -1) {
          glds3(voff, ks + 8192 + j * 6144, kd + 8192 + j * 6144);
          glds3(voff, ks + (OFF_V - OFF_K) + 8192 + j * 6144, kd + L_V + 8192 + j * 6144);
          glds3(voff, ks + 11264 + j * 6144, kd + 11264 + j * 6144);
          glds3(voff, ks + (OFF_V - OFF_K) + 11264 + j * 6144, kd + L_V + 11264 + j * 6144);
        }
        if (ph == 0) {
          WAITV(0);
          if (lane == 0) __hip_atomic_fetch_add(lcnt + 4, 1u, __ATOMIC_RELAXED, __HIP_MEMORY_SCOPE_WORKGROUP);
#pragma unroll 1
          for (unsigned spins = 0; spins < 4000000u; ++spins)
            if (__builtin_amdgcn_readfirstlane(__hip_atomic_load(lcnt + 4, __ATOMIC_RELAXED, __HIP_MEMORY_SCOPE_WORKGROUP)) >= 4u * (layer + 1)) break;
          asm volatile("" ::: "memory");
        }
        if (ph == 1) __builtin_amdgcn_s_setprio(2);
        if (ph == 2) __builtin_amdgcn_s_setprio(1);
        if (ph == 3) __builtin_amdgcn_s_setprio(0);
        if (layer == 0 && ph >= 0) {
          const int q4 = (lane & 15) >> 2, b16 = (lane >> 4) & 1;
          const int scr = L_CMB + (half ? j * 5120 : 20480 + j * 2048);
          const int jo = ((ph - 1) * 8 + w) * 256 + bid;
          if ((ph == 1 || ph == 2) && jo < 3712) {
            const bool gu = jo < 1792;
            LDSP(const char) sp = (LDSP(const char))smem + scr + (lane & 3) * 8 +
                                  (gu ? (16 * hh + 4 * b16 + q4) * 32 : (4 * hh + q4) * 64 + b16 * 32);
            const int ho = gu ? 256 : 512;
            const char* pd;
            if (gu) { const int ft = jo >> 4, S = jo & 15, nb = ft / 7, ii = ft - nb * 7; pd = ws + OFF_WGU + ((size_t)((nb * 8 + (S >> 1)) * 14 + 2 * ii) * 2 + (S & 1)) * 1024; }
            else if (jo < 2048) pd = ws + OFF_WO + (size_t)(jo - 1792) * 2048;
            else if (jo < 2944) pd = ws + OFF_WD + (size_t)(jo - 2048) * 2048;
            else { const int t = (jo - 2944) >> 4, nb = t / 3, ii = t - nb * 3, s0 = (jo & 15) * 2; pd = ws + OFF_WQ + (size_t)(((nb * 8 + (s0 >> 2)) * 3 + ii) * 4 + (s0 & 3)) * 1024; }
            WAITV(0);
            const u32x4 fa = __builtin_bit_cast(u32x4, trfrag(sp, ho)), fb = __builtin_bit_cast(u32x4, trfrag(sp + 1024, ho));
            st16s<0>(voff, pd, fa); st16s<0>(voff, pd + (gu ? 2048 : 1024), fb);
            if (gu || jo >= 2944) {
              float ca = (lo16(fa[0]) + hi16(fa[0])) + (lo16(fa[1]) + hi16(fa[1])) + (lo16(fa[2]) + hi16(fa[2])) + (lo16(fa[3]) + hi16(fa[3]));
              float cb = (lo16(fb[0]) + hi16(fb[0])) + (lo16(fb[1]) + hi16(fb[1])) + (lo16(fb[2]) + hi16(fb[2])) + (lo16(fb[3]) + hi16(fb[3]));
              if (gu) { ca = xhalf_add(x16_add(ca)); cb = xhalf_add(x16_add(cb)); ca = lane < 16 ? ca : cb; }
              else ca = xhalf_add(ca + cb);
              const char* cp = ws + OFF_CSP + ((jo & 15) * 5120 + ((gu ? jo : jo - 2944 + 1792) >> 4) * 32) * 4;
              if (lane < 32) asm volatile("global_store_dword %0, %1, %2 sc1" :: "v"(lane * 4), "v"(ca), "s"(cp) : "memory");
            }
          }
          const int jn = jo + 2048;
          if (ph < 2 && jn < 3712) {
            const char *sa, *sb; unsigned gv;
            if (jn < 1792) {
              const int o = 32 * (jn & 15) * (F_ * 2) + (jn >> 4) * 32;
              gv = (lane >> 1) * (F_ * 2) + (lane & 1) * 16; sa = karg(4) + o; sb = karg(5) + o;
            } else {
              int nt, s0, ld = 1024, ai;
              if (jn < 2048) { const int f = (jn - 1792) * 2; nt = f >> 5; s0 = f & 31; ai = 3; }
              else if (jn < 2944) { const int f = (jn - 2048) * 2; nt = f / KSF; s0 = f - nt * KSF; ai = 6; }
              else { const int t = (jn - 2944) >> 4, nb = t / 3; s0 = (jn & 15) * 2; nt = (t - nb * 3) * 16 + nb; ai = 2; ld = 3072; }
              gv = (lane >> 2) * ld + (lane & 3) * 16; sa = karg(ai) + (16 * s0 * ld + nt * 64); sb = sa + 16 * ld;
            }
            __builtin_amdgcn_s_waitcnt(0xc07f);
            glds1(gv, sa, lds0 + scr); glds1(gv, sb, lds0 + scr + 1024);
          }
          if (ph == 3) arrive4(lcnt, cnt + (C_PREP + xcd) * 128, lane);
        }
        int n = ph <= 0 ? ph + 3 : ph == 3 ? 2 : 4;
#pragma unroll 1
        do { TILE(0, kA, kB); TILE(1, kB, kA); kq += 2048; vq += vstep; } while (--n > 0);
      }
      o1 = MFMA(vp, BC8(p1), o1);
#undef TILE
#undef STEPN
      PSTAMP(3);
      LDSP(char) xp = lb + L_CMB + j * 5120;
      if (half == 1) {
        const f32x4 a = {o0[0], o0[1], o0[2], o0[3]}, b = {o0[4], o0[5], o0[6], o0[7]}, c = {o1[0], o1[1], o1[2], o1[3]}, d = {o1[4], o1[5], o1[6], o1[7]};
        const f32x2 l = {o0[8], o1[8]};
        *(LDSP(f32x4))xp = a; *(LDSP(f32x4))(xp + 1024) = b; *(LDSP(f32x4))(xp + 2048) = c; *(LDSP(f32x4))(xp + 3072) = d; *(LDSP(f32x2))(xp + 4096) = l;
      }
      WAITBAR_L();
      if (half == 0) {
        PSTAMP(11);
        const f32x4 a = *(LDSP(const f32x4))xp, b = *(LDSP(const f32x4))(xp + 1024), c = *(LDSP(const f32x4))(xp + 2048), d = *(LDSP(const f32x4))(xp + 3072);
        const f32x2 l = *(LDSP(const f32x2))(xp + 4096);
#pragma unroll
        for (int i = 0; i < 4; ++i) { o0[i] += a[i]; o0[4 + i] += b[i]; o1[i] += c[i]; o1[4 + i] += d[i]; }
        o0[8] += l[0]; o1[8] += l[1];
        float chk = (o0[8] + o1[8]) * 5.4210109e-20f;
#pragma unroll
        for (int i = 0; i < 8; ++i) chk = __builtin_fmaf(o0[i], 0.f, __builtin_fmaf(o1[i], 0.f, chk));
        const bool bad = !(chk < 1.f);
        if (__builtin_expect(__any(bad), 0)) {
#pragma unroll 1
          for (int z = 0; z < 2; ++z) {
            const bf16x8 q = z ? q1 : q0;
            float mx = -INFINITY;
            f32x16 o = f32x16{};
            kq = kp; vq = vp0;
#pragma unroll 1
            for (int t = 0; t < 64; ++t) {
              const bf16x8 k = ldfrag(kq), v0 = ldfrag(vq), v1 = ldfrag(vq + 512);
              f32x16 s = MFMA(k, q, f32x16{});
              float tm = s[0];
#pragma unroll
              for (int i = 1; i < 16; ++i) tm = fmaxf(tm, s[i]);
              tm = xhalf_max(tm);
              const float mn = fmaxf(mx, tm);
              const float alpha = __builtin_amdgcn_exp2f(mx - mn);
              mx = mn;
#pragma unroll
              for (int i = 0; i < 16; ++i) { o[i] *= alpha; s[i] = __builtin_amdgcn_exp2f(s[i] - mn); }
              const u32x4 pa = {pk2(s[0], s[1]), pk2(s[2], s[3]), pk2(s[4], s[5]), pk2(s[6], s[7])};
              const u32x4 pb = {pk2(s[8], s[9]), pk2(s[10], s[11]), pk2(s[12], s[13]), pk2(s[14], s[15])};
              o = MFMA(v0, BC8(pa), o);
              o = MFMA(v1, BC8(pb), o);
              kq += 1024; vq += vstep >> 1;
            }
            if (z) o1 = o; else o0 = o;
          }
        }
        const float i0 = __builtin_amdgcn_rcpf(o0[8]), i1 = __builtin_amdgcn_rcpf(o1[8]);
        const u32x4 a0 = {pk2(o0[0] * i0, o0[1] * i0), pk2(o0[2] * i0, o0[3] * i0), pk2(o0[4] * i0, o0[5] * i0), pk2(o0[6] * i0, o0[7] * i0)};
        const u32x4 a1 = {pk2(o1[0] * i1, o1[1] * i1), pk2(o1[2] * i1, o1[3] * i1), pk2(o1[4] * i1, o1[5] * i1), pk2(o1[6] * i1, o1[7] * i1)};
        char* op = wl + OFF_O + ((size_t)tt0 * KSD + head) * 1024 + voff;
        st16(op, a0); st16(op + 32768, a1);
        PSTAMP(12);
        WAITBAR_L();
        arrive4(lcnt, cnt + (C_O + layer * 8 + qb) * 128, lane);
      }
    }
    PSTAMP(4);
    if (layer == 0) poll(ws_ + OFF_CNT + C_PREP * 128, (threadIdx.x & 7) * 128, 64u);
    if (w < 4) {
      {
        FRESH();
        const char* wb = ws + OFF_WGU + (size_t)gnb * 8 * GW + j * 7168;
#pragma unroll 1
        for (int c = 0; c < 2; ++c) { glds4(voff, wb + c * GW, lds0 + c * GCH + j * 7168); glds3(voff, wb + c * GW + 4096, lds0 + c * GCH + j * 7168 + 4096); }
        colsum_part(ws + OFF_CSP + (size_t)j * 81920 + gnb * 896, voff, lb + L_CS + j * 1024);
      }
      PSTAMP(14);
      WAITBAR_L();
      PSTAMP(15);
      {
        FRESH();
        const int g4 = lane >> 4, c16 = lane & 15;
        f32x4 ac[2][14];
#pragma unroll
        for (int i = 0; i < 14; ++i) { ac[0][i] = f32x4{}; ac[1][i] = f32x4{}; }
        LDSP(const char) lx = (LDSP(const char))smem + GW + j * 4096 + (g4 >> 1) * 1024 + ((g4 & 1) * 32 + c16) * 16;
        bf16x8 wf[2][7], xf[2][2];
        xf[1][0] = bf16x8{}; xf[1][1] = bf16x8{};
#pragma unroll
        for (int i = 0; i < 7; ++i) wf[1][i] = bf16x8{};
#define RDX(u, so, a) xf[u][a] = ldfrag(lx + (so) + (u) * 2048 + (a) * 256)
#define RDW(hf, u, so, i) wf[hf][i] = ldfrag(lb + (so) + ((7 * (hf) + (i)) * 2 + (u)) * 1024)
#define M1(B, XP, i, a) do { SBAR(); ac[a][7 * (B) + (i)] = __builtin_amdgcn_mfma_f32_16x16x32_bf16(wf[B][i], xf[XP][a], ac[a][7 * (B) + (i)], 0, 0, 0); SBAR(); } while (0)
#define MM(B, XP, i) do { M1(B, XP, i, 0); M1(B, XP, i, 1); } while (0)
#define GSTEP(B, XP, u, so) do { \
    M1(B, XP, 0, 0); RDW((B) ^ 1, u, so, 0); M1(B, XP, 0, 1); if ((B) == 1) RDX(u, so, 0); M1(B, XP, 1, 0); if ((B) == 1) RDX(u, so, 1); M1(B, XP, 1, 1); RDW((B) ^ 1, u, so, 1); \
    M1(B, XP, 2, 0); RDW((B) ^ 1, u, so, 2); M1(B, XP, 2, 1); RDW((B) ^ 1, u, so, 3); M1(B, XP, 3, 0); RDW((B) ^ 1, u, so, 4); M1(B, XP, 3, 1); RDW((B) ^ 1, u, so, 5); \
    M1(B, XP, 4, 0); RDW((B) ^ 1, u, so, 6); M1(B, XP, 4, 1); MM(B, XP, 5); MM(B, XP, 6); } while (0)
        WAITBAR_L();
        PSTAMP(5);
        int so = 0;
#pragma unroll 1
        for (int c = 0; c < 8; ++c) {
          GSTEP(1, 1, 0, so); GSTEP(0, 0, 0, so); GSTEP(1, 0, 1, so); GSTEP(0, 1, 1, so);
          asm volatile("" ::: "memory"); __builtin_amdgcn_s_waitcnt(0xc07f);
          if (c != 7) __builtin_amdgcn_s_barrier();
          asm volatile("" ::: "memory");
          so = so == 2 * GCH ? 0 : so + GCH;
        }
        LDSP(const float) sx = (LDSP(const float))((LDSP(char))smem + L_ST) + j * 64 + c16;
        const float rs0 = sx[0], rs1 = sx[16], m0 = -sx[32], m1 = -sx[48];
        f32x4 c4[14];
#pragma unroll
        for (int i = 0; i < 14; ++i) c4[i] = *(LDSP(const f32x4))((LDSP(const char))smem + L_CS + i * 64 + g4 * 16);
        const char* ob = wl + OFF_H + ((size_t)(gtb * 4 + j) * KSF + gnb * 7) * 1024;
        unsigned oo = (((g4 & 1) * 32) + (g4 >> 1) * 16 + c16) * 16;
        asm volatile("" : "+s"(ob)); PIN(oo);
        MM(1, 1, 0); MM(1, 1, 1); MM(1, 1, 2); MM(1, 1, 3); MM(1, 1, 4); MM(1, 1, 5); MM(1, 1, 6);
#undef RDX
#undef RDW
#undef M1
#undef MM
#undef GSTEP
        PSTAMP(6);
        const f32x4 vm0 = {m0, m0, m0, m0}, vm1 = {m1, m1, m1, m1};
#define HOUT(i) do { \
          const f32x4 ga = __builtin_elementwise_fma(c4[2 * (i)], vm0, ac[0][2 * (i)]), ua = __builtin_elementwise_fma(c4[2 * (i) + 1], vm0, ac[0][2 * (i) + 1]) * rs0; \
          const f32x4 gb = __builtin_elementwise_fma(c4[2 * (i)], vm1, ac[1][2 * (i)]), ub = __builtin_elementwise_fma(c4[2 * (i) + 1], vm1, ac[1][2 * (i) + 1]) * rs1; \
          const f32x4 h0 = __builtin_elementwise_max(ga, f32x4{}) * ua, h1 = __builtin_elementwise_max(gb, f32x4{}) * ub; \
          auto e0 = __builtin_amdgcn_permlane32_swap(pk2(h0[0], h0[1]), pk2(h1[0], h1[1]), false, false); \
          auto e1 = __builtin_amdgcn_permlane32_swap(pk2(h0[2], h0[3]), pk2(h1[2], h1[3]), false, false); \
          const u32x4 a_ = {e0[0], e1[0], e0[1], e1[1]}; \
          st16s<((i) & 3) * 1024>(oo, ob + ((i) >> 2) * 4096, a_); } while (0)
        HOUT(0); HOUT(1); HOUT(2); HOUT(3); HOUT(4); HOUT(5); HOUT(6);
#undef HOUT
        PSTAMP(13);
        if (lane == 0) __hip_atomic_fetch_add(lcnt + 3, 1u, __ATOMIC_RELAXED, __HIP_MEMORY_SCOPE_WORKGROUP);
        arrive4(lcnt, cnt + (C_H + layer * 16 + gtb) * 128, lane);
        PSTAMP(7);
      }
      WAITBAR_L();
      if (layer == 0) {
        FRESH();
        __builtin_amdgcn_s_barrier();
        { const char* wb = ws + OFF_WQ + (size_t)qnb * 8 * QW + j * 3072;
#pragma unroll 1
          for (int c = 0; c < 4; ++c) glds3(voff, wb + c * QW, lds0 + c * QW + j * 3072); }
        colsum_part(ws + OFF_CSP + (size_t)j * 81920 + (3584 + qnb * 96) * 4, voff, lb + L_CS + j * 1024);
        QINIT();
#define RX(B, u) xf[B] = ldfrag(sxp + (u) * 1024)
#define RW(B, i, u) wf[B][i] = ldfrag(swp + (i) * 4096 + (u) * 1024)
        WAITBAR_L();
        PSTAMP(8);
#pragma unroll 1
        for (int c = 0; c < 8; ++c) {
          LDSP(const char) swp = lb + (c & 3) * QW;
          LDSP(const char) sxp = lb + L_QX + (c & 3) * 16384 + j * 4096;
          QCHUNK();
          WAITBAR_L();
        }
        LDSP(const float) cx = (LDSP(const float))((LDSP(char))smem + L_CS);
        LDSP(const float) sx = (LDSP(const float))((LDSP(char))smem + L_ST) + j * 64;
        f32x4 cq[8], ra[4], rb[4];
        const float rstd = sx[r], mr = sx[32 + r], cv = cx[64 + r];
#pragma unroll
        for (int g = 0; g < 8; ++g) cq[g] = *(LDSP(const f32x4))(cx + 8 * g + 4 * hh);
#pragma unroll
        for (int g = 0; g < 4; ++g) { ra[g] = *(LDSP(const f32x4))(sx + 8 * g + 4 * hh); rb[g] = *(LDSP(const f32x4))(sx + 32 + 8 * g + 4 * hh); }
        SBAR();
        QFINISH();
        SBAR();
#undef RX
#undef RW
        PSTAMP(9);
        char* wn = ws + LSTRIDE;
        const int tt = qtb * 4 + j;
        float v[16];
        const float rq = rstd * QSCALE, mq = mr * QSCALE;
#pragma unroll
        for (int e = 0; e < 16; ++e) v[e] = acc[0][e] * rq - mq * cq[e >> 2][e & 3];
        qk_store(v, wn + OFF_Q + ((size_t)(2 * qnb) * NTT + tt) * 1024 + voff);
#pragma unroll
        for (int e = 0; e < 16; ++e) v[e] = acc[1][e] * rstd - mr * cq[4 + (e >> 2)][e & 3];
        qk_store(v, wn + OFF_K + ((size_t)(2 * qnb) * NTT + tt) * 1024 + voff);
#pragma unroll
        for (int e = 0; e < 16; ++e) v[e] = acc[2][e] * ra[e >> 2][e & 3] - rb[e >> 2][e & 3] * cv;
        v_store(v, wn + OFF_V + ((size_t)(2 * qnb + (r >> 4)) * NTT + tt) * 1024 + hh * 256 + (r & 15) * 16);
        arrive4(lcnt, cnt + (C_KV + qnb) * 128, lane);
        PSTAMP(10);
      }
    } else {
      {
        FRESH();
        bf16x8 w0[8], w1[8], x0[8], x1[8];
        const char* wa = ws + OFF_WO + ((size_t)ont * KSD + j * 8) * 1024;
        const char* wb = ws + OFF_WO + ((size_t)(ont ^ 1) * KSD + j * 8) * 1024;
#define LD4(D, o, base) do { gld16<0>(D[o], voff, base); gld16<1024>(D[(o) + 1], voff, base); gld16<2048>(D[(o) + 2], voff, base); gld16<3072>(D[(o) + 3], voff, base); } while (0)
        __builtin_amdgcn_s_barrier();
        LD4(w0, 0, wa); LD4(w0, 4, wa + 4096); LD4(w1, 0, wb); LD4(w1, 4, wb + 4096);
        u32x2 rn[4]; unsigned res[8];
        res_load(rn, layer ? voff : r * 1024 + hh * 8, (layer ? ws + OFF_X3 + ont * 2048 : karg(1) + ont * 64) + (size_t)ott * 32768, layer ? 8 : 16, layer ? 1024 : 32);
        const char* xa = wl + OFF_O + ((size_t)ott * KSD + j * 8) * 1024;
        const char* xb = wl + OFF_O + ((size_t)(ott ^ 1) * KSD + j * 8) * 1024;
        asm volatile("" : "+s"(xa), "+s"(xb));
        poll_b(cnt + (C_O + layer * 8 + xcd) * 128, 32u, lcnt + 1, layer * 4 + 1, j);
        PSTAMP(5);
        #define LD2(s) do { gld16<((s) & 3) * 1024>(x0[s], voff, xa + ((s) >> 2) * 4096); gld16<((s) & 3) * 1024>(x1[s], voff, xb + ((s) >> 2) * 4096); } while (0)
        LD2(0); LD2(1); LD2(2); LD2(3); LD2(4); LD2(5); LD2(6); LD2(7);
#undef LD2
        f32x16 acc[4];
#pragma unroll
        for (int i = 0; i < 4; ++i) acc[i] = f32x16{};
#define MM4(W0, W1, X0, X1) do { acc[0] = MFMA(W0, X0, acc[0]); acc[1] = MFMA(W0, X1, acc[1]); acc[2] = MFMA(W1, X0, acc[2]); acc[3] = MFMA(W1, X1, acc[3]); } while (0)
#define PIN8(n, X, Y, o) asm volatile("s_waitcnt vmcnt(%8)" : "+v"(X[o]), "+v"(X[(o) + 1]), "+v"(X[(o) + 2]), "+v"(X[(o) + 3]), "+v"(Y[o]), "+v"(Y[(o) + 1]), "+v"(Y[(o) + 2]), "+v"(Y[(o) + 3]) : "i"(n) : "memory")
        PIN8(16, w0, w1, 0); PIN8(16, w0, w1, 4);
#define WX(b, n) asm volatile("s_waitcnt vmcnt(%2)" : "+v"(x0[b]), "+v"(x1[b]) : "i"(n) : "memory")
#define ST(s) do { WX(s, 14 - 2 * (s)); MM4(w0[s], w1[s], x0[s], x1[s]); SBAR(); } while (0)
        ST(0); ST(1); ST(2); ST(3); ST(4); ST(5); ST(6); ST(7);
#undef ST
#undef WX
        PSTAMP(6);
        RES_FINISH((i - 1) * GCH + GW + (j ^ i) * 4096, (i - 1) * GCH + GW + j * 4096);
        PSTAMP(13);
        res_store_tiled(res, wl + OFF_X2 + ((size_t)ott * KSD + 2 * ont) * 1024 + voff, wl + OFF_ST2 + (size_t)ott * 4096 + ont * 256 + r * 8, lane);
        PSTAMP(14);
        arrive4(lcnt, cnt + (C_X2 + layer * 16 + grp) * 128, lane);
        PSTAMP(7);
      }
      {
        FRESH();
        { LDSP(char) cp = (LDSP(char))smem + L_CS + j * 256 + lane * 4;
          *(LDSP(float))cp = (*(LDSP(float))cp + *(LDSP(float))(cp + 1024)) + (*(LDSP(float))(cp + 2048) + *(LDSP(float))(cp + 3072)); }
        bf16x8 w0[12], w1[12], x0[8], x1[8];
        const char* wa = ws + OFF_WD + ((size_t)ont * KSF + j * 28) * 1024;
        const char* wb = ws + OFF_WD + ((size_t)(ont ^ 1) * KSF + j * 28) * 1024;
        {
          const int tt = gtb * 4 + j;
          const char* gx = wl + OFF_X2 + (size_t)tt * 32768;
          const char* gw = ws + OFF_WGU + (size_t)gnb * 8 * GW + j * 7168;
          f32x4 sp[4];
          asm volatile("" : "+s"(gx), "+s"(gw));
          poll_b(cnt + (C_X2 + layer * 16 + gtb) * 128, 16u, lcnt + 1, layer * 4 + 2, j);
          PSTAMP(8);
          __builtin_amdgcn_s_waitcnt(0xc07f);
#define GISSUE(c, d) do { glds4(voff, gx + (c) * 4096, (d) + GW + j * 4096); glds4(voff, gw + (c) * GW, (d) + j * 7168); glds3(voff, gw + (c) * GW + 4096, (d) + j * 7168 + 4096); } while (0)
          glds4(voff, gx, lds0 + GW + j * 4096);
          glds4(voff, gx + 4096, lds0 + GCH + GW + j * 4096);
          stat_issue(sp, wl + OFF_ST2 + (size_t)tt * 4096, voff);
          WAITBAR_V(8);
          GISSUE(2, lds0 + 2 * GCH);
          LD4(w0, 0, wa); LD4(w1, 0, wb);
          WAITBAR_V(23);
          unsigned d = lds0;
#pragma unroll 1
          for (int c = 3; c < 8; ++c) {
            GISSUE(c, d);
            d = d == lds0 + 2 * GCH ? lds0 : d + GCH;
            WAITBAR_V(11);
          }
#undef GISSUE
          LD4(w0, 4, wa + 4096); LD4(w1, 4, wb + 4096); LD4(w0, 8, wa + 8192); LD4(w1, 8, wb + 8192);
          { float mean, var; stat_finish(sp, mean, var);
            ((LDSP(float))((LDSP(char))smem + L_ST))[j * 64 + hh * 32 + (lane & 15) * 2 + ((lane >> 4) & 1)] = hh ? mean : 1.f / var; }
          WAITBAR_V(16);
        }
#define DISSUE(c, P) do { glds4(voff, wa + 12288 + (c) * 4096, lds0 + (P) + j * 8192); glds4(voff, wb + 12288 + (c) * 4096, lds0 + (P) + j * 8192 + 4096); } while (0)
        DISSUE(0, L_D0); DISSUE(1, L_D1); DISSUE(2, L_D2);
#pragma unroll 1
        for (unsigned spins = 0; spins < 4000000u; ++spins) {
          if (__builtin_amdgcn_readfirstlane(__hip_atomic_load(lcnt + 2, __ATOMIC_RELAXED, __HIP_MEMORY_SCOPE_WORKGROUP)) >= 4u * (layer + 1)) break;
          __builtin_amdgcn_s_sleep(1);
        }
        asm volatile("" ::: "memory");
        DISSUE(3, L_D3);
#undef DISSUE
        const char* xa = wl + OFF_H + ((size_t)ott * KSF + j * 28) * 1024;
        const char* xb = wl + OFF_H + ((size_t)(ott ^ 1) * KSF + j * 28) * 1024;
        asm volatile("" : "+s"(xa), "+s"(xb));
        poll_b(cnt + (C_H + layer * 16 + grp) * 128, 16u, lcnt + 1, layer * 4 + 3, j);
        PSTAMP(9);
        PIN8(0, w0, w1, 0); PIN8(0, w0, w1, 4); PIN8(0, w0, w1, 8);
#define LDX(b, blk) do { gld16<((b) & 3) * 1024>(x0[b], voff, xa + (blk) * 4096); gld16<((b) & 3) * 1024>(x1[b], voff, xb + (blk) * 4096); } while (0)
#define WX(b, n) asm volatile("s_waitcnt vmcnt(%2)" : "+v"(x0[b]), "+v"(x1[b]) : "i"(n) : "memory")
        LDX(0, 0); LDX(1, 0); LDX(2, 0); LDX(3, 0); LDX(4, 1); LDX(5, 1); LDX(6, 1); LDX(7, 1);
        f32x16 acc[4];
#pragma unroll
        for (int i = 0; i < 4; ++i) acc[i] = f32x16{};
        LDSP(const char) lw = lb + j * 8192;
#define BLK(o, h, nb, n0) do { _Pragma("unroll") for (int s = 0; s < 1; ++s) {} \
          WX((h) + 0, n0); MM4(w0[(o) + 0], w1[(o) + 0], x0[(h) + 0], x1[(h) + 0]); SBAR(); if ((nb) >= 0) LDX((h) + 0, nb); SBAR(); \
          WX((h) + 1, (nb) >= 0 ? n0 : n0 - 2); MM4(w0[(o) + 1], w1[(o) + 1], x0[(h) + 1], x1[(h) + 1]); SBAR(); if ((nb) >= 0) LDX((h) + 1, nb); SBAR(); \
          WX((h) + 2, (nb) >= 0 ? n0 : n0 - 4); MM4(w0[(o) + 2], w1[(o) + 2], x0[(h) + 2], x1[(h) + 2]); SBAR(); if ((nb) >= 0) LDX((h) + 2, nb); SBAR(); \
          WX((h) + 3, (nb) >= 0 ? n0 : n0 - 6); MM4(w0[(o) + 3], w1[(o) + 3], x0[(h) + 3], x1[(h) + 3]); SBAR(); if ((nb) >= 0) LDX((h) + 3, nb); SBAR(); } while (0)
#define RDL(o, c) do { _Pragma("unroll") for (int s = 0; s < 4; ++s) { w0[(o) + s] = ldfrag(lw + (c) + s * 1024); w1[(o) + s] = ldfrag(lw + (c) + 4096 + s * 1024); } SBAR(); } while (0)
        BLK(0, 0, 2, 14); RDL(0, L_D0);
        BLK(4, 4, 3, 14); RDL(4, L_D1);
        BLK(8, 0, 4, 14); RDL(8, L_D2);
        BLK(0, 4, 5, 14); RDL(0, L_D3);
        BLK(4, 0, 6, 14);
        u32x2 rn[4]; unsigned res[8];
        res_load(rn, voff, wl + OFF_X2 + (size_t)ott * 32768 + ont * 2048, 8, 1024);
        BLK(8, 4, -1, 18);
        BLK(0, 0, -1, 10);
#undef RDL
#undef BLK
#undef LDX
#undef WX
        PSTAMP(10);
        RES_FINISH((i == 3 ? L_D1 : L_D0 + (i - 1) * 4096) + j * 8192, (i == 3 ? L_D1 : L_D0 + (i - 1) * 4096) + (j ^ i) * 8192);
        if (layer == 0) {
          res_store_tiled(res, ws + OFF_X3 + ((size_t)ott * KSD + 2 * ont) * 1024 + voff, ws + OFF_ST3 + (size_t)ott * 4096 + ont * 256 + r * 8, lane);
          arrive4(lcnt, cnt + (C_X3 + grp) * 128, lane);
        } else {
          auto s0 = __builtin_amdgcn_permlane32_swap(res[0], res[2], false, false);
          auto s1 = __builtin_amdgcn_permlane32_swap(res[1], res[3], false, false);
          auto s2 = __builtin_amdgcn_permlane32_swap(res[4], res[6], false, false);
          auto s3 = __builtin_amdgcn_permlane32_swap(res[5], res[7], false, false);
          const u32x4 a = {s0[0], s1[0], s0[1], s1[1]}, b = {s2[0], s3[0], s2[1], s3[1]};
          char* o = (char*)karg(7) + ((size_t)(ott * 32 + r) * D_ + ont * 32) * 2 + hh * 16;
          st16(o, a);
          st16(o + 32, b);
        }
        PSTAMP(11);
      }
#undef LD4
#undef MM4
#undef PIN8
      if (layer == 0) {
        FRESH();
        const int tt = qtb * 4 + j;
        const char* xb = ws + OFF_X3 + (size_t)tt * 32768;
        const char* wb = ws + OFF_WQ + (size_t)qnb * 8 * QW + j * 3072;
        f32x4 sp[4];
        asm volatile("" : "+s"(xb), "+s"(wb));
        __builtin_amdgcn_s_barrier();
        poll_b(cnt + (C_X3 + qtb) * 128, 16u, lcnt + 1, 4, j);
        PSTAMP(12);
#define QISSUE(c) do { glds4(voff, xb + (c) * 4096, lds0 + L_QX + ((c) & 3) * 16384 + j * 4096); glds3(voff, wb + (c) * QW, lds0 + ((c) & 3) * QW + j * 3072); } while (0)
        glds4(voff, xb, lds0 + L_QX + j * 4096);
        glds4(voff, xb + 4096, lds0 + L_QX + 16384 + j * 4096);
        stat_issue(sp, ws + OFF_ST3 + (size_t)tt * 4096, voff);
        glds4(voff, xb + 8192, lds0 + L_QX + 32768 + j * 4096);
        glds4(voff, xb + 12288, lds0 + L_QX + 49152 + j * 4096);
        WAITBAR_V(16);
        { LDSP(char) cp = (LDSP(char))smem + L_CS + j * 256 + lane * 4;
          *(LDSP(float))cp = (*(LDSP(float))cp + *(LDSP(float))(cp + 1024)) + (*(LDSP(float))(cp + 2048) + *(LDSP(float))(cp + 3072)); }
        WAITBAR_V(12);
        QISSUE(4); WAITBAR_V(11);
#pragma unroll 1
        for (int c = 5; c < 8; ++c) { QISSUE(c); WAITBAR_V(14); }
#undef QISSUE
        { float mean, var; stat_finish(sp, mean, var); const float rs = rsqrtf(var);
          ((LDSP(float))((LDSP(char))smem + L_ST))[j * 64 + hh * 32 + (lane & 15) * 2 + ((lane >> 4) & 1)] = hh ? mean * rs : rs; }
        WAITBAR_V(7);
        WAITBAR_V(0);
        __builtin_amdgcn_s_barrier();
      }
    }
  }
}

extern "C" void kernel_launch(void* const* d_in, const int* in_sizes, int n_in, void* d_out, int out_size, void* d_ws, size_t ws_size,
                              hipStream_t stream) {
  const char* x = (const char*)d_in[0];
  const char* wqkv = (const char*)d_in[1];
  const char* wo = (const char*)d_in[2];
  const char* wg = (const char*)d_in[4];
  const char* wu = (const char*)d_in[5];
  const char* wd = (const char*)d_in[6];
  char* ws = (char*)d_ws;
  k_qkv1<<<256, 768, 0, stream>>>(x, wqkv, ws DIAG_PASS(0));
  k_main<<<256, 512, 0, stream>>>(ws, x, wqkv, wo, wg, wu, wd, (char*)d_out DIAG_PASS(1));
}
```

```cpp
#include <hip/hip_runtime.h>
#include <hip/hip_bf16.h>
#include <stdint.h>

typedef short bf16x8 __attribute__((ext_vector_type(8)));
typedef float f32x16 __attribute__((ext_vector_type(16)));
typedef float f32x4 __attribute__((ext_vector_type(4)));
typedef float f32x2 __attribute__((ext_vector_type(2)));
typedef unsigned u32x4 __attribute__((ext_vector_type(4)));
typedef unsigned u32x2 __attribute__((ext_vector_type(2)));
typedef __bf16 bf16v2 __attribute__((ext_vector_type(2)));
typedef unsigned short u16;

#define STAMP(i)
#define STAMP_DECL
#define PSTAMP(i)
#define STAMP_STORE()
#define DIAG_ARG
#define DIAG_PASS(k)
#define DI __device__ __forceinline__
#define MFMA(a, b, c) __builtin_amdgcn_mfma_f32_32x32x16_bf16((a), (b), (c), 0, 0, 0)
#define LDSP(T) __attribute__((address_space(3))) T*

constexpr int S_ = 2048, D_ = 512, H_ = 32, F_ = 1792;
constexpr int NTT = S_ / 32;
constexpr int KSD = D_ / 16;
constexpr int KSF = F_ / 16;
constexpr float QSCALE = 0.25f * 1.4426950408889634f;
constexpr float LN_EPS = 1e-5f;

constexpr size_t KB = 1024;
constexpr size_t OFF_CNT = 0;
constexpr size_t OFF_WO = 16 * KB;
constexpr size_t OFF_WGU = OFF_WO + 512 * KB;
constexpr size_t OFF_WD = OFF_WGU + 3584 * KB;
constexpr size_t OFF_WQ = OFF_WD + 1792 * KB;
constexpr size_t OFF_CSP = OFF_WQ + 1536 * KB;
constexpr size_t OFF_ST3 = OFF_CSP + 320 * KB;
constexpr size_t OFF_X3 = OFF_ST3 + 256 * KB;
constexpr size_t OFF_L = OFF_X3 + 2048 * KB;
constexpr size_t OFF_Q = OFF_L;
constexpr size_t OFF_K = OFF_Q + 2048 * KB;
constexpr size_t OFF_V = OFF_K + 2048 * KB;
constexpr size_t OFF_O = OFF_V + 2048 * KB;
constexpr size_t OFF_X2 = OFF_O + 2048 * KB;
constexpr size_t OFF_ST2 = OFF_X2 + 2048 * KB;
constexpr size_t OFF_H = OFF_ST2 + 256 * KB;
constexpr size_t LSTRIDE = OFF_H + 7168 * KB - OFF_L;
constexpr int C_O = 0, C_X2 = 16, C_H = 48, C_X3 = 80, C_KV = 96, C_PREP = 112, C_N = 128;

DI unsigned pk2(float lo, float hi) {
  f32x2 v = {lo, hi};
  bf16v2 r = __builtin_convertvector(v, bf16v2);
  return __builtin_bit_cast(unsigned, r);
}
DI float lo16(unsigned u) { return __uint_as_float(u << 16); }
DI float hi16(unsigned u) { return __uint_as_float(u & 0xffff0000u); }
DI float xhalf_add(float v) {
  auto r = __builtin_amdgcn_permlane32_swap(__float_as_uint(v), __float_as_uint(v), false, false);
  return __uint_as_float(r[0]) + __uint_as_float(r[1]);
}
DI float xhalf_max(float v) {
  auto r = __builtin_amdgcn_permlane32_swap(__float_as_uint(v), __float_as_uint(v), false, false);
  return fmaxf(__uint_as_float(r[0]), __uint_as_float(r[1]));
}
DI float x16_add(float v) {
  auto r = __builtin_amdgcn_permlane16_swap(__float_as_uint(v), __float_as_uint(v), false, false);
  return __uint_as_float(r[0]) + __uint_as_float(r[1]);
}
DI void st16(void* p, u32x4 v) { asm volatile("global_store_dwordx4 %0, %1, off sc1\n\ts_nop 1" :: "v"(p), "v"(v) : "memory"); }
template <int IMM>
DI void st16s(unsigned voff, const char* sbase, u32x4 v) {
  asm volatile("global_store_dwordx4 %0, %1, %2 offset:%3 sc1\n\ts_nop 1" :: "v"(voff), "v"(v), "s"(sbase), "i"(IMM) : "memory");
}
template <int IMM>
DI void gld16(bf16x8& d, unsigned voff, const char* sbase) {
  asm volatile("global_load_dwordx4 %0, %1, %2 offset:%3 sc1" : "=v"(d) : "v"(voff), "s"(sbase), "i"(IMM) : "memory");
}
#define GLDS_HEAD "s_mov_b32 %0, m0\n\ts_mov_b32 m0, %3\n\ts_nop 0\n\t"
#define GLDS_OP(o) "global_load_lds_dwordx4 %1, %2 offset:" #o " sc1\n\t"
#define GLDS_TAIL "s_mov_b32 m0, %0" : "=&s"(keep) : "v"(voff), "s"(sbase), "s"(lds) : "memory"
DI void glds1(unsigned voff, const char* sbase, unsigned lds) { unsigned keep; asm volatile(GLDS_HEAD GLDS_OP(0) GLDS_TAIL); }
DI void glds2(unsigned voff, const char* sbase, unsigned lds) { unsigned keep; asm volatile(GLDS_HEAD GLDS_OP(0) GLDS_OP(1024) GLDS_TAIL); }
DI void glds3(unsigned voff, const char* sbase, unsigned lds) { unsigned keep; asm volatile(GLDS_HEAD GLDS_OP(0) GLDS_OP(1024) GLDS_OP(2048) GLDS_TAIL); }
DI void glds4(unsigned voff, const char* sbase, unsigned lds) { unsigned keep; asm volatile(GLDS_HEAD GLDS_OP(0) GLDS_OP(1024) GLDS_OP(2048) GLDS_OP(3072) GLDS_TAIL); }
#define SBAR() __builtin_amdgcn_sched_barrier(0)
#define PIN(x) asm volatile("" : "+v"(x))
#define WAITV(n) asm volatile("s_waitcnt vmcnt(%0)" :: "i"(n) : "memory")
#define WAITBAR_V(n) asm volatile("s_waitcnt vmcnt(%0) lgkmcnt(0)\n\ts_barrier" :: "i"(n) : "memory")
#define WAITBAR_L() do { asm volatile("" ::: "memory"); __builtin_amdgcn_s_waitcnt(0xc07f); __builtin_amdgcn_s_barrier(); asm volatile("" ::: "memory"); } while (0)
typedef short s16x4 __attribute__((ext_vector_type(4)));
DI s16x4 trread(LDSP(const char) p) { return __builtin_amdgcn_ds_read_tr16_b64_v4i16((LDSP(s16x4))p); }
DI bf16x8 trfrag(LDSP(const char) p, int off2) { const s16x4 lo = trread(p), hi = trread(p + off2); return __builtin_shufflevector(lo, hi, 0, 1, 2, 3, 4, 5, 6, 7); }
DI bf16x8 ldfrag(LDSP(const char) p) { return *(LDSP(const bf16x8))p; }
DI float dot2(unsigned a, unsigned b, float c) { return __builtin_amdgcn_fdot2_f32_bf16(__builtin_bit_cast(bf16v2, a), __builtin_bit_cast(bf16v2, b), c, false); }
constexpr unsigned ONE2 = 0x3f803f80u;
#define BC8(x) __builtin_bit_cast(bf16x8, x)

template <int LOOK, int PER> DI void ring_wait(int i) {
  if (i <= 8 - LOOK) WAITBAR_V((LOOK - 1) * PER);
  else if (LOOK > 3 && i == 5) WAITBAR_V(2 * PER);
  else if (LOOK > 2 && i == 6) WAITBAR_V(PER);
  else WAITBAR_V(0);
}

DI void qk_store(const float (&v)[16], char* o) {
  u32x4 a = {pk2(v[0], v[1]), pk2(v[2], v[3]), pk2(v[4], v[5]), pk2(v[6], v[7])};
  u32x4 b = {pk2(v[8], v[9]), pk2(v[10], v[11]), pk2(v[12], v[13]), pk2(v[14], v[15])};
  st16(o, a);
  st16(o + NTT * 1024, b);
}
DI void v_store(const float (&v)[16], char* o) {
  u32x4 a = {pk2(v[0], v[1]), pk2(v[2], v[3]), pk2(v[4], v[5]), pk2(v[6], v[7])};
  u32x4 b = {pk2(v[8], v[9]), pk2(v[10], v[11]), pk2(v[12], v[13]), pk2(v[14], v[15])};
  st16(o, a);
  st16(o + 512, b);
}
DI float row16_sum(float v) {
  v += __builtin_bit_cast(float, __builtin_amdgcn_update_dpp(0, __builtin_bit_cast(int, v), 0x128, 0xf, 0xf, false));
  v += __builtin_bit_cast(float, __builtin_amdgcn_update_dpp(0, __builtin_bit_cast(int, v), 0x124, 0xf, 0xf, false));
  v += __builtin_bit_cast(float, __builtin_amdgcn_update_dpp(0, __builtin_bit_cast(int, v), 0x122, 0xf, 0xf, false));
  v += __builtin_bit_cast(float, __builtin_amdgcn_update_dpp(0, __builtin_bit_cast(int, v), 0x121, 0xf, 0xf, false));
  return v;
}
#define KSTEP(B, u) do { SBAR(); RX((B) ^ 1, u); RW((B) ^ 1, 0, u); SBAR(); acc[0] = MFMA(wf[B][0], xf[B], acc[0]); SBAR(); RW((B) ^ 1, 1, u); RW((B) ^ 1, 2, u); SBAR(); \
                         acc[1] = MFMA(wf[B][1], xf[B], acc[1]); acc[2] = MFMA(xf[B], wf[B][2], acc[2]); SBAR(); } while (0)
#define QCHUNK() do { KSTEP(1, 0); KSTEP(0, 1); KSTEP(1, 2); KSTEP(0, 3); } while (0)
#define QINIT() \
  f32x16 acc[3]; bf16x8 wf[2][3], xf[2]; \
  _Pragma("unroll") for (int i = 0; i < 3; ++i) { acc[i] = f32x16{}; wf[1][i] = bf16x8{}; } \
  xf[1] = bf16x8{}
#define QFINISH() do { acc[0] = MFMA(wf[1][0], xf[1], acc[0]); acc[1] = MFMA(wf[1][1], xf[1], acc[1]); acc[2] = MFMA(xf[1], wf[1][2], acc[2]); } while (0)

extern "C" __global__ __launch_bounds__(768, 3) void k_qkv1(const char* __restrict__ x, const char* __restrict__ wn, char* __restrict__ ws DIAG_ARG) {
  STAMP_DECL; STAMP(0);
  constexpr int LOOK = 3, CH = 24576, LXN = 4 * CH;
  __shared__ __attribute__((aligned(16))) char smem[LXN + 65536];
  const int tid = threadIdx.x, lane = tid & 63, r = lane & 31, hh = lane >> 5;
  const int w = __builtin_amdgcn_readfirstlane(tid >> 6);
  const int xcd = blockIdx.x & 7, idx = blockIdx.x >> 3;
  const int tb = xcd * 4 + (idx & 3), nq = idx >> 2;
  const unsigned lds0 = (unsigned)(uintptr_t)smem;
  const int rot = 2 * (idx & 3);
  if (w >= 8) {
    const int j = w - 8;
    const unsigned wv = (lane >> 3) * 3072 + (((lane & 7) ^ (((lane >> 4) & 1) << 2)) * 16);
    const char* wb = wn + (size_t)j * 16 * 3072 + nq * 128;
    if (blockIdx.x == 0 && lane < 32) *(unsigned*)(ws + OFF_CNT + (j * 32 + lane) * 128) = 0u;
    asm volatile("s_sleep 2\n\ts_nop 4" ::: "memory");
#pragma unroll 1
    for (int c = 0; c < 8 + LOOK; ++c) {
      if (c >= LOOK) ring_wait<LOOK, 6>(c - LOOK);
      if (c < 8) {
        const char* ws_ = wb + (size_t)((c + rot) & 7) * 64 * 3072;
        const unsigned d = lds0 + (c & 3) * CH + j * 2048;
        glds1(wv, ws_, d); glds1(wv, ws_ + 1024, d + 8192); glds1(wv, ws_ + 2048, d + 16384);
        glds1(wv, ws_ + 8 * 3072, d + 1024); glds1(wv, ws_ + 8 * 3072 + 1024, d + 8192 + 1024); glds1(wv, ws_ + 8 * 3072 + 2048, d + 16384 + 1024);
      }
    }
    return;
  }
  {
    const int n = w;
    const u32x4* xr = (const u32x4*)(x + (size_t)(tb * 64 + n * 8) * 1024) + lane;
    u32x4 v[8];
#pragma unroll
    for (int i = 0; i < 8; ++i) v[i] = xr[i * 64];
    float s[8], q[8];
#pragma unroll
    for (int i = 0; i < 8; ++i) {
      s[i] = 0.f; q[i] = 0.f;
#pragma unroll
      for (int e = 0; e < 4; ++e) { s[i] = dot2(v[i][e], ONE2, s[i]); q[i] = dot2(v[i][e], v[i][e], q[i]); }
    }
    float s4[4], q4[4], s2[2], q2[2];
#pragma unroll
    for (int i = 0; i < 4; ++i) {
      const int lo = (i & 1) + 4 * (i >> 1);
      auto a = __builtin_amdgcn_permlane32_swap(__float_as_uint(s[lo]), __float_as_uint(s[lo + 2]), false, false);
      auto b = __builtin_amdgcn_permlane32_swap(__float_as_uint(q[lo]), __float_as_uint(q[lo + 2]), false, false);
      s4[i] = __uint_as_float(a[0]) + __uint_as_float(a[1]); q4[i] = __uint_as_float(b[0]) + __uint_as_float(b[1]);
    }
#pragma unroll
    for (int i = 0; i < 2; ++i) {
      auto a = __builtin_amdgcn_permlane16_swap(__float_as_uint(s4[2 * i]), __float_as_uint(s4[2 * i + 1]), false, false);
      auto b = __builtin_amdgcn_permlane16_swap(__float_as_uint(q4[2 * i]), __float_as_uint(q4[2 * i + 1]), false, false);
      s2[i] = row16_sum(__uint_as_float(a[0]) + __uint_as_float(a[1])); q2[i] = row16_sum(__uint_as_float(b[0]) + __uint_as_float(b[1]));
    }
    float ra[2], rb[2];
#pragma unroll
    for (int i = 0; i < 2; ++i) {
      const float mean = s2[i] * (1.f / D_);
      ra[i] = rsqrtf(fmaxf(q2[i] * (1.f / D_) - mean * mean, 0.f) + LN_EPS);
      rb[i] = -mean * ra[i];
    }
    LDSP(char) xd = (LDSP(char))smem + LXN + (lane >> 3) * 8192 + n * 1024;
#pragma unroll
    for (int i = 0; i < 8; ++i) {
      const float a = __builtin_bit_cast(float, __builtin_amdgcn_readlane(__builtin_bit_cast(int, ra[i >> 2]), (i & 3) * 16));
      const float b = __builtin_bit_cast(float, __builtin_amdgcn_readlane(__builtin_bit_cast(int, rb[i >> 2]), (i & 3) * 16));
      u32x4 o;
#pragma unroll
      for (int e = 0; e < 4; ++e) o[e] = pk2(lo16(v[i][e]) * a + b, hi16(v[i][e]) * a + b);
      *(LDSP(u32x4))(xd + i * 128 + (((lane & 7) ^ ((4 * n + (i >> 1)) & 7)) * 16)) = o;
    }
  }
  STAMP(1);
  WAITBAR_L();
  if (w >= 4) return;
  STAMP(2);
  const int ta = w & 1, hp = w >> 1, q4 = (lane & 15) >> 2, xsw = (r >> 1) & 7;
  LDSP(const char) lw = (LDSP(const char))smem + (8 * hh + q4) * 128 + ((hp ^ (q4 >> 1)) * 64) + ((lane >> 4) & 1) * 32 + (lane & 3) * 8;
  LDSP(const char) lx = (LDSP(const char))smem + LXN + ta * 4096 + r * 128;
  int xo[4];
#pragma unroll
  for (int u = 0; u < 4; ++u) xo[u] = ((2 * u + hh) ^ xsw) * 16;
  QINIT();
#define RX(B, u) xf[B] = ldfrag(sx + xo[u])
#define RW(B, i, u) wf[B][i] = trfrag(sw + (i) * 8192 + (u) * 2048, 512)
#pragma unroll 1
  for (int c = 0; c < 8; ++c) {
    LDSP(const char) sw = lw + (c & 3) * CH;
    LDSP(const char) sx = lx + ((c + rot) & 7) * 8192;
    QCHUNK();
    WAITBAR_L();
  }
  QFINISH();
#undef RX
#undef RW
  STAMP(4);
  const int tt = tb * 2 + ta, nb = nq * 2 + hp;
  float v[16];
#pragma unroll
  for (int e = 0; e < 16; ++e) v[e] = acc[0][e] * QSCALE;
  qk_store(v, ws + OFF_Q + ((size_t)(2 * nb) * NTT + tt) * 1024 + lane * 16);
#pragma unroll
  for (int e = 0; e < 16; ++e) v[e] = acc[1][e];
  qk_store(v, ws + OFF_K + ((size_t)(2 * nb) * NTT + tt) * 1024 + lane * 16);
#pragma unroll
  for (int e = 0; e < 16; ++e) v[e] = acc[2][e];
  v_store(v, ws + OFF_V + ((size_t)(2 * nb + (r >> 4)) * NTT + tt) * 1024 + hh * 256 + (r & 15) * 16);
  STAMP(5); STAMP_STORE();
}

DI void poll(const char* cnt, unsigned voff, unsigned target) {
#pragma unroll 1
  for (unsigned spins = 0; spins < 200000u; ++spins) {
    unsigned v;
    asm volatile("global_load_dword %0, %1, %2 sc1\n\ts_waitcnt vmcnt(0)" : "=v"(v) : "v"(voff), "s"(cnt) : "memory");
    if (__all(v >= target)) break;
  }
}
DI void poll_b(const char* cnt, unsigned target, LDSP(unsigned) go, unsigned seq, int j) {
  if (j == 0) {
    poll(cnt, 0u, target);
    __hip_atomic_store(go, seq, __ATOMIC_RELAXED, __HIP_MEMORY_SCOPE_WORKGROUP);
  } else {
    WAITV(0);
#pragma unroll 1
    for (unsigned spins = 0; spins < 4000000u; ++spins) {
      if (__builtin_amdgcn_readfirstlane(__hip_atomic_load(go, __ATOMIC_RELAXED, __HIP_MEMORY_SCOPE_WORKGROUP)) >= seq) break;
    }
  }
  asm volatile("" ::: "memory");
}
DI void arrive4(LDSP(unsigned) lw, char* cnt, int lane) {
  WAITV(0);
  if (lane == 0) {
    const unsigned old = __hip_atomic_fetch_add(lw, 1u, __ATOMIC_RELAXED, __HIP_MEMORY_SCOPE_WORKGROUP);
    if ((old & 3u) == 3u) asm volatile("global_atomic_add %0, %1, %2" :: "v"(0u), "v"(1u), "s"(cnt) : "memory");
  }
  asm volatile("" ::: "memory");
}
DI void stat_issue(f32x4 (&p)[4], const char* sb, unsigned voff) {
  asm volatile("global_load_dwordx4 %0, %4, %5 sc1\n\tglobal_load_dwordx4 %1, %4, %5 offset:1024 sc1\n\tglobal_load_dwordx4 %2, %4, %5 offset:2048 sc1\n\t"
               "global_load_dwordx4 %3, %4, %5 offset:3072 sc1" : "=&v"(p[0]), "=&v"(p[1]), "=&v"(p[2]), "=&v"(p[3]) : "v"(voff), "s"(sb) : "memory");
}
DI void stat_finish(f32x4 (&p)[4], float& mean, float& var) {
  asm volatile("" : "+v"(p[0]), "+v"(p[1]), "+v"(p[2]), "+v"(p[3]));
  const f32x4 t = (p[0] + p[1]) + (p[2] + p[3]);
  auto a = __builtin_amdgcn_permlane16_swap(__float_as_uint(t[0]), __float_as_uint(t[2]), false, false);
  auto b = __builtin_amdgcn_permlane16_swap(__float_as_uint(t[1]), __float_as_uint(t[3]), false, false);
  mean = xhalf_add(__uint_as_float(a[0]) + __uint_as_float(a[1])) * (1.f / D_);
  var = fmaxf(xhalf_add(__uint_as_float(b[0]) + __uint_as_float(b[1])) * (1.f / D_) - mean * mean, 0.f) + LN_EPS;
}
DI void colsum_part(const char* src, unsigned voff, LDSP(char) dst) {
  f32x4 p[4];
  asm volatile("global_load_dwordx4 %0, %4, %5 sc1\n\tglobal_load_dwordx4 %1, %4, %6 sc1\n\tglobal_load_dwordx4 %2, %4, %7 sc1\n\t"
               "global_load_dwordx4 %3, %4, %8 sc1\n\ts_waitcnt vmcnt(0)" : "=&v"(p[0]), "=&v"(p[1]), "=&v"(p[2]), "=&v"(p[3])
               : "v"(voff), "s"(src), "s"(src + 20480), "s"(src + 40960), "s"(src + 61440) : "memory");
  *(LDSP(f32x4))dst = (p[0] + p[1]) + (p[2] + p[3]);
}

#define RES_FINISH(WR, RD) do { \
    _Pragma("unroll") for (int i = 1; i < 4; ++i) _Pragma("unroll") for (int g = 0; g < 4; ++g) { \
      const f32x4 v_ = {acc[i][4 * g], acc[i][4 * g + 1], acc[i][4 * g + 2], acc[i][4 * g + 3]}; \
      *(LDSP(f32x4))(lb + (WR) + g * 1024) = v_; } \
    asm volatile("s_waitcnt vmcnt(0)" : "+v"(rn[0]), "+v"(rn[1]), "+v"(rn[2]), "+v"(rn[3]) :: "memory"); \
    WAITBAR_L(); \
    _Pragma("unroll") for (int g = 0; g < 4; ++g) { \
      f32x4 t_ = {acc[0][4 * g], acc[0][4 * g + 1], acc[0][4 * g + 2], acc[0][4 * g + 3]}; \
      f32x4 u_[3]; \
      _Pragma("unroll") for (int i = 1; i < 4; ++i) u_[i - 1] = *(LDSP(const f32x4))(lb + (RD) + g * 1024); \
      t_ = (u_[0] + u_[1]) + (u_[2] + t_); \
      res[2 * g] = pk2(t_[0] + lo16(rn[g][0]), t_[1] + hi16(rn[g][0])); \
      res[2 * g + 1] = pk2(t_[2] + lo16(rn[g][1]), t_[3] + hi16(rn[g][1])); } } while (0)
DI void res_load(u32x2 (&rn)[4], unsigned vo, const char* p0, int d1, int d2) {
  asm volatile("global_load_dwordx2 %0, %4, %5 sc1\n\tglobal_load_dwordx2 %1, %4, %6 sc1\n\tglobal_load_dwordx2 %2, %4, %7 sc1\n\tglobal_load_dwordx2 %3, %4, %8 sc1"
               : "=&v"(rn[0]), "=&v"(rn[1]), "=&v"(rn[2]), "=&v"(rn[3]) : "v"(vo), "s"(p0), "s"(p0 + d1), "s"(p0 + d2), "s"(p0 + d2 + d1) : "memory");
}
DI void res_store_tiled(const unsigned (&res)[8], char* o, char* st, int lane) {
  const u32x4 p0 = {res[0], res[1], res[2], res[3]}, p1 = {res[4], res[5], res[6], res[7]};
  st16(o, p0);
  st16(o + 1024, p1);
  float s1 = 0.f, s2 = 0.f, t1 = 0.f, t2 = 0.f;
#pragma unroll
  for (int e = 0; e < 4; ++e) { s1 = dot2(res[e], ONE2, s1); s2 = dot2(res[e], res[e], s2); t1 = dot2(res[4 + e], ONE2, t1); t2 = dot2(res[4 + e], res[4 + e], t2); }
  s1 = xhalf_add(s1 + t1); s2 = xhalf_add(s2 + t2);
  if (lane < 32) { const f32x2 v = {s1, s2}; asm volatile("global_store_dwordx2 %0, %1, off sc1" :: "v"(st), "v"(v) : "memory"); }
}

DI const char* karg(int i) {
  const __attribute__((address_space(4))) unsigned long long* ka = (const __attribute__((address_space(4))) unsigned long long*)__builtin_amdgcn_kernarg_segment_ptr();
  asm volatile("" : "+s"(ka));
  return (const char*)ka[i];
}
extern "C" __global__ __launch_bounds__(512, 2) void k_main(char* __restrict__ ws_, const char* x_, const char* wq_, const char* wo_, const char* wg_,
                                                            const char* wu_, const char* wd_, char* out_ DIAG_ARG) {
  constexpr int L_K = 0, L_V = 65536, L_ONE = 131072, L_CMB = 133120;
  constexpr int GW = 28672, GCH = GW + 16384;
  constexpr int L_D0 = 2 * GCH, L_D1 = L_D0 + 32768, L_D2 = 0, L_D3 = GCH;
  constexpr int QW = 12288, L_QX = 49152;
  constexpr int L_CS = 157696, L_ST = L_CS + 4096, L_CNT = L_ST + 1024, L_DBG = L_CNT + 64;
  __shared__ __attribute__((aligned(16))) char smem[163840];
  const int w = __builtin_amdgcn_readfirstlane(threadIdx.x >> 6);
  const unsigned lds0 = (unsigned)(uintptr_t)smem;
#define FRESH() \
  int tid = threadIdx.x; PIN(tid); int bid = blockIdx.x, j = w & 3; char* ws = ws_; asm volatile("" : "+s"(bid), "+s"(j), "+s"(ws)); \
  const int lane = tid & 63, r = lane & 31, hh = lane >> 5, xcd = bid & 7, idx = bid >> 3, grp = xcd * 2 + (idx >> 4), m = idx & 15; \
  const unsigned voff = lane * 16; LDSP(char) lb = (LDSP(char))smem + lane * 16; char* cnt = ws + OFF_CNT; char* wl = ws + layer * LSTRIDE; \
  LDSP(unsigned) lcnt = (LDSP(unsigned))((LDSP(char))smem + L_CNT) + (w >> 2); \
  const int ont = (m >> 1) * 2 + (j >> 1), ott = grp * 4 + (m & 1) * 2 + (j & 1); \
  const int gtb = (xcd & 1) * 8 + (idx & 7), gnb = (xcd >> 1) * 4 + (idx >> 3), qtb = (xcd & 3) * 4 + (idx & 3), qnb = (xcd >> 2) * 8 + (idx >> 2); \
  (void)r; (void)hh; (void)voff; (void)lb; (void)cnt; (void)wl; (void)lcnt; (void)ont; (void)ott; (void)gtb; (void)gnb; (void)qtb; (void)qnb
  if (threadIdx.x < 8) ((LDSP(unsigned))((LDSP(char))smem + L_CNT))[threadIdx.x] = 0u;
#pragma unroll 1
  for (int layer = 0; layer < 2; ++layer) {
    {
      FRESH();
      const int head = xcd * 4 + (idx & 3), qb = idx >> 2, half = w >> 2, tt0 = qb * 8 + 2 * j;
      PSTAMP(0);
      if (layer) { if (w == 4) poll(cnt + (C_KV + (head >> 1)) * 128, 0u, 16u); WAITBAR_L(); }
      PSTAMP(1);
      const char* ks = wl + OFF_K + (size_t)head * 65536 + half * 32768;
      const unsigned kd = lds0 + L_K + half * 32768;
      bf16x8 q0, q1;
      gld16<0>(q0, voff, wl + OFF_Q + ((size_t)head * NTT + tt0) * 1024);
      gld16<1024>(q1, voff, wl + OFF_Q + ((size_t)head * NTT + tt0) * 1024);
      glds2(voff, ks + j * 2048, kd + j * 2048);
      glds2(voff, ks + (OFF_V - OFF_K) + j * 2048, kd + L_V + j * 2048);
      *(LDSP(unsigned))((LDSP(char))smem + L_ONE + tid * 4) = ((tid >> 2) & 11) == 0 ? ONE2 : 0u;
      asm volatile("s_waitcnt vmcnt(0) lgkmcnt(0)\n\ts_barrier" : "+v"(q0), "+v"(q1) :: "memory");
      PSTAMP(2);
      LDSP(const char) kp = lb + L_K;
      LDSP(const char) vp0 = (LDSP(const char))smem + (r < 16 ? L_V : L_ONE) + hh * 256 + (r & 15) * 16;
      const int vstep = r < 16 ? 2048 : 0;
      LDSP(const char) kq = kp + half * 32768; LDSP(const char) vq = vp0 + half * 16 * vstep;
      f32x16 n0, n1, sA, sB, o0 = f32x16{}, o1 = f32x16{};
      bf16x8 kA, kB, vp = bf16x8{};
      u32x4 p1 = {0u, 0u, 0u, 0u};
      {
        const bf16x8 k0 = ldfrag(kp);
        kA = ldfrag(kq);
        const f32x16 s = MFMA(k0, q0, f32x16{}), t = MFMA(k0, q1, f32x16{});
        float a = fmaxf(fmaxf(s[0], s[1]), s[2]), b = fmaxf(fmaxf(t[0], t[1]), t[2]);
#pragma unroll
        for (int i = 3; i < 15; i += 2) { a = fmaxf(fmaxf(a, s[i]), s[i + 1]); b = fmaxf(fmaxf(b, t[i]), t[i + 1]); }
        a = xhalf_max(fmaxf(a, s[15])); b = xhalf_max(fmaxf(b, t[15]));
#pragma unroll
        for (int i = 0; i < 16; ++i) { n0[i] = -a; n1[i] = -b; }
        PIN(n0); PIN(n1);
        sA = MFMA(kA, q0, n0);
      }
#define EX(S, i) S[i] = __builtin_amdgcn_exp2f(S[i])
#define STEPN(CUR, NXT, KN_, QN, NN, O, VA, OP, VP, READS) do { \
      EX(CUR, 0); EX(CUR, 1); PIN(CUR); SBAR(); OP = MFMA(VP, BC8(p1), OP); SBAR(); READS; SBAR(); \
      EX(CUR, 2); EX(CUR, 3); EX(CUR, 4); EX(CUR, 5); PIN(CUR); SBAR(); NXT = MFMA(KN_, QN, NN); SBAR(); \
      u32x4 p0; EX(CUR, 6); EX(CUR, 7); p0[0] = pk2(CUR[0], CUR[1]); p0[1] = pk2(CUR[2], CUR[3]); PIN(CUR); SBAR(); \
      EX(CUR, 8); EX(CUR, 9); p0[2] = pk2(CUR[4], CUR[5]); p0[3] = pk2(CUR[6], CUR[7]); PIN(p0); PIN(CUR); SBAR(); \
      EX(CUR, 10); EX(CUR, 11); PIN(CUR); SBAR(); O = MFMA(VA, BC8(p0), O); SBAR(); \
      EX(CUR, 12); EX(CUR, 13); EX(CUR, 14); EX(CUR, 15); \
      p1[0] = pk2(CUR[8], CUR[9]); p1[1] = pk2(CUR[10], CUR[11]); p1[2] = pk2(CUR[12], CUR[13]); p1[3] = pk2(CUR[14], CUR[15]); PIN(p1); SBAR(); } while (0)
#define TILE(t, KA, KB) do { bf16x8 va, vb; \
      STEPN(sA, sB, KA, q1, n1, o0, va, o1, vp, (va = ldfrag(vq + (t) * 1024), vb = ldfrag(vq + (t) * 1024 + 512), KB = ldfrag(kq + ((t) + 1) * 1024))); \
      STEPN(sB, sA, KB, q0, n0, o1, va, o0, vb, (vp = vb)); } while (0)
      __builtin_amdgcn_s_setprio(3);
#pragma unroll 1
      for (int ph = -2; ph < 4; ++ph) {
        if (ph == -1) {
          glds3(voff, ks + 8192 + j * 6144, kd + 8192 + j * 6144);
          glds3(voff, ks + (OFF_V - OFF_K) + 8192 + j * 6144, kd + L_V + 8192 + j * 6144);
          glds3(voff, ks + 11264 + j * 6144, kd + 11264 + j * 6144);
          glds3(voff, ks + (OFF_V - OFF_K) + 11264 + j * 6144, kd + L_V + 11264 + j * 6144);
        }
        if (ph == 0) {
          WAITV(0);
          if (lane == 0) __hip_atomic_fetch_add(lcnt + 4, 1u, __ATOMIC_RELAXED, __HIP_MEMORY_SCOPE_WORKGROUP);
#pragma unroll 1
          for (unsigned spins = 0; spins < 4000000u; ++spins)
            if (__builtin_amdgcn_readfirstlane(__hip_atomic_load(lcnt + 4, __ATOMIC_RELAXED, __HIP_MEMORY_SCOPE_WORKGROUP)) >= 4u * (layer + 1)) break;
          asm volatile("" ::: "memory");
        }
        if (ph == 1) __builtin_amdgcn_s_setprio(2);
        if (ph == 2) __builtin_amdgcn_s_setprio(1);
        if (ph == 3) __builtin_amdgcn_s_setprio(0);
        if (layer == 0 && ph >= 0) {
          const int q4 = (lane & 15) >> 2, b16 = (lane >> 4) & 1;
          const int scr = L_CMB + (half ? j * 5120 : 20480 + j * 2048);
          const int jo = ((ph - 1) * 8 + w) * 256 + bid;
          if ((ph == 1 || ph == 2) && jo < 3712) {
            const bool gu = jo < 1792;
            LDSP(const char) sp = (LDSP(const char))smem + scr + (lane & 3) * 8 +
                                  (gu ? (16 * hh + 4 * b16 + q4) * 32 : (4 * hh + q4) * 64 + b16 * 32);
            const int ho = gu ? 256 : 512;
            const char* pd;
            if (gu) { const int ft = jo >> 4, S = jo & 15, nb = ft / 7, ii = ft - nb * 7; pd = ws + OFF_WGU + ((size_t)((nb * 8 + (S >> 1)) * 14 + 2 * ii) * 2 + (S & 1)) * 1024; }
            else if (jo < 2048) pd = ws + OFF_WO + (size_t)(jo - 1792) * 2048;
            else if (jo < 2944) pd = ws + OFF_WD + (size_t)(jo - 2048) * 2048;
            else { const int t = (jo - 2944) >> 4, nb = t / 3, ii = t - nb * 3, s0 = (jo & 15) * 2; pd = ws + OFF_WQ + (size_t)(((nb * 8 + (s0 >> 2)) * 3 + ii) * 4 + (s0 & 3)) * 1024; }
            WAITV(0);
            const u32x4 fa = __builtin_bit_cast(u32x4, trfrag(sp, ho)), fb = __builtin_bit_cast(u32x4, trfrag(sp + 1024, ho));
            st16s<0>(voff, pd, fa); st16s<0>(voff, pd + (gu ? 2048 : 1024), fb);
            if (gu || jo >= 2944) {
              float ca = (lo16(fa[0]) + hi16(fa[0])) + (lo16(fa[1]) + hi16(fa[1])) + (lo16(fa[2]) + hi16(fa[2])) + (lo16(fa[3]) + hi16(fa[3]));
              float cb = (lo16(fb[0]) + hi16(fb[0])) + (lo16(fb[1]) + hi16(fb[1])) + (lo16(fb[2]) + hi16(fb[2])) + (lo16(fb[3]) + hi16(fb[3]));
              if (gu) { ca = xhalf_add(x16_add(ca)); cb = xhalf_add(x16_add(cb)); ca = lane < 16 ? ca : cb; }
              else ca = xhalf_add(ca + cb);
              const char* cp = ws + OFF_CSP + ((jo & 15) * 5120 + ((gu ? jo : jo - 2944 + 1792) >> 4) * 32) * 4;
              if (lane < 32) asm volatile("global_store_dword %0, %1, %2 sc1" :: "v"(lane * 4), "v"(ca), "s"(cp) : "memory");
            }
          }
          const int jn = jo + 2048;
          if (ph < 2 && jn < 3712) {
            const char *sa, *sb; unsigned gv;
            if (jn < 1792) {
              const int o = 32 * (jn & 15) * (F_ * 2) + (jn >> 4) * 32;
              gv = (lane >> 1) * (F_ * 2) + (lane & 1) * 16; sa = karg(4) + o; sb = karg(5) + o;
            } else {
              int nt, s0, ld = 1024, ai;
              if (jn < 2048) { const int f = (jn - 1792) * 2; nt = f >> 5; s0 = f & 31; ai = 3; }
              else if (jn < 2944) { const int f = (jn - 2048) * 2; nt = f / KSF; s0 = f - nt * KSF; ai = 6; }
              else { const int t = (jn - 2944) >> 4, nb = t / 3; s0 = (jn & 15) * 2; nt = (t - nb * 3) * 16 + nb; ai = 2; ld = 3072; }
              gv = (lane >> 2) * ld + (lane & 3) * 16; sa = karg(ai) + (16 * s0 * ld + nt * 64); sb = sa + 16 * ld;
            }
            __builtin_amdgcn_s_waitcnt(0xc07f);
            glds1(gv, sa, lds0 + scr); glds1(gv, sb, lds0 + scr + 1024);
          }
          if (ph == 3) arrive4(lcnt, cnt + (C_PREP + xcd) * 128, lane);
        }
        int n = ph <= 0 ? ph + 3 : ph == 3 ? 2 : 4;
#pragma unroll 1
        do { TILE(0, kA, kB); TILE(1, kB, kA); kq += 2048; vq += vstep; } while (--n > 0);
      }
      o1 = MFMA(vp, BC8(p1), o1);
#undef TILE
#undef STEPN
      PSTAMP(3);
      LDSP(char) xp = lb + L_CMB + j * 5120;
      if (half == 1) {
        const f32x4 a = {o0[0], o0[1], o0[2], o0[3]}, b = {o0[4], o0[5], o0[6], o0[7]}, c = {o1[0], o1[1], o1[2], o1[3]}, d = {o1[4], o1[5], o1[6], o1[7]};
        const f32x2 l = {o0[8], o1[8]};
        *(LDSP(f32x4))xp = a; *(LDSP(f32x4))(xp + 1024) = b; *(LDSP(f32x4))(xp + 2048) = c; *(LDSP(f32x4))(xp + 3072) = d; *(LDSP(f32x2))(xp + 4096) = l;
      }
      WAITBAR_L();
      if (half == 0) {
        PSTAMP(11);
        const f32x4 a = *(LDSP(const f32x4))xp, b = *(LDSP(const f32x4))(xp + 1024), c = *(LDSP(const f32x4))(xp + 2048), d = *(LDSP(const f32x4))(xp + 3072);
        const f32x2 l = *(LDSP(const f32x2))(xp + 4096);
#pragma unroll
        for (int i = 0; i < 4; ++i) { o0[i] += a[i]; o0[4 + i] += b[i]; o1[i] += c[i]; o1[4 + i] += d[i]; }
        o0[8] += l[0]; o1[8] += l[1];
        float chk = (o0[8] + o1[8]) * 5.4210109e-20f;
#pragma unroll
        for (int i = 0; i < 8; ++i) chk = __builtin_fmaf(o0[i], 0.f, __builtin_fmaf(o1[i], 0.f, chk));
        const bool bad = !(chk < 1.f);
        if (__builtin_expect(__any(bad), 0)) {
#pragma unroll 1
          for (int z = 0; z < 2; ++z) {
            const bf16x8 q = z ? q1 : q0;
            float mx = -INFINITY;
            f32x16 o = f32x16{};
            kq = kp; vq = vp0;
#pragma unroll 1
            for (int t = 0; t < 64; ++t) {
              const bf16x8 k = ldfrag(kq), v0 = ldfrag(vq), v1 = ldfrag(vq + 512);
              f32x16 s = MFMA(k, q, f32x16{});
              float tm = s[0];
#pragma unroll
              for (int i = 1; i < 16; ++i) tm = fmaxf(tm, s[i]);
              tm = xhalf_max(tm);
              const float mn = fmaxf(mx, tm);
              const float alpha = __builtin_amdgcn_exp2f(mx - mn);
              mx = mn;
#pragma unroll
              for (int i = 0; i < 16; ++i) { o[i] *= alpha; s[i] = __builtin_amdgcn_exp2f(s[i] - mn); }
              const u32x4 pa = {pk2(s[0], s[1]), pk2(s[2], s[3]), pk2(s[4], s[5]), pk2(s[6], s[7])};
              const u32x4 pb = {pk2(s[8], s[9]), pk2(s[10], s[11]), pk2(s[12], s[13]), pk2(s[14], s[15])};
              o = MFMA(v0, BC8(pa), o);
              o = MFMA(v1, BC8(pb), o);
              kq += 1024; vq += vstep >> 1;
            }
            if (z) o1 = o; else o0 = o;
          }
        }
        const float i0 = __builtin_amdgcn_rcpf(o0[8]), i1 = __builtin_amdgcn_rcpf(o1[8]);
        const u32x4 a0 = {pk2(o0[0] * i0, o0[1] * i0), pk2(o0[2] * i0, o0[3] * i0), pk2(o0[4] * i0, o0[5] * i0), pk2(o0[6] * i0, o0[7] * i0)};
        const u32x4 a1 = {pk2(o1[0] * i1, o1[1] * i1), pk2(o1[2] * i1, o1[3] * i1), pk2(o1[4] * i1, o1[5] * i1), pk2(o1[6] * i1, o1[7] * i1)};
        char* op = wl + OFF_O + ((size_t)tt0 * KSD + head) * 1024 + voff;
        st16(op, a0); st16(op + 32768, a1);
        PSTAMP(12);
        WAITBAR_L();
        arrive4(lcnt, cnt + (C_O + layer * 8 + qb) * 128, lane);
      }
    }
    PSTAMP(4);
    if (layer == 0) poll(ws_ + OFF_CNT + C_PREP * 128, (threadIdx.x & 7) * 128, 64u);
    if (w < 4) {
      {
        FRESH();
        const char* wb = ws + OFF_WGU + (size_t)gnb * 8 * GW + j * 7168;
#pragma unroll 1
        for (int c = 0; c < 2; ++c) { glds4(voff, wb + c * GW, lds0 + c * GCH + j * 7168); glds3(voff, wb + c * GW + 4096, lds0 + c * GCH + j * 7168 + 4096); }
        colsum_part(ws + OFF_CSP + (size_t)j * 81920 + gnb * 896, voff, lb + L_CS + j * 1024);
      }
      PSTAMP(14);
      WAITBAR_L();
      PSTAMP(15);
      {
        FRESH();
        const int g4 = lane >> 4, c16 = lane & 15;
        f32x4 ac[2][14];
#pragma unroll
        for (int i = 0; i < 14; ++i) { ac[0][i] = f32x4{}; ac[1][i] = f32x4{}; }
        LDSP(const char) lx = (LDSP(const char))smem + GW + j * 4096 + (g4 >> 1) * 1024 + ((g4 & 1) * 32 + c16) * 16;
        bf16x8 wf[2][7], xf[2][2];
        xf[1][0] = bf16x8{}; xf[1][1] = bf16x8{};
#pragma unroll
        for (int i = 0; i < 7; ++i) wf[1][i] = bf16x8{};
#define RDX(u, so, a) xf[u][a] = ldfrag(lx + (so) + (u) * 2048 + (a) * 256)
#define RDW(hf, u, so, i) wf[hf][i] = ldfrag(lb + (so) + ((7 * (hf) + (i)) * 2 + (u)) * 1024)
#define M1(B, XP, i, a) do { SBAR(); ac[a][7 * (B) + (i)] = __builtin_amdgcn_mfma_f32_16x16x32_bf16(wf[B][i], xf[XP][a], ac[a][7 * (B) + (i)], 0, 0, 0); SBAR(); } while (0)
#define MM(B, XP, i) do { M1(B, XP, i, 0); M1(B, XP, i, 1); } while (0)
#define GSTEP(B, XP, u, so) do { \
    M1(B, XP, 0, 0); RDW((B) ^ 1, u, so, 0); M1(B, XP, 0, 1); if ((B) == 1) RDX(u, so, 0); M1(B, XP, 1, 0); if ((B) == 1) RDX(u, so, 1); M1(B, XP, 1, 1); RDW((B) ^ 1, u, so, 1); \
    M1(B, XP, 2, 0); RDW((B) ^ 1, u, so, 2); M1(B, XP, 2, 1); RDW((B) ^ 1, u, so, 3); M1(B, XP, 3, 0); RDW((B) ^ 1, u, so, 4); M1(B, XP, 3, 1); RDW((B) ^ 1, u, so, 5); \
    M1(B, XP, 4, 0); RDW((B) ^ 1, u, so, 6); M1(B, XP, 4, 1); MM(B, XP, 5); MM(B, XP, 6); } while (0)
        WAITBAR_L();
        PSTAMP(5);
        int so = 0;
#pragma unroll 1
        for (int c = 0; c < 8; ++c) {
          GSTEP(1, 1, 0, so); GSTEP(0, 0, 0, so); GSTEP(1, 0, 1, so); GSTEP(0, 1, 1, so);
          asm volatile("" ::: "memory"); __builtin_amdgcn_s_waitcnt(0xc07f);
          if (c != 7) __builtin_amdgcn_s_barrier();
          asm volatile("" ::: "memory");
          so = so == 2 * GCH ? 0 : so + GCH;
        }
        LDSP(const float) sx = (LDSP(const float))((LDSP(char))smem + L_ST) + j * 64 + c16;
        const float rs0 = sx[0], rs1 = sx[16], m0 = -sx[32], m1 = -sx[48];
        f32x4 c4[14];
#pragma unroll
        for (int i = 0; i < 14; ++i) c4[i] = *(LDSP(const f32x4))((LDSP(const char))smem + L_CS + i * 64 + g4 * 16);
        const char* ob = wl + OFF_H + ((size_t)(gtb * 4 + j) * KSF + gnb * 7) * 1024;
        unsigned oo = (((g4 & 1) * 32) + (g4 >> 1) * 16 + c16) * 16;
        asm volatile("" : "+s"(ob)); PIN(oo);
        MM(1, 1, 0); MM(1, 1, 1); MM(1, 1, 2); MM(1, 1, 3); MM(1, 1, 4); MM(1, 1, 5); MM(1, 1, 6);
#undef RDX
#undef RDW
#undef M1
#undef MM
#undef GSTEP
        PSTAMP(6);
        const f32x4 vm0 = {m0, m0, m0, m0}, vm1 = {m1, m1, m1, m1};
#define HOUT(i) do { \
          const f32x4 ga = __builtin_elementwise_fma(c4[2 * (i)], vm0, ac[0][2 * (i)]), ua = __builtin_elementwise_fma(c4[2 * (i) + 1], vm0, ac[0][2 * (i) + 1]) * rs0; \
          const f32x4 gb = __builtin_elementwise_fma(c4[2 * (i)], vm1, ac[1][2 * (i)]), ub = __builtin_elementwise_fma(c4[2 * (i) + 1], vm1, ac[1][2 * (i) + 1]) * rs1; \
          const f32x4 h0 = __builtin_elementwise_max(ga, f32x4{}) * ua, h1 = __builtin_elementwise_max(gb, f32x4{}) * ub; \
          auto e0 = __builtin_amdgcn_permlane32_swap(pk2(h0[0], h0[1]), pk2(h1[0], h1[1]), false, false); \
          auto e1 = __builtin_amdgcn_permlane32_swap(pk2(h0[2], h0[3]), pk2(h1[2], h1[3]), false, false); \
          const u32x4 a_ = {e0[0], e1[0], e0[1], e1[1]}; \
          st16s<((i) & 3) * 1024>(oo, ob + ((i) >> 2) * 4096, a_); } while (0)
        HOUT(0); HOUT(1); HOUT(2); HOUT(3); HOUT(4); HOUT(5); HOUT(6);
#undef HOUT
        PSTAMP(13);
        if (lane == 0) __hip_atomic_fetch_add(lcnt + 3, 1u, __ATOMIC_RELAXED, __HIP_MEMORY_SCOPE_WORKGROUP);
        arrive4(lcnt, cnt + (C_H + layer * 16 + gtb) * 128, lane);
        PSTAMP(7);
      }
      WAITBAR_L();
      if (layer == 0) {
        FRESH();
        __builtin_amdgcn_s_barrier();
        { const char* wb = ws + OFF_WQ + (size_t)qnb * 8 * QW + j * 3072;
#pragma unroll 1
          for (int c = 0; c < 4; ++c) glds3(voff, wb + c * QW, lds0 + c * QW + j * 3072); }
        colsum_part(ws + OFF_CSP + (size_t)j * 81920 + (3584 + qnb * 96) * 4, voff, lb + L_CS + j * 1024);
        QINIT();
#define RX(B, u) xf[B] = ldfrag(sxp + (u) * 1024)
#define RW(B, i, u) wf[B][i] = ldfrag(swp + (i) * 4096 + (u) * 1024)
        WAITBAR_L();
        PSTAMP(8);
#pragma unroll 1
        for (int c = 0; c < 8; ++c) {
          LDSP(const char) swp = lb + (c & 3) * QW;
          LDSP(const char) sxp = lb + L_QX + (c & 3) * 16384 + j * 4096;
          QCHUNK();
          WAITBAR_L();
        }
        LDSP(const float) cx = (LDSP(const float))((LDSP(char))smem + L_CS);
        LDSP(const float) sx = (LDSP(const float))((LDSP(char))smem + L_ST) + j * 64;
        f32x4 cq[8], ra[4], rb[4];
        const float rstd = sx[r], mr = sx[32 + r], cv = cx[64 + r];
#pragma unroll
        for (int g = 0; g < 8; ++g) cq[g] = *(LDSP(const f32x4))(cx + 8 * g + 4 * hh);
#pragma unroll
        for (int g = 0; g < 4; ++g) { ra[g] = *(LDSP(const f32x4))(sx + 8 * g + 4 * hh); rb[g] = *(LDSP(const f32x4))(sx + 32 + 8 * g + 4 * hh); }
        SBAR();
        QFINISH();
        SBAR();
#undef RX
#undef RW
        PSTAMP(9);
        char* wn = ws + LSTRIDE;
        const int tt = qtb * 4 + j;
        float v[16];
        const float rq = rstd * QSCALE, mq = mr * QSCALE;
#pragma unroll
        for (int e = 0; e < 16; ++e) v[e] = acc[0][e] * rq - mq * cq[e >> 2][e & 3];
        qk_store(v, wn + OFF_Q + ((size_t)(2 * qnb) * NTT + tt) * 1024 + voff);
#pragma unroll
        for (int e = 0; e < 16; ++e) v[e] = acc[1][e] * rstd - mr * cq[4 + (e >> 2)][e & 3];
        qk_store(v, wn + OFF_K + ((size_t)(2 * qnb) * NTT + tt) * 1024 + voff);
#pragma unroll
        for (int e = 0; e < 16; ++e) v[e] = acc[2][e] * ra[e >> 2][e & 3] - rb[e >> 2][e & 3] * cv;
        v_store(v, wn + OFF_V + ((size_t)(2 * qnb + (r >> 4)) * NTT + tt) * 1024 + hh * 256 + (r & 15) * 16);
        arrive4(lcnt, cnt + (C_KV + qnb) * 128, lane);
        PSTAMP(10);
      }
    } else {
      {
        FRESH();
        bf16x8 w0[8], w1[8], x0[8], x1[8];
        const char* wa = ws + OFF_WO + ((size_t)ont * KSD + j * 8) * 1024;
        const char* wb = ws + OFF_WO + ((size_t)(ont ^ 1) * KSD + j * 8) * 1024;
#define LD4(D, o, base) do { gld16<0>(D[o], voff, base); gld16<1024>(D[(o) + 1], voff, base); gld16<2048>(D[(o) + 2], voff, base); gld16<3072>(D[(o) + 3], voff, base); } while (0)
        __builtin_amdgcn_s_barrier();
        LD4(w0, 0, wa); LD4(w0, 4, wa + 4096); LD4(w1, 0, wb); LD4(w1, 4, wb + 4096);
        u32x2 rn[4]; unsigned res[8];
        res_load(rn, layer ? voff : r * 1024 + hh * 8, (layer ? ws + OFF_X3 + ont * 2048 : karg(1) + ont * 64) + (size_t)ott * 32768, layer ? 8 : 16, layer ? 1024 : 32);
        const char* xa = wl + OFF_O + ((size_t)ott * KSD + j * 8) * 1024;
        const char* xb = wl + OFF_O + ((size_t)(ott ^ 1) * KSD + j * 8) * 1024;
        asm volatile("" : "+s"(xa), "+s"(xb));
        poll_b(cnt + (C_O + layer * 8 + xcd) * 128, 32u, lcnt + 1, layer * 4 + 1, j);
        PSTAMP(5);
        #define LD2(s) do { gld16<((s) & 3) * 1024>(x0[s], voff, xa + ((s) >> 2) * 4096); gld16<((s) & 3) * 1024>(x1[s], voff, xb + ((s) >> 2) * 4096); } while (0)
        LD2(0); LD2(1); LD2(2); LD2(3); LD2(4); LD2(5); LD2(6); LD2(7);
#undef LD2
        f32x16 acc[4];
#pragma unroll
        for (int i = 0; i < 4; ++i) acc[i] = f32x16{};
#define MM4(W0, W1, X0, X1) do { acc[1] = MFMA(W0, X1, acc[1]); acc[2] = MFMA(W1, X0, acc[2]); acc[3] = MFMA(W1, X1, acc[3]); acc[0] = MFMA(W0, X0, acc[0]); } while (0)
#define PIN8(n, X, Y, o) asm volatile("s_waitcnt vmcnt(%8)" : "+v"(X[o]), "+v"(X[(o) + 1]), "+v"(X[(o) + 2]), "+v"(X[(o) + 3]), "+v"(Y[o]), "+v"(Y[(o) + 1]), "+v"(Y[(o) + 2]), "+v"(Y[(o) + 3]) : "i"(n) : "memory")
        PIN8(16, w0, w1, 0); PIN8(16, w0, w1, 4);
#define WX(b, n) asm volatile("s_waitcnt vmcnt(%2)" : "+v"(x0[b]), "+v"(x1[b]) : "i"(n) : "memory")
#define ST(s) do { WX(s, 14 - 2 * (s)); MM4(w0[s], w1[s], x0[s], x1[s]); SBAR(); } while (0)
        ST(0); ST(1); ST(2); ST(3); ST(4); ST(5); ST(6); ST(7);
#undef ST
#undef WX
        PSTAMP(6);
        RES_FINISH((i - 1) * GCH + GW + (j ^ i) * 4096, (i - 1) * GCH + GW + j * 4096);
        PSTAMP(13);
        res_store_tiled(res, wl + OFF_X2 + ((size_t)ott * KSD + 2 * ont) * 1024 + voff, wl + OFF_ST2 + (size_t)ott * 4096 + ont * 256 + r * 8, lane);
        PSTAMP(14);
        arrive4(lcnt, cnt + (C_X2 + layer * 16 + grp) * 128, lane);
        PSTAMP(7);
      }
      {
        FRESH();
        { LDSP(char) cp = (LDSP(char))smem + L_CS + j * 256 + lane * 4;
          *(LDSP(float))cp = (*(LDSP(float))cp + *(LDSP(float))(cp + 1024)) + (*(LDSP(float))(cp + 2048) + *(LDSP(float))(cp + 3072)); }
        bf16x8 w0[12], w1[12], x0[8], x1[8];
        const char* wa = ws + OFF_WD + ((size_t)ont * KSF + j * 28) * 1024;
        const char* wb = ws + OFF_WD + ((size_t)(ont ^ 1) * KSF + j * 28) * 1024;
        {
          const int tt = gtb * 4 + j;
          const char* gx = wl + OFF_X2 + (size_t)tt * 32768;
          const char* gw = ws + OFF_WGU + (size_t)gnb * 8 * GW + j * 7168;
          f32x4 sp[4];
          asm volatile("" : "+s"(gx), "+s"(gw));
          poll_b(cnt + (C_X2 + layer * 16 + gtb) * 128, 16u, lcnt + 1, layer * 4 + 2, j);
          PSTAMP(8);
          __builtin_amdgcn_s_waitcnt(0xc07f);
#define GISSUE(c, d) do { glds4(voff, gx + (c) * 4096, (d) + GW + j * 4096); glds4(voff, gw + (c) * GW, (d) + j * 7168); glds3(voff, gw + (c) * GW + 4096, (d) + j * 7168 + 4096); } while (0)
          glds4(voff, gx, lds0 + GW + j * 4096);
          glds4(voff, gx + 4096, lds0 + GCH + GW + j * 4096);
          stat_issue(sp, wl + OFF_ST2 + (size_t)tt * 4096, voff);
          WAITBAR_V(8);
          GISSUE(2, lds0 + 2 * GCH);
          LD4(w0, 0, wa); LD4(w1, 0, wb);
          WAITBAR_V(23);
          unsigned d = lds0;
#pragma unroll 1
          for (int c = 3; c < 8; ++c) {
            GISSUE(c, d);
            d = d == lds0 + 2 * GCH ? lds0 : d + GCH;
            WAITBAR_V(11);
          }
#undef GISSUE
          LD4(w0, 4, wa + 4096); LD4(w1, 4, wb + 4096); LD4(w0, 8, wa + 8192); LD4(w1, 8, wb + 8192);
          { float mean, var; stat_finish(sp, mean, var);
            ((LDSP(float))((LDSP(char))smem + L_ST))[j * 64 + hh * 32 + (lane & 15) * 2 + ((lane >> 4) & 1)] = hh ? mean : 1.f / var; }
          WAITBAR_V(16);
        }
#define DISSUE(c, P) do { glds4(voff, wa + 12288 + (c) * 4096, lds0 + (P) + j * 8192); glds4(voff, wb + 12288 + (c) * 4096, lds0 + (P) + j * 8192 + 4096); } while (0)
        DISSUE(0, L_D0); DISSUE(1, L_D1); DISSUE(2, L_D2);
#pragma unroll 1
        for (unsigned spins = 0; spins < 4000000u; ++spins) {
          if (__builtin_amdgcn_readfirstlane(__hip_atomic_load(lcnt + 2, __ATOMIC_RELAXED, __HIP_MEMORY_SCOPE_WORKGROUP)) >= 4u * (layer + 1)) break;
          __builtin_amdgcn_s_sleep(1);
        }
        asm volatile("" ::: "memory");
        DISSUE(3, L_D3);
#undef DISSUE
        const char* xa = wl + OFF_H + ((size_t)ott * KSF + j * 28) * 1024;
        const char* xb = wl + OFF_H + ((size_t)(ott ^ 1) * KSF + j * 28) * 1024;
        asm volatile("" : "+s"(xa), "+s"(xb));
        poll_b(cnt + (C_H + layer * 16 + grp) * 128, 16u, lcnt + 1, layer * 4 + 3, j);
        PSTAMP(9);
        PIN8(0, w0, w1, 0); PIN8(0, w0, w1, 4); PIN8(0, w0, w1, 8);
#define LDX(b, blk) do { gld16<((b) & 3) * 1024>(x0[b], voff, xa + (blk) * 4096); gld16<((b) & 3) * 1024>(x1[b], voff, xb + (blk) * 4096); } while (0)
#define WX(b, n) asm volatile("s_waitcnt vmcnt(%2)" : "+v"(x0[b]), "+v"(x1[b]) : "i"(n) : "memory")
        LDX(0, 0); LDX(1, 0); LDX(2, 0); LDX(3, 0); LDX(4, 1); LDX(5, 1); LDX(6, 1); LDX(7, 1);
        f32x16 acc[4];
#pragma unroll
        for (int i = 0; i < 4; ++i) acc[i] = f32x16{};
        LDSP(const char) lw = lb + j * 8192;
#define BLK(o, h, nb, n0) do { _Pragma("unroll") for (int s = 0; s < 1; ++s) {} \
          WX((h) + 0, n0); MM4(w0[(o) + 0], w1[(o) + 0], x0[(h) + 0], x1[(h) + 0]); SBAR(); if ((nb) >= 0) LDX((h) + 0, nb); SBAR(); \
          WX((h) + 1, (nb) >= 0 ? n0 : n0 - 2); MM4(w0[(o) + 1], w1[(o) + 1], x0[(h) + 1], x1[(h) + 1]); SBAR(); if ((nb) >= 0) LDX((h) + 1, nb); SBAR(); \
          WX((h) + 2, (nb) >= 0 ? n0 : n0 - 4); MM4(w0[(o) + 2], w1[(o) + 2], x0[(h) + 2], x1[(h) + 2]); SBAR(); if ((nb) >= 0) LDX((h) + 2, nb); SBAR(); \
          WX((h) + 3, (nb) >= 0 ? n0 : n0 - 6); MM4(w0[(o) + 3], w1[(o) + 3], x0[(h) + 3], x1[(h) + 3]); SBAR(); if ((nb) >= 0) LDX((h) + 3, nb); SBAR(); } while (0)
#define RDL(o, c) do { _Pragma("unroll") for (int s = 0; s < 4; ++s) { w0[(o) + s] = ldfrag(lw + (c) + s * 1024); w1[(o) + s] = ldfrag(lw + (c) + 4096 + s * 1024); } SBAR(); } while (0)
        BLK(0, 0, 2, 14); RDL(0, L_D0);
        BLK(4, 4, 3, 14); RDL(4, L_D1);
        BLK(8, 0, 4, 14); RDL(8, L_D2);
        BLK(0, 4, 5, 14); RDL(0, L_D3);
        BLK(4, 0, 6, 14);
        u32x2 rn[4]; unsigned res[8];
        res_load(rn, voff, wl + OFF_X2 + (size_t)ott * 32768 + ont * 2048, 8, 1024);
        BLK(8, 4, -1, 18);
        BLK(0, 0, -1, 10);
#undef RDL
#undef BLK
#undef LDX
#undef WX
        PSTAMP(10);
        RES_FINISH((i == 3 ? L_D1 : L_D0 + (i - 1) * 4096) + j * 8192, (i == 3 ? L_D1 : L_D0 + (i - 1) * 4096) + (j ^ i) * 8192);
        if (layer == 0) {
          res_store_tiled(res, ws + OFF_X3 + ((size_t)ott * KSD + 2 * ont) * 1024 + voff, ws + OFF_ST3 + (size_t)ott * 4096 + ont * 256 + r * 8, lane);
          arrive4(lcnt, cnt + (C_X3 + grp) * 128, lane);
        } else {
          auto s0 = __builtin_amdgcn_permlane32_swap(res[0], res[2], false, false);
          auto s1 = __builtin_amdgcn_permlane32_swap(res[1], res[3], false, false);
          auto s2 = __builtin_amdgcn_permlane32_swap(res[4], res[6], false, false);
          auto s3 = __builtin_amdgcn_permlane32_swap(res[5], res[7], false, false);
          const u32x4 a = {s0[0], s1[0], s0[1], s1[1]}, b = {s2[0], s3[0], s2[1], s3[1]};
          char* o = (char*)karg(7) + ((size_t)(ott * 32 + r) * D_ + ont * 32) * 2 + hh * 16;
          st16(o, a);
          st16(o + 32, b);
        }
        PSTAMP(11);
      }
#undef LD4
#undef MM4
#undef PIN8
      if (layer == 0) {
        FRESH();
        const int tt = qtb * 4 + j;
        const char* xb = ws + OFF_X3 + (size_t)tt * 32768;
        const char* wb = ws + OFF_WQ + (size_t)qnb * 8 * QW + j * 3072;
        f32x4 sp[4];
        asm volatile("" : "+s"(xb), "+s"(wb));
        __builtin_amdgcn_s_barrier();
        poll_b(cnt + (C_X3 + qtb) * 128, 16u, lcnt + 1, 4, j);
        PSTAMP(12);
#define QISSUE(c) do { glds4(voff, xb + (c) * 4096, lds0 + L_QX + ((c) & 3) * 16384 + j * 4096); glds3(voff, wb + (c) * QW, lds0 + ((c) & 3) * QW + j * 3072); } while (0)
        glds4(voff, xb, lds0 + L_QX + j * 4096);
        glds4(voff, xb + 4096, lds0 + L_QX + 16384 + j * 4096);
        stat_issue(sp, ws + OFF_ST3 + (size_t)tt * 4096, voff);
        glds4(voff, xb + 8192, lds0 + L_QX + 32768 + j * 4096);
        glds4(voff, xb + 12288, lds0 + L_QX + 49152 + j * 4096);
        WAITBAR_V(16);
        { LDSP(char) cp = (LDSP(char))smem + L_CS + j * 256 + lane * 4;
          *(LDSP(float))cp = (*(LDSP(float))cp + *(LDSP(float))(cp + 1024)) + (*(LDSP(float))(cp + 2048) + *(LDSP(float))(cp + 3072)); }
        WAITBAR_V(12);
        QISSUE(4); WAITBAR_V(11);
#pragma unroll 1
        for (int c = 5; c < 8; ++c) { QISSUE(c); WAITBAR_V(14); }
#undef QISSUE
        { float mean, var; stat_finish(sp, mean, var); const float rs = rsqrtf(var);
          ((LDSP(float))((LDSP(char))smem + L_ST))[j * 64 + hh * 32 + (lane & 15) * 2 + ((lane >> 4) & 1)] = hh ? mean * rs : rs; }
        WAITBAR_V(7);
        WAITBAR_V(0);
        __builtin_amdgcn_s_barrier();
      }
    }
  }
}

extern "C" void kernel_launch(void* const* d_in, const int* in_sizes, int n_in, void* d_out, int out_size, void* d_ws, size_t ws_size,
                              hipStream_t stream) {
  const char* x = (const char*)d_in[0];
  const char* wqkv = (const char*)d_in[1];
  const char* wo = (const char*)d_in[2];
  const char* wg = (const char*)d_in[4];
  const char* wu = (const char*)d_in[5];
  const char* wd = (const char*)d_in[6];
  char* ws = (char*)d_ws;
  k_qkv1<<<256, 768, 0, stream>>>(x, wqkv, ws DIAG_PASS(0));
  k_main<<<256, 512, 0, stream>>>(ws, x, wqkv, wo, wg, wu, wd, (char*)d_out DIAG_PASS(1));
}
```

```cpp
#include <hip/hip_runtime.h>
#include <hip/hip_bf16.h>
#include <stdint.h>

typedef short bf16x8 __attribute__((ext_vector_type(8)));
typedef float f32x16 __attribute__((ext_vector_type(16)));
typedef float f32x4 __attribute__((ext_vector_type(4)));
typedef float f32x2 __attribute__((ext_vector_type(2)));
typedef unsigned u32x4 __attribute__((ext_vector_type(4)));
typedef unsigned u32x2 __attribute__((ext_vector_type(2)));
typedef __bf16 bf16v2 __attribute__((ext_vector_type(2)));
typedef unsigned short u16;

#define STAMP(i)
#define STAMP_DECL
#define PSTAMP(i)
#define STAMP_STORE()
#define DIAG_ARG
#define DIAG_PASS(k)
#define DI __device__ __forceinline__
#define MFMA(a, b, c) __builtin_amdgcn_mfma_f32_32x32x16_bf16((a), (b), (c), 0, 0, 0)
#define LDSP(T) __attribute__((address_space(3))) T*

constexpr int S_ = 2048, D_ = 512, H_ = 32, F_ = 1792;
constexpr int NTT = S_ / 32;
constexpr int KSD = D_ / 16;
constexpr int KSF = F_ / 16;
constexpr float QSCALE = 0.25f * 1.4426950408889634f;
constexpr float LN_EPS = 1e-5f;

constexpr size_t KB = 1024;
constexpr size_t OFF_CNT = 0;
constexpr size_t OFF_WO = 128 * KB;
constexpr size_t OFF_WGU = OFF_WO + 512 * KB;
constexpr size_t OFF_WD = OFF_WGU + 3584 * KB;
constexpr size_t OFF_WQ = OFF_WD + 1792 * KB;
constexpr size_t OFF_CSP = OFF_WQ + 1536 * KB;
constexpr size_t OFF_ST3 = OFF_CSP + 320 * KB;
constexpr size_t OFF_X3 = OFF_ST3 + 256 * KB;
constexpr size_t OFF_L = OFF_X3 + 2048 * KB;
constexpr size_t OFF_Q = OFF_L;
constexpr size_t OFF_K = OFF_Q + 2048 * KB;
constexpr size_t OFF_V = OFF_K + 2048 * KB;
constexpr size_t OFF_O = OFF_V + 2048 * KB;
constexpr size_t OFF_X2 = OFF_O + 2048 * KB;
constexpr size_t OFF_ST2 = OFF_X2 + 2048 * KB;
constexpr size_t OFF_H = OFF_ST2 + 256 * KB;
constexpr size_t LSTRIDE = OFF_H + 7168 * KB - OFF_L;
constexpr int KMAIN_KB = 20;
constexpr int C_O = 0, C_X2 = 16, C_H = 48, C_X3 = 80, C_KV = 96, C_PREP = 112, C_N = 128;

DI unsigned pk2(float lo, float hi) {
  f32x2 v = {lo, hi};
  bf16v2 r = __builtin_convertvector(v, bf16v2);
  return __builtin_bit_cast(unsigned, r);
}
DI float lo16(unsigned u) { return __uint_as_float(u << 16); }
DI float hi16(unsigned u) { return __uint_as_float(u & 0xffff0000u); }
DI float xhalf_add(float v) {
  auto r = __builtin_amdgcn_permlane32_swap(__float_as_uint(v), __float_as_uint(v), false, false);
  return __uint_as_float(r[0]) + __uint_as_float(r[1]);
}
DI float xhalf_max(float v) {
  auto r = __builtin_amdgcn_permlane32_swap(__float_as_uint(v), __float_as_uint(v), false, false);
  return fmaxf(__uint_as_float(r[0]), __uint_as_float(r[1]));
}
DI float x16_add(float v) {
  auto r = __builtin_amdgcn_permlane16_swap(__float_as_uint(v), __float_as_uint(v), false, false);
  return __uint_as_float(r[0]) + __uint_as_float(r[1]);
}
DI void st16(void* p, u32x4 v) { asm volatile("global_store_dwordx4 %0, %1, off sc1\n\ts_nop 1" :: "v"(p), "v"(v) : "memory"); }
template <int IMM>
DI void st16s(unsigned voff, const char* sbase, u32x4 v) {
  asm volatile("global_store_dwordx4 %0, %1, %2 offset:%3 sc1\n\ts_nop 1" :: "v"(voff), "v"(v), "s"(sbase), "i"(IMM) : "memory");
}
template <int IMM>
DI void gld16(bf16x8& d, unsigned voff, const char* sbase) {
  asm volatile("global_load_dwordx4 %0, %1, %2 offset:%3 sc1" : "=v"(d) : "v"(voff), "s"(sbase), "i"(IMM) : "memory");
}
#define GLDS_HEAD "s_mov_b32 %0, m0\n\ts_mov_b32 m0, %3\n\ts_nop 0\n\t"
#define GLDS_OP(o) "global_load_lds_dwordx4 %1, %2 offset:" #o " sc1\n\t"
#define GLDS_TAIL "s_mov_b32 m0, %0" : "=&s"(keep) : "v"(voff), "s"(sbase), "s"(lds) : "memory"
DI void glds1(unsigned voff, const char* sbase, unsigned lds) { unsigned keep; asm volatile(GLDS_HEAD GLDS_OP(0) GLDS_TAIL); }
DI void glds2(unsigned voff, const char* sbase, unsigned lds) { unsigned keep; asm volatile(GLDS_HEAD GLDS_OP(0) GLDS_OP(1024) GLDS_TAIL); }
DI void glds3(unsigned voff, const char* sbase, unsigned lds) { unsigned keep; asm volatile(GLDS_HEAD GLDS_OP(0) GLDS_OP(1024) GLDS_OP(2048) GLDS_TAIL); }
DI void glds4(unsigned voff, const char* sbase, unsigned lds) { unsigned keep; asm volatile(GLDS_HEAD GLDS_OP(0) GLDS_OP(1024) GLDS_OP(2048) GLDS_OP(3072) GLDS_TAIL); }
#define SBAR() __builtin_amdgcn_sched_barrier(0)
#define PIN(x) asm volatile("" : "+v"(x))
#define WAITV(n) asm volatile("s_waitcnt vmcnt(%0)" :: "i"(n) : "memory")
#define WAITBAR_V(n) asm volatile("s_waitcnt vmcnt(%0) lgkmcnt(0)\n\ts_barrier" :: "i"(n) : "memory")
#define WAITBAR_L() do { asm volatile("" ::: "memory"); __builtin_amdgcn_s_waitcnt(0xc07f); __builtin_amdgcn_s_barrier(); asm volatile("" ::: "memory"); } while (0)
typedef short s16x4 __attribute__((ext_vector_type(4)));
DI s16x4 trread(LDSP(const char) p) { return __builtin_amdgcn_ds_read_tr16_b64_v4i16((LDSP(s16x4))p); }
DI bf16x8 trfrag(LDSP(const char) p, int off2) { const s16x4 lo = trread(p), hi = trread(p + off2); return __builtin_shufflevector(lo, hi, 0, 1, 2, 3, 4, 5, 6, 7); }
DI bf16x8 ldfrag(LDSP(const char) p) { return *(LDSP(const bf16x8))p; }
DI float dot2(unsigned a, unsigned b, float c) { return __builtin_amdgcn_fdot2_f32_bf16(__builtin_bit_cast(bf16v2, a), __builtin_bit_cast(bf16v2, b), c, false); }
constexpr unsigned ONE2 = 0x3f803f80u;
#define BC8(x) __builtin_bit_cast(bf16x8, x)

template <int LOOK, int PER> DI void ring_wait(int i) {
  if (i <= 8 - LOOK) WAITBAR_V((LOOK - 1) * PER);
  else if (LOOK > 3 && i == 5) WAITBAR_V(2 * PER);
  else if (LOOK > 2 && i == 6) WAITBAR_V(PER);
  else WAITBAR_V(0);
}

DI void qk_store(const float (&v)[16], char* o) {
  u32x4 a = {pk2(v[0], v[1]), pk2(v[2], v[3]), pk2(v[4], v[5]), pk2(v[6], v[7])};
  u32x4 b = {pk2(v[8], v[9]), pk2(v[10], v[11]), pk2(v[12], v[13]), pk2(v[14], v[15])};
  st16(o, a);
  st16(o + NTT * 1024, b);
}
DI void v_store(const float (&v)[16], char* o) {
  u32x4 a = {pk2(v[0], v[1]), pk2(v[2], v[3]), pk2(v[4], v[5]), pk2(v[6], v[7])};
  u32x4 b = {pk2(v[8], v[9]), pk2(v[10], v[11]), pk2(v[12], v[13]), pk2(v[14], v[15])};
  st16(o, a);
  st16(o + 512, b);
}
DI float row16_sum(float v) {
  v += __builtin_bit_cast(float, __builtin_amdgcn_update_dpp(0, __builtin_bit_cast(int, v), 0x128, 0xf, 0xf, false));
  v += __builtin_bit_cast(float, __builtin_amdgcn_update_dpp(0, __builtin_bit_cast(int, v), 0x124, 0xf, 0xf, false));
  v += __builtin_bit_cast(float, __builtin_amdgcn_update_dpp(0, __builtin_bit_cast(int, v), 0x122, 0xf, 0xf, false));
  v += __builtin_bit_cast(float, __builtin_amdgcn_update_dpp(0, __builtin_bit_cast(int, v), 0x121, 0xf, 0xf, false));
  return v;
}
#define KSTEP(B, u) do { SBAR(); RX((B) ^ 1, u); RW((B) ^ 1, 0, u); SBAR(); acc[0] = MFMA(wf[B][0], xf[B], acc[0]); SBAR(); RW((B) ^ 1, 1, u); RW((B) ^ 1, 2, u); SBAR(); \
                         acc[1] = MFMA(wf[B][1], xf[B], acc[1]); acc[2] = MFMA(xf[B], wf[B][2], acc[2]); SBAR(); } while (0)
#define QCHUNK() do { KSTEP(1, 0); KSTEP(0, 1); KSTEP(1, 2); KSTEP(0, 3); } while (0)
#define QINIT() \
  f32x16 acc[3]; bf16x8 wf[2][3], xf[2]; \
  _Pragma("unroll") for (int i = 0; i < 3; ++i) { acc[i] = f32x16{}; wf[1][i] = bf16x8{}; } \
  xf[1] = bf16x8{}
#define QFINISH() do { acc[0] = MFMA(wf[1][0], xf[1], acc[0]); acc[1] = MFMA(wf[1][1], xf[1], acc[1]); acc[2] = MFMA(xf[1], wf[1][2], acc[2]); } while (0)

extern "C" __global__ __launch_bounds__(768, 3) void k_qkv1(const char* __restrict__ x, const char* __restrict__ wn, char* __restrict__ ws DIAG_ARG) {
  STAMP_DECL; STAMP(0);
  constexpr int LOOK = 3, CH = 24576, LXN = 4 * CH;
  __shared__ __attribute__((aligned(16))) char smem[LXN + 65536];
  const int tid = threadIdx.x, lane = tid & 63, r = lane & 31, hh = lane >> 5;
  const int w = __builtin_amdgcn_readfirstlane(tid >> 6);
  const int xcd = blockIdx.x & 7, idx = blockIdx.x >> 3;
  const int tb = xcd * 4 + (idx & 3), nq = idx >> 2;
  const unsigned lds0 = (unsigned)(uintptr_t)smem;
  const int rot = 2 * (idx & 3);
  if (w >= 8) {
    const int j = w - 8;
    const unsigned wv = (lane >> 3) * 3072 + (((lane & 7) ^ (((lane >> 4) & 1) << 2)) * 16);
    const char* wb = wn + (size_t)j * 16 * 3072 + nq * 128;
    if (blockIdx.x < 4) *(unsigned*)(ws + OFF_CNT + ((blockIdx.x * 4 + j) * 64 + lane) * 128) = 0u;
    asm volatile("s_sleep 8\n\ts_nop 4" ::: "memory");
#pragma unroll 1
    for (int c = 0; c < 8 + LOOK; ++c) {
      if (c >= LOOK) ring_wait<LOOK, 6>(c - LOOK);
      if (c < 8) {
        const char* ws_ = wb + (size_t)((c + rot) & 7) * 64 * 3072;
        const unsigned d = lds0 + (c & 3) * CH + j * 2048;
        glds1(wv, ws_, d); glds1(wv, ws_ + 1024, d + 8192); glds1(wv, ws_ + 2048, d + 16384);
        glds1(wv, ws_ + 8 * 3072, d + 1024); glds1(wv, ws_ + 8 * 3072 + 1024, d + 8192 + 1024); glds1(wv, ws_ + 8 * 3072 + 2048, d + 16384 + 1024);
      }
    }
    return;
  }
  {
    const int n = w;
    const u32x4* xr = (const u32x4*)(x + (size_t)(tb * 64 + n * 8) * 1024) + lane;
    u32x4 v[8];
#pragma unroll
    for (int i = 0; i < 8; ++i) v[i] = xr[i * 64];
    float s[8], q[8];
#pragma unroll
    for (int i = 0; i < 8; ++i) {
      s[i] = 0.f; q[i] = 0.f;
#pragma unroll
      for (int e = 0; e < 4; ++e) { s[i] = dot2(v[i][e], ONE2, s[i]); q[i] = dot2(v[i][e], v[i][e], q[i]); }
    }
    float s4[4], q4[4], s2[2], q2[2];
#pragma unroll
    for (int i = 0; i < 4; ++i) {
      const int lo = (i & 1) + 4 * (i >> 1);
      auto a = __builtin_amdgcn_permlane32_swap(__float_as_uint(s[lo]), __float_as_uint(s[lo + 2]), false, false);
      auto b = __builtin_amdgcn_permlane32_swap(__float_as_uint(q[lo]), __float_as_uint(q[lo + 2]), false, false);
      s4[i] = __uint_as_float(a[0]) + __uint_as_float(a[1]); q4[i] = __uint_as_float(b[0]) + __uint_as_float(b[1]);
    }
#pragma unroll
    for (int i = 0; i < 2; ++i) {
      auto a = __builtin_amdgcn_permlane16_swap(__float_as_uint(s4[2 * i]), __float_as_uint(s4[2 * i + 1]), false, false);
      auto b = __builtin_amdgcn_permlane16_swap(__float_as_uint(q4[2 * i]), __float_as_uint(q4[2 * i + 1]), false, false);
      s2[i] = row16_sum(__uint_as_float(a[0]) + __uint_as_float(a[1])); q2[i] = row16_sum(__uint_as_float(b[0]) + __uint_as_float(b[1]));
    }
    float ra[2], rb[2];
#pragma unroll
    for (int i = 0; i < 2; ++i) {
      const float mean = s2[i] * (1.f / D_);
      ra[i] = rsqrtf(fmaxf(q2[i] * (1.f / D_) - mean * mean, 0.f) + LN_EPS);
      rb[i] = -mean * ra[i];
    }
    LDSP(char) xd = (LDSP(char))smem + LXN + (lane >> 3) * 8192 + n * 1024;
#pragma unroll
    for (int i = 0; i < 8; ++i) {
      const float a = __builtin_bit_cast(float, __builtin_amdgcn_readlane(__builtin_bit_cast(int, ra[i >> 2]), (i & 3) * 16));
      const float b = __builtin_bit_cast(float, __builtin_amdgcn_readlane(__builtin_bit_cast(int, rb[i >> 2]), (i & 3) * 16));
      u32x4 o;
#pragma unroll
      for (int e = 0; e < 4; ++e) o[e] = pk2(lo16(v[i][e]) * a + b, hi16(v[i][e]) * a + b);
      *(LDSP(u32x4))(xd + i * 128 + (((lane & 7) ^ ((4 * n + (i >> 1)) & 7)) * 16)) = o;
    }
  }
  STAMP(1);
  WAITBAR_L();
  if (w >= 4) {
    if (w == 4 && idx < KMAIN_KB) {
      bf16x8 t;
      asm volatile("s_getpc_b64 s[20:21]\n\ts_add_u32 s20, s20, k_main@rel32@lo+4\n\ts_addc_u32 s21, s21, k_main@rel32@hi+12\n\t"
                   "global_load_dwordx4 %0, %1, s[20:21]\n\ts_waitcnt vmcnt(0)" : "=v"(t) : "v"(lane * 16 + idx * 1024) : "s20", "s21", "scc", "memory");
    }
    return;
  }
  STAMP(2);
  const int ta = w & 1, hp = w >> 1, q4 = (lane & 15) >> 2, xsw = (r >> 1) & 7;
  LDSP(const char) lw = (LDSP(const char))smem + (8 * hh + q4) * 128 + ((hp ^ (q4 >> 1)) * 64) + ((lane >> 4) & 1) * 32 + (lane & 3) * 8;
  LDSP(const char) lx = (LDSP(const char))smem + LXN + ta * 4096 + r * 128;
  int xo[4];
#pragma unroll
  for (int u = 0; u < 4; ++u) xo[u] = ((2 * u + hh) ^ xsw) * 16;
  QINIT();
#define RX(B, u) xf[B] = ldfrag(sx + xo[u])
#define RW(B, i, u) wf[B][i] = trfrag(sw + (i) * 8192 + (u) * 2048, 512)
#pragma unroll 1
  for (int c = 0; c < 8; ++c) {
    LDSP(const char) sw = lw + (c & 3) * CH;
    LDSP(const char) sx = lx + ((c + rot) & 7) * 8192;
    QCHUNK();
    WAITBAR_L();
  }
  QFINISH();
#undef RX
#undef RW
  STAMP(4);
  const int tt = tb * 2 + ta, nb = nq * 2 + hp;
  float v[16];
#pragma unroll
  for (int e = 0; e < 16; ++e) v[e] = acc[0][e] * QSCALE;
  qk_store(v, ws + OFF_Q + ((size_t)(2 * nb) * NTT + tt) * 1024 + lane * 16);
#pragma unroll
  for (int e = 0; e < 16; ++e) v[e] = acc[1][e];
  qk_store(v, ws + OFF_K + ((size_t)(2 * nb) * NTT + tt) * 1024 + lane * 16);
#pragma unroll
  for (int e = 0; e < 16; ++e) v[e] = acc[2][e];
  v_store(v, ws + OFF_V + ((size_t)(2 * nb + (r >> 4)) * NTT + tt) * 1024 + hh * 256 + (r & 15) * 16);
  STAMP(5); STAMP_STORE();
}

DI void poll(const char* cnt, unsigned voff, unsigned target) {
#pragma unroll 1
  for (unsigned spins = 0; spins < 200000u; ++spins) {
    unsigned v;
    asm volatile("global_load_dword %0, %1, %2 sc1\n\ts_waitcnt vmcnt(0)" : "=v"(v) : "v"(voff), "s"(cnt) : "memory");
    if (__all(v >= target)) break;
  }
}
DI void poll_b(const char* cnt, unsigned target, LDSP(unsigned) go, unsigned seq, int j) {
  if (j == 0) {
    poll(cnt, 0u, target);
    __hip_atomic_store(go, seq, __ATOMIC_RELAXED, __HIP_MEMORY_SCOPE_WORKGROUP);
  } else {
    WAITV(0);
#pragma unroll 1
    for (unsigned spins = 0; spins < 4000000u; ++spins) {
      if (__builtin_amdgcn_readfirstlane(__hip_atomic_load(go, __ATOMIC_RELAXED, __HIP_MEMORY_SCOPE_WORKGROUP)) >= seq) break;
    }
  }
  asm volatile("" ::: "memory");
}
DI void arrive4(LDSP(unsigned) lw, char* cnt, int lane) {
  WAITV(0);
  unsigned old = 0;
  if (lane == 0) old = __hip_atomic_fetch_add(lw, 1u, __ATOMIC_RELAXED, __HIP_MEMORY_SCOPE_WORKGROUP);
  old = __builtin_amdgcn_readfirstlane(old);
  if ((old & 3u) == 3u && lane < 8) asm volatile("global_atomic_add %0, %1, %2" :: "v"(lane * 128), "v"(1u), "s"(cnt) : "memory");
  asm volatile("" ::: "memory");
}
DI void stat_issue(f32x4 (&p)[4], const char* sb, unsigned voff) {
  asm volatile("global_load_dwordx4 %0, %4, %5 sc1\n\tglobal_load_dwordx4 %1, %4, %5 offset:1024 sc1\n\tglobal_load_dwordx4 %2, %4, %5 offset:2048 sc1\n\t"
               "global_load_dwordx4 %3, %4, %5 offset:3072 sc1" : "=&v"(p[0]), "=&v"(p[1]), "=&v"(p[2]), "=&v"(p[3]) : "v"(voff), "s"(sb) : "memory");
}
DI void stat_finish(f32x4 (&p)[4], float& mean, float& var) {
  asm volatile("" : "+v"(p[0]), "+v"(p[1]), "+v"(p[2]), "+v"(p[3]));
  const f32x4 t = (p[0] + p[1]) + (p[2] + p[3]);
  auto a = __builtin_amdgcn_permlane16_swap(__float_as_uint(t[0]), __float_as_uint(t[2]), false, false);
  auto b = __builtin_amdgcn_permlane16_swap(__float_as_uint(t[1]), __float_as_uint(t[3]), false, false);
  mean = xhalf_add(__uint_as_float(a[0]) + __uint_as_float(a[1])) * (1.f / D_);
  var = fmaxf(xhalf_add(__uint_as_float(b[0]) + __uint_as_float(b[1])) * (1.f / D_) - mean * mean, 0.f) + LN_EPS;
}
DI void colsum_part(const char* src, unsigned voff, LDSP(char) dst) {
  f32x4 p[4];
  asm volatile("global_load_dwordx4 %0, %4, %5 sc1\n\tglobal_load_dwordx4 %1, %4, %6 sc1\n\tglobal_load_dwordx4 %2, %4, %7 sc1\n\t"
               "global_load_dwordx4 %3, %4, %8 sc1\n\ts_waitcnt vmcnt(0)" : "=&v"(p[0]), "=&v"(p[1]), "=&v"(p[2]), "=&v"(p[3])
               : "v"(voff), "s"(src), "s"(src + 20480), "s"(src + 40960), "s"(src + 61440) : "memory");
  *(LDSP(f32x4))dst = (p[0] + p[1]) + (p[2] + p[3]);
}

#define RES_FINISH(WR, RD) do { \
    _Pragma("unroll") for (int i = 1; i < 4; ++i) _Pragma("unroll") for (int g = 0; g < 4; ++g) { \
      const f32x4 v_ = {acc[i][4 * g], acc[i][4 * g + 1], acc[i][4 * g + 2], acc[i][4 * g + 3]}; \
      *(LDSP(f32x4))(lb + (WR) + g * 1024) = v_; } \
    asm volatile("s_waitcnt vmcnt(0)" : "+v"(rn[0]), "+v"(rn[1]), "+v"(rn[2]), "+v"(rn[3]) :: "memory"); \
    WAITBAR_L(); \
    _Pragma("unroll") for (int g = 0; g < 4; ++g) { \
      f32x4 t_ = {acc[0][4 * g], acc[0][4 * g + 1], acc[0][4 * g + 2], acc[0][4 * g + 3]}; \
      f32x4 u_[3]; \
      _Pragma("unroll") for (int i = 1; i < 4; ++i) u_[i - 1] = *(LDSP(const f32x4))(lb + (RD) + g * 1024); \
      t_ = (u_[0] + u_[1]) + (u_[2] + t_); \
      res[2 * g] = pk2(t_[0] + lo16(rn[g][0]), t_[1] + hi16(rn[g][0])); \
      res[2 * g + 1] = pk2(t_[2] + lo16(rn[g][1]), t_[3] + hi16(rn[g][1])); } } while (0)
DI void res_load(u32x2 (&rn)[4], unsigned vo, const char* p0, int d1, int d2) {
  asm volatile("global_load_dwordx2 %0, %4, %5 sc1\n\tglobal_load_dwordx2 %1, %4, %6 sc1\n\tglobal_load_dwordx2 %2, %4, %7 sc1\n\tglobal_load_dwordx2 %3, %4, %8 sc1"
               : "=&v"(rn[0]), "=&v"(rn[1]), "=&v"(rn[2]), "=&v"(rn[3]) : "v"(vo), "s"(p0), "s"(p0 + d1), "s"(p0 + d2), "s"(p0 + d2 + d1) : "memory");
}
DI void res_store_tiled(const unsigned (&res)[8], char* o, char* st, int lane) {
  const u32x4 p0 = {res[0], res[1], res[2], res[3]}, p1 = {res[4], res[5], res[6], res[7]};
  st16(o, p0);
  st16(o + 1024, p1);
  float s1 = 0.f, s2 = 0.f, t1 = 0.f, t2 = 0.f;
#pragma unroll
  for (int e = 0; e < 4; ++e) { s1 = dot2(res[e], ONE2, s1); s2 = dot2(res[e], res[e], s2); t1 = dot2(res[4 + e], ONE2, t1); t2 = dot2(res[4 + e], res[4 + e], t2); }
  s1 = xhalf_add(s1 + t1); s2 = xhalf_add(s2 + t2);
  if (lane < 32) { const f32x2 v = {s1, s2}; asm volatile("global_store_dwordx2 %0, %1, off sc1" :: "v"(st), "v"(v) : "memory"); }
}

DI const char* karg(int i) {
  const __attribute__((address_space(4))) unsigned long long* ka = (const __attribute__((address_space(4))) unsigned long long*)__builtin_amdgcn_kernarg_segment_ptr();
  asm volatile("" : "+s"(ka));
  return (const char*)ka[i];
}
extern "C" __global__ __launch_bounds__(512, 2) void k_main(char* __restrict__ ws_, const char* x_, const char* wq_, const char* wo_, const char* wg_,
                                                            const char* wu_, const char* wd_, char* out_ DIAG_ARG) {
  constexpr int L_K = 0, L_V = 65536, L_ONE = 131072, L_CMB = 133120;
  constexpr int GW = 28672, GCH = GW + 16384;
  constexpr int L_D0 = 2 * GCH, L_D1 = L_D0 + 32768, L_D2 = 0, L_D3 = GCH;
  constexpr int QW = 12288, L_QX = 49152;
  constexpr int L_CS = 157696, L_ST = L_CS + 4096, L_CNT = L_ST + 1024, L_DBG = L_CNT + 64;
  __shared__ __attribute__((aligned(16))) char smem[163840];
  const int w = __builtin_amdgcn_readfirstlane(threadIdx.x >> 6);
  const unsigned lds0 = (unsigned)(uintptr_t)smem;
#define FRESH() \
  int tid = threadIdx.x; PIN(tid); int bid = blockIdx.x, j = w & 3; char* ws = ws_; asm volatile("" : "+s"(bid), "+s"(j), "+s"(ws)); \
  const int lane = tid & 63, r = lane & 31, hh = lane >> 5, xcd = bid & 7, idx = bid >> 3, grp = xcd * 2 + (idx >> 4), m = idx & 15; \
  const unsigned voff = lane * 16; LDSP(char) lb = (LDSP(char))smem + lane * 16; char* cnt = ws + OFF_CNT; char* wl = ws + layer * LSTRIDE; \
  LDSP(unsigned) lcnt = (LDSP(unsigned))((LDSP(char))smem + L_CNT) + (w >> 2); \
  const int ont = (m >> 1) * 2 + (j >> 1), ott = grp * 4 + (m & 1) * 2 + (j & 1); \
  const int gtb = (xcd & 1) * 8 + (idx & 7), gnb = (xcd >> 1) * 4 + (idx >> 3), qtb = (xcd & 3) * 4 + (idx & 3), qnb = (xcd >> 2) * 8 + (idx >> 2); \
  (void)r; (void)hh; (void)voff; (void)lb; (void)cnt; (void)wl; (void)lcnt; (void)ont; (void)ott; (void)gtb; (void)gnb; (void)qtb; (void)qnb
  if (threadIdx.x < 8) ((LDSP(unsigned))((LDSP(char))smem + L_CNT))[threadIdx.x] = 0u;
#pragma unroll 1
  for (int layer = 0; layer < 2; ++layer) {
    {
      FRESH();
      const int head = xcd * 4 + (idx & 3), qb = idx >> 2, half = w >> 2, tt0 = qb * 8 + 2 * j;
      PSTAMP(0);
      if (layer) { if (w == 4) poll(cnt + (C_KV + (head >> 1)) * 1024 + qb * 128, 0u, 16u); WAITBAR_L(); }
      PSTAMP(1);
      const char* ks = wl + OFF_K + (size_t)head * 65536 + half * 32768;
      const unsigned kd = lds0 + L_K + half * 32768;
      bf16x8 q0, q1;
      gld16<0>(q0, voff, wl + OFF_Q + ((size_t)head * NTT + tt0) * 1024);
      gld16<1024>(q1, voff, wl + OFF_Q + ((size_t)head * NTT + tt0) * 1024);
      glds2(voff, ks + j * 2048, kd + j * 2048);
      glds2(voff, ks + (OFF_V - OFF_K) + j * 2048, kd + L_V + j * 2048);
      *(LDSP(unsigned))((LDSP(char))smem + L_ONE + tid * 4) = ((tid >> 2) & 11) == 0 ? ONE2 : 0u;
      asm volatile("s_waitcnt vmcnt(0) lgkmcnt(0)\n\ts_barrier" : "+v"(q0), "+v"(q1) :: "memory");
      PSTAMP(2);
      LDSP(const char) kp = lb + L_K;
      LDSP(const char) vp0 = (LDSP(const char))smem + (r < 16 ? L_V : L_ONE) + hh * 256 + (r & 15) * 16;
      const int vstep = r < 16 ? 2048 : 0;
      LDSP(const char) kq = kp + half * 32768; LDSP(const char) vq = vp0 + half * 16 * vstep;
      f32x16 n0, n1, sA, sB, o0 = f32x16{}, o1 = f32x16{};
      bf16x8 kA, kB, vp = bf16x8{};
      u32x4 p1 = {0u, 0u, 0u, 0u};
      {
        const bf16x8 k0 = ldfrag(kp);
        kA = ldfrag(kq);
        const f32x16 s = MFMA(k0, q0, f32x16{}), t = MFMA(k0, q1, f32x16{});
        float a = fmaxf(fmaxf(s[0], s[1]), s[2]), b = fmaxf(fmaxf(t[0], t[1]), t[2]);
#pragma unroll
        for (int i = 3; i < 15; i += 2) { a = fmaxf(fmaxf(a, s[i]), s[i + 1]); b = fmaxf(fmaxf(b, t[i]), t[i + 1]); }
        a = xhalf_max(fmaxf(a, s[15])); b = xhalf_max(fmaxf(b, t[15]));
#pragma unroll
        for (int i = 0; i < 16; ++i) { n0[i] = -a; n1[i] = -b; }
        PIN(n0); PIN(n1);
        sA = MFMA(kA, q0, n0);
      }
#define EX(S, i) S[i] = __builtin_amdgcn_exp2f(S[i])
#define STEPN(CUR, NXT, KN_, QN, NN, O, VA, OP, VP, READS) do { \
      EX(CUR, 0); EX(CUR, 1); PIN(CUR); SBAR(); OP = MFMA(VP, BC8(p1), OP); SBAR(); READS; SBAR(); \
      EX(CUR, 2); EX(CUR, 3); EX(CUR, 4); EX(CUR, 5); PIN(CUR); SBAR(); NXT = MFMA(KN_, QN, NN); SBAR(); \
      u32x4 p0; EX(CUR, 6); EX(CUR, 7); SBAR(); p0[0] = pk2(CUR[0], CUR[1]); p0[1] = pk2(CUR[2], CUR[3]); SBAR(); \
      EX(CUR, 8); EX(CUR, 9); SBAR(); p0[2] = pk2(CUR[4], CUR[5]); p0[3] = pk2(CUR[6], CUR[7]); SBAR(); \
      EX(CUR, 10); EX(CUR, 11); PIN(CUR); SBAR(); O = MFMA(VA, BC8(p0), O); SBAR(); \
      EX(CUR, 12); EX(CUR, 13); EX(CUR, 14); EX(CUR, 15); \
      p1[0] = pk2(CUR[8], CUR[9]); p1[1] = pk2(CUR[10], CUR[11]); p1[2] = pk2(CUR[12], CUR[13]); p1[3] = pk2(CUR[14], CUR[15]); PIN(p1); SBAR(); } while (0)
#define TILE(t, KA, KB) do { bf16x8 va, vb; \
      STEPN(sA, sB, KA, q1, n1, o0, va, o1, vp, (va = ldfrag(vq + (t) * 1024), vb = ldfrag(vq + (t) * 1024 + 512), KB = ldfrag(kq + ((t) + 1) * 1024))); \
      STEPN(sB, sA, KB, q0, n0, o1, va, o0, vb, (vp = vb)); } while (0)
      __builtin_amdgcn_s_setprio(3);
#pragma unroll 1
      for (int ph = -2; ph < 5; ++ph) {
        if (ph == -1) {
          glds3(voff, ks + 8192 + j * 6144, kd + 8192 + j * 6144);
          glds3(voff, ks + (OFF_V - OFF_K) + 8192 + j * 6144, kd + L_V + 8192 + j * 6144);
          glds3(voff, ks + 11264 + j * 6144, kd + 11264 + j * 6144);
          glds3(voff, ks + (OFF_V - OFF_K) + 11264 + j * 6144, kd + L_V + 11264 + j * 6144);
        }
        if (ph == 0) {
          WAITV(0);
          if (lane == 0) __hip_atomic_fetch_add(lcnt + 4, 1u, __ATOMIC_RELAXED, __HIP_MEMORY_SCOPE_WORKGROUP);
#pragma unroll 1
          for (unsigned spins = 0; spins < 4000000u; ++spins)
            if (__builtin_amdgcn_readfirstlane(__hip_atomic_load(lcnt + 4, __ATOMIC_RELAXED, __HIP_MEMORY_SCOPE_WORKGROUP)) >= 4u * (layer + 1)) break;
          asm volatile("" ::: "memory");
        }
        if (ph == 1) __builtin_amdgcn_s_setprio(2);
        if (ph == 3) __builtin_amdgcn_s_setprio(1);
        if (ph == 4) __builtin_amdgcn_s_setprio(0);
        if (layer == 0 && ph >= 0) {
          const int q4 = (lane & 15) >> 2, b16 = (lane >> 4) & 1;
          const int scr = L_CMB + (half ? j * 5120 : 20480 + j * 2048);
          const int jo = ((ph - 1) * 8 + w) * 256 + bid;
          if ((ph == 1 || ph == 2) && jo < 3712) {
            const bool gu = jo < 1792;
            LDSP(const char) sp = (LDSP(const char))smem + scr + (lane & 3) * 8 +
                                  (gu ? (16 * hh + 4 * b16 + q4) * 32 : (4 * hh + q4) * 64 + b16 * 32);
            const int ho = gu ? 256 : 512;
            const char* pd;
            if (gu) { const int ft = jo >> 4, S = jo & 15, nb = ft / 7, ii = ft - nb * 7; pd = ws + OFF_WGU + ((size_t)((nb * 8 + (S >> 1)) * 14 + 2 * ii) * 2 + (S & 1)) * 1024; }
            else if (jo < 2048) pd = ws + OFF_WO + (size_t)(jo - 1792) * 2048;
            else if (jo < 2944) pd = ws + OFF_WD + (size_t)(jo - 2048) * 2048;
            else { const int t = (jo - 2944) >> 4, nb = t / 3, ii = t - nb * 3, s0 = (jo & 15) * 2; pd = ws + OFF_WQ + (size_t)(((nb * 8 + (s0 >> 2)) * 3 + ii) * 4 + (s0 & 3)) * 1024; }
            WAITV(0);
            const u32x4 fa = __builtin_bit_cast(u32x4, trfrag(sp, ho)), fb = __builtin_bit_cast(u32x4, trfrag(sp + 1024, ho));
            st16s<0>(voff, pd, fa); st16s<0>(voff, pd + (gu ? 2048 : 1024), fb);
            if (gu || jo >= 2944) {
              float ca = (lo16(fa[0]) + hi16(fa[0])) + (lo16(fa[1]) + hi16(fa[1])) + (lo16(fa[2]) + hi16(fa[2])) + (lo16(fa[3]) + hi16(fa[3]));
              float cb = (lo16(fb[0]) + hi16(fb[0])) + (lo16(fb[1]) + hi16(fb[1])) + (lo16(fb[2]) + hi16(fb[2])) + (lo16(fb[3]) + hi16(fb[3]));
              if (gu) { ca = xhalf_add(x16_add(ca)); cb = xhalf_add(x16_add(cb)); ca = lane < 16 ? ca : cb; }
              else ca = xhalf_add(ca + cb);
              const char* cp = ws + OFF_CSP + ((jo & 15) * 5120 + ((gu ? jo : jo - 2944 + 1792) >> 4) * 32) * 4;
              if (lane < 32) asm volatile("global_store_dword %0, %1, %2 sc1" :: "v"(lane * 4), "v"(ca), "s"(cp) : "memory");
            }
          }
          const int jn = jo + 2048;
          if (ph < 2 && jn < 3712) {
            const char *sa, *sb; unsigned gv;
            if (jn < 1792) {
              const int o = 32 * (jn & 15) * (F_ * 2) + (jn >> 4) * 32;
              gv = (lane >> 1) * (F_ * 2) + (lane & 1) * 16; sa = karg(4) + o; sb = karg(5) + o;
            } else {
              int nt, s0, ld = 1024, ai;
              if (jn < 2048) { const int f = (jn - 1792) * 2; nt = f >> 5; s0 = f & 31; ai = 3; }
              else if (jn < 2944) { const int f = (jn - 2048) * 2; nt = f / KSF; s0 = f - nt * KSF; ai = 6; }
              else { const int t = (jn - 2944) >> 4, nb = t / 3; s0 = (jn & 15) * 2; nt = (t - nb * 3) * 16 + nb; ai = 2; ld = 3072; }
              gv = (lane >> 2) * ld + (lane & 3) * 16; sa = karg(ai) + (16 * s0 * ld + nt * 64); sb = sa + 16 * ld;
            }
            __builtin_amdgcn_s_waitcnt(0xc07f);
            glds1(gv, sa, lds0 + scr); glds1(gv, sb, lds0 + scr + 1024);
          }
          if (ph == 3) arrive4(lcnt, cnt + (C_PREP + xcd) * 1024, lane);
        }
        int n = ph <= 0 ? ph + 3 : ph == 2 ? 2 : ph == 4 ? 2 : 3;
#pragma unroll 1
        do { TILE(0, kA, kB); TILE(1, kB, kA); kq += 2048; vq += vstep; } while (--n > 0);
      }
      o1 = MFMA(vp, BC8(p1), o1);
#undef TILE
#undef STEPN
      PSTAMP(3);
      LDSP(char) xp = lb + L_CMB + j * 5120;
      if (half == 1) {
        const f32x4 a = {o0[0], o0[1], o0[2], o0[3]}, b = {o0[4], o0[5], o0[6], o0[7]}, c = {o1[0], o1[1], o1[2], o1[3]}, d = {o1[4], o1[5], o1[6], o1[7]};
        const f32x2 l = {o0[8], o1[8]};
        *(LDSP(f32x4))xp = a; *(LDSP(f32x4))(xp + 1024) = b; *(LDSP(f32x4))(xp + 2048) = c; *(LDSP(f32x4))(xp + 3072) = d; *(LDSP(f32x2))(xp + 4096) = l;
      }
      if (layer == 0 && w == 0) poll(cnt + C_PREP * 1024 + (idx & 7) * 128, (lane & 7) * 1024, 64u);
      WAITBAR_L();
      if (half == 0) {
        PSTAMP(11);
        const f32x4 a = *(LDSP(const f32x4))xp, b = *(LDSP(const f32x4))(xp + 1024), c = *(LDSP(const f32x4))(xp + 2048), d = *(LDSP(const f32x4))(xp + 3072);
        const f32x2 l = *(LDSP(const f32x2))(xp + 4096);
#pragma unroll
        for (int i = 0; i < 4; ++i) { o0[i] += a[i]; o0[4 + i] += b[i]; o1[i] += c[i]; o1[4 + i] += d[i]; }
        o0[8] += l[0]; o1[8] += l[1];
        float chk = (o0[8] + o1[8]) * 5.4210109e-20f;
#pragma unroll
        for (int i = 0; i < 8; ++i) chk = __builtin_fmaf(o0[i], 0.f, __builtin_fmaf(o1[i], 0.f, chk));
        const bool bad = !(chk < 1.f);
        if (__builtin_expect(__any(bad), 0)) {
#pragma unroll 1
          for (int z = 0; z < 2; ++z) {
            const bf16x8 q = z ? q1 : q0;
            float mx = -INFINITY;
            f32x16 o = f32x16{};
            kq = kp; vq = vp0;
#pragma unroll 1
            for (int t = 0; t < 64; ++t) {
              const bf16x8 k = ldfrag(kq), v0 = ldfrag(vq), v1 = ldfrag(vq + 512);
              f32x16 s = MFMA(k, q, f32x16{});
              float tm = s[0];
#pragma unroll
              for (int i = 1; i < 16; ++i) tm = fmaxf(tm, s[i]);
              tm = xhalf_max(tm);
              const float mn = fmaxf(mx, tm);
              const float alpha = __builtin_amdgcn_exp2f(mx - mn);
              mx = mn;
#pragma unroll
              for (int i = 0; i < 16; ++i) { o[i] *= alpha; s[i] = __builtin_amdgcn_exp2f(s[i] - mn); }
              const u32x4 pa = {pk2(s[0], s[1]), pk2(s[2], s[3]), pk2(s[4], s[5]), pk2(s[6], s[7])};
              const u32x4 pb = {pk2(s[8], s[9]), pk2(s[10], s[11]), pk2(s[12], s[13]), pk2(s[14], s[15])};
              o = MFMA(v0, BC8(pa), o);
              o = MFMA(v1, BC8(pb), o);
              kq += 1024; vq += vstep >> 1;
            }
            if (z) o1 = o; else o0 = o;
          }
        }
        const float i0 = __builtin_amdgcn_rcpf(o0[8]), i1 = __builtin_amdgcn_rcpf(o1[8]);
        const u32x4 a0 = {pk2(o0[0] * i0, o0[1] * i0), pk2(o0[2] * i0, o0[3] * i0), pk2(o0[4] * i0, o0[5] * i0), pk2(o0[6] * i0, o0[7] * i0)};
        const u32x4 a1 = {pk2(o1[0] * i1, o1[1] * i1), pk2(o1[2] * i1, o1[3] * i1), pk2(o1[4] * i1, o1[5] * i1), pk2(o1[6] * i1, o1[7] * i1)};
        char* op = wl + OFF_O + ((size_t)tt0 * KSD + head) * 1024 + voff;
        st16(op, a0); st16(op + 32768, a1);
        PSTAMP(12);
        WAITBAR_L();
        arrive4(lcnt, cnt + (C_O + layer * 8 + qb) * 1024, lane);
      }
    }
    PSTAMP(4);
    int role = w >> 2;
    asm volatile("" : "+s"(role));
    if (role == 0) {
      {
        FRESH();
        const char* wb = ws + OFF_WGU + (size_t)gnb * 8 * GW + j * 7168;
#pragma unroll 1
        for (int c = 0; c < 2; ++c) { glds4(voff, wb + c * GW, lds0 + c * GCH + j * 7168); glds3(voff, wb + c * GW + 4096, lds0 + c * GCH + j * 7168 + 4096); }
        colsum_part(ws + OFF_CSP + (size_t)j * 81920 + gnb * 896, voff, lb + L_CS + j * 1024);
      }
      PSTAMP(14);
      WAITBAR_L();
      PSTAMP(15);
      {
        FRESH();
        const int g4 = lane >> 4, c16 = lane & 15;
        f32x4 ac[2][14];
#pragma unroll
        for (int i = 0; i < 14; ++i) { ac[0][i] = f32x4{}; ac[1][i] = f32x4{}; }
        LDSP(const char) lx = (LDSP(const char))smem + GW + j * 4096 + (g4 >> 1) * 1024 + ((g4 & 1) * 32 + c16) * 16;
        bf16x8 wf[2][7], xf[2][2];
        xf[1][0] = bf16x8{}; xf[1][1] = bf16x8{};
#pragma unroll
        for (int i = 0; i < 7; ++i) wf[1][i] = bf16x8{};
#define RDX(u, so, a) xf[u][a] = ldfrag(lx + (so) + (u) * 2048 + (a) * 256)
#define RDW(hf, u, so, i) wf[hf][i] = ldfrag(lb + (so) + ((7 * (hf) + (i)) * 2 + (u)) * 1024)
#define M1(B, XP, i, a) do { SBAR(); ac[a][7 * (B) + (i)] = __builtin_amdgcn_mfma_f32_16x16x32_bf16(wf[B][i], xf[XP][a], ac[a][7 * (B) + (i)], 0, 0, 0); SBAR(); } while (0)
#define MM(B, XP, i) do { M1(B, XP, i, 0); M1(B, XP, i, 1); } while (0)
#define GSTEP(B, XP, u, so) do { \
    M1(B, XP, 0, 0); RDW((B) ^ 1, u, so, 0); M1(B, XP, 0, 1); if ((B) == 1) RDX(u, so, 0); M1(B, XP, 1, 0); if ((B) == 1) RDX(u, so, 1); M1(B, XP, 1, 1); RDW((B) ^ 1, u, so, 1); \
    M1(B, XP, 2, 0); RDW((B) ^ 1, u, so, 2); M1(B, XP, 2, 1); RDW((B) ^ 1, u, so, 3); M1(B, XP, 3, 0); RDW((B) ^ 1, u, so, 4); M1(B, XP, 3, 1); RDW((B) ^ 1, u, so, 5); \
    M1(B, XP, 4, 0); RDW((B) ^ 1, u, so, 6); M1(B, XP, 4, 1); MM(B, XP, 5); MM(B, XP, 6); } while (0)
        WAITBAR_L();
        PSTAMP(5);
        int so = 0;
#pragma unroll 1
        for (int c = 0; c < 8; ++c) {
          GSTEP(1, 1, 0, so); GSTEP(0, 0, 0, so); GSTEP(1, 0, 1, so); GSTEP(0, 1, 1, so);
          asm volatile("" ::: "memory"); __builtin_amdgcn_s_waitcnt(0xc07f);
          if (c != 7) __builtin_amdgcn_s_barrier();
          asm volatile("" ::: "memory");
          so = so == 2 * GCH ? 0 : so + GCH;
        }
        LDSP(const float) sx = (LDSP(const float))((LDSP(char))smem + L_ST) + j * 64 + c16;
        const float rs0 = sx[0], rs1 = sx[16], m0 = -sx[32], m1 = -sx[48];
        f32x4 c4[14];
#pragma unroll
        for (int i = 0; i < 14; ++i) c4[i] = *(LDSP(const f32x4))((LDSP(const char))smem + L_CS + i * 64 + g4 * 16);
        const char* ob = wl + OFF_H + ((size_t)(gtb * 4 + j) * KSF + gnb * 7) * 1024;
        unsigned oo = (((g4 & 1) * 32) + (g4 >> 1) * 16 + c16) * 16;
        asm volatile("" : "+s"(ob)); PIN(oo);
        MM(1, 1, 0); MM(1, 1, 1); MM(1, 1, 2); MM(1, 1, 3); MM(1, 1, 4); MM(1, 1, 5); MM(1, 1, 6);
#undef RDX
#undef RDW
#undef M1
#undef MM
#undef GSTEP
        PSTAMP(6);
        const f32x4 vm0 = {m0, m0, m0, m0}, vm1 = {m1, m1, m1, m1};
#define HOUT(i) do { \
          const f32x4 ga = __builtin_elementwise_fma(c4[2 * (i)], vm0, ac[0][2 * (i)]), ua = __builtin_elementwise_fma(c4[2 * (i) + 1], vm0, ac[0][2 * (i) + 1]) * rs0; \
          const f32x4 gb = __builtin_elementwise_fma(c4[2 * (i)], vm1, ac[1][2 * (i)]), ub = __builtin_elementwise_fma(c4[2 * (i) + 1], vm1, ac[1][2 * (i) + 1]) * rs1; \
          const f32x4 h0 = __builtin_elementwise_max(ga, f32x4{}) * ua, h1 = __builtin_elementwise_max(gb, f32x4{}) * ub; \
          auto e0 = __builtin_amdgcn_permlane32_swap(pk2(h0[0], h0[1]), pk2(h1[0], h1[1]), false, false); \
          auto e1 = __builtin_amdgcn_permlane32_swap(pk2(h0[2], h0[3]), pk2(h1[2], h1[3]), false, false); \
          const u32x4 a_ = {e0[0], e1[0], e0[1], e1[1]}; \
          st16s<((i) & 3) * 1024>(oo, ob + ((i) >> 2) * 4096, a_); } while (0)
        HOUT(0); HOUT(1); HOUT(2); HOUT(3); HOUT(4); HOUT(5); HOUT(6);
#undef HOUT
        PSTAMP(13);
        if (lane == 0) __hip_atomic_fetch_add(lcnt + 3, 1u, __ATOMIC_RELAXED, __HIP_MEMORY_SCOPE_WORKGROUP);
        arrive4(lcnt, cnt + (C_H + layer * 16 + gtb) * 1024, lane);
        PSTAMP(7);
      }
      WAITBAR_L();
      if (layer == 0) {
        FRESH();
        __builtin_amdgcn_s_barrier();
        { const char* wb = ws + OFF_WQ + (size_t)qnb * 8 * QW + j * 3072;
#pragma unroll 1
          for (int c = 0; c < 4; ++c) glds3(voff, wb + c * QW, lds0 + c * QW + j * 3072); }
        colsum_part(ws + OFF_CSP + (size_t)j * 81920 + (3584 + qnb * 96) * 4, voff, lb + L_CS + j * 1024);
        QINIT();
#define RX(B, u) xf[B] = ldfrag(sxp + (u) * 1024)
#define RW(B, i, u) wf[B][i] = ldfrag(swp + (i) * 4096 + (u) * 1024)
        WAITBAR_L();
        PSTAMP(8);
#pragma unroll 1
        for (int c = 0; c < 8; ++c) {
          LDSP(const char) swp = lb + (c & 3) * QW;
          LDSP(const char) sxp = lb + L_QX + (c & 3) * 16384 + j * 4096;
          QCHUNK();
          WAITBAR_L();
        }
        LDSP(const float) cx = (LDSP(const float))((LDSP(char))smem + L_CS);
        LDSP(const float) sx = (LDSP(const float))((LDSP(char))smem + L_ST) + j * 64;
        f32x4 cq[8], ra[4], rb[4];
        const float rstd = sx[r], mr = sx[32 + r], cv = cx[64 + r];
#pragma unroll
        for (int g = 0; g < 8; ++g) cq[g] = *(LDSP(const f32x4))(cx + 8 * g + 4 * hh);
#pragma unroll
        for (int g = 0; g < 4; ++g) { ra[g] = *(LDSP(const f32x4))(sx + 8 * g + 4 * hh); rb[g] = *(LDSP(const f32x4))(sx + 32 + 8 * g + 4 * hh); }
        SBAR();
        QFINISH();
        SBAR();
#undef RX
#undef RW
        PSTAMP(9);
        char* wn = ws + LSTRIDE;
        const int tt = qtb * 4 + j;
        float v[16];
        const float rq = rstd * QSCALE, mq = mr * QSCALE;
#pragma unroll
        for (int e = 0; e < 16; ++e) v[e] = acc[0][e] * rq - mq * cq[e >> 2][e & 3];
        qk_store(v, wn + OFF_Q + ((size_t)(2 * qnb) * NTT + tt) * 1024 + voff);
#pragma unroll
        for (int e = 0; e < 16; ++e) v[e] = acc[1][e] * rstd - mr * cq[4 + (e >> 2)][e & 3];
        qk_store(v, wn + OFF_K + ((size_t)(2 * qnb) * NTT + tt) * 1024 + voff);
#pragma unroll
        for (int e = 0; e < 16; ++e) v[e] = acc[2][e] * ra[e >> 2][e & 3] - rb[e >> 2][e & 3] * cv;
        v_store(v, wn + OFF_V + ((size_t)(2 * qnb + (r >> 4)) * NTT + tt) * 1024 + hh * 256 + (r & 15) * 16);
        arrive4(lcnt, cnt + (C_KV + qnb) * 1024, lane);
        PSTAMP(10);
      }
    } else {
      {
        FRESH();
        bf16x8 w0[8], w1[8], x0[8], x1[8];
        const char* wa = ws + OFF_WO + ((size_t)ont * KSD + j * 8) * 1024;
        const char* wb = ws + OFF_WO + ((size_t)(ont ^ 1) * KSD + j * 8) * 1024;
#define LD4(D, o, base) do { gld16<0>(D[o], voff, base); gld16<1024>(D[(o) + 1], voff, base); gld16<2048>(D[(o) + 2], voff, base); gld16<3072>(D[(o) + 3], voff, base); } while (0)
        __builtin_amdgcn_s_barrier();
        LD4(w0, 0, wa); LD4(w0, 4, wa + 4096); LD4(w1, 0, wb); LD4(w1, 4, wb + 4096);
        u32x2 rn[4]; unsigned res[8];
        res_load(rn, layer ? voff : r * 1024 + hh * 8, (layer ? ws + OFF_X3 + ont * 2048 : karg(1) + ont * 64) + (size_t)ott * 32768, layer ? 8 : 16, layer ? 1024 : 32);
        const char* xa = wl + OFF_O + ((size_t)ott * KSD + j * 8) * 1024;
        const char* xb = wl + OFF_O + ((size_t)(ott ^ 1) * KSD + j * 8) * 1024;
        asm volatile("" : "+s"(xa), "+s"(xb));
        poll_b(cnt + (C_O + layer * 8 + xcd) * 1024 + (idx & 7) * 128, 32u, lcnt + 1, layer * 4 + 1, j);
        PSTAMP(5);
        #define LD2(s) do { gld16<((s) & 3) * 1024>(x0[s], voff, xa + ((s) >> 2) * 4096); gld16<((s) & 3) * 1024>(x1[s], voff, xb + ((s) >> 2) * 4096); } while (0)
        LD2(0); LD2(1); LD2(2); LD2(3); LD2(4); LD2(5); LD2(6); LD2(7);
#undef LD2
        f32x16 acc[4];
#pragma unroll
        for (int i = 0; i < 4; ++i) acc[i] = f32x16{};
#define MM4(W0, W1, X0, X1) do { acc[1] = MFMA(W0, X1, acc[1]); acc[2] = MFMA(W1, X0, acc[2]); acc[3] = MFMA(W1, X1, acc[3]); acc[0] = MFMA(W0, X0, acc[0]); } while (0)
#define PIN8(n, X, Y, o) asm volatile("s_waitcnt vmcnt(%8)" : "+v"(X[o]), "+v"(X[(o) + 1]), "+v"(X[(o) + 2]), "+v"(X[(o) + 3]), "+v"(Y[o]), "+v"(Y[(o) + 1]), "+v"(Y[(o) + 2]), "+v"(Y[(o) + 3]) : "i"(n) : "memory")
        PIN8(16, w0, w1, 0); PIN8(16, w0, w1, 4);
#define WX(b, n) asm volatile("s_waitcnt vmcnt(%2)" : "+v"(x0[b]), "+v"(x1[b]) : "i"(n) : "memory")
#define ST(s) do { WX(s, 14 - 2 * (s)); MM4(w0[s], w1[s], x0[s], x1[s]); SBAR(); } while (0)
        ST(0); ST(1); ST(2); ST(3); ST(4); ST(5); ST(6); ST(7);
#undef ST
#undef WX
        PSTAMP(6);
        RES_FINISH((i - 1) * GCH + GW + (j ^ i) * 4096, (i - 1) * GCH + GW + j * 4096);
        PSTAMP(13);
        res_store_tiled(res, wl + OFF_X2 + ((size_t)ott * KSD + 2 * ont) * 1024 + voff, wl + OFF_ST2 + (size_t)ott * 4096 + ont * 256 + r * 8, lane);
        PSTAMP(14);
        arrive4(lcnt, cnt + (C_X2 + layer * 16 + grp) * 1024, lane);
        PSTAMP(7);
      }
      {
        FRESH();
        { LDSP(char) cp = (LDSP(char))smem + L_CS + j * 256 + lane * 4;
          *(LDSP(float))cp = (*(LDSP(float))cp + *(LDSP(float))(cp + 1024)) + (*(LDSP(float))(cp + 2048) + *(LDSP(float))(cp + 3072)); }
        bf16x8 w0[12], w1[12], x0[8], x1[8];
        const char* wa = ws + OFF_WD + ((size_t)ont * KSF + j * 28) * 1024;
        const char* wb = ws + OFF_WD + ((size_t)(ont ^ 1) * KSF + j * 28) * 1024;
        {
          const int tt = gtb * 4 + j;
          const char* gx = wl + OFF_X2 + (size_t)tt * 32768;
          const char* gw = ws + OFF_WGU + (size_t)gnb * 8 * GW + j * 7168;
          f32x4 sp[4];
          asm volatile("" : "+s"(gx), "+s"(gw));
          poll_b(cnt + (C_X2 + layer * 16 + gtb) * 1024 + (gnb & 7) * 128, 16u, lcnt + 1, layer * 4 + 2, j);
          PSTAMP(8);
          __builtin_amdgcn_s_waitcnt(0xc07f);
#define GISSUE(c, d) do { glds4(voff, gx + (c) * 4096, (d) + GW + j * 4096); glds4(voff, gw + (c) * GW, (d) + j * 7168); glds3(voff, gw + (c) * GW + 4096, (d) + j * 7168 + 4096); } while (0)
          glds4(voff, gx, lds0 + GW + j * 4096);
          glds4(voff, gx + 4096, lds0 + GCH + GW + j * 4096);
          stat_issue(sp, wl + OFF_ST2 + (size_t)tt * 4096, voff);
          WAITBAR_V(8);
          GISSUE(2, lds0 + 2 * GCH);
          LD4(w0, 0, wa); LD4(w1, 0, wb);
          WAITBAR_V(23);
          unsigned d = lds0;
#pragma unroll 1
          for (int c = 3; c < 8; ++c) {
            GISSUE(c, d);
            d = d == lds0 + 2 * GCH ? lds0 : d + GCH;
            WAITBAR_V(11);
          }
#undef GISSUE
          LD4(w0, 4, wa + 4096); LD4(w1, 4, wb + 4096); LD4(w0, 8, wa + 8192); LD4(w1, 8, wb + 8192);
          { float mean, var; stat_finish(sp, mean, var);
            ((LDSP(float))((LDSP(char))smem + L_ST))[j * 64 + hh * 32 + (lane & 15) * 2 + ((lane >> 4) & 1)] = hh ? mean : 1.f / var; }
          WAITBAR_V(16);
        }
#define DISSUE(c, P) do { glds4(voff, wa + 12288 + (c) * 4096, lds0 + (P) + j * 8192); glds4(voff, wb + 12288 + (c) * 4096, lds0 + (P) + j * 8192 + 4096); } while (0)
        DISSUE(0, L_D0); DISSUE(1, L_D1); DISSUE(2, L_D2);
#pragma unroll 1
        for (unsigned spins = 0; spins < 4000000u; ++spins) {
          if (__builtin_amdgcn_readfirstlane(__hip_atomic_load(lcnt + 2, __ATOMIC_RELAXED, __HIP_MEMORY_SCOPE_WORKGROUP)) >= 4u * (layer + 1)) break;
          __builtin_amdgcn_s_sleep(1);
        }
        asm volatile("" ::: "memory");
        DISSUE(3, L_D3);
#undef DISSUE
        const char* xa = wl + OFF_H + ((size_t)ott * KSF + j * 28) * 1024;
        const char* xb = wl + OFF_H + ((size_t)(ott ^ 1) * KSF + j * 28) * 1024;
        asm volatile("" : "+s"(xa), "+s"(xb));
        poll_b(cnt + (C_H + layer * 16 + grp) * 1024 + (idx & 7) * 128, 16u, lcnt + 1, layer * 4 + 3, j);
        PSTAMP(9);
        PIN8(0, w0, w1, 0); PIN8(0, w0, w1, 4); PIN8(0, w0, w1, 8);
#define LDX(b, blk) do { gld16<((b) & 3) * 1024>(x0[b], voff, xa + (blk) * 4096); gld16<((b) & 3) * 1024>(x1[b], voff, xb + (blk) * 4096); } while (0)
#define WX(b, n) asm volatile("s_waitcnt vmcnt(%2)" : "+v"(x0[b]), "+v"(x1[b]) : "i"(n) : "memory")
        LDX(0, 0); LDX(1, 0); LDX(2, 0); LDX(3, 0); LDX(4, 1); LDX(5, 1); LDX(6, 1); LDX(7, 1);
        f32x16 acc[4];
#pragma unroll
        for (int i = 0; i < 4; ++i) acc[i] = f32x16{};
        LDSP(const char) lw = lb + j * 8192;
#define BLK(o, h, nb, n0) do { _Pragma("unroll") for (int s = 0; s < 1; ++s) {} \
          WX((h) + 0, n0); MM4(w0[(o) + 0], w1[(o) + 0], x0[(h) + 0], x1[(h) + 0]); SBAR(); if ((nb) >= 0) LDX((h) + 0, nb); SBAR(); \
          WX((h) + 1, (nb) >= 0 ? n0 : n0 - 2); MM4(w0[(o) + 1], w1[(o) + 1], x0[(h) + 1], x1[(h) + 1]); SBAR(); if ((nb) >= 0) LDX((h) + 1, nb); SBAR(); \
          WX((h) + 2, (nb) >= 0 ? n0 : n0 - 4); MM4(w0[(o) + 2], w1[(o) + 2], x0[(h) + 2], x1[(h) + 2]); SBAR(); if ((nb) >= 0) LDX((h) + 2, nb); SBAR(); \
          WX((h) + 3, (nb) >= 0 ? n0 : n0 - 6); MM4(w0[(o) + 3], w1[(o) + 3], x0[(h) + 3], x1[(h) + 3]); SBAR(); if ((nb) >= 0) LDX((h) + 3, nb); SBAR(); } while (0)
#define RDL(o, c) do { _Pragma("unroll") for (int s = 0; s < 4; ++s) { w0[(o) + s] = ldfrag(lw + (c) + s * 1024); w1[(o) + s] = ldfrag(lw + (c) + 4096 + s * 1024); } SBAR(); } while (0)
        BLK(0, 0, 2, 14); RDL(0, L_D0);
        BLK(4, 4, 3, 14); RDL(4, L_D1);
        BLK(8, 0, 4, 14); RDL(8, L_D2);
        BLK(0, 4, 5, 14); RDL(0, L_D3);
        BLK(4, 0, 6, 14);
        u32x2 rn[4]; unsigned res[8];
        res_load(rn, voff, wl + OFF_X2 + (size_t)ott * 32768 + ont * 2048, 8, 1024);
        BLK(8, 4, -1, 18);
        BLK(0, 0, -1, 10);
#undef RDL
#undef BLK
#undef LDX
#undef WX
        PSTAMP(10);
        RES_FINISH((i == 3 ? L_D1 : L_D0 + (i - 1) * 4096) + j * 8192, (i == 3 ? L_D1 : L_D0 + (i - 1) * 4096) + (j ^ i) * 8192);
        if (layer == 0) {
          res_store_tiled(res, ws + OFF_X3 + ((size_t)ott * KSD + 2 * ont) * 1024 + voff, ws + OFF_ST3 + (size_t)ott * 4096 + ont * 256 + r * 8, lane);
          arrive4(lcnt, cnt + (C_X3 + grp) * 1024, lane);
        } else {
          auto s0 = __builtin_amdgcn_permlane32_swap(res[0], res[2], false, false);
          auto s1 = __builtin_amdgcn_permlane32_swap(res[1], res[3], false, false);
          auto s2 = __builtin_amdgcn_permlane32_swap(res[4], res[6], false, false);
          auto s3 = __builtin_amdgcn_permlane32_swap(res[5], res[7], false, false);
          const u32x4 a = {s0[0], s1[0], s0[1], s1[1]}, b = {s2[0], s3[0], s2[1], s3[1]};
          char* o = (char*)karg(7) + ((size_t)(ott * 32 + r) * D_ + ont * 32) * 2 + hh * 16;
          st16(o, a);
          st16(o + 32, b);
        }
        PSTAMP(11);
      }
#undef LD4
#undef MM4
#undef PIN8
      if (layer == 0) {
        FRESH();
        const int tt = qtb * 4 + j;
        const char* xb = ws + OFF_X3 + (size_t)tt * 32768;
        const char* wb = ws + OFF_WQ + (size_t)qnb * 8 * QW + j * 3072;
        f32x4 sp[4];
        asm volatile("" : "+s"(xb), "+s"(wb));
        __builtin_amdgcn_s_barrier();
        poll_b(cnt + (C_X3 + qtb) * 1024 + (qnb & 7) * 128, 16u, lcnt + 1, 4, j);
        PSTAMP(12);
#define QISSUE(c) do { glds4(voff, xb + (c) * 4096, lds0 + L_QX + ((c) & 3) * 16384 + j * 4096); glds3(voff, wb + (c) * QW, lds0 + ((c) & 3) * QW + j * 3072); } while (0)
        glds4(voff, xb, lds0 + L_QX + j * 4096);
        glds4(voff, xb + 4096, lds0 + L_QX + 16384 + j * 4096);
        stat_issue(sp, ws + OFF_ST3 + (size_t)tt * 4096, voff);
        glds4(voff, xb + 8192, lds0 + L_QX + 32768 + j * 4096);
        glds4(voff, xb + 12288, lds0 + L_QX + 49152 + j * 4096);
        WAITBAR_V(16);
        { LDSP(char) cp = (LDSP(char))smem + L_CS + j * 256 + lane * 4;
          *(LDSP(float))cp = (*(LDSP(float))cp + *(LDSP(float))(cp + 1024)) + (*(LDSP(float))(cp + 2048) + *(LDSP(float))(cp + 3072)); }
        WAITBAR_V(12);
        QISSUE(4); WAITBAR_V(11);
#pragma unroll 1
        for (int c = 5; c < 8; ++c) { QISSUE(c); WAITBAR_V(14); }
#undef QISSUE
        { float mean, var; stat_finish(sp, mean, var); const float rs = rsqrtf(var);
          ((LDSP(float))((LDSP(char))smem + L_ST))[j * 64 + hh * 32 + (lane & 15) * 2 + ((lane >> 4) & 1)] = hh ? mean * rs : rs; }
        WAITBAR_V(7);
        WAITBAR_V(0);
        __builtin_amdgcn_s_barrier();
      }
    }
  }
}

extern "C" void kernel_launch(void* const* d_in, const int* in_sizes, int n_in, void* d_out, int out_size, void* d_ws, size_t ws_size,
                              hipStream_t stream) {
  const char* x = (const char*)d_in[0];
  const char* wqkv = (const char*)d_in[1];
  const char* wo = (const char*)d_in[2];
  const char* wg = (const char*)d_in[4];
  const char* wu = (const char*)d_in[5];
  const char* wd = (const char*)d_in[6];
  char* ws = (char*)d_ws;
  k_qkv1<<<256, 768, 0, stream>>>(x, wqkv, ws DIAG_PASS(0));
  k_main<<<256, 512, 0, stream>>>(ws, x, wqkv, wo, wg, wu, wd, (char*)d_out DIAG_PASS(1));
}
```

```cpp
#include <hip/hip_runtime.h>
#include <hip/hip_bf16.h>
#include <stdint.h>

typedef short bf16x8 __attribute__((ext_vector_type(8)));
typedef float f32x16 __attribute__((ext_vector_type(16)));
typedef float f32x4 __attribute__((ext_vector_type(4)));
typedef float f32x2 __attribute__((ext_vector_type(2)));
typedef unsigned u32x4 __attribute__((ext_vector_type(4)));
typedef unsigned u32x2 __attribute__((ext_vector_type(2)));
typedef __bf16 bf16v2 __attribute__((ext_vector_type(2)));
typedef unsigned short u16;

#define STAMP(i)
#define STAMP_DECL
#define PSTAMP(i)
#define STAMP_STORE()
#define DIAG_ARG
#define DIAG_PASS(k)
#define DI __device__ __forceinline__
#define MFMA(a, b, c) __builtin_amdgcn_mfma_f32_32x32x16_bf16((a), (b), (c), 0, 0, 0)
#define LDSP(T) __attribute__((address_space(3))) T*

constexpr int S_ = 2048, D_ = 512, H_ = 32, F_ = 1792;
constexpr int NTT = S_ / 32;
constexpr int KSD = D_ / 16;
constexpr int KSF = F_ / 16;
constexpr float QSCALE = 0.25f * 1.4426950408889634f;
constexpr float LN_EPS = 1e-5f;

constexpr size_t KB = 1024;
constexpr size_t OFF_CNT = 0;
constexpr size_t OFF_WO = 128 * KB;
constexpr size_t OFF_WGU = OFF_WO + 512 * KB;
constexpr size_t OFF_WD = OFF_WGU + 3584 * KB;
constexpr size_t OFF_WQ = OFF_WD + 1792 * KB;
constexpr size_t OFF_CSP = OFF_WQ + 1536 * KB;
constexpr size_t OFF_ST3 = OFF_CSP + 320 * KB;
constexpr size_t OFF_X3 = OFF_ST3 + 256 * KB;
constexpr size_t OFF_L = OFF_X3 + 2048 * KB;
constexpr size_t OFF_Q = OFF_L;
constexpr size_t OFF_K = OFF_Q + 2048 * KB;
constexpr size_t OFF_V = OFF_K + 2048 * KB;
constexpr size_t OFF_O = OFF_V + 2048 * KB;
constexpr size_t OFF_X2 = OFF_O + 2048 * KB;
constexpr size_t OFF_ST2 = OFF_X2 + 2048 * KB;
constexpr size_t OFF_H = OFF_ST2 + 256 * KB;
constexpr size_t LSTRIDE = OFF_H + 7168 * KB - OFF_L;
constexpr int KMAIN_KB = 20;
constexpr int C_O = 0, C_X2 = 16, C_H = 48, C_X3 = 80, C_KV = 96, C_PREP = 112, C_N = 128;

DI unsigned pk2(float lo, float hi) {
  f32x2 v = {lo, hi};
  bf16v2 r = __builtin_convertvector(v, bf16v2);
  return __builtin_bit_cast(unsigned, r);
}
DI float lo16(unsigned u) { return __uint_as_float(u << 16); }
DI float hi16(unsigned u) { return __uint_as_float(u & 0xffff0000u); }
DI float xhalf_add(float v) {
  auto r = __builtin_amdgcn_permlane32_swap(__float_as_uint(v), __float_as_uint(v), false, false);
  return __uint_as_float(r[0]) + __uint_as_float(r[1]);
}
DI float xhalf_max(float v) {
  auto r = __builtin_amdgcn_permlane32_swap(__float_as_uint(v), __float_as_uint(v), false, false);
  return fmaxf(__uint_as_float(r[0]), __uint_as_float(r[1]));
}
DI float x16_add(float v) {
  auto r = __builtin_amdgcn_permlane16_swap(__float_as_uint(v), __float_as_uint(v), false, false);
  return __uint_as_float(r[0]) + __uint_as_float(r[1]);
}
DI void st16(void* p, u32x4 v) { asm volatile("global_store_dwordx4 %0, %1, off sc1\n\ts_nop 1" :: "v"(p), "v"(v) : "memory"); }
template <int IMM>
DI void st16s(unsigned voff, const char* sbase, u32x4 v) {
  asm volatile("global_store_dwordx4 %0, %1, %2 offset:%3 sc1\n\ts_nop 1" :: "v"(voff), "v"(v), "s"(sbase), "i"(IMM) : "memory");
}
template <int IMM>
DI void gld16(bf16x8& d, unsigned voff, const char* sbase) {
  asm volatile("global_load_dwordx4 %0, %1, %2 offset:%3 sc1" : "=v"(d) : "v"(voff), "s"(sbase), "i"(IMM) : "memory");
}
#define GLDS_HEAD "s_mov_b32 %0, m0\n\ts_mov_b32 m0, %3\n\ts_nop 0\n\t"
#define GLDS_OP(o) "global_load_lds_dwordx4 %1, %2 offset:" #o " sc1\n\t"
#define GLDS_TAIL "s_mov_b32 m0, %0" : "=&s"(keep) : "v"(voff), "s"(sbase), "s"(lds) : "memory"
DI void glds1(unsigned voff, const char* sbase, unsigned lds) { unsigned keep; asm volatile(GLDS_HEAD GLDS_OP(0) GLDS_TAIL); }
DI void glds2(unsigned voff, const char* sbase, unsigned lds) { unsigned keep; asm volatile(GLDS_HEAD GLDS_OP(0) GLDS_OP(1024) GLDS_TAIL); }
DI void glds3(unsigned voff, const char* sbase, unsigned lds) { unsigned keep; asm volatile(GLDS_HEAD GLDS_OP(0) GLDS_OP(1024) GLDS_OP(2048) GLDS_TAIL); }
DI void glds4(unsigned voff, const char* sbase, unsigned lds) { unsigned keep; asm volatile(GLDS_HEAD GLDS_OP(0) GLDS_OP(1024) GLDS_OP(2048) GLDS_OP(3072) GLDS_TAIL); }
#define SBAR() __builtin_amdgcn_sched_barrier(0)
#define PIN(x) asm volatile("" : "+v"(x))
#define WAITV(n) asm volatile("s_waitcnt vmcnt(%0)" :: "i"(n) : "memory")
#define WAITBAR_V(n) asm volatile("s_waitcnt vmcnt(%0) lgkmcnt(0)\n\ts_barrier" :: "i"(n) : "memory")
#define WAITBAR_L() do { asm volatile("" ::: "memory"); __builtin_amdgcn_s_waitcnt(0xc07f); __builtin_amdgcn_s_barrier(); asm volatile("" ::: "memory"); } while (0)
typedef short s16x4 __attribute__((ext_vector_type(4)));
DI s16x4 trread(LDSP(const char) p) { return __builtin_amdgcn_ds_read_tr16_b64_v4i16((LDSP(s16x4))p); }
DI bf16x8 trfrag(LDSP(const char) p, int off2) { const s16x4 lo = trread(p), hi = trread(p + off2); return __builtin_shufflevector(lo, hi, 0, 1, 2, 3, 4, 5, 6, 7); }
DI bf16x8 ldfrag(LDSP(const char) p) { return *(LDSP(const bf16x8))p; }
DI float dot2(unsigned a, unsigned b, float c) { return __builtin_amdgcn_fdot2_f32_bf16(__builtin_bit_cast(bf16v2, a), __builtin_bit_cast(bf16v2, b), c, false); }
constexpr unsigned ONE2 = 0x3f803f80u;
#define BC8(x) __builtin_bit_cast(bf16x8, x)

template <int LOOK, int PER> DI void ring_wait(int i) {
  if (i <= 8 - LOOK) WAITBAR_V((LOOK - 1) * PER);
  else if (LOOK > 3 && i == 5) WAITBAR_V(2 * PER);
  else if (LOOK > 2 && i == 6) WAITBAR_V(PER);
  else WAITBAR_V(0);
}

DI void qk_store(const float (&v)[16], char* o) {
  u32x4 a = {pk2(v[0], v[1]), pk2(v[2], v[3]), pk2(v[4], v[5]), pk2(v[6], v[7])};
  u32x4 b = {pk2(v[8], v[9]), pk2(v[10], v[11]), pk2(v[12], v[13]), pk2(v[14], v[15])};
  st16(o, a);
  st16(o + NTT * 1024, b);
}
DI void v_store(const float (&v)[16], char* o) {
  auto s0 = __builtin_amdgcn_permlane32_swap(pk2(v[0], v[1]), pk2(v[4], v[5]), false, false);
  auto s1 = __builtin_amdgcn_permlane32_swap(pk2(v[2], v[3]), pk2(v[6], v[7]), false, false);
  auto s2 = __builtin_amdgcn_permlane32_swap(pk2(v[8], v[9]), pk2(v[12], v[13]), false, false);
  auto s3 = __builtin_amdgcn_permlane32_swap(pk2(v[10], v[11]), pk2(v[14], v[15]), false, false);
  const u32x4 a = {s0[0], s1[0], s0[1], s1[1]}, b = {s2[0], s3[0], s2[1], s3[1]};
  st16(o, a);
  st16(o + 512, b);
}
typedef __bf16 bfv8 __attribute__((ext_vector_type(8)));
typedef __bf16 bfv16 __attribute__((ext_vector_type(16)));
typedef float f32x8 __attribute__((ext_vector_type(8)));
template <int H> DI f32x16 smf(bf16x8 a, const f32x16& s, f32x16 c, int idx) {
  const f32x8 p = __builtin_shufflevector(s, s, 8 * H, 8 * H + 1, 8 * H + 2, 8 * H + 3, 8 * H + 4, 8 * H + 5, 8 * H + 6, 8 * H + 7);
  return __builtin_amdgcn_smfmac_f32_32x32x32_bf16(__builtin_bit_cast(bfv8, a), __builtin_bit_cast(bfv16, p), c, idx, 0, 0);
}
DI float row16_sum(float v) {
  v += __builtin_bit_cast(float, __builtin_amdgcn_update_dpp(0, __builtin_bit_cast(int, v), 0x128, 0xf, 0xf, false));
  v += __builtin_bit_cast(float, __builtin_amdgcn_update_dpp(0, __builtin_bit_cast(int, v), 0x124, 0xf, 0xf, false));
  v += __builtin_bit_cast(float, __builtin_amdgcn_update_dpp(0, __builtin_bit_cast(int, v), 0x122, 0xf, 0xf, false));
  v += __builtin_bit_cast(float, __builtin_amdgcn_update_dpp(0, __builtin_bit_cast(int, v), 0x121, 0xf, 0xf, false));
  return v;
}
#define KSTEP(B, u) do { SBAR(); RX((B) ^ 1, u); RW((B) ^ 1, 0, u); SBAR(); acc[0] = MFMA(wf[B][0], xf[B], acc[0]); SBAR(); RW((B) ^ 1, 1, u); RW((B) ^ 1, 2, u); SBAR(); \
                         acc[1] = MFMA(wf[B][1], xf[B], acc[1]); acc[2] = MFMA(xf[B], wf[B][2], acc[2]); SBAR(); } while (0)
#define QCHUNK() do { KSTEP(1, 0); KSTEP(0, 1); KSTEP(1, 2); KSTEP(0, 3); } while (0)
#define QINIT() \
  f32x16 acc[3]; bf16x8 wf[2][3], xf[2]; \
  _Pragma("unroll") for (int i = 0; i < 3; ++i) { acc[i] = f32x16{}; wf[1][i] = bf16x8{}; } \
  xf[1] = bf16x8{}
#define QFINISH() do { acc[0] = MFMA(wf[1][0], xf[1], acc[0]); acc[1] = MFMA(wf[1][1], xf[1], acc[1]); acc[2] = MFMA(xf[1], wf[1][2], acc[2]); } while (0)

extern "C" __global__ __launch_bounds__(768, 3) void k_qkv1(const char* __restrict__ x, const char* __restrict__ wn, char* __restrict__ ws DIAG_ARG) {
  STAMP_DECL; STAMP(0);
  constexpr int LOOK = 3, CH = 24576, LXN = 4 * CH;
  __shared__ __attribute__((aligned(16))) char smem[LXN + 65536];
  const int tid = threadIdx.x, lane = tid & 63, r = lane & 31, hh = lane >> 5;
  const int w = __builtin_amdgcn_readfirstlane(tid >> 6);
  const int xcd = blockIdx.x & 7, idx = blockIdx.x >> 3;
  const int tb = (xcd & 3) * 8 + (idx & 7), nq = (xcd >> 2) * 4 + (idx >> 3);
  const unsigned lds0 = (unsigned)(uintptr_t)smem;
  const int rot = 2 * (idx & 3);
  if (w >= 8) {
    const int j = w - 8;
    const unsigned wv = (lane >> 3) * 3072 + (((lane & 7) ^ (((lane >> 4) & 1) << 2)) * 16);
    const char* wb = wn + (size_t)j * 16 * 3072 + nq * 128;
    if (blockIdx.x < 4) *(unsigned*)(ws + OFF_CNT + ((blockIdx.x * 4 + j) * 64 + lane) * 128) = 0u;
    asm volatile("s_sleep 8\n\ts_nop 4" ::: "memory");
#pragma unroll 1
    for (int c = 0; c < 8 + LOOK; ++c) {
      if (c >= LOOK) ring_wait<LOOK, 6>(c - LOOK);
      if (c < 8) {
        const char* ws_ = wb + (size_t)((c + rot) & 7) * 64 * 3072;
        const unsigned d = lds0 + (c & 3) * CH + j * 2048;
        glds1(wv, ws_, d); glds1(wv, ws_ + 1024, d + 8192); glds1(wv, ws_ + 2048, d + 16384);
        glds1(wv, ws_ + 8 * 3072, d + 1024); glds1(wv, ws_ + 8 * 3072 + 1024, d + 8192 + 1024); glds1(wv, ws_ + 8 * 3072 + 2048, d + 16384 + 1024);
      }
    }
    return;
  }
  {
    const int n = w;
    const u32x4* xr = (const u32x4*)(x + (size_t)(tb * 64 + n * 8) * 1024) + lane;
    u32x4 v[8];
#pragma unroll
    for (int i = 0; i < 8; ++i) v[i] = xr[i * 64];
    float s[8], q[8];
#pragma unroll
    for (int i = 0; i < 8; ++i) {
      s[i] = 0.f; q[i] = 0.f;
#pragma unroll
      for (int e = 0; e < 4; ++e) { s[i] = dot2(v[i][e], ONE2, s[i]); q[i] = dot2(v[i][e], v[i][e], q[i]); }
    }
    float s4[4], q4[4], s2[2], q2[2];
#pragma unroll
    for (int i = 0; i < 4; ++i) {
      const int lo = (i & 1) + 4 * (i >> 1);
      auto a = __builtin_amdgcn_permlane32_swap(__float_as_uint(s[lo]), __float_as_uint(s[lo + 2]), false, false);
      auto b = __builtin_amdgcn_permlane32_swap(__float_as_uint(q[lo]), __float_as_uint(q[lo + 2]), false, false);
      s4[i] = __uint_as_float(a[0]) + __uint_as_float(a[1]); q4[i] = __uint_as_float(b[0]) + __uint_as_float(b[1]);
    }
#pragma unroll
    for (int i = 0; i < 2; ++i) {
      auto a = __builtin_amdgcn_permlane16_swap(__float_as_uint(s4[2 * i]), __float_as_uint(s4[2 * i + 1]), false, false);
      auto b = __builtin_amdgcn_permlane16_swap(__float_as_uint(q4[2 * i]), __float_as_uint(q4[2 * i + 1]), false, false);
      s2[i] = row16_sum(__uint_as_float(a[0]) + __uint_as_float(a[1])); q2[i] = row16_sum(__uint_as_float(b[0]) + __uint_as_float(b[1]));
    }
    float ra[2], rb[2];
#pragma unroll
    for (int i = 0; i < 2; ++i) {
      const float mean = s2[i] * (1.f / D_);
      ra[i] = rsqrtf(fmaxf(q2[i] * (1.f / D_) - mean * mean, 0.f) + LN_EPS);
      rb[i] = -mean * ra[i];
    }
    LDSP(char) xd = (LDSP(char))smem + LXN + (lane >> 3) * 8192 + n * 1024;
#pragma unroll
    for (int i = 0; i < 8; ++i) {
      const float a = __builtin_bit_cast(float, __builtin_amdgcn_readlane(__builtin_bit_cast(int, ra[i >> 2]), (i & 3) * 16));
      const float b = __builtin_bit_cast(float, __builtin_amdgcn_readlane(__builtin_bit_cast(int, rb[i >> 2]), (i & 3) * 16));
      u32x4 o;
#pragma unroll
      for (int e = 0; e < 4; ++e) o[e] = pk2(lo16(v[i][e]) * a + b, hi16(v[i][e]) * a + b);
      *(LDSP(u32x4))(xd + i * 128 + (((lane & 7) ^ ((4 * n + (i >> 1)) & 7)) * 16)) = o;
    }
  }
  STAMP(1);
  WAITBAR_L();
  if (w >= 4) {
    if (w == 4 && idx < KMAIN_KB) {
      bf16x8 t;
      asm volatile("s_getpc_b64 s[20:21]\n\ts_add_u32 s20, s20, k_main@rel32@lo+4\n\ts_addc_u32 s21, s21, k_main@rel32@hi+12\n\t"
                   "global_load_dwordx4 %0, %1, s[20:21]\n\ts_waitcnt vmcnt(0)" : "=v"(t) : "v"(lane * 16 + idx * 1024) : "s20", "s21", "scc", "memory");
    }
    return;
  }
  STAMP(2);
  const int ta = w & 1, hp = w >> 1, q4 = (lane & 15) >> 2, xsw = (r >> 1) & 7;
  LDSP(const char) lw = (LDSP(const char))smem + (8 * hh + q4) * 128 + ((hp ^ (q4 >> 1)) * 64) + ((lane >> 4) & 1) * 32 + (lane & 3) * 8;
  LDSP(const char) lx = (LDSP(const char))smem + LXN + ta * 4096 + r * 128;
  int xo[4];
#pragma unroll
  for (int u = 0; u < 4; ++u) xo[u] = ((2 * u + hh) ^ xsw) * 16;
  QINIT();
#define RX(B, u) xf[B] = ldfrag(sx + xo[u])
#define RW(B, i, u) wf[B][i] = trfrag(sw + (i) * 8192 + (u) * 2048, 512)
#pragma unroll 1
  for (int c = 0; c < 8; ++c) {
    LDSP(const char) sw = lw + (c & 3) * CH;
    LDSP(const char) sx = lx + ((c + rot) & 7) * 8192;
    QCHUNK();
    WAITBAR_L();
  }
  QFINISH();
#undef RX
#undef RW
  STAMP(4);
  const int tt = tb * 2 + ta, nb = nq * 2 + hp;
  float v[16];
#pragma unroll
  for (int e = 0; e < 16; ++e) v[e] = acc[0][e] * QSCALE;
  qk_store(v, ws + OFF_Q + ((size_t)(2 * nb) * NTT + tt) * 1024 + lane * 16);
#pragma unroll
  for (int e = 0; e < 16; ++e) v[e] = acc[1][e];
  qk_store(v, ws + OFF_K + ((size_t)(2 * nb) * NTT + tt) * 1024 + lane * 16);
#pragma unroll
  for (int e = 0; e < 16; ++e) v[e] = acc[2][e];
  v_store(v, ws + OFF_V + ((size_t)(2 * nb + (r >> 4)) * NTT + tt) * 1024 + hh * 256 + (r & 15) * 16);
  STAMP(5); STAMP_STORE();
}

DI void poll(const char* cnt, unsigned voff, unsigned target) {
#pragma unroll 1
  for (unsigned spins = 0; spins < 200000u; ++spins) {
    unsigned v;
    asm volatile("global_load_dword %0, %1, %2 sc1\n\ts_waitcnt vmcnt(0)" : "=v"(v) : "v"(voff), "s"(cnt) : "memory");
    if (__all(v >= target)) break;
  }
}
DI void poll_b(const char* cnt, unsigned target, LDSP(unsigned) go, unsigned seq, int j) {
  if (j == 0) {
    poll(cnt, 0u, target);
    __hip_atomic_store(go, seq, __ATOMIC_RELAXED, __HIP_MEMORY_SCOPE_WORKGROUP);
  } else {
    WAITV(0);
#pragma unroll 1
    for (unsigned spins = 0; spins < 4000000u; ++spins) {
      if (__builtin_amdgcn_readfirstlane(__hip_atomic_load(go, __ATOMIC_RELAXED, __HIP_MEMORY_SCOPE_WORKGROUP)) >= seq) break;
    }
  }
  asm volatile("" ::: "memory");
}
DI void arrive4(LDSP(unsigned) lw, char* cnt, int lane) {
  WAITV(0);
  unsigned old = 0;
  if (lane == 0) old = __hip_atomic_fetch_add(lw, 1u, __ATOMIC_RELAXED, __HIP_MEMORY_SCOPE_WORKGROUP);
  old = __builtin_amdgcn_readfirstlane(old);
  if ((old & 3u) == 3u && lane < 8) asm volatile("global_atomic_add %0, %1, %2" :: "v"(lane * 128), "v"(1u), "s"(cnt) : "memory");
  asm volatile("" ::: "memory");
}
DI void stat_issue(f32x4 (&p)[4], const char* sb, unsigned voff) {
  asm volatile("global_load_dwordx4 %0, %4, %5 sc1\n\tglobal_load_dwordx4 %1, %4, %5 offset:1024 sc1\n\tglobal_load_dwordx4 %2, %4, %5 offset:2048 sc1\n\t"
               "global_load_dwordx4 %3, %4, %5 offset:3072 sc1" : "=&v"(p[0]), "=&v"(p[1]), "=&v"(p[2]), "=&v"(p[3]) : "v"(voff), "s"(sb) : "memory");
}
DI void stat_finish(f32x4 (&p)[4], float& mean, float& var) {
  asm volatile("" : "+v"(p[0]), "+v"(p[1]), "+v"(p[2]), "+v"(p[3]));
  const f32x4 t = (p[0] + p[1]) + (p[2] + p[3]);
  auto a = __builtin_amdgcn_permlane16_swap(__float_as_uint(t[0]), __float_as_uint(t[2]), false, false);
  auto b = __builtin_amdgcn_permlane16_swap(__float_as_uint(t[1]), __float_as_uint(t[3]), false, false);
  mean = xhalf_add(__uint_as_float(a[0]) + __uint_as_float(a[1])) * (1.f / D_);
  var = fmaxf(xhalf_add(__uint_as_float(b[0]) + __uint_as_float(b[1])) * (1.f / D_) - mean * mean, 0.f) + LN_EPS;
}
DI void colsum_part(const char* src, unsigned voff, LDSP(char) dst) {
  f32x4 p[4];
  asm volatile("global_load_dwordx4 %0, %4, %5 sc1\n\tglobal_load_dwordx4 %1, %4, %6 sc1\n\tglobal_load_dwordx4 %2, %4, %7 sc1\n\t"
               "global_load_dwordx4 %3, %4, %8 sc1\n\ts_waitcnt vmcnt(0)" : "=&v"(p[0]), "=&v"(p[1]), "=&v"(p[2]), "=&v"(p[3])
               : "v"(voff), "s"(src), "s"(src + 20480), "s"(src + 40960), "s"(src + 61440) : "memory");
  *(LDSP(f32x4))dst = (p[0] + p[1]) + (p[2] + p[3]);
}

#define RES_FINISH(WR, RD) do { \
    _Pragma("unroll") for (int i = 1; i < 4; ++i) _Pragma("unroll") for (int g = 0; g < 4; ++g) { \
      const f32x4 v_ = {acc[i][4 * g], acc[i][4 * g + 1], acc[i][4 * g + 2], acc[i][4 * g + 3]}; \
      *(LDSP(f32x4))(lb + (WR) + g * 1024) = v_; } \
    asm volatile("s_waitcnt vmcnt(0)" : "+v"(rn[0]), "+v"(rn[1]), "+v"(rn[2]), "+v"(rn[3]) :: "memory"); \
    WAITBAR_L(); \
    _Pragma("unroll") for (int g = 0; g < 4; ++g) { \
      f32x4 t_ = {acc[0][4 * g], acc[0][4 * g + 1], acc[0][4 * g + 2], acc[0][4 * g + 3]}; \
      f32x4 u_[3]; \
      _Pragma("unroll") for (int i = 1; i < 4; ++i) u_[i - 1] = *(LDSP(const f32x4))(lb + (RD) + g * 1024); \
      t_ = (u_[0] + u_[1]) + (u_[2] + t_); \
      res[2 * g] = pk2(t_[0] + lo16(rn[g][0]), t_[1] + hi16(rn[g][0])); \
      res[2 * g + 1] = pk2(t_[2] + lo16(rn[g][1]), t_[3] + hi16(rn[g][1])); } } while (0)
DI void res_load(u32x2 (&rn)[4], unsigned vo, const char* p0, int d1, int d2) {
  asm volatile("global_load_dwordx2 %0, %4, %5 sc1\n\tglobal_load_dwordx2 %1, %4, %6 sc1\n\tglobal_load_dwordx2 %2, %4, %7 sc1\n\tglobal_load_dwordx2 %3, %4, %8 sc1"
               : "=&v"(rn[0]), "=&v"(rn[1]), "=&v"(rn[2]), "=&v"(rn[3]) : "v"(vo), "s"(p0), "s"(p0 + d1), "s"(p0 + d2), "s"(p0 + d2 + d1) : "memory");
}
DI void res_store_tiled(const unsigned (&res)[8], char* o, char* st, int lane) {
  const u32x4 p0 = {res[0], res[1], res[2], res[3]}, p1 = {res[4], res[5], res[6], res[7]};
  st16(o, p0);
  st16(o + 1024, p1);
  float s1 = 0.f, s2 = 0.f, t1 = 0.f, t2 = 0.f;
#pragma unroll
  for (int e = 0; e < 4; ++e) { s1 = dot2(res[e], ONE2, s1); s2 = dot2(res[e], res[e], s2); t1 = dot2(res[4 + e], ONE2, t1); t2 = dot2(res[4 + e], res[4 + e], t2); }
  s1 = xhalf_add(s1 + t1); s2 = xhalf_add(s2 + t2);
  if (lane < 32) { const f32x2 v = {s1, s2}; asm volatile("global_store_dwordx2 %0, %1, off sc1" :: "v"(st), "v"(v) : "memory"); }
}

DI const char* karg(int i) {
  const __attribute__((address_space(4))) unsigned long long* ka = (const __attribute__((address_space(4))) unsigned long long*)__builtin_amdgcn_kernarg_segment_ptr();
  asm volatile("" : "+s"(ka));
  return (const char*)ka[i];
}
extern "C" __global__ __launch_bounds__(512, 2) void k_main(char* __restrict__ ws_, const char* x_, const char* wq_, const char* wo_, const char* wg_,
                                                            const char* wu_, const char* wd_, char* out_ DIAG_ARG) {
  constexpr int L_K = 0, L_V = 65536, L_ONE = 131072, L_CMB = 133120;
  constexpr int GW = 28672, GCH = GW + 16384;
  constexpr int L_D0 = 2 * GCH, L_D1 = L_D0 + 32768, L_D2 = 0, L_D3 = GCH;
  constexpr int QW = 12288, L_QX = 49152;
  constexpr int L_CS = 157696, L_ST = L_CS + 4096, L_CNT = L_ST + 1024, L_DBG = L_CNT + 64;
  __shared__ __attribute__((aligned(16))) char smem[163840];
  const int w = __builtin_amdgcn_readfirstlane(threadIdx.x >> 6);
  const unsigned lds0 = (unsigned)(uintptr_t)smem;
#define FRESH() \
  int tid = threadIdx.x; PIN(tid); int bid = blockIdx.x, j = w & 3; char* ws = ws_; asm volatile("" : "+s"(bid), "+s"(j), "+s"(ws)); \
  const int lane = tid & 63, r = lane & 31, hh = lane >> 5, xcd = bid & 7, idx = bid >> 3, grp = xcd * 2 + (idx >> 4), m = idx & 15; \
  const unsigned voff = lane * 16; LDSP(char) lb = (LDSP(char))smem + lane * 16; char* cnt = ws + OFF_CNT; char* wl = ws + layer * LSTRIDE; \
  LDSP(unsigned) lcnt = (LDSP(unsigned))((LDSP(char))smem + L_CNT) + (w >> 2); \
  const int ont = (m >> 1) * 2 + (j >> 1), ott = grp * 4 + (m & 1) * 2 + (j & 1); \
  const int gtb = (xcd & 1) * 8 + (idx & 7), gnb = (xcd >> 1) * 4 + (idx >> 3), qtb = (xcd & 3) * 4 + (idx & 3), qnb = (xcd >> 2) * 8 + (idx >> 2); \
  (void)r; (void)hh; (void)voff; (void)lb; (void)cnt; (void)wl; (void)lcnt; (void)ont; (void)ott; (void)gtb; (void)gnb; (void)qtb; (void)qnb
  if (threadIdx.x < 8) ((LDSP(unsigned))((LDSP(char))smem + L_CNT))[threadIdx.x] = 0u;
#pragma unroll 1
  for (int layer = 0; layer < 2; ++layer) {
    {
      FRESH();
      const int head = xcd * 4 + (idx & 3), qb = idx >> 2, half = w >> 2, tt0 = qb * 8 + 2 * j;
      PSTAMP(0);
      if (layer) { if (w == 4) poll(cnt + (C_KV + (head >> 1)) * 1024 + qb * 128, 0u, 16u); WAITBAR_L(); }
      PSTAMP(1);
      const char* ks = wl + OFF_K + (size_t)head * 65536 + half * 32768;
      const unsigned kd = lds0 + L_K + half * 32768;
      bf16x8 q0, q1;
      gld16<0>(q0, voff, wl + OFF_Q + ((size_t)head * NTT + tt0) * 1024);
      gld16<1024>(q1, voff, wl + OFF_Q + ((size_t)head * NTT + tt0) * 1024);
      glds2(voff, ks + j * 2048, kd + j * 2048);
      glds2(voff, ks + (OFF_V - OFF_K) + j * 2048, kd + L_V + j * 2048);
      *(LDSP(unsigned))((LDSP(char))smem + L_ONE + tid * 4) = ((tid >> 2) & 11) == 0 ? ONE2 : 0u;
      asm volatile("s_waitcnt vmcnt(0) lgkmcnt(0)\n\ts_barrier" : "+v"(q0), "+v"(q1) :: "memory");
      PSTAMP(2);
      LDSP(const char) kp = lb + L_K;
      LDSP(const char) vp0 = (LDSP(const char))smem + (r < 16 ? L_V : L_ONE) + hh * 256 + (r & 15) * 16;
      const int vstep = r < 16 ? 2048 : 0;
      LDSP(const char) kq = kp + half * 32768; LDSP(const char) vq = vp0 + half * 16 * vstep;
      f32x16 n0, n1, sA, sB = f32x16{}, o0 = f32x16{}, o1 = f32x16{};
      bf16x8 kA, kB, vp = bf16x8{};
      int sidx = (int)0xDDDDDDDDu; PIN(sidx);
      {
        const bf16x8 k0 = ldfrag(kp);
        kA = ldfrag(kq);
        const f32x16 s = MFMA(k0, q0, f32x16{}), t = MFMA(k0, q1, f32x16{});
        float a = fmaxf(fmaxf(s[0], s[1]), s[2]), b = fmaxf(fmaxf(t[0], t[1]), t[2]);
#pragma unroll
        for (int i = 3; i < 15; i += 2) { a = fmaxf(fmaxf(a, s[i]), s[i + 1]); b = fmaxf(fmaxf(b, t[i]), t[i + 1]); }
        a = xhalf_max(fmaxf(a, s[15])); b = xhalf_max(fmaxf(b, t[15]));
#pragma unroll
        for (int i = 0; i < 16; ++i) { n0[i] = -a; n1[i] = -b; }
        PIN(n0); PIN(n1);
        sA = MFMA(kA, q0, n0);
      }
#define EX(S, i) S[i] = __builtin_amdgcn_exp2f(S[i])
#define STEPN(CUR, NXT, KN_, QN, NN, O, VA, OP, VP, READS) do { \
      EX(CUR, 0); EX(CUR, 1); PIN(CUR); SBAR(); OP = smf<1>(VP, NXT, OP, sidx); SBAR(); READS; SBAR(); \
      EX(CUR, 2); EX(CUR, 3); EX(CUR, 4); EX(CUR, 5); PIN(CUR); SBAR(); NXT = MFMA(KN_, QN, NN); SBAR(); \
      EX(CUR, 6); EX(CUR, 7); EX(CUR, 8); EX(CUR, 9); EX(CUR, 10); PIN(CUR); SBAR(); O = smf<0>(VA, CUR, O, sidx); SBAR(); \
      EX(CUR, 11); EX(CUR, 12); EX(CUR, 13); EX(CUR, 14); EX(CUR, 15); PIN(CUR); SBAR(); } while (0)
#define TILE(t, KA, KB) do { bf16x8 va, vb; \
      STEPN(sA, sB, KA, q1, n1, o0, va, o1, vp, (va = ldfrag(vq + (t) * 1024), vb = ldfrag(vq + (t) * 1024 + 512), KB = ldfrag(kq + ((t) + 1) * 1024))); \
      STEPN(sB, sA, KB, q0, n0, o1, va, o0, vb, (vp = vb)); } while (0)
      __builtin_amdgcn_s_setprio(3);
#pragma unroll 1
      for (int ph = -2; ph < 5; ++ph) {
        if (ph == -1) {
          glds3(voff, ks + 8192 + j * 6144, kd + 8192 + j * 6144);
          glds3(voff, ks + (OFF_V - OFF_K) + 8192 + j * 6144, kd + L_V + 8192 + j * 6144);
          glds3(voff, ks + 11264 + j * 6144, kd + 11264 + j * 6144);
          glds3(voff, ks + (OFF_V - OFF_K) + 11264 + j * 6144, kd + L_V + 11264 + j * 6144);
        }
        if (ph == 0) {
          WAITV(0);
          if (lane == 0) __hip_atomic_fetch_add(lcnt + 4, 1u, __ATOMIC_RELAXED, __HIP_MEMORY_SCOPE_WORKGROUP);
#pragma unroll 1
          for (unsigned spins = 0; spins < 4000000u; ++spins)
            if (__builtin_amdgcn_readfirstlane(__hip_atomic_load(lcnt + 4, __ATOMIC_RELAXED, __HIP_MEMORY_SCOPE_WORKGROUP)) >= 4u * (layer + 1)) break;
          asm volatile("" ::: "memory");
        }
        if (ph == 1) __builtin_amdgcn_s_setprio(2);
        if (ph == 3) __builtin_amdgcn_s_setprio(1);
        if (ph == 4) __builtin_amdgcn_s_setprio(0);
        if (layer == 0 && ph >= 0) {
          const int q4 = (lane & 15) >> 2, b16 = (lane >> 4) & 1;
          const int scr = L_CMB + (half ? j * 5120 : 20480 + j * 2048);
          const int jo = ((ph - 1) * 8 + w) * 256 + bid;
          if ((ph == 1 || ph == 2) && jo < 3712) {
            const bool gu = jo < 1792;
            LDSP(const char) sp = (LDSP(const char))smem + scr + (lane & 3) * 8 +
                                  (gu ? (16 * hh + 4 * b16 + q4) * 32 : (4 * hh + q4) * 64 + b16 * 32);
            const int ho = gu ? 256 : 512;
            const char* pd;
            if (gu) { const int ft = jo >> 4, S = jo & 15, nb = ft / 7, ii = ft - nb * 7; pd = ws + OFF_WGU + ((size_t)((nb * 8 + (S >> 1)) * 14 + 2 * ii) * 2 + (S & 1)) * 1024; }
            else if (jo < 2048) pd = ws + OFF_WO + (size_t)(jo - 1792) * 2048;
            else if (jo < 2944) pd = ws + OFF_WD + (size_t)(jo - 2048) * 2048;
            else { const int t = (jo - 2944) >> 4, nb = t / 3, ii = t - nb * 3, s0 = (jo & 15) * 2; pd = ws + OFF_WQ + (size_t)(((nb * 8 + (s0 >> 2)) * 3 + ii) * 4 + (s0 & 3)) * 1024; }
            WAITV(0);
            const u32x4 fa = __builtin_bit_cast(u32x4, trfrag(sp, ho)), fb = __builtin_bit_cast(u32x4, trfrag(sp + 1024, ho));
            st16s<0>(voff, pd, fa); st16s<0>(voff, pd + (gu ? 2048 : 1024), fb);
            if (gu || jo >= 2944) {
              float ca = (lo16(fa[0]) + hi16(fa[0])) + (lo16(fa[1]) + hi16(fa[1])) + (lo16(fa[2]) + hi16(fa[2])) + (lo16(fa[3]) + hi16(fa[3]));
              float cb = (lo16(fb[0]) + hi16(fb[0])) + (lo16(fb[1]) + hi16(fb[1])) + (lo16(fb[2]) + hi16(fb[2])) + (lo16(fb[3]) + hi16(fb[3]));
              if (gu) { ca = xhalf_add(x16_add(ca)); cb = xhalf_add(x16_add(cb)); ca = lane < 16 ? ca : cb; }
              else ca = xhalf_add(ca + cb);
              const char* cp = ws + OFF_CSP + ((jo & 15) * 5120 + ((gu ? jo : jo - 2944 + 1792) >> 4) * 32) * 4;
              if (lane < 32) asm volatile("global_store_dword %0, %1, %2 sc1" :: "v"(lane * 4), "v"(ca), "s"(cp) : "memory");
            }
          }
          const int jn = jo + 2048;
          if (ph < 2 && jn < 3712) {
            const char *sa, *sb; unsigned gv;
            if (jn < 1792) {
              const int o = 32 * (jn & 15) * (F_ * 2) + (jn >> 4) * 32;
              gv = (lane >> 1) * (F_ * 2) + (lane & 1) * 16; sa = karg(4) + o; sb = karg(5) + o;
            } else {
              int nt, s0, ld = 1024, ai;
              if (jn < 2048) { const int f = (jn - 1792) * 2; nt = f >> 5; s0 = f & 31; ai = 3; }
              else if (jn < 2944) { const int f = (jn - 2048) * 2; nt = f / KSF; s0 = f - nt * KSF; ai = 6; }
              else { const int t = (jn - 2944) >> 4, nb = t / 3; s0 = (jn & 15) * 2; nt = (t - nb * 3) * 16 + nb; ai = 2; ld = 3072; }
              gv = (lane >> 2) * ld + (lane & 3) * 16; sa = karg(ai) + (16 * s0 * ld + nt * 64); sb = sa + 16 * ld;
            }
            __builtin_amdgcn_s_waitcnt(0xc07f);
            glds1(gv, sa, lds0 + scr); glds1(gv, sb, lds0 + scr + 1024);
          }
          if (ph == 3) arrive4(lcnt, cnt + (C_PREP + xcd) * 1024, lane);
        }
        int n = ph <= 0 ? ph + 3 : ph == 2 ? 2 : ph == 4 ? 2 : 3;
#pragma unroll 1
        do { TILE(0, kA, kB); TILE(1, kB, kA); kq += 2048; vq += vstep; } while (--n > 0);
      }
      o1 = smf<1>(vp, sB, o1, sidx);
#undef TILE
#undef STEPN
      PSTAMP(3);
      LDSP(char) xp = lb + L_CMB + j * 5120;
      if (half == 1) {
        const f32x4 a = {o0[0], o0[1], o0[2], o0[3]}, b = {o0[4], o0[5], o0[6], o0[7]}, c = {o1[0], o1[1], o1[2], o1[3]}, d = {o1[4], o1[5], o1[6], o1[7]};
        const f32x2 l = {o0[8], o1[8]};
        *(LDSP(f32x4))xp = a; *(LDSP(f32x4))(xp + 1024) = b; *(LDSP(f32x4))(xp + 2048) = c; *(LDSP(f32x4))(xp + 3072) = d; *(LDSP(f32x2))(xp + 4096) = l;
      }
      if (layer == 0 && w == 0) poll(cnt + C_PREP * 1024 + (idx & 7) * 128, (lane & 7) * 1024, 64u);
      WAITBAR_L();
      if (half == 0) {
        PSTAMP(11);
        const f32x4 a = *(LDSP(const f32x4))xp, b = *(LDSP(const f32x4))(xp + 1024), c = *(LDSP(const f32x4))(xp + 2048), d = *(LDSP(const f32x4))(xp + 3072);
        const f32x2 l = *(LDSP(const f32x2))(xp + 4096);
#pragma unroll
        for (int i = 0; i < 4; ++i) { o0[i] += a[i]; o0[4 + i] += b[i]; o1[i] += c[i]; o1[4 + i] += d[i]; }
        o0[8] += l[0]; o1[8] += l[1];
        float chk = (o0[8] + o1[8]) * 5.4210109e-20f;
#pragma unroll
        for (int i = 0; i < 8; ++i) chk = __builtin_fmaf(o0[i], 0.f, __builtin_fmaf(o1[i], 0.f, chk));
        const bool bad = !(chk < 1.f);
        if (__builtin_expect(__any(bad), 0)) {
#pragma unroll 1
          for (int z = 0; z < 2; ++z) {
            const bf16x8 q = z ? q1 : q0;
            float mx = -INFINITY;
            f32x16 o = f32x16{};
            kq = kp; vq = vp0;
#pragma unroll 1
            for (int t = 0; t < 64; ++t) {
              const bf16x8 k = ldfrag(kq), v0 = ldfrag(vq), v1 = ldfrag(vq + 512);
              f32x16 s = MFMA(k, q, f32x16{});
              float tm = s[0];
#pragma unroll
              for (int i = 1; i < 16; ++i) tm = fmaxf(tm, s[i]);
              tm = xhalf_max(tm);
              const float mn = fmaxf(mx, tm);
              const float alpha = __builtin_amdgcn_exp2f(mx - mn);
              mx = mn;
#pragma unroll
              for (int i = 0; i < 16; ++i) { o[i] *= alpha; s[i] = __builtin_amdgcn_exp2f(s[i] - mn); }
              o = smf<0>(v0, s, o, sidx);
              o = smf<1>(v1, s, o, sidx);
              kq += 1024; vq += vstep >> 1;
            }
            if (z) o1 = o; else o0 = o;
          }
        }
        const float i0 = __builtin_amdgcn_rcpf(o0[8]), i1 = __builtin_amdgcn_rcpf(o1[8]);
        const u32x4 a0 = {pk2(o0[0] * i0, o0[1] * i0), pk2(o0[2] * i0, o0[3] * i0), pk2(o0[4] * i0, o0[5] * i0), pk2(o0[6] * i0, o0[7] * i0)};
        const u32x4 a1 = {pk2(o1[0] * i1, o1[1] * i1), pk2(o1[2] * i1, o1[3] * i1), pk2(o1[4] * i1, o1[5] * i1), pk2(o1[6] * i1, o1[7] * i1)};
        char* op = wl + OFF_O + ((size_t)tt0 * KSD + head) * 1024 + voff;
        st16(op, a0); st16(op + 32768, a1);
        PSTAMP(12);
        WAITBAR_L();
        arrive4(lcnt, cnt + (C_O + layer * 8 + qb) * 1024, lane);
      }
    }
    PSTAMP(4);
    int role = w >> 2;
    asm volatile("" : "+s"(role));
    if (role == 0) {
      {
        FRESH();
        const char* wb = ws + OFF_WGU + (size_t)gnb * 8 * GW + j * 7168;
#pragma unroll 1
        for (int c = 0; c < 2; ++c) { glds4(voff, wb + c * GW, lds0 + c * GCH + j * 7168); glds3(voff, wb + c * GW + 4096, lds0 + c * GCH + j * 7168 + 4096); }
        colsum_part(ws + OFF_CSP + (size_t)j * 81920 + gnb * 896, voff, lb + L_CS + j * 1024);
      }
      PSTAMP(14);
      WAITBAR_L();
      PSTAMP(15);
      {
        FRESH();
        const int g4 = lane >> 4, c16 = lane & 15;
        f32x4 ac[2][14];
#pragma unroll
        for (int i = 0; i < 14; ++i) { ac[0][i] = f32x4{}; ac[1][i] = f32x4{}; }
        LDSP(const char) lx = (LDSP(const char))smem + GW + j * 4096 + (g4 >> 1) * 1024 + ((g4 & 1) * 32 + c16) * 16;
        bf16x8 wf[2][7], xf[2][2];
        xf[1][0] = bf16x8{}; xf[1][1] = bf16x8{};
#pragma unroll
        for (int i = 0; i < 7; ++i) wf[1][i] = bf16x8{};
#define RDX(u, so, a) xf[u][a] = ldfrag(lx + (so) + (u) * 2048 + (a) * 256)
#define RDW(hf, u, so, i) wf[hf][i] = ldfrag(lb + (so) + ((7 * (hf) + (i)) * 2 + (u)) * 1024)
#define M1(B, XP, i, a) do { SBAR(); ac[a][7 * (B) + (i)] = __builtin_amdgcn_mfma_f32_16x16x32_bf16(wf[B][i], xf[XP][a], ac[a][7 * (B) + (i)], 0, 0, 0); SBAR(); } while (0)
#define MM(B, XP, i) do { M1(B, XP, i, 0); M1(B, XP, i, 1); } while (0)
#define GSTEP(B, XP, u, so) do { \
    M1(B, XP, 0, 0); RDW((B) ^ 1, u, so, 0); M1(B, XP, 0, 1); if ((B) == 1) RDX(u, so, 0); M1(B, XP, 1, 0); if ((B) == 1) RDX(u, so, 1); M1(B, XP, 1, 1); RDW((B) ^ 1, u, so, 1); \
    M1(B, XP, 2, 0); RDW((B) ^ 1, u, so, 2); M1(B, XP, 2, 1); RDW((B) ^ 1, u, so, 3); M1(B, XP, 3, 0); RDW((B) ^ 1, u, so, 4); M1(B, XP, 3, 1); RDW((B) ^ 1, u, so, 5); \
    M1(B, XP, 4, 0); RDW((B) ^ 1, u, so, 6); M1(B, XP, 4, 1); MM(B, XP, 5); MM(B, XP, 6); } while (0)
        WAITBAR_L();
        PSTAMP(5);
        int so = 0;
#pragma unroll 1
        for (int c = 0; c < 8; ++c) {
          GSTEP(1, 1, 0, so); GSTEP(0, 0, 0, so); GSTEP(1, 0, 1, so); GSTEP(0, 1, 1, so);
          asm volatile("" ::: "memory"); __builtin_amdgcn_s_waitcnt(0xc07f);
          if (c != 7) __builtin_amdgcn_s_barrier();
          asm volatile("" ::: "memory");
          so = so == 2 * GCH ? 0 : so + GCH;
        }
        LDSP(const float) sx = (LDSP(const float))((LDSP(char))smem + L_ST) + j * 64 + c16;
        const float rs0 = sx[0], rs1 = sx[16], m0 = -sx[32], m1 = -sx[48];
        f32x4 c4[14];
#pragma unroll
        for (int i = 0; i < 14; ++i) c4[i] = *(LDSP(const f32x4))((LDSP(const char))smem + L_CS + i * 64 + g4 * 16);
        const char* ob = wl + OFF_H + ((size_t)(gtb * 4 + j) * KSF + gnb * 7) * 1024;
        unsigned oo = (((g4 & 1) * 32) + (g4 >> 1) * 16 + c16) * 16;
        asm volatile("" : "+s"(ob)); PIN(oo);
        MM(1, 1, 0); MM(1, 1, 1); MM(1, 1, 2); MM(1, 1, 3); MM(1, 1, 4); MM(1, 1, 5); MM(1, 1, 6);
#undef RDX
#undef RDW
#undef M1
#undef MM
#undef GSTEP
        PSTAMP(6);
        const f32x4 vm0 = {m0, m0, m0, m0}, vm1 = {m1, m1, m1, m1};
#define HOUT(i) do { \
          const f32x4 ga = __builtin_elementwise_fma(c4[2 * (i)], vm0, ac[0][2 * (i)]), ua = __builtin_elementwise_fma(c4[2 * (i) + 1], vm0, ac[0][2 * (i) + 1]) * rs0; \
          const f32x4 gb = __builtin_elementwise_fma(c4[2 * (i)], vm1, ac[1][2 * (i)]), ub = __builtin_elementwise_fma(c4[2 * (i) + 1], vm1, ac[1][2 * (i) + 1]) * rs1; \
          const f32x4 h0 = __builtin_elementwise_max(ga, f32x4{}) * ua, h1 = __builtin_elementwise_max(gb, f32x4{}) * ub; \
          auto e0 = __builtin_amdgcn_permlane32_swap(pk2(h0[0], h0[1]), pk2(h1[0], h1[1]), false, false); \
          auto e1 = __builtin_amdgcn_permlane32_swap(pk2(h0[2], h0[3]), pk2(h1[2], h1[3]), false, false); \
          const u32x4 a_ = {e0[0], e1[0], e0[1], e1[1]}; \
          st16s<((i) & 3) * 1024>(oo, ob + ((i) >> 2) * 4096, a_); } while (0)
        HOUT(0); HOUT(1); HOUT(2); HOUT(3); HOUT(4); HOUT(5); HOUT(6);
#undef HOUT
        PSTAMP(13);
        if (lane == 0) __hip_atomic_fetch_add(lcnt + 3, 1u, __ATOMIC_RELAXED, __HIP_MEMORY_SCOPE_WORKGROUP);
        arrive4(lcnt, cnt + (C_H + layer * 16 + gtb) * 1024, lane);
        PSTAMP(7);
      }
      WAITBAR_L();
      if (layer == 0) {
        FRESH();
        __builtin_amdgcn_s_barrier();
        { const char* wb = ws + OFF_WQ + (size_t)qnb * 8 * QW + j * 3072;
#pragma unroll 1
          for (int c = 0; c < 4; ++c) glds3(voff, wb + c * QW, lds0 + c * QW + j * 3072); }
        colsum_part(ws + OFF_CSP + (size_t)j * 81920 + (3584 + qnb * 96) * 4, voff, lb + L_CS + j * 1024);
        QINIT();
#define RX(B, u) xf[B] = ldfrag(sxp + (u) * 1024)
#define RW(B, i, u) wf[B][i] = ldfrag(swp + (i) * 4096 + (u) * 1024)
        WAITBAR_L();
        PSTAMP(8);
#pragma unroll 1
        for (int c = 0; c < 8; ++c) {
          LDSP(const char) swp = lb + (c & 3) * QW;
          LDSP(const char) sxp = lb + L_QX + (c & 3) * 16384 + j * 4096;
          QCHUNK();
          WAITBAR_L();
        }
        LDSP(const float) cx = (LDSP(const float))((LDSP(char))smem + L_CS);
        LDSP(const float) sx = (LDSP(const float))((LDSP(char))smem + L_ST) + j * 64;
        f32x4 cq[8], ra[4], rb[4];
        const float rstd = sx[r], mr = sx[32 + r], cv = cx[64 + r];
#pragma unroll
        for (int g = 0; g < 8; ++g) cq[g] = *(LDSP(const f32x4))(cx + 8 * g + 4 * hh);
#pragma unroll
        for (int g = 0; g < 4; ++g) { ra[g] = *(LDSP(const f32x4))(sx + 8 * g + 4 * hh); rb[g] = *(LDSP(const f32x4))(sx + 32 + 8 * g + 4 * hh); }
        SBAR();
        QFINISH();
        SBAR();
#undef RX
#undef RW
        PSTAMP(9);
        char* wn = ws + LSTRIDE;
        const int tt = qtb * 4 + j;
        float v[16];
        const float rq = rstd * QSCALE, mq = mr * QSCALE;
#pragma unroll
        for (int e = 0; e < 16; ++e) v[e] = acc[0][e] * rq - mq * cq[e >> 2][e & 3];
        qk_store(v, wn + OFF_Q + ((size_t)(2 * qnb) * NTT + tt) * 1024 + voff);
#pragma unroll
        for (int e = 0; e < 16; ++e) v[e] = acc[1][e] * rstd - mr * cq[4 + (e >> 2)][e & 3];
        qk_store(v, wn + OFF_K + ((size_t)(2 * qnb) * NTT + tt) * 1024 + voff);
#pragma unroll
        for (int e = 0; e < 16; ++e) v[e] = acc[2][e] * ra[e >> 2][e & 3] - rb[e >> 2][e & 3] * cv;
        v_store(v, wn + OFF_V + ((size_t)(2 * qnb + (r >> 4)) * NTT + tt) * 1024 + hh * 256 + (r & 15) * 16);
        arrive4(lcnt, cnt + (C_KV + qnb) * 1024, lane);
        PSTAMP(10);
      }
    } else {
      {
        FRESH();
        bf16x8 w0[8], w1[8], x0[8], x1[8];
        const char* wa = ws + OFF_WO + ((size_t)ont * KSD + j * 8) * 1024;
        const char* wb = ws + OFF_WO + ((size_t)(ont ^ 1) * KSD + j * 8) * 1024;
#define LD4(D, o, base) do { gld16<0>(D[o], voff, base); gld16<1024>(D[(o) + 1], voff, base); gld16<2048>(D[(o) + 2], voff, base); gld16<3072>(D[(o) + 3], voff, base); } while (0)
        __builtin_amdgcn_s_barrier();
        LD4(w0, 0, wa); LD4(w0, 4, wa + 4096); LD4(w1, 0, wb); LD4(w1, 4, wb + 4096);
        u32x2 rn[4]; unsigned res[8];
        res_load(rn, layer ? voff : r * 1024 + hh * 8, (layer ? ws + OFF_X3 + ont * 2048 : karg(1) + ont * 64) + (size_t)ott * 32768, layer ? 8 : 16, layer ? 1024 : 32);
        const char* xa = wl + OFF_O + ((size_t)ott * KSD + j * 8) * 1024;
        const char* xb = wl + OFF_O + ((size_t)(ott ^ 1) * KSD + j * 8) * 1024;
        asm volatile("" : "+s"(xa), "+s"(xb));
        poll_b(cnt + (C_O + layer * 8 + xcd) * 1024 + (idx & 7) * 128, 32u, lcnt + 1, layer * 4 + 1, j);
        PSTAMP(5);
        #define LD2(s) do { gld16<((s) & 3) * 1024>(x0[s], voff, xa + ((s) >> 2) * 4096); gld16<((s) & 3) * 1024>(x1[s], voff, xb + ((s) >> 2) * 4096); } while (0)
        LD2(0); LD2(1); LD2(2); LD2(3); LD2(4); LD2(5); LD2(6); LD2(7);
#undef LD2
        f32x16 acc[4];
#pragma unroll
        for (int i = 0; i < 4; ++i) acc[i] = f32x16{};
#define MM4(W0, W1, X0, X1) do { acc[1] = MFMA(W0, X1, acc[1]); acc[2] = MFMA(W1, X0, acc[2]); acc[3] = MFMA(W1, X1, acc[3]); acc[0] = MFMA(W0, X0, acc[0]); } while (0)
#define PIN8(n, X, Y, o) asm volatile("s_waitcnt vmcnt(%8)" : "+v"(X[o]), "+v"(X[(o) + 1]), "+v"(X[(o) + 2]), "+v"(X[(o) + 3]), "+v"(Y[o]), "+v"(Y[(o) + 1]), "+v"(Y[(o) + 2]), "+v"(Y[(o) + 3]) : "i"(n) : "memory")
        PIN8(16, w0, w1, 0); PIN8(16, w0, w1, 4);
#define WX(b, n) asm volatile("s_waitcnt vmcnt(%2)" : "+v"(x0[b]), "+v"(x1[b]) : "i"(n) : "memory")
#define ST(s) do { WX(s, 14 - 2 * (s)); MM4(w0[s], w1[s], x0[s], x1[s]); SBAR(); } while (0)
        ST(0); ST(1); ST(2); ST(3); ST(4); ST(5); ST(6); ST(7);
#undef ST
#undef WX
        PSTAMP(6);
        RES_FINISH((i - 1) * GCH + GW + (j ^ i) * 4096, (i - 1) * GCH + GW + j * 4096);
        PSTAMP(13);
        res_store_tiled(res, wl + OFF_X2 + ((size_t)ott * KSD + 2 * ont) * 1024 + voff, wl + OFF_ST2 + (size_t)ott * 4096 + ont * 256 + r * 8, lane);
        PSTAMP(14);
        arrive4(lcnt, cnt + (C_X2 + layer * 16 + grp) * 1024, lane);
        PSTAMP(7);
      }
      {
        FRESH();
        { LDSP(char) cp = (LDSP(char))smem + L_CS + j * 256 + lane * 4;
          *(LDSP(float))cp = (*(LDSP(float))cp + *(LDSP(float))(cp + 1024)) + (*(LDSP(float))(cp + 2048) + *(LDSP(float))(cp + 3072)); }
        bf16x8 w0[12], w1[12], x0[8], x1[8];
        const char* wa = ws + OFF_WD + ((size_t)ont * KSF + j * 28) * 1024;
        const char* wb = ws + OFF_WD + ((size_t)(ont ^ 1) * KSF + j * 28) * 1024;
        {
          const int tt = gtb * 4 + j;
          const char* gx = wl + OFF_X2 + (size_t)tt * 32768;
          const char* gw = ws + OFF_WGU + (size_t)gnb * 8 * GW + j * 7168;
          f32x4 sp[4];
          asm volatile("" : "+s"(gx), "+s"(gw));
          poll_b(cnt + (C_X2 + layer * 16 + gtb) * 1024 + (gnb & 7) * 128, 16u, lcnt + 1, layer * 4 + 2, j);
          PSTAMP(8);
          __builtin_amdgcn_s_waitcnt(0xc07f);
#define GISSUE(c, d) do { glds4(voff, gx + (c) * 4096, (d) + GW + j * 4096); glds4(voff, gw + (c) * GW, (d) + j * 7168); glds3(voff, gw + (c) * GW + 4096, (d) + j * 7168 + 4096); } while (0)
          glds4(voff, gx, lds0 + GW + j * 4096);
          glds4(voff, gx + 4096, lds0 + GCH + GW + j * 4096);
          stat_issue(sp, wl + OFF_ST2 + (size_t)tt * 4096, voff);
          WAITBAR_V(8);
          GISSUE(2, lds0 + 2 * GCH);
          LD4(w0, 0, wa); LD4(w1, 0, wb);
          WAITBAR_V(23);
          unsigned d = lds0;
#pragma unroll 1
          for (int c = 3; c < 8; ++c) {
            GISSUE(c, d);
            d = d == lds0 + 2 * GCH ? lds0 : d + GCH;
            WAITBAR_V(11);
          }
#undef GISSUE
          LD4(w0, 4, wa + 4096); LD4(w1, 4, wb + 4096); LD4(w0, 8, wa + 8192); LD4(w1, 8, wb + 8192);
          { float mean, var; stat_finish(sp, mean, var);
            ((LDSP(float))((LDSP(char))smem + L_ST))[j * 64 + hh * 32 + (lane & 15) * 2 + ((lane >> 4) & 1)] = hh ? mean : 1.f / var; }
          WAITBAR_V(16);
        }
#define DISSUE(c, P) do { glds4(voff, wa + 12288 + (c) * 4096, lds0 + (P) + j * 8192); glds4(voff, wb + 12288 + (c) * 4096, lds0 + (P) + j * 8192 + 4096); } while (0)
        DISSUE(0, L_D0); DISSUE(1, L_D1); DISSUE(2, L_D2);
#pragma unroll 1
        for (unsigned spins = 0; spins < 4000000u; ++spins) {
          if (__builtin_amdgcn_readfirstlane(__hip_atomic_load(lcnt + 2, __ATOMIC_RELAXED, __HIP_MEMORY_SCOPE_WORKGROUP)) >= 4u * (layer + 1)) break;
          __builtin_amdgcn_s_sleep(1);
        }
        asm volatile("" ::: "memory");
        DISSUE(3, L_D3);
#undef DISSUE
        const char* xa = wl + OFF_H + ((size_t)ott * KSF + j * 28) * 1024;
        const char* xb = wl + OFF_H + ((size_t)(ott ^ 1) * KSF + j * 28) * 1024;
        asm volatile("" : "+s"(xa), "+s"(xb));
        poll_b(cnt + (C_H + layer * 16 + grp) * 1024 + (idx & 7) * 128, 16u, lcnt + 1, layer * 4 + 3, j);
        PSTAMP(9);
        PIN8(0, w0, w1, 0); PIN8(0, w0, w1, 4); PIN8(0, w0, w1, 8);
#define LDX(b, blk) do { gld16<((b) & 3) * 1024>(x0[b], voff, xa + (blk) * 4096); gld16<((b) & 3) * 1024>(x1[b], voff, xb + (blk) * 4096); } while (0)
#define WX(b, n) asm volatile("s_waitcnt vmcnt(%2)" : "+v"(x0[b]), "+v"(x1[b]) : "i"(n) : "memory")
        LDX(0, 0); LDX(1, 0); LDX(2, 0); LDX(3, 0); LDX(4, 1); LDX(5, 1); LDX(6, 1); LDX(7, 1);
        f32x16 acc[4];
#pragma unroll
        for (int i = 0; i < 4; ++i) acc[i] = f32x16{};
        LDSP(const char) lw = lb + j * 8192;
#define BLK(o, h, nb, n0) do { _Pragma("unroll") for (int s = 0; s < 1; ++s) {} \
          WX((h) + 0, n0); MM4(w0[(o) + 0], w1[(o) + 0], x0[(h) + 0], x1[(h) + 0]); SBAR(); if ((nb) >= 0) LDX((h) + 0, nb); SBAR(); \
          WX((h) + 1, (nb) >= 0 ? n0 : n0 - 2); MM4(w0[(o) + 1], w1[(o) + 1], x0[(h) + 1], x1[(h) + 1]); SBAR(); if ((nb) >= 0) LDX((h) + 1, nb); SBAR(); \
          WX((h) + 2, (nb) >= 0 ? n0 : n0 - 4); MM4(w0[(o) + 2], w1[(o) + 2], x0[(h) + 2], x1[(h) + 2]); SBAR(); if ((nb) >= 0) LDX((h) + 2, nb); SBAR(); \
          WX((h) + 3, (nb) >= 0 ? n0 : n0 - 6); MM4(w0[(o) + 3], w1[(o) + 3], x0[(h) + 3], x1[(h) + 3]); SBAR(); if ((nb) >= 0) LDX((h) + 3, nb); SBAR(); } while (0)
#define RDL(o, c) do { _Pragma("unroll") for (int s = 0; s < 4; ++s) { w0[(o) + s] = ldfrag(lw + (c) + s * 1024); w1[(o) + s] = ldfrag(lw + (c) + 4096 + s * 1024); } SBAR(); } while (0)
        BLK(0, 0, 2, 14); RDL(0, L_D0);
        BLK(4, 4, 3, 14); RDL(4, L_D1);
        BLK(8, 0, 4, 14); RDL(8, L_D2);
        BLK(0, 4, 5, 14); RDL(0, L_D3);
        BLK(4, 0, 6, 14);
        u32x2 rn[4]; unsigned res[8];
        res_load(rn, voff, wl + OFF_X2 + (size_t)ott * 32768 + ont * 2048, 8, 1024);
        BLK(8, 4, -1, 18);
        BLK(0, 0, -1, 10);
#undef RDL
#undef BLK
#undef LDX
#undef WX
        PSTAMP(10);
        RES_FINISH((i == 3 ? L_D1 : L_D0 + (i - 1) * 4096) + j * 8192, (i == 3 ? L_D1 : L_D0 + (i - 1) * 4096) + (j ^ i) * 8192);
        if (layer == 0) {
          res_store_tiled(res, ws + OFF_X3 + ((size_t)ott * KSD + 2 * ont) * 1024 + voff, ws + OFF_ST3 + (size_t)ott * 4096 + ont * 256 + r * 8, lane);
          arrive4(lcnt, cnt + (C_X3 + grp) * 1024, lane);
        } else {
          auto s0 = __builtin_amdgcn_permlane32_swap(res[0], res[2], false, false);
          auto s1 = __builtin_amdgcn_permlane32_swap(res[1], res[3], false, false);
          auto s2 = __builtin_amdgcn_permlane32_swap(res[4], res[6], false, false);
          auto s3 = __builtin_amdgcn_permlane32_swap(res[5], res[7], false, false);
          const u32x4 a = {s0[0], s1[0], s0[1], s1[1]}, b = {s2[0], s3[0], s2[1], s3[1]};
          char* o = (char*)karg(7) + ((size_t)(ott * 32 + r) * D_ + ont * 32) * 2 + hh * 16;
          st16(o, a);
          st16(o + 32, b);
        }
        PSTAMP(11);
      }
#undef LD4
#undef MM4
#undef PIN8
      if (layer == 0) {
        FRESH();
        const int tt = qtb * 4 + j;
        const char* xb = ws + OFF_X3 + (size_t)tt * 32768;
        const char* wb = ws + OFF_WQ + (size_t)qnb * 8 * QW + j * 3072;
        f32x4 sp[4];
        asm volatile("" : "+s"(xb), "+s"(wb));
        __builtin_amdgcn_s_barrier();
        poll_b(cnt + (C_X3 + qtb) * 1024 + (qnb & 7) * 128, 16u, lcnt + 1, 4, j);
        PSTAMP(12);
#define QISSUE(c) do { glds4(voff, xb + (c) * 4096, lds0 + L_QX + ((c) & 3) * 16384 + j * 4096); glds3(voff, wb + (c) * QW, lds0 + ((c) & 3) * QW + j * 3072); } while (0)
        glds4(voff, xb, lds0 + L_QX + j * 4096);
        glds4(voff, xb + 4096, lds0 + L_QX + 16384 + j * 4096);
        stat_issue(sp, ws + OFF_ST3 + (size_t)tt * 4096, voff);
        glds4(voff, xb + 8192, lds0 + L_QX + 32768 + j * 4096);
        glds4(voff, xb + 12288, lds0 + L_QX + 49152 + j * 4096);
        WAITBAR_V(16);
        { LDSP(char) cp = (LDSP(char))smem + L_CS + j * 256 + lane * 4;
          *(LDSP(float))cp = (*(LDSP(float))cp + *(LDSP(float))(cp + 1024)) + (*(LDSP(float))(cp + 2048) + *(LDSP(float))(cp + 3072)); }
        WAITBAR_V(12);
        QISSUE(4); WAITBAR_V(11);
#pragma unroll 1
        for (int c = 5; c < 8; ++c) { QISSUE(c); WAITBAR_V(14); }
#undef QISSUE
        { float mean, var; stat_finish(sp, mean, var); const float rs = rsqrtf(var);
          ((LDSP(float))((LDSP(char))smem + L_ST))[j * 64 + hh * 32 + (lane & 15) * 2 + ((lane >> 4) & 1)] = hh ? mean * rs : rs; }
        WAITBAR_V(7);
        WAITBAR_V(0);
        __builtin_amdgcn_s_barrier();
      }
    }
  }
}

extern "C" void kernel_launch(void* const* d_in, const int* in_sizes, int n_in, void* d_out, int out_size, void* d_ws, size_t ws_size,
                              hipStream_t stream) {
  const char* x = (const char*)d_in[0];
  const char* wqkv = (const char*)d_in[1];
  const char* wo = (const char*)d_in[2];
  const char* wg = (const char*)d_in[4];
  const char* wu = (const char*)d_in[5];
  const char* wd = (const char*)d_in[6];
  char* ws = (char*)d_ws;
  k_qkv1<<<256, 768, 0, stream>>>(x, wqkv, ws DIAG_PASS(0));
  k_main<<<256, 512, 0, stream>>>(ws, x, wqkv, wo, wg, wu, wd, (char*)d_out DIAG_PASS(1));
}
```

```cpp
#include <hip/hip_runtime.h>
#include <hip/hip_bf16.h>
#include <stdint.h>

typedef short bf16x8 __attribute__((ext_vector_type(8)));
typedef float f32x16 __attribute__((ext_vector_type(16)));
typedef float f32x4 __attribute__((ext_vector_type(4)));
typedef float f32x2 __attribute__((ext_vector_type(2)));
typedef unsigned u32x4 __attribute__((ext_vector_type(4)));
typedef unsigned u32x2 __attribute__((ext_vector_type(2)));
typedef __bf16 bf16v2 __attribute__((ext_vector_type(2)));
typedef unsigned short u16;

#define DI __device__ __forceinline__
#define MFMA(a, b, c) __builtin_amdgcn_mfma_f32_32x32x16_bf16((a), (b), (c), 0, 0, 0)
#define LDSP(T) __attribute__((address_space(3))) T*

constexpr int S_ = 2048, D_ = 512, H_ = 32, F_ = 1792;
constexpr int NTT = S_ / 32;
constexpr int KSD = D_ / 16;
constexpr int KSF = F_ / 16;
constexpr float QSCALE = 0.25f * 1.4426950408889634f;
constexpr float LN_EPS = 1e-5f;

constexpr size_t KB = 1024;
constexpr size_t OFF_CNT = 0;
constexpr size_t OFF_WO = 128 * KB;
constexpr size_t OFF_WGU = OFF_WO + 512 * KB;
constexpr size_t OFF_WD = OFF_WGU + 3584 * KB;
constexpr size_t OFF_WQ = OFF_WD + 1792 * KB;
constexpr size_t OFF_CSP = OFF_WQ + 1536 * KB;
constexpr size_t OFF_ST3 = OFF_CSP + 320 * KB;
constexpr size_t OFF_X3 = OFF_ST3 + 256 * KB;
constexpr size_t OFF_L = OFF_X3 + 2048 * KB;
constexpr size_t OFF_Q = OFF_L;
constexpr size_t OFF_K = OFF_Q + 2048 * KB;
constexpr size_t OFF_V = OFF_K + 2048 * KB;
constexpr size_t OFF_O = OFF_V + 2048 * KB;
constexpr size_t OFF_X2 = OFF_O + 2048 * KB;
constexpr size_t OFF_ST2 = OFF_X2 + 2048 * KB;
constexpr size_t OFF_H = OFF_ST2 + 256 * KB;
constexpr size_t LSTRIDE = OFF_H + 7168 * KB - OFF_L;
constexpr int KMAIN_KB = 20;
constexpr int C_O = 0, C_X2 = 16, C_H = 48, C_X3 = 80, C_KV = 96, C_PREP = 112, C_N = 128;

DI unsigned pk2(float lo, float hi) {
  f32x2 v = {lo, hi};
  bf16v2 r = __builtin_convertvector(v, bf16v2);
  return __builtin_bit_cast(unsigned, r);
}
DI float lo16(unsigned u) { return __uint_as_float(u << 16); }
DI float hi16(unsigned u) { return __uint_as_float(u & 0xffff0000u); }
DI float xhalf_add(float v) {
  auto r = __builtin_amdgcn_permlane32_swap(__float_as_uint(v), __float_as_uint(v), false, false);
  return __uint_as_float(r[0]) + __uint_as_float(r[1]);
}
DI float xhalf_max(float v) {
  auto r = __builtin_amdgcn_permlane32_swap(__float_as_uint(v), __float_as_uint(v), false, false);
  return fmaxf(__uint_as_float(r[0]), __uint_as_float(r[1]));
}
DI float x16_add(float v) {
  auto r = __builtin_amdgcn_permlane16_swap(__float_as_uint(v), __float_as_uint(v), false, false);
  return __uint_as_float(r[0]) + __uint_as_float(r[1]);
}
DI void st16(void* p, u32x4 v) { asm volatile("global_store_dwordx4 %0, %1, off sc1\n\ts_nop 1" :: "v"(p), "v"(v) : "memory"); }
template <int IMM>
DI void st16s(unsigned voff, const char* sbase, u32x4 v) {
  asm volatile("global_store_dwordx4 %0, %1, %2 offset:%3 sc1\n\ts_nop 1" :: "v"(voff), "v"(v), "s"(sbase), "i"(IMM) : "memory");
}
template <int IMM>
DI void gld16(bf16x8& d, unsigned voff, const char* sbase) {
  asm volatile("global_load_dwordx4 %0, %1, %2 offset:%3 sc1" : "=v"(d) : "v"(voff), "s"(sbase), "i"(IMM) : "memory");
}
#define GLDS_HEAD "s_mov_b32 %0, m0\n\ts_mov_b32 m0, %3\n\ts_nop 0\n\t"
#define GLDS_OP(o) "global_load_lds_dwordx4 %1, %2 offset:" #o " sc1\n\t"
#define GLDS_TAIL "s_mov_b32 m0, %0" : "=&s"(keep) : "v"(voff), "s"(sbase), "s"(lds) : "memory"
DI void glds1(unsigned voff, const char* sbase, unsigned lds) { unsigned keep; asm volatile(GLDS_HEAD GLDS_OP(0) GLDS_TAIL); }
DI void glds2(unsigned voff, const char* sbase, unsigned lds) { unsigned keep; asm volatile(GLDS_HEAD GLDS_OP(0) GLDS_OP(1024) GLDS_TAIL); }
DI void glds3(unsigned voff, const char* sbase, unsigned lds) { unsigned keep; asm volatile(GLDS_HEAD GLDS_OP(0) GLDS_OP(1024) GLDS_OP(2048) GLDS_TAIL); }
DI void glds4(unsigned voff, const char* sbase, unsigned lds) { unsigned keep; asm volatile(GLDS_HEAD GLDS_OP(0) GLDS_OP(1024) GLDS_OP(2048) GLDS_OP(3072) GLDS_TAIL); }
#define SBAR() __builtin_amdgcn_sched_barrier(0)
#define PIN(x) asm volatile("" : "+v"(x))
#define WAITV(n) asm volatile("s_waitcnt vmcnt(%0)" :: "i"(n) : "memory")
#define WAITBAR_V(n) asm volatile("s_waitcnt vmcnt(%0) lgkmcnt(0)\n\ts_barrier" :: "i"(n) : "memory")
#define WAITBAR_L() do { asm volatile("" ::: "memory"); __builtin_amdgcn_s_waitcnt(0xc07f); __builtin_amdgcn_s_barrier(); asm volatile("" ::: "memory"); } while (0)
typedef short s16x4 __attribute__((ext_vector_type(4)));
DI s16x4 trread(LDSP(const char) p) { return __builtin_amdgcn_ds_read_tr16_b64_v4i16((LDSP(s16x4))p); }
DI bf16x8 trfrag(LDSP(const char) p, int off2) { const s16x4 lo = trread(p), hi = trread(p + off2); return __builtin_shufflevector(lo, hi, 0, 1, 2, 3, 4, 5, 6, 7); }
DI bf16x8 ldfrag(LDSP(const char) p) { return *(LDSP(const bf16x8))p; }
DI float dot2(unsigned a, unsigned b, float c) { return __builtin_amdgcn_fdot2_f32_bf16(__builtin_bit_cast(bf16v2, a), __builtin_bit_cast(bf16v2, b), c, false); }
constexpr unsigned ONE2 = 0x3f803f80u;
#define BC8(x) __builtin_bit_cast(bf16x8, x)

template <int LOOK, int PER> DI void ring_wait(int i) {
  if (i <= 8 - LOOK) WAITBAR_V((LOOK - 1) * PER);
  else if (LOOK > 3 && i == 5) WAITBAR_V(2 * PER);
  else if (LOOK > 2 && i == 6) WAITBAR_V(PER);
  else WAITBAR_V(0);
}

DI void qk_store(const float (&v)[16], char* o) {
  u32x4 a = {pk2(v[0], v[1]), pk2(v[2], v[3]), pk2(v[4], v[5]), pk2(v[6], v[7])};
  u32x4 b = {pk2(v[8], v[9]), pk2(v[10], v[11]), pk2(v[12], v[13]), pk2(v[14], v[15])};
  st16(o, a);
  st16(o + NTT * 1024, b);
}
DI void v_store(const float (&v)[16], char* o) {
  auto s0 = __builtin_amdgcn_permlane32_swap(pk2(v[0], v[1]), pk2(v[4], v[5]), false, false);
  auto s1 = __builtin_amdgcn_permlane32_swap(pk2(v[2], v[3]), pk2(v[6], v[7]), false, false);
  auto s2 = __builtin_amdgcn_permlane32_swap(pk2(v[8], v[9]), pk2(v[12], v[13]), false, false);
  auto s3 = __builtin_amdgcn_permlane32_swap(pk2(v[10], v[11]), pk2(v[14], v[15]), false, false);
  const u32x4 a = {s0[0], s1[0], s0[1], s1[1]}, b = {s2[0], s3[0], s2[1], s3[1]};
  st16(o, a);
  st16(o + 512, b);
}
typedef __bf16 bfv8 __attribute__((ext_vector_type(8)));
typedef __bf16 bfv16 __attribute__((ext_vector_type(16)));
typedef float f32x8 __attribute__((ext_vector_type(8)));
template <int H> DI f32x16 smf(bf16x8 a, const f32x16& s, f32x16 c, int idx) {
  const f32x8 p = __builtin_shufflevector(s, s, 8 * H, 8 * H + 1, 8 * H + 2, 8 * H + 3, 8 * H + 4, 8 * H + 5, 8 * H + 6, 8 * H + 7);
  return __builtin_amdgcn_smfmac_f32_32x32x32_bf16(__builtin_bit_cast(bfv8, a), __builtin_bit_cast(bfv16, p), c, idx, 0, 0);
}
DI float row16_sum(float v) {
  v += __builtin_bit_cast(float, __builtin_amdgcn_update_dpp(0, __builtin_bit_cast(int, v), 0x128, 0xf, 0xf, false));
  v += __builtin_bit_cast(float, __builtin_amdgcn_update_dpp(0, __builtin_bit_cast(int, v), 0x124, 0xf, 0xf, false));
  v += __builtin_bit_cast(float, __builtin_amdgcn_update_dpp(0, __builtin_bit_cast(int, v), 0x122, 0xf, 0xf, false));
  v += __builtin_bit_cast(float, __builtin_amdgcn_update_dpp(0, __builtin_bit_cast(int, v), 0x121, 0xf, 0xf, false));
  return v;
}
#define KSTEP(B, u) do { SBAR(); RX((B) ^ 1, u); RW((B) ^ 1, 0, u); SBAR(); acc[0] = MFMA(wf[B][0], xf[B], acc[0]); SBAR(); RW((B) ^ 1, 1, u); RW((B) ^ 1, 2, u); SBAR(); \
                         acc[1] = MFMA(wf[B][1], xf[B], acc[1]); acc[2] = MFMA(xf[B], wf[B][2], acc[2]); SBAR(); } while (0)
#define QCHUNK() do { KSTEP(1, 0); KSTEP(0, 1); KSTEP(1, 2); KSTEP(0, 3); } while (0)
#define QINIT() \
  f32x16 acc[3]; bf16x8 wf[2][3], xf[2]; \
  _Pragma("unroll") for (int i = 0; i < 3; ++i) { acc[i] = f32x16{}; wf[1][i] = bf16x8{}; } \
  xf[1] = bf16x8{}
#define QFINISH() do { acc[0] = MFMA(wf[1][0], xf[1], acc[0]); acc[1] = MFMA(wf[1][1], xf[1], acc[1]); acc[2] = MFMA(xf[1], wf[1][2], acc[2]); } while (0)

extern "C" __global__ __launch_bounds__(768, 3) void k_qkv1(const char* __restrict__ x, const char* __restrict__ wn, char* __restrict__ ws) {
  constexpr int LOOK = 3, CH = 24576, LXN = 4 * CH;
  __shared__ __attribute__((aligned(16))) char smem[LXN + 65536];
  const int tid = threadIdx.x, lane = tid & 63, r = lane & 31, hh = lane >> 5;
  const int w = __builtin_amdgcn_readfirstlane(tid >> 6);
  const int xcd = blockIdx.x & 7, idx = blockIdx.x >> 3;
  const int tb = (xcd & 3) * 8 + (idx & 7), nq = (xcd >> 2) * 4 + (idx >> 3);
  const unsigned lds0 = (unsigned)(uintptr_t)smem;
  const int rot = 2 * (idx & 3);
  if (w >= 8) {
    const int j = w - 8;
    const unsigned wv = (lane >> 3) * 3072 + (((lane & 7) ^ (((lane >> 4) & 1) << 2)) * 16);
    const char* wb = wn + (size_t)j * 16 * 3072 + nq * 128;
    if (blockIdx.x < 4) *(unsigned*)(ws + OFF_CNT + ((blockIdx.x * 4 + j) * 64 + lane) * 128) = 0u;
    asm volatile("s_sleep 8\n\ts_nop 4" ::: "memory");
#pragma unroll 1
    for (int c = 0; c < 8 + LOOK; ++c) {
      if (c >= LOOK) ring_wait<LOOK, 6>(c - LOOK);
      if (c < 8) {
        const char* ws_ = wb + (size_t)((c + rot) & 7) * 64 * 3072;
        const unsigned d = lds0 + (c & 3) * CH + j * 2048;
        glds1(wv, ws_, d); glds1(wv, ws_ + 1024, d + 8192); glds1(wv, ws_ + 2048, d + 16384);
        glds1(wv, ws_ + 8 * 3072, d + 1024); glds1(wv, ws_ + 8 * 3072 + 1024, d + 8192 + 1024); glds1(wv, ws_ + 8 * 3072 + 2048, d + 16384 + 1024);
      }
    }
    return;
  }
  {
    const int n = w;
    const u32x4* xr = (const u32x4*)(x + (size_t)(tb * 64 + n * 8) * 1024) + lane;
    u32x4 v[8];
#pragma unroll
    for (int i = 0; i < 8; ++i) v[i] = xr[i * 64];
    float s[8], q[8];
#pragma unroll
    for (int i = 0; i < 8; ++i) {
      s[i] = 0.f; q[i] = 0.f;
#pragma unroll
      for (int e = 0; e < 4; ++e) { s[i] = dot2(v[i][e], ONE2, s[i]); q[i] = dot2(v[i][e], v[i][e], q[i]); }
    }
    float s4[4], q4[4], s2[2], q2[2];
#pragma unroll
    for (int i = 0; i < 4; ++i) {
      const int lo = (i & 1) + 4 * (i >> 1);
      auto a = __builtin_amdgcn_permlane32_swap(__float_as_uint(s[lo]), __float_as_uint(s[lo + 2]), false, false);
      auto b = __builtin_amdgcn_permlane32_swap(__float_as_uint(q[lo]), __float_as_uint(q[lo + 2]), false, false);
      s4[i] = __uint_as_float(a[0]) + __uint_as_float(a[1]); q4[i] = __uint_as_float(b[0]) + __uint_as_float(b[1]);
    }
#pragma unroll
    for (int i = 0; i < 2; ++i) {
      auto a = __builtin_amdgcn_permlane16_swap(__float_as_uint(s4[2 * i]), __float_as_uint(s4[2 * i + 1]), false, false);
      auto b = __builtin_amdgcn_permlane16_swap(__float_as_uint(q4[2 * i]), __float_as_uint(q4[2 * i + 1]), false, false);
      s2[i] = row16_sum(__uint_as_float(a[0]) + __uint_as_float(a[1])); q2[i] = row16_sum(__uint_as_float(b[0]) + __uint_as_float(b[1]));
    }
    float ra[2], rb[2];
#pragma unroll
    for (int i = 0; i < 2; ++i) {
      const float mean = s2[i] * (1.f / D_);
      ra[i] = rsqrtf(fmaxf(q2[i] * (1.f / D_) - mean * mean, 0.f) + LN_EPS);
      rb[i] = -mean * ra[i];
    }
    LDSP(char) xd = (LDSP(char))smem + LXN + (lane >> 3) * 8192 + n * 1024;
#pragma unroll
    for (int i = 0; i < 8; ++i) {
      const float a = __builtin_bit_cast(float, __builtin_amdgcn_readlane(__builtin_bit_cast(int, ra[i >> 2]), (i & 3) * 16));
      const float b = __builtin_bit_cast(float, __builtin_amdgcn_readlane(__builtin_bit_cast(int, rb[i >> 2]), (i & 3) * 16));
      u32x4 o;
#pragma unroll
      for (int e = 0; e < 4; ++e) o[e] = pk2(lo16(v[i][e]) * a + b, hi16(v[i][e]) * a + b);
      *(LDSP(u32x4))(xd + i * 128 + (((lane & 7) ^ ((4 * n + (i >> 1)) & 7)) * 16)) = o;
    }
  }
  WAITBAR_L();
  if (w >= 4) {
    if (w == 4 && idx < KMAIN_KB) {
      bf16x8 t;
      asm volatile("s_getpc_b64 s[20:21]\n\ts_add_u32 s20, s20, k_main@rel32@lo+4\n\ts_addc_u32 s21, s21, k_main@rel32@hi+12\n\t"
                   "global_load_dwordx4 %0, %1, s[20:21]\n\ts_waitcnt vmcnt(0)" : "=v"(t) : "v"(lane * 16 + idx * 1024) : "s20", "s21", "scc", "memory");
    }
    return;
  }
  const int ta = w & 1, hp = w >> 1, q4 = (lane & 15) >> 2, xsw = (r >> 1) & 7;
  LDSP(const char) lw = (LDSP(const char))smem + (8 * hh + q4) * 128 + ((hp ^ (q4 >> 1)) * 64) + ((lane >> 4) & 1) * 32 + (lane & 3) * 8;
  LDSP(const char) lx = (LDSP(const char))smem + LXN + ta * 4096 + r * 128;
  int xo[4];
#pragma unroll
  for (int u = 0; u < 4; ++u) xo[u] = ((2 * u + hh) ^ xsw) * 16;
  QINIT();
#define RX(B, u) xf[B] = ldfrag(sx + xo[u])
#define RW(B, i, u) wf[B][i] = trfrag(sw + (i) * 8192 + (u) * 2048, 512)
#pragma unroll 1
  for (int c = 0; c < 8; ++c) {
    LDSP(const char) sw = lw + (c & 3) * CH;
    LDSP(const char) sx = lx + ((c + rot) & 7) * 8192;
    QCHUNK();
    WAITBAR_L();
  }
  QFINISH();
#undef RX
#undef RW
  const int tt = tb * 2 + ta, nb = nq * 2 + hp;
  float v[16];
#pragma unroll
  for (int e = 0; e < 16; ++e) v[e] = acc[0][e] * QSCALE;
  qk_store(v, ws + OFF_Q + ((size_t)(2 * nb) * NTT + tt) * 1024 + lane * 16);
#pragma unroll
  for (int e = 0; e < 16; ++e) v[e] = acc[1][e];
  qk_store(v, ws + OFF_K + ((size_t)(2 * nb) * NTT + tt) * 1024 + lane * 16);
#pragma unroll
  for (int e = 0; e < 16; ++e) v[e] = acc[2][e];
  v_store(v, ws + OFF_V + ((size_t)(2 * nb + (r >> 4)) * NTT + tt) * 1024 + hh * 256 + (r & 15) * 16);
}

DI void poll(const char* cnt, unsigned voff, unsigned target) {
#pragma unroll 1
  for (unsigned spins = 0; spins < 200000u; ++spins) {
    unsigned v;
    asm volatile("global_load_dword %0, %1, %2 sc1\n\ts_waitcnt vmcnt(0)" : "=v"(v) : "v"(voff), "s"(cnt) : "memory");
    if (__all(v >= target)) break;
  }
}
DI void poll_b(const char* cnt, unsigned target, LDSP(unsigned) go, unsigned seq, int j) {
  if (j == 0) {
    poll(cnt, 0u, target);
    __hip_atomic_store(go, seq, __ATOMIC_RELAXED, __HIP_MEMORY_SCOPE_WORKGROUP);
  } else {
    WAITV(0);
#pragma unroll 1
    for (unsigned spins = 0; spins < 4000000u; ++spins) {
      if (__builtin_amdgcn_readfirstlane(__hip_atomic_load(go, __ATOMIC_RELAXED, __HIP_MEMORY_SCOPE_WORKGROUP)) >= seq) break;
    }
  }
  asm volatile("" ::: "memory");
}
DI void arrive4(LDSP(unsigned) lw, char* cnt, int lane) {
  WAITV(0);
  unsigned old = 0;
  if (lane == 0) old = __hip_atomic_fetch_add(lw, 1u, __ATOMIC_RELAXED, __HIP_MEMORY_SCOPE_WORKGROUP);
  old = __builtin_amdgcn_readfirstlane(old);
  if ((old & 3u) == 3u && lane < 8) asm volatile("global_atomic_add %0, %1, %2" :: "v"(lane * 128), "v"(1u), "s"(cnt) : "memory");
  asm volatile("" ::: "memory");
}
DI void stat_issue(f32x4 (&p)[4], const char* sb, unsigned voff) {
  asm volatile("global_load_dwordx4 %0, %4, %5 sc1\n\tglobal_load_dwordx4 %1, %4, %5 offset:1024 sc1\n\tglobal_load_dwordx4 %2, %4, %5 offset:2048 sc1\n\t"
               "global_load_dwordx4 %3, %4, %5 offset:3072 sc1" : "=&v"(p[0]), "=&v"(p[1]), "=&v"(p[2]), "=&v"(p[3]) : "v"(voff), "s"(sb) : "memory");
}
DI void stat_finish(f32x4 (&p)[4], float& mean, float& var) {
  asm volatile("" : "+v"(p[0]), "+v"(p[1]), "+v"(p[2]), "+v"(p[3]));
  const f32x4 t = (p[0] + p[1]) + (p[2] + p[3]);
  auto a = __builtin_amdgcn_permlane16_swap(__float_as_uint(t[0]), __float_as_uint(t[2]), false, false);
  auto b = __builtin_amdgcn_permlane16_swap(__float_as_uint(t[1]), __float_as_uint(t[3]), false, false);
  mean = xhalf_add(__uint_as_float(a[0]) + __uint_as_float(a[1])) * (1.f / D_);
  var = fmaxf(xhalf_add(__uint_as_float(b[0]) + __uint_as_float(b[1])) * (1.f / D_) - mean * mean, 0.f) + LN_EPS;
}
DI void colsum_part(const char* src, unsigned voff, LDSP(char) dst) {
  f32x4 p[4];
  asm volatile("global_load_dwordx4 %0, %4, %5 sc1\n\tglobal_load_dwordx4 %1, %4, %6 sc1\n\tglobal_load_dwordx4 %2, %4, %7 sc1\n\t"
               "global_load_dwordx4 %3, %4, %8 sc1\n\ts_waitcnt vmcnt(0)" : "=&v"(p[0]), "=&v"(p[1]), "=&v"(p[2]), "=&v"(p[3])
               : "v"(voff), "s"(src), "s"(src + 20480), "s"(src + 40960), "s"(src + 61440) : "memory");
  *(LDSP(f32x4))dst = (p[0] + p[1]) + (p[2] + p[3]);
}

#define RES_FINISH(WR, RD) do { \
    _Pragma("unroll") for (int i = 1; i < 4; ++i) _Pragma("unroll") for (int g = 0; g < 4; ++g) { \
      const f32x4 v_ = {acc[i][4 * g], acc[i][4 * g + 1], acc[i][4 * g + 2], acc[i][4 * g + 3]}; \
      *(LDSP(f32x4))(lb + (WR) + g * 1024) = v_; } \
    asm volatile("s_waitcnt vmcnt(0)" : "+v"(rn[0]), "+v"(rn[1]), "+v"(rn[2]), "+v"(rn[3]) :: "memory"); \
    WAITBAR_L(); \
    _Pragma("unroll") for (int g = 0; g < 4; ++g) { \
      f32x4 t_ = {acc[0][4 * g], acc[0][4 * g + 1], acc[0][4 * g + 2], acc[0][4 * g + 3]}; \
      f32x4 u_[3]; \
      _Pragma("unroll") for (int i = 1; i < 4; ++i) u_[i - 1] = *(LDSP(const f32x4))(lb + (RD) + g * 1024); \
      t_ = (u_[0] + u_[1]) + (u_[2] + t_); \
      res[2 * g] = pk2(t_[0] + lo16(rn[g][0]), t_[1] + hi16(rn[g][0])); \
      res[2 * g + 1] = pk2(t_[2] + lo16(rn[g][1]), t_[3] + hi16(rn[g][1])); } } while (0)
DI void res_load(u32x2 (&rn)[4], unsigned vo, const char* p0, int d1, int d2) {
  asm volatile("global_load_dwordx2 %0, %4, %5 sc1\n\tglobal_load_dwordx2 %1, %4, %6 sc1\n\tglobal_load_dwordx2 %2, %4, %7 sc1\n\tglobal_load_dwordx2 %3, %4, %8 sc1"
               : "=&v"(rn[0]), "=&v"(rn[1]), "=&v"(rn[2]), "=&v"(rn[3]) : "v"(vo), "s"(p0), "s"(p0 + d1), "s"(p0 + d2), "s"(p0 + d2 + d1) : "memory");
}
DI void res_store_tiled(const unsigned (&res)[8], char* o, char* st, int lane) {
  const u32x4 p0 = {res[0], res[1], res[2], res[3]}, p1 = {res[4], res[5], res[6], res[7]};
  st16(o, p0);
  st16(o + 1024, p1);
  float s1 = 0.f, s2 = 0.f, t1 = 0.f, t2 = 0.f;
#pragma unroll
  for (int e = 0; e < 4; ++e) { s1 = dot2(res[e], ONE2, s1); s2 = dot2(res[e], res[e], s2); t1 = dot2(res[4 + e], ONE2, t1); t2 = dot2(res[4 + e], res[4 + e], t2); }
  s1 = xhalf_add(s1 + t1); s2 = xhalf_add(s2 + t2);
  if (lane < 32) { const f32x2 v = {s1, s2}; asm volatile("global_store_dwordx2 %0, %1, off sc1" :: "v"(st), "v"(v) : "memory"); }
}

DI const char* karg(int i) {
  const __attribute__((address_space(4))) unsigned long long* ka = (const __attribute__((address_space(4))) unsigned long long*)__builtin_amdgcn_kernarg_segment_ptr();
  asm volatile("" : "+s"(ka));
  return (const char*)ka[i];
}
extern "C" __global__ __launch_bounds__(512, 2) void k_main(char* __restrict__ ws_, const char* x_, const char* wq_, const char* wo_, const char* wg_,
                                                            const char* wu_, const char* wd_, char* out_) {
  constexpr int L_K = 0, L_V = 65536, L_ONE = 131072, L_CMB = 133120;
  constexpr int GW = 28672, GCH = GW + 16384;
  constexpr int L_D0 = 2 * GCH, L_D1 = L_D0 + 32768, L_D2 = 0, L_D3 = GCH;
  constexpr int QW = 12288, L_QX = 49152;
  constexpr int L_CS = 157696, L_ST = L_CS + 4096, L_CNT = L_ST + 1024;
  __shared__ __attribute__((aligned(16))) char smem[163840];
  const int w = __builtin_amdgcn_readfirstlane(threadIdx.x >> 6);
  const unsigned lds0 = (unsigned)(uintptr_t)smem;
#define FRESH() \
  int tid = threadIdx.x; PIN(tid); int bid = blockIdx.x, j = w & 3; char* ws = ws_; asm volatile("" : "+s"(bid), "+s"(j), "+s"(ws)); \
  const int lane = tid & 63, r = lane & 31, hh = lane >> 5, xcd = bid & 7, idx = bid >> 3, grp = xcd * 2 + (idx >> 4), m = idx & 15; \
  const unsigned voff = lane * 16; LDSP(char) lb = (LDSP(char))smem + lane * 16; char* cnt = ws + OFF_CNT; char* wl = ws + layer * LSTRIDE; \
  LDSP(unsigned) lcnt = (LDSP(unsigned))((LDSP(char))smem + L_CNT) + (w >> 2); \
  const int ont = (m >> 1) * 2 + (j >> 1), ott = grp * 4 + (m & 1) * 2 + (j & 1); \
  const int gtb = (xcd & 1) * 8 + (idx & 7), gnb = (xcd >> 1) * 4 + (idx >> 3), qtb = (xcd & 3) * 4 + (idx & 3), qnb = (xcd >> 2) * 8 + (idx >> 2); \
  (void)r; (void)hh; (void)voff; (void)lb; (void)cnt; (void)wl; (void)lcnt; (void)ont; (void)ott; (void)gtb; (void)gnb; (void)qtb; (void)qnb
  if (threadIdx.x < 8) ((LDSP(unsigned))((LDSP(char))smem + L_CNT))[threadIdx.x] = 0u;
#pragma unroll 1
  for (int layer = 0; layer < 2; ++layer) {
    {
      FRESH();
      const int head = xcd * 4 + (idx & 3), qb = idx >> 2, half = w >> 2, tt0 = qb * 8 + 2 * j;
      if (layer) { if (w == 4) poll(cnt + (C_KV + (head >> 1)) * 1024 + qb * 128, 0u, 16u); WAITBAR_L(); }
      const char* ks = wl + OFF_K + (size_t)head * 65536 + half * 32768;
      const unsigned kd = lds0 + L_K + half * 32768;
      bf16x8 q0, q1;
      gld16<0>(q0, voff, wl + OFF_Q + ((size_t)head * NTT + tt0) * 1024);
      gld16<1024>(q1, voff, wl + OFF_Q + ((size_t)head * NTT + tt0) * 1024);
      glds2(voff, ks + j * 2048, kd + j * 2048);
      glds2(voff, ks + (OFF_V - OFF_K) + j * 2048, kd + L_V + j * 2048);
      *(LDSP(unsigned))((LDSP(char))smem + L_ONE + tid * 4) = ((tid >> 2) & 11) == 0 ? ONE2 : 0u;
      asm volatile("s_waitcnt vmcnt(0) lgkmcnt(0)\n\ts_barrier" : "+v"(q0), "+v"(q1) :: "memory");
      LDSP(const char) kp = lb + L_K;
      LDSP(const char) vp0 = (LDSP(const char))smem + (r < 16 ? L_V : L_ONE) + hh * 256 + (r & 15) * 16;
      const int vstep = r < 16 ? 2048 : 0;
      LDSP(const char) kq = kp + half * 32768; LDSP(const char) vq = vp0 + half * 16 * vstep;
      f32x16 n0, n1, sA, sB = f32x16{}, o0 = f32x16{}, o1 = f32x16{};
      bf16x8 kA, kB, vp = bf16x8{};
      int sidx = (int)0xDDDDDDDDu; PIN(sidx);
      {
        const bf16x8 k0 = ldfrag(kp);
        kA = ldfrag(kq);
        const f32x16 s = MFMA(k0, q0, f32x16{}), t = MFMA(k0, q1, f32x16{});
        float a = fmaxf(fmaxf(s[0], s[1]), s[2]), b = fmaxf(fmaxf(t[0], t[1]), t[2]);
#pragma unroll
        for (int i = 3; i < 15; i += 2) { a = fmaxf(fmaxf(a, s[i]), s[i + 1]); b = fmaxf(fmaxf(b, t[i]), t[i + 1]); }
        a = xhalf_max(fmaxf(a, s[15])); b = xhalf_max(fmaxf(b, t[15]));
#pragma unroll
        for (int i = 0; i < 16; ++i) { n0[i] = -a; n1[i] = -b; }
        PIN(n0); PIN(n1);
        sA = MFMA(kA, q0, n0);
      }
#define EX(S, i) S[i] = __builtin_amdgcn_exp2f(S[i])
#define STEPN(CUR, NXT, KN_, QN, NN, O, VA, OP, VP, READS) do { \
      EX(CUR, 0); EX(CUR, 1); PIN(CUR); SBAR(); OP = smf<1>(VP, NXT, OP, sidx); SBAR(); READS; SBAR(); \
      EX(CUR, 2); EX(CUR, 3); EX(CUR, 4); EX(CUR, 5); PIN(CUR); SBAR(); NXT = MFMA(KN_, QN, NN); SBAR(); \
      EX(CUR, 6); EX(CUR, 7); EX(CUR, 8); EX(CUR, 9); EX(CUR, 10); PIN(CUR); SBAR(); O = smf<0>(VA, CUR, O, sidx); SBAR(); \
      EX(CUR, 11); EX(CUR, 12); EX(CUR, 13); EX(CUR, 14); EX(CUR, 15); PIN(CUR); SBAR(); } while (0)
#define TILE(t, KA, KB) do { bf16x8 va, vb; \
      STEPN(sA, sB, KA, q1, n1, o0, va, o1, vp, (va = ldfrag(vq + (t) * 1024), vb = ldfrag(vq + (t) * 1024 + 512), KB = ldfrag(kq + ((t) + 1) * 1024))); \
      STEPN(sB, sA, KB, q0, n0, o1, va, o0, vb, (vp = vb)); } while (0)
      __builtin_amdgcn_s_setprio(3);
#pragma unroll 1
      for (int ph = -2; ph < 5; ++ph) {
        if (ph == -1) {
          glds3(voff, ks + 8192 + j * 6144, kd + 8192 + j * 6144);
          glds3(voff, ks + (OFF_V - OFF_K) + 8192 + j * 6144, kd + L_V + 8192 + j * 6144);
          glds3(voff, ks + 11264 + j * 6144, kd + 11264 + j * 6144);
          glds3(voff, ks + (OFF_V - OFF_K) + 11264 + j * 6144, kd + L_V + 11264 + j * 6144);
        }
        if (ph == 0) {
          WAITV(0);
          if (lane == 0) __hip_atomic_fetch_add(lcnt + 4, 1u, __ATOMIC_RELAXED, __HIP_MEMORY_SCOPE_WORKGROUP);
#pragma unroll 1
          for (unsigned spins = 0; spins < 4000000u; ++spins)
            if (__builtin_amdgcn_readfirstlane(__hip_atomic_load(lcnt + 4, __ATOMIC_RELAXED, __HIP_MEMORY_SCOPE_WORKGROUP)) >= 4u * (layer + 1)) break;
          asm volatile("" ::: "memory");
        }
        if (ph == 1) __builtin_amdgcn_s_setprio(2);
        if (ph == 3) __builtin_amdgcn_s_setprio(1);
        if (ph == 4) __builtin_amdgcn_s_setprio(0);
        if (layer == 0 && ph >= 0) {
          const int q4 = (lane & 15) >> 2, b16 = (lane >> 4) & 1;
          const int scr = L_CMB + (half ? j * 5120 : 20480 + j * 2048);
          const int jo = ((ph - 1) * 8 + w) * 256 + bid;
          if ((ph == 1 || ph == 2) && jo < 3712) {
            const bool gu = jo < 1792;
            LDSP(const char) sp = (LDSP(const char))smem + scr + (lane & 3) * 8 +
                                  (gu ? (16 * hh + 4 * b16 + q4) * 32 : (4 * hh + q4) * 64 + b16 * 32);
            const int ho = gu ? 256 : 512;
            const char* pd;
            if (gu) { const int ft = jo >> 4, S = jo & 15, nb = ft / 7, ii = ft - nb * 7; pd = ws + OFF_WGU + ((size_t)((nb * 8 + (S >> 1)) * 14 + 2 * ii) * 2 + (S & 1)) * 1024; }
            else if (jo < 2048) pd = ws + OFF_WO + (size_t)(jo - 1792) * 2048;
            else if (jo < 2944) pd = ws + OFF_WD + (size_t)(jo - 2048) * 2048;
            else { const int t = (jo - 2944) >> 4, nb = t / 3, ii = t - nb * 3, s0 = (jo & 15) * 2; pd = ws + OFF_WQ + (size_t)(((nb * 8 + (s0 >> 2)) * 3 + ii) * 4 + (s0 & 3)) * 1024; }
            WAITV(0);
            const u32x4 fa = __builtin_bit_cast(u32x4, trfrag(sp, ho)), fb = __builtin_bit_cast(u32x4, trfrag(sp + 1024, ho));
            st16s<0>(voff, pd, fa); st16s<0>(voff, pd + (gu ? 2048 : 1024), fb);
            if (gu || jo >= 2944) {
              float ca = (lo16(fa[0]) + hi16(fa[0])) + (lo16(fa[1]) + hi16(fa[1])) + (lo16(fa[2]) + hi16(fa[2])) + (lo16(fa[3]) + hi16(fa[3]));
              float cb = (lo16(fb[0]) + hi16(fb[0])) + (lo16(fb[1]) + hi16(fb[1])) + (lo16(fb[2]) + hi16(fb[2])) + (lo16(fb[3]) + hi16(fb[3]));
              if (gu) { ca = xhalf_add(x16_add(ca)); cb = xhalf_add(x16_add(cb)); ca = lane < 16 ? ca : cb; }
              else ca = xhalf_add(ca + cb);
              const char* cp = ws + OFF_CSP + ((jo & 15) * 5120 + ((gu ? jo : jo - 2944 + 1792) >> 4) * 32) * 4;
              if (lane < 32) asm volatile("global_store_dword %0, %1, %2 sc1" :: "v"(lane * 4), "v"(ca), "s"(cp) : "memory");
            }
          }
          const int jn = jo + 2048;
          if (ph < 2 && jn < 3712) {
            const char *sa, *sb; unsigned gv;
            if (jn < 1792) {
              const int o = 32 * (jn & 15) * (F_ * 2) + (jn >> 4) * 32;
              gv = (lane >> 1) * (F_ * 2) + (lane & 1) * 16; sa = karg(4) + o; sb = karg(5) + o;
            } else {
              int nt, s0, ld = 1024, ai;
              if (jn < 2048) { const int f = (jn - 1792) * 2; nt = f >> 5; s0 = f & 31; ai = 3; }
              else if (jn < 2944) { const int f = (jn - 2048) * 2; nt = f / KSF; s0 = f - nt * KSF; ai = 6; }
              else { const int t = (jn - 2944) >> 4, nb = t / 3; s0 = (jn & 15) * 2; nt = (t - nb * 3) * 16 + nb; ai = 2; ld = 3072; }
              gv = (lane >> 2) * ld + (lane & 3) * 16; sa = karg(ai) + (16 * s0 * ld + nt * 64); sb = sa + 16 * ld;
            }
            __builtin_amdgcn_s_waitcnt(0xc07f);
            glds1(gv, sa, lds0 + scr); glds1(gv, sb, lds0 + scr + 1024);
          }
          if (ph == 3) arrive4(lcnt, cnt + (C_PREP + xcd) * 1024, lane);
        }
        int n = ph <= 0 ? ph + 3 : ph == 2 ? 2 : ph == 4 ? 2 : 3;
#pragma unroll 1
        do { TILE(0, kA, kB); TILE(1, kB, kA); kq += 2048; vq += vstep; } while (--n > 0);
      }
      o1 = smf<1>(vp, sB, o1, sidx);
#undef TILE
#undef STEPN
      LDSP(char) xp = lb + L_CMB + j * 5120;
      if (half == 1) {
        const f32x4 a = {o0[0], o0[1], o0[2], o0[3]}, b = {o0[4], o0[5], o0[6], o0[7]}, c = {o1[0], o1[1], o1[2], o1[3]}, d = {o1[4], o1[5], o1[6], o1[7]};
        const f32x2 l = {o0[8], o1[8]};
        *(LDSP(f32x4))xp = a; *(LDSP(f32x4))(xp + 1024) = b; *(LDSP(f32x4))(xp + 2048) = c; *(LDSP(f32x4))(xp + 3072) = d; *(LDSP(f32x2))(xp + 4096) = l;
      }
      if (layer == 0 && w == 0) poll(cnt + C_PREP * 1024 + (idx & 7) * 128, (lane & 7) * 1024, 64u);
      WAITBAR_L();
      if (half == 0) {
        __builtin_amdgcn_s_setprio(3);
        const f32x4 a = *(LDSP(const f32x4))xp, b = *(LDSP(const f32x4))(xp + 1024), c = *(LDSP(const f32x4))(xp + 2048), d = *(LDSP(const f32x4))(xp + 3072);
        const f32x2 l = *(LDSP(const f32x2))(xp + 4096);
#pragma unroll
        for (int i = 0; i < 4; ++i) { o0[i] += a[i]; o0[4 + i] += b[i]; o1[i] += c[i]; o1[4 + i] += d[i]; }
        o0[8] += l[0]; o1[8] += l[1];
        float chk = (o0[8] + o1[8]) * 5.4210109e-20f;
#pragma unroll
        for (int i = 0; i < 8; ++i) chk = __builtin_fmaf(o0[i], 0.f, __builtin_fmaf(o1[i], 0.f, chk));
        const bool bad = !(chk < 1.f);
        if (__builtin_expect(__any(bad), 0)) {
#pragma unroll 1
          for (int z = 0; z < 2; ++z) {
            const bf16x8 q = z ? q1 : q0;
            float mx = -INFINITY;
            f32x16 o = f32x16{};
            kq = kp; vq = vp0;
#pragma unroll 1
            for (int t = 0; t < 64; ++t) {
              const bf16x8 k = ldfrag(kq), v0 = ldfrag(vq), v1 = ldfrag(vq + 512);
              f32x16 s = MFMA(k, q, f32x16{});
              float tm = s[0];
#pragma unroll
              for (int i = 1; i < 16; ++i) tm = fmaxf(tm, s[i]);
              tm = xhalf_max(tm);
              const float mn = fmaxf(mx, tm);
              const float alpha = __builtin_amdgcn_exp2f(mx - mn);
              mx = mn;
#pragma unroll
              for (int i = 0; i < 16; ++i) { o[i] *= alpha; s[i] = __builtin_amdgcn_exp2f(s[i] - mn); }
              o = smf<0>(v0, s, o, sidx);
              o = smf<1>(v1, s, o, sidx);
              kq += 1024; vq += vstep >> 1;
            }
            if (z) o1 = o; else o0 = o;
          }
        }
        const float i0 = __builtin_amdgcn_rcpf(o0[8]), i1 = __builtin_amdgcn_rcpf(o1[8]);
        const u32x4 a0 = {pk2(o0[0] * i0, o0[1] * i0), pk2(o0[2] * i0, o0[3] * i0), pk2(o0[4] * i0, o0[5] * i0), pk2(o0[6] * i0, o0[7] * i0)};
        const u32x4 a1 = {pk2(o1[0] * i1, o1[1] * i1), pk2(o1[2] * i1, o1[3] * i1), pk2(o1[4] * i1, o1[5] * i1), pk2(o1[6] * i1, o1[7] * i1)};
        char* op = wl + OFF_O + ((size_t)tt0 * KSD + head) * 1024 + voff;
        st16(op, a0); st16(op + 32768, a1);
        WAITBAR_L();
        arrive4(lcnt, cnt + (C_O + layer * 8 + qb) * 1024, lane);
      }
    }
    int role = w >> 2;
    asm volatile("" : "+s"(role));
    if (role == 0) {
      {
        FRESH();
        const char* wb = ws + OFF_WGU + (size_t)gnb * 8 * GW + j * 7168;
#pragma unroll 1
        for (int c = 0; c < 2; ++c) { glds4(voff, wb + c * GW, lds0 + c * GCH + j * 7168); glds3(voff, wb + c * GW + 4096, lds0 + c * GCH + j * 7168 + 4096); }
        colsum_part(ws + OFF_CSP + (size_t)j * 81920 + gnb * 896, voff, lb + L_CS + j * 1024);
      }
      WAITBAR_L();
      {
        FRESH();
        const int g4 = lane >> 4, c16 = lane & 15;
        f32x4 ac[2][14];
#pragma unroll
        for (int i = 0; i < 14; ++i) { ac[0][i] = f32x4{}; ac[1][i] = f32x4{}; }
        LDSP(const char) lx = (LDSP(const char))smem + GW + j * 4096 + (g4 >> 1) * 1024 + ((g4 & 1) * 32 + c16) * 16;
        bf16x8 wf[2][7], xf[2][2];
        xf[1][0] = bf16x8{}; xf[1][1] = bf16x8{};
#pragma unroll
        for (int i = 0; i < 7; ++i) wf[1][i] = bf16x8{};
#define RDX(u, so, a) xf[u][a] = ldfrag(lx + (so) + (u) * 2048 + (a) * 256)
#define RDW(hf, u, so, i) wf[hf][i] = ldfrag(lb + (so) + ((7 * (hf) + (i)) * 2 + (u)) * 1024)
#define M1(B, XP, i, a) do { SBAR(); ac[a][7 * (B) + (i)] = __builtin_amdgcn_mfma_f32_16x16x32_bf16(wf[B][i], xf[XP][a], ac[a][7 * (B) + (i)], 0, 0, 0); SBAR(); } while (0)
#define MM(B, XP, i) do { M1(B, XP, i, 0); M1(B, XP, i, 1); } while (0)
#define GSTEP(B, XP, u, so) do { \
    M1(B, XP, 0, 0); RDW((B) ^ 1, u, so, 0); M1(B, XP, 0, 1); if ((B) == 1) RDX(u, so, 0); M1(B, XP, 1, 0); if ((B) == 1) RDX(u, so, 1); M1(B, XP, 1, 1); RDW((B) ^ 1, u, so, 1); \
    M1(B, XP, 2, 0); RDW((B) ^ 1, u, so, 2); M1(B, XP, 2, 1); RDW((B) ^ 1, u, so, 3); M1(B, XP, 3, 0); RDW((B) ^ 1, u, so, 4); M1(B, XP, 3, 1); RDW((B) ^ 1, u, so, 5); \
    M1(B, XP, 4, 0); RDW((B) ^ 1, u, so, 6); M1(B, XP, 4, 1); MM(B, XP, 5); MM(B, XP, 6); } while (0)
        WAITBAR_L();
        int so = 0;
#pragma unroll 1
        for (int c = 0; c < 8; ++c) {
          GSTEP(1, 1, 0, so); GSTEP(0, 0, 0, so); GSTEP(1, 0, 1, so); GSTEP(0, 1, 1, so);
          asm volatile("" ::: "memory"); __builtin_amdgcn_s_waitcnt(0xc07f);
          if (c != 7) __builtin_amdgcn_s_barrier();
          asm volatile("" ::: "memory");
          so = so == 2 * GCH ? 0 : so + GCH;
        }
        LDSP(const float) sx = (LDSP(const float))((LDSP(char))smem + L_ST) + j * 64 + c16;
        const float rs0 = sx[0], rs1 = sx[16], m0 = -sx[32], m1 = -sx[48];
        f32x4 c4[14];
#pragma unroll
        for (int i = 0; i < 14; ++i) c4[i] = *(LDSP(const f32x4))((LDSP(const char))smem + L_CS + i * 64 + g4 * 16);
        const char* ob = wl + OFF_H + ((size_t)(gtb * 4 + j) * KSF + gnb * 7) * 1024;
        unsigned oo = (((g4 & 1) * 32) + (g4 >> 1) * 16 + c16) * 16;
        asm volatile("" : "+s"(ob)); PIN(oo);
        MM(1, 1, 0); MM(1, 1, 1); MM(1, 1, 2); MM(1, 1, 3); MM(1, 1, 4); MM(1, 1, 5); MM(1, 1, 6);
#undef RDX
#undef RDW
#undef M1
#undef MM
#undef GSTEP
        const f32x4 vm0 = {m0, m0, m0, m0}, vm1 = {m1, m1, m1, m1};
#define HOUT(i) do { \
          const f32x4 ga = __builtin_elementwise_fma(c4[2 * (i)], vm0, ac[0][2 * (i)]), ua = __builtin_elementwise_fma(c4[2 * (i) + 1], vm0, ac[0][2 * (i) + 1]) * rs0; \
          const f32x4 gb = __builtin_elementwise_fma(c4[2 * (i)], vm1, ac[1][2 * (i)]), ub = __builtin_elementwise_fma(c4[2 * (i) + 1], vm1, ac[1][2 * (i) + 1]) * rs1; \
          const f32x4 h0 = __builtin_elementwise_max(ga, f32x4{}) * ua, h1 = __builtin_elementwise_max(gb, f32x4{}) * ub; \
          auto e0 = __builtin_amdgcn_permlane32_swap(pk2(h0[0], h0[1]), pk2(h1[0], h1[1]), false, false); \
          auto e1 = __builtin_amdgcn_permlane32_swap(pk2(h0[2], h0[3]), pk2(h1[2], h1[3]), false, false); \
          const u32x4 a_ = {e0[0], e1[0], e0[1], e1[1]}; \
          st16s<((i) & 3) * 1024>(oo, ob + ((i) >> 2) * 4096, a_); } while (0)
        HOUT(0); HOUT(1); HOUT(2); HOUT(3); HOUT(4); HOUT(5); HOUT(6);
#undef HOUT
        if (lane == 0) __hip_atomic_fetch_add(lcnt + 3, 1u, __ATOMIC_RELAXED, __HIP_MEMORY_SCOPE_WORKGROUP);
        arrive4(lcnt, cnt + (C_H + layer * 16 + gtb) * 1024, lane);
      }
      WAITBAR_L();
      if (layer == 0) {
        FRESH();
        __builtin_amdgcn_s_barrier();
        { const char* wb = ws + OFF_WQ + (size_t)qnb * 8 * QW + j * 3072;
#pragma unroll 1
          for (int c = 0; c < 4; ++c) glds3(voff, wb + c * QW, lds0 + c * QW + j * 3072); }
        colsum_part(ws + OFF_CSP + (size_t)j * 81920 + (3584 + qnb * 96) * 4, voff, lb + L_CS + j * 1024);
        QINIT();
#define RX(B, u) xf[B] = ldfrag(sxp + (u) * 1024)
#define RW(B, i, u) wf[B][i] = ldfrag(swp + (i) * 4096 + (u) * 1024)
        WAITBAR_L();
#pragma unroll 1
        for (int c = 0; c < 8; ++c) {
          LDSP(const char) swp = lb + (c & 3) * QW;
          LDSP(const char) sxp = lb + L_QX + (c & 3) * 16384 + j * 4096;
          QCHUNK();
          WAITBAR_L();
        }
        LDSP(const float) cx = (LDSP(const float))((LDSP(char))smem + L_CS);
        LDSP(const float) sx = (LDSP(const float))((LDSP(char))smem + L_ST) + j * 64;
        f32x4 cq[8], ra[4], rb[4];
        const float rstd = sx[r], mr = sx[32 + r], cv = cx[64 + r];
#pragma unroll
        for (int g = 0; g < 8; ++g) cq[g] = *(LDSP(const f32x4))(cx + 8 * g + 4 * hh);
#pragma unroll
        for (int g = 0; g < 4; ++g) { ra[g] = *(LDSP(const f32x4))(sx + 8 * g + 4 * hh); rb[g] = *(LDSP(const f32x4))(sx + 32 + 8 * g + 4 * hh); }
        SBAR();
        QFINISH();
        SBAR();
#undef RX
#undef RW
        char* wn = ws + LSTRIDE;
        const int tt = qtb * 4 + j;
        float v[16];
        const float rq = rstd * QSCALE, mq = mr * QSCALE;
#pragma unroll
        for (int e = 0; e < 16; ++e) v[e] = acc[0][e] * rq - mq * cq[e >> 2][e & 3];
        qk_store(v, wn + OFF_Q + ((size_t)(2 * qnb) * NTT + tt) * 1024 + voff);
#pragma unroll
        for (int e = 0; e < 16; ++e) v[e] = acc[1][e] * rstd - mr * cq[4 + (e >> 2)][e & 3];
        qk_store(v, wn + OFF_K + ((size_t)(2 * qnb) * NTT + tt) * 1024 + voff);
#pragma unroll
        for (int e = 0; e < 16; ++e) v[e] = acc[2][e] * ra[e >> 2][e & 3] - rb[e >> 2][e & 3] * cv;
        v_store(v, wn + OFF_V + ((size_t)(2 * qnb + (r >> 4)) * NTT + tt) * 1024 + hh * 256 + (r & 15) * 16);
        arrive4(lcnt, cnt + (C_KV + qnb) * 1024, lane);
      }
    } else {
      {
        FRESH();
        bf16x8 w0[8], w1[8], x0[8], x1[8];
        const char* wa = ws + OFF_WO + ((size_t)ont * KSD + j * 8) * 1024;
        const char* wb = ws + OFF_WO + ((size_t)(ont ^ 1) * KSD + j * 8) * 1024;
#define LD4(D, o, base) do { gld16<0>(D[o], voff, base); gld16<1024>(D[(o) + 1], voff, base); gld16<2048>(D[(o) + 2], voff, base); gld16<3072>(D[(o) + 3], voff, base); } while (0)
        __builtin_amdgcn_s_barrier();
        LD4(w0, 0, wa); LD4(w0, 4, wa + 4096); LD4(w1, 0, wb); LD4(w1, 4, wb + 4096);
        u32x2 rn[4]; unsigned res[8];
        res_load(rn, layer ? voff : r * 1024 + hh * 8, (layer ? ws + OFF_X3 + ont * 2048 : karg(1) + ont * 64) + (size_t)ott * 32768, layer ? 8 : 16, layer ? 1024 : 32);
        const char* xa = wl + OFF_O + ((size_t)ott * KSD + j * 8) * 1024;
        const char* xb = wl + OFF_O + ((size_t)(ott ^ 1) * KSD + j * 8) * 1024;
        asm volatile("" : "+s"(xa), "+s"(xb));
        poll_b(cnt + (C_O + layer * 8 + xcd) * 1024 + (idx & 7) * 128, 32u, lcnt + 1, layer * 4 + 1, j);
        #define LD2(s) do { gld16<((s) & 3) * 1024>(x0[s], voff, xa + ((s) >> 2) * 4096); gld16<((s) & 3) * 1024>(x1[s], voff, xb + ((s) >> 2) * 4096); } while (0)
        LD2(0); LD2(1); LD2(2); LD2(3); LD2(4); LD2(5); LD2(6); LD2(7);
#undef LD2
        f32x16 acc[4];
#pragma unroll
        for (int i = 0; i < 4; ++i) acc[i] = f32x16{};
#define MM4(W0, W1, X0, X1) do { acc[1] = MFMA(W0, X1, acc[1]); acc[2] = MFMA(W1, X0, acc[2]); acc[3] = MFMA(W1, X1, acc[3]); acc[0] = MFMA(W0, X0, acc[0]); } while (0)
#define PIN8(n, X, Y, o) asm volatile("s_waitcnt vmcnt(%8)" : "+v"(X[o]), "+v"(X[(o) + 1]), "+v"(X[(o) + 2]), "+v"(X[(o) + 3]), "+v"(Y[o]), "+v"(Y[(o) + 1]), "+v"(Y[(o) + 2]), "+v"(Y[(o) + 3]) : "i"(n) : "memory")
        PIN8(16, w0, w1, 0); PIN8(16, w0, w1, 4);
#define WX(b, n) asm volatile("s_waitcnt vmcnt(%2)" : "+v"(x0[b]), "+v"(x1[b]) : "i"(n) : "memory")
#define ST(s) do { WX(s, 14 - 2 * (s)); MM4(w0[s], w1[s], x0[s], x1[s]); SBAR(); } while (0)
        ST(0); ST(1); ST(2); ST(3); ST(4); ST(5); ST(6); ST(7);
#undef ST
#undef WX
        RES_FINISH((i - 1) * GCH + GW + (j ^ i) * 4096, (i - 1) * GCH + GW + j * 4096);
        res_store_tiled(res, wl + OFF_X2 + ((size_t)ott * KSD + 2 * ont) * 1024 + voff, wl + OFF_ST2 + (size_t)ott * 4096 + ont * 256 + r * 8, lane);
        arrive4(lcnt, cnt + (C_X2 + layer * 16 + grp) * 1024, lane);
      }
      {
        FRESH();
        { LDSP(char) cp = (LDSP(char))smem + L_CS + j * 256 + lane * 4;
          *(LDSP(float))cp = (*(LDSP(float))cp + *(LDSP(float))(cp + 1024)) + (*(LDSP(float))(cp + 2048) + *(LDSP(float))(cp + 3072)); }
        bf16x8 w0[12], w1[12], x0[8], x1[8];
        const char* wa = ws + OFF_WD + ((size_t)ont * KSF + j * 28) * 1024;
        const char* wb = ws + OFF_WD + ((size_t)(ont ^ 1) * KSF + j * 28) * 1024;
        {
          const int tt = gtb * 4 + j;
          const char* gx = wl + OFF_X2 + (size_t)tt * 32768;
          const char* gw = ws + OFF_WGU + (size_t)gnb * 8 * GW + j * 7168;
          f32x4 sp[4];
          asm volatile("" : "+s"(gx), "+s"(gw));
          poll_b(cnt + (C_X2 + layer * 16 + gtb) * 1024 + (gnb & 7) * 128, 16u, lcnt + 1, layer * 4 + 2, j);
          __builtin_amdgcn_s_waitcnt(0xc07f);
#define GISSUE(c, d) do { glds4(voff, gx + (c) * 4096, (d) + GW + j * 4096); glds4(voff, gw + (c) * GW, (d) + j * 7168); glds3(voff, gw + (c) * GW + 4096, (d) + j * 7168 + 4096); } while (0)
          glds4(voff, gx, lds0 + GW + j * 4096);
          glds4(voff, gx + 4096, lds0 + GCH + GW + j * 4096);
          stat_issue(sp, wl + OFF_ST2 + (size_t)tt * 4096, voff);
          WAITBAR_V(8);
          GISSUE(2, lds0 + 2 * GCH);
          LD4(w0, 0, wa); LD4(w1, 0, wb);
          WAITBAR_V(23);
          unsigned d = lds0;
#pragma unroll 1
          for (int c = 3; c < 8; ++c) {
            GISSUE(c, d);
            d = d == lds0 + 2 * GCH ? lds0 : d + GCH;
            WAITBAR_V(11);
          }
#undef GISSUE
          LD4(w0, 4, wa + 4096); LD4(w1, 4, wb + 4096); LD4(w0, 8, wa + 8192); LD4(w1, 8, wb + 8192);
          { float mean, var; stat_finish(sp, mean, var);
            ((LDSP(float))((LDSP(char))smem + L_ST))[j * 64 + hh * 32 + (lane & 15) * 2 + ((lane >> 4) & 1)] = hh ? mean : 1.f / var; }
          WAITBAR_V(16);
        }
#define DISSUE(c, P) do { glds4(voff, wa + 12288 + (c) * 4096, lds0 + (P) + j * 8192); glds4(voff, wb + 12288 + (c) * 4096, lds0 + (P) + j * 8192 + 4096); } while (0)
        DISSUE(0, L_D0); DISSUE(1, L_D1); DISSUE(2, L_D2);
#pragma unroll 1
        for (unsigned spins = 0; spins < 4000000u; ++spins) {
          if (__builtin_amdgcn_readfirstlane(__hip_atomic_load(lcnt + 2, __ATOMIC_RELAXED, __HIP_MEMORY_SCOPE_WORKGROUP)) >= 4u * (layer + 1)) break;
          __builtin_amdgcn_s_sleep(1);
        }
        asm volatile("" ::: "memory");
        DISSUE(3, L_D3);
#undef DISSUE
        const char* xa = wl + OFF_H + ((size_t)ott * KSF + j * 28) * 1024;
        const char* xb = wl + OFF_H + ((size_t)(ott ^ 1) * KSF + j * 28) * 1024;
        asm volatile("" : "+s"(xa), "+s"(xb));
        poll_b(cnt + (C_H + layer * 16 + grp) * 1024 + (idx & 7) * 128, 16u, lcnt + 1, layer * 4 + 3, j);
        PIN8(0, w0, w1, 0); PIN8(0, w0, w1, 4); PIN8(0, w0, w1, 8);
#define LDX(b, blk) do { gld16<((b) & 3) * 1024>(x0[b], voff, xa + (blk) * 4096); gld16<((b) & 3) * 1024>(x1[b], voff, xb + (blk) * 4096); } while (0)
#define WX(b, n) asm volatile("s_waitcnt vmcnt(%2)" : "+v"(x0[b]), "+v"(x1[b]) : "i"(n) : "memory")
        LDX(0, 0); LDX(1, 0); LDX(2, 0); LDX(3, 0); LDX(4, 1); LDX(5, 1); LDX(6, 1); LDX(7, 1);
        f32x16 acc[4];
#pragma unroll
        for (int i = 0; i < 4; ++i) acc[i] = f32x16{};
        LDSP(const char) lw = lb + j * 8192;
#define BLK(o, h, nb, n0) do { _Pragma("unroll") for (int s = 0; s < 1; ++s) {} \
          WX((h) + 0, n0); MM4(w0[(o) + 0], w1[(o) + 0], x0[(h) + 0], x1[(h) + 0]); SBAR(); if ((nb) >= 0) LDX((h) + 0, nb); SBAR(); \
          WX((h) + 1, (nb) >= 0 ? n0 : n0 - 2); MM4(w0[(o) + 1], w1[(o) + 1], x0[(h) + 1], x1[(h) + 1]); SBAR(); if ((nb) >= 0) LDX((h) + 1, nb); SBAR(); \
          WX((h) + 2, (nb) >= 0 ? n0 : n0 - 4); MM4(w0[(o) + 2], w1[(o) + 2], x0[(h) + 2], x1[(h) + 2]); SBAR(); if ((nb) >= 0) LDX((h) + 2, nb); SBAR(); \
          WX((h) + 3, (nb) >= 0 ? n0 : n0 - 6); MM4(w0[(o) + 3], w1[(o) + 3], x0[(h) + 3], x1[(h) + 3]); SBAR(); if ((nb) >= 0) LDX((h) + 3, nb); SBAR(); } while (0)
#define RDL(o, c) do { _Pragma("unroll") for (int s = 0; s < 4; ++s) { w0[(o) + s] = ldfrag(lw + (c) + s * 1024); w1[(o) + s] = ldfrag(lw + (c) + 4096 + s * 1024); } SBAR(); } while (0)
        BLK(0, 0, 2, 14); RDL(0, L_D0);
        BLK(4, 4, 3, 14); RDL(4, L_D1);
        BLK(8, 0, 4, 14); RDL(8, L_D2);
        BLK(0, 4, 5, 14); RDL(0, L_D3);
        BLK(4, 0, 6, 14);
        u32x2 rn[4]; unsigned res[8];
        res_load(rn, voff, wl + OFF_X2 + (size_t)ott * 32768 + ont * 2048, 8, 1024);
        BLK(8, 4, -1, 18);
        BLK(0, 0, -1, 10);
#undef RDL
#undef BLK
#undef LDX
#undef WX
        RES_FINISH((i == 3 ? L_D1 : L_D0 + (i - 1) * 4096) + j * 8192, (i == 3 ? L_D1 : L_D0 + (i - 1) * 4096) + (j ^ i) * 8192);
        if (layer == 0) {
          res_store_tiled(res, ws + OFF_X3 + ((size_t)ott * KSD + 2 * ont) * 1024 + voff, ws + OFF_ST3 + (size_t)ott * 4096 + ont * 256 + r * 8, lane);
          arrive4(lcnt, cnt + (C_X3 + grp) * 1024, lane);
        } else {
          auto s0 = __builtin_amdgcn_permlane32_swap(res[0], res[2], false, false);
          auto s1 = __builtin_amdgcn_permlane32_swap(res[1], res[3], false, false);
          auto s2 = __builtin_amdgcn_permlane32_swap(res[4], res[6], false, false);
          auto s3 = __builtin_amdgcn_permlane32_swap(res[5], res[7], false, false);
          const u32x4 a = {s0[0], s1[0], s0[1], s1[1]}, b = {s2[0], s3[0], s2[1], s3[1]};
          char* o = (char*)karg(7) + ((size_t)(ott * 32 + r) * D_ + ont * 32) * 2 + hh * 16;
          st16(o, a);
          st16(o + 32, b);
        }
      }
#undef LD4
#undef MM4
#undef PIN8
      if (layer == 0) {
        FRESH();
        const int tt = qtb * 4 + j;
        const char* xb = ws + OFF_X3 + (size_t)tt * 32768;
        const char* wb = ws + OFF_WQ + (size_t)qnb * 8 * QW + j * 3072;
        f32x4 sp[4];
        asm volatile("" : "+s"(xb), "+s"(wb));
        __builtin_amdgcn_s_barrier();
        poll_b(cnt + (C_X3 + qtb) * 1024 + (qnb & 7) * 128, 16u, lcnt + 1, 4, j);
#define QISSUE(c) do { glds4(voff, xb + (c) * 4096, lds0 + L_QX + ((c) & 3) * 16384 + j * 4096); glds3(voff, wb + (c) * QW, lds0 + ((c) & 3) * QW + j * 3072); } while (0)
        glds4(voff, xb, lds0 + L_QX + j * 4096);
        glds4(voff, xb + 4096, lds0 + L_QX + 16384 + j * 4096);
        stat_issue(sp, ws + OFF_ST3 + (size_t)tt * 4096, voff);
        glds4(voff, xb + 8192, lds0 + L_QX + 32768 + j * 4096);
        glds4(voff, xb + 12288, lds0 + L_QX + 49152 + j * 4096);
        WAITBAR_V(16);
        { LDSP(char) cp = (LDSP(char))smem + L_CS + j * 256 + lane * 4;
          *(LDSP(float))cp = (*(LDSP(float))cp + *(LDSP(float))(cp + 1024)) + (*(LDSP(float))(cp + 2048) + *(LDSP(float))(cp + 3072)); }
        WAITBAR_V(12);
        QISSUE(4); WAITBAR_V(11);
#pragma unroll 1
        for (int c = 5; c < 8; ++c) { QISSUE(c); WAITBAR_V(14); }
#undef QISSUE
        { float mean, var; stat_finish(sp, mean, var); const float rs = rsqrtf(var);
          ((LDSP(float))((LDSP(char))smem + L_ST))[j * 64 + hh * 32 + (lane & 15) * 2 + ((lane >> 4) & 1)] = hh ? mean * rs : rs; }
        WAITBAR_V(7);
        WAITBAR_V(0);
        __builtin_amdgcn_s_barrier();
      }
    }
  }
}

extern "C" void kernel_launch(void* const* d_in, const int* in_sizes, int n_in, void* d_out, int out_size, void* d_ws, size_t ws_size,
                              hipStream_t stream) {
  const char* x = (const char*)d_in[0];
  const char* wqkv = (const char*)d_in[1];
  const char* wo = (const char*)d_in[2];
  const char* wg = (const char*)d_in[4];
  const char* wu = (const char*)d_in[5];
  const char* wd = (const char*)d_in[6];
  char* ws = (char*)d_ws;
  k_qkv1<<<256, 768, 0, stream>>>(x, wqkv, ws);
  k_main<<<256, 512, 0, stream>>>(ws, x, wqkv, wo, wg, wu, wd, (char*)d_out);
}
```

```cpp
#include <hip/hip_runtime.h>
#include <hip/hip_bf16.h>
#include <stdint.h>

typedef short bf16x8 __attribute__((ext_vector_type(8)));
typedef float f32x16 __attribute__((ext_vector_type(16)));
typedef float f32x4 __attribute__((ext_vector_type(4)));
typedef float f32x2 __attribute__((ext_vector_type(2)));
typedef unsigned u32x4 __attribute__((ext_vector_type(4)));
typedef unsigned u32x2 __attribute__((ext_vector_type(2)));
typedef __bf16 bf16v2 __attribute__((ext_vector_type(2)));
typedef unsigned short u16;

#define DI __device__ __forceinline__
#define MFMA(a, b, c) __builtin_amdgcn_mfma_f32_32x32x16_bf16((a), (b), (c), 0, 0, 0)
#define LDSP(T) __attribute__((address_space(3))) T*

constexpr int S_ = 2048, D_ = 512, H_ = 32, F_ = 1792;
constexpr int NTT = S_ / 32;
constexpr int KSD = D_ / 16;
constexpr int KSF = F_ / 16;
constexpr float QSCALE = 0.25f * 1.4426950408889634f;
constexpr float LN_EPS = 1e-5f;

constexpr size_t KB = 1024;
constexpr size_t OFF_CNT = 0;
constexpr size_t OFF_WO = 192 * KB;
constexpr size_t OFF_WGU = OFF_WO + 512 * KB;
constexpr size_t OFF_WD = OFF_WGU + 3584 * KB;
constexpr size_t OFF_WQ = OFF_WD + 1792 * KB;
constexpr size_t OFF_CSP = OFF_WQ + 1536 * KB;
constexpr size_t OFF_ST3 = OFF_CSP + 324 * KB;
constexpr size_t OFF_X3 = OFF_ST3 + 256 * KB;
constexpr size_t OFF_L = OFF_X3 + 2048 * KB;
constexpr size_t OFF_Q = OFF_L;
constexpr size_t OFF_K = OFF_Q + 2048 * KB;
constexpr size_t OFF_V = OFF_K + 2048 * KB;
constexpr size_t OFF_O = OFF_V + 2048 * KB;
constexpr size_t OFF_X2 = OFF_O + 2048 * KB;
constexpr size_t OFF_ST2 = OFF_X2 + 2048 * KB;
constexpr size_t OFF_H = OFF_ST2 + 256 * KB;
constexpr size_t LSTRIDE = OFF_H + 7168 * KB - OFF_L;
constexpr int KMAIN_KB = 26;
constexpr int C_O = 0, C_X2 = 16, C_H = 48, C_X3 = 80, C_KV = 96, C_PREP = 112, C_H2 = 128, C_N = 160;

template <int I> struct IC { static constexpr int value = I; };
template <int B, int E, class F> DI void sfor(F&& f) { if constexpr (B < E) { f(IC<B>{}); sfor<B + 1, E>(f); } }
DI unsigned pk2(float lo, float hi) {
  f32x2 v = {lo, hi};
  bf16v2 r = __builtin_convertvector(v, bf16v2);
  return __builtin_bit_cast(unsigned, r);
}
DI float lo16(unsigned u) { return __uint_as_float(u << 16); }
DI float hi16(unsigned u) { return __uint_as_float(u & 0xffff0000u); }
DI float xhalf_add(float v) {
  auto r = __builtin_amdgcn_permlane32_swap(__float_as_uint(v), __float_as_uint(v), false, false);
  return __uint_as_float(r[0]) + __uint_as_float(r[1]);
}
DI float xhalf_max(float v) {
  auto r = __builtin_amdgcn_permlane32_swap(__float_as_uint(v), __float_as_uint(v), false, false);
  return fmaxf(__uint_as_float(r[0]), __uint_as_float(r[1]));
}
DI float x16_add(float v) {
  auto r = __builtin_amdgcn_permlane16_swap(__float_as_uint(v), __float_as_uint(v), false, false);
  return __uint_as_float(r[0]) + __uint_as_float(r[1]);
}
DI void st16(void* p, u32x4 v) { asm volatile("global_store_dwordx4 %0, %1, off sc1\n\ts_nop 1" :: "v"(p), "v"(v) : "memory"); }
template <int IMM>
DI void st16s(unsigned voff, const char* sbase, u32x4 v) {
  asm volatile("global_store_dwordx4 %0, %1, %2 offset:%3 sc1\n\ts_nop 1" :: "v"(voff), "v"(v), "s"(sbase), "i"(IMM) : "memory");
}
template <int IMM>
DI void gld16(bf16x8& d, unsigned voff, const char* sbase) {
  asm volatile("global_load_dwordx4 %0, %1, %2 offset:%3 sc1" : "=v"(d) : "v"(voff), "s"(sbase), "i"(IMM) : "memory");
}
#define GLDS_HEAD "s_mov_b32 %0, m0\n\ts_mov_b32 m0, %3\n\ts_nop 0\n\t"
#define GLDS_OP(o) "global_load_lds_dwordx4 %1, %2 offset:" #o " sc1\n\t"
#define GLDS_TAIL "s_mov_b32 m0, %0" : "=&s"(keep) : "v"(voff), "s"(sbase), "s"(lds) : "memory"
DI void glds1(unsigned voff, const char* sbase, unsigned lds) { unsigned keep; asm volatile(GLDS_HEAD GLDS_OP(0) GLDS_TAIL); }
DI void glds2(unsigned voff, const char* sbase, unsigned lds) { unsigned keep; asm volatile(GLDS_HEAD GLDS_OP(0) GLDS_OP(1024) GLDS_TAIL); }
DI void glds3(unsigned voff, const char* sbase, unsigned lds) { unsigned keep; asm volatile(GLDS_HEAD GLDS_OP(0) GLDS_OP(1024) GLDS_OP(2048) GLDS_TAIL); }
DI void glds4(unsigned voff, const char* sbase, unsigned lds) { unsigned keep; asm volatile(GLDS_HEAD GLDS_OP(0) GLDS_OP(1024) GLDS_OP(2048) GLDS_OP(3072) GLDS_TAIL); }
#define SBAR() __builtin_amdgcn_sched_barrier(0)
#define PIN(x) asm volatile("" : "+v"(x))
#define WAITV(n) asm volatile("s_waitcnt vmcnt(%0)" :: "i"(n) : "memory")
#define WAITBAR_V(n) asm volatile("s_waitcnt vmcnt(%0) lgkmcnt(0)\n\ts_barrier" :: "i"(n) : "memory")
#define WAITBAR_L() do { asm volatile("" ::: "memory"); __builtin_amdgcn_s_waitcnt(0xc07f); __builtin_amdgcn_s_barrier(); asm volatile("" ::: "memory"); } while (0)
typedef short s16x4 __attribute__((ext_vector_type(4)));
DI s16x4 trread(LDSP(const char) p) { return __builtin_amdgcn_ds_read_tr16_b64_v4i16((LDSP(s16x4))p); }
DI bf16x8 trfrag(LDSP(const char) p, int off2) { const s16x4 lo = trread(p), hi = trread(p + off2); return __builtin_shufflevector(lo, hi, 0, 1, 2, 3, 4, 5, 6, 7); }
DI bf16x8 ldfrag(LDSP(const char) p) { return *(LDSP(const bf16x8))p; }
DI float dot2(unsigned a, unsigned b, float c) { return __builtin_amdgcn_fdot2_f32_bf16(__builtin_bit_cast(bf16v2, a), __builtin_bit_cast(bf16v2, b), c, false); }
constexpr unsigned ONE2 = 0x3f803f80u;
#define BC8(x) __builtin_bit_cast(bf16x8, x)

template <int LOOK, int PER> DI void ring_wait(int i) {
  if (i <= 8 - LOOK) WAITBAR_V((LOOK - 1) * PER);
  else if (LOOK > 3 && i == 5) WAITBAR_V(2 * PER);
  else if (LOOK > 2 && i == 6) WAITBAR_V(PER);
  else WAITBAR_V(0);
}

DI void qk_store(const float (&v)[16], char* o) {
  u32x4 a = {pk2(v[0], v[1]), pk2(v[2], v[3]), pk2(v[4], v[5]), pk2(v[6], v[7])};
  u32x4 b = {pk2(v[8], v[9]), pk2(v[10], v[11]), pk2(v[12], v[13]), pk2(v[14], v[15])};
  st16(o, a);
  st16(o + NTT * 1024, b);
}
DI void v_store(const float (&v)[16], char* o) {
  auto s0 = __builtin_amdgcn_permlane32_swap(pk2(v[0], v[1]), pk2(v[4], v[5]), false, false);
  auto s1 = __builtin_amdgcn_permlane32_swap(pk2(v[2], v[3]), pk2(v[6], v[7]), false, false);
  auto s2 = __builtin_amdgcn_permlane32_swap(pk2(v[8], v[9]), pk2(v[12], v[13]), false, false);
  auto s3 = __builtin_amdgcn_permlane32_swap(pk2(v[10], v[11]), pk2(v[14], v[15]), false, false);
  const u32x4 a = {s0[0], s1[0], s0[1], s1[1]}, b = {s2[0], s3[0], s2[1], s3[1]};
  st16(o, a);
  st16(o + 512, b);
}
typedef __bf16 bfv8 __attribute__((ext_vector_type(8)));
typedef __bf16 bfv16 __attribute__((ext_vector_type(16)));
typedef float f32x8 __attribute__((ext_vector_type(8)));
template <int H> DI f32x16 smf(bf16x8 a, const f32x16& s, f32x16 c, int idx) {
  const f32x8 p = __builtin_shufflevector(s, s, 8 * H, 8 * H + 1, 8 * H + 2, 8 * H + 3, 8 * H + 4, 8 * H + 5, 8 * H + 6, 8 * H + 7);
  return __builtin_amdgcn_smfmac_f32_32x32x32_bf16(__builtin_bit_cast(bfv8, a), __builtin_bit_cast(bfv16, p), c, idx, 0, 0);
}
DI float row16_sum(float v) {
  v += __builtin_bit_cast(float, __builtin_amdgcn_update_dpp(0, __builtin_bit_cast(int, v), 0x128, 0xf, 0xf, false));
  v += __builtin_bit_cast(float, __builtin_amdgcn_update_dpp(0, __builtin_bit_cast(int, v), 0x124, 0xf, 0xf, false));
  v += __builtin_bit_cast(float, __builtin_amdgcn_update_dpp(0, __builtin_bit_cast(int, v), 0x122, 0xf, 0xf, false));
  v += __builtin_bit_cast(float, __builtin_amdgcn_update_dpp(0, __builtin_bit_cast(int, v), 0x121, 0xf, 0xf, false));
  return v;
}
#define KSTEP(B, u) do { SBAR(); RX((B) ^ 1, u); RW((B) ^ 1, 0, u); SBAR(); acc[0] = MFMA(wf[B][0], xf[B], acc[0]); SBAR(); RW((B) ^ 1, 1, u); RW((B) ^ 1, 2, u); SBAR(); \
                         acc[1] = MFMA(wf[B][1], xf[B], acc[1]); acc[2] = MFMA(xf[B], wf[B][2], acc[2]); SBAR(); } while (0)
#define QCHUNK() do { KSTEP(1, 0); KSTEP(0, 1); KSTEP(1, 2); KSTEP(0, 3); } while (0)
#define QINIT() \
  f32x16 acc[3]; bf16x8 wf[2][3], xf[2]; \
  _Pragma("unroll") for (int i = 0; i < 3; ++i) { acc[i] = f32x16{}; wf[1][i] = bf16x8{}; } \
  xf[1] = bf16x8{}
#define QFINISH() do { acc[0] = MFMA(wf[1][0], xf[1], acc[0]); acc[1] = MFMA(wf[1][1], xf[1], acc[1]); acc[2] = MFMA(xf[1], wf[1][2], acc[2]); } while (0)

extern "C" __global__ __launch_bounds__(768, 3) void k_qkv1(const char* __restrict__ x, const char* __restrict__ wn, char* __restrict__ ws) {
  constexpr int LOOK = 3, CH = 24576, LXN = 4 * CH;
  __shared__ __attribute__((aligned(16))) char smem[LXN + 65536];
  const int tid = threadIdx.x, lane = tid & 63, r = lane & 31, hh = lane >> 5;
  const int w = __builtin_amdgcn_readfirstlane(tid >> 6);
  const int xcd = blockIdx.x & 7, idx = blockIdx.x >> 3;
  const int tb = (xcd & 3) * 8 + (idx & 7), nq = (xcd >> 2) * 4 + (idx >> 3);
  const unsigned lds0 = (unsigned)(uintptr_t)smem;
  const int rot = 2 * (idx & 3);
  if (w >= 8) {
    const int j = w - 8;
    const unsigned wv = (lane >> 3) * 3072 + (((lane & 7) ^ (((lane >> 4) & 1) << 2)) * 16);
    const char* wb = wn + (size_t)j * 16 * 3072 + nq * 128;
    if (blockIdx.x < 5) *(unsigned*)(ws + OFF_CNT + ((blockIdx.x * 4 + j) * 64 + lane) * 128) = 0u;
    asm volatile("s_sleep 8\n\ts_nop 4" ::: "memory");
#pragma unroll 1
    for (int c = 0; c < 8 + LOOK; ++c) {
      if (c >= LOOK) ring_wait<LOOK, 6>(c - LOOK);
      if (c < 8) {
        const char* ws_ = wb + (size_t)((c + rot) & 7) * 64 * 3072;
        const unsigned d = lds0 + (c & 3) * CH + j * 2048;
        glds1(wv, ws_, d); glds1(wv, ws_ + 1024, d + 8192); glds1(wv, ws_ + 2048, d + 16384);
        glds1(wv, ws_ + 8 * 3072, d + 1024); glds1(wv, ws_ + 8 * 3072 + 1024, d + 8192 + 1024); glds1(wv, ws_ + 8 * 3072 + 2048, d + 16384 + 1024);
      }
    }
    return;
  }
  {
    const int n = w;
    const u32x4* xr = (const u32x4*)(x + (size_t)(tb * 64 + n * 8) * 1024) + lane;
    u32x4 v[8];
#pragma unroll
    for (int i = 0; i < 8; ++i) v[i] = xr[i * 64];
    float s[8], q[8];
#pragma unroll
    for (int i = 0; i < 8; ++i) {
      s[i] = 0.f; q[i] = 0.f;
#pragma unroll
      for (int e = 0; e < 4; ++e) { s[i] = dot2(v[i][e], ONE2, s[i]); q[i] = dot2(v[i][e], v[i][e], q[i]); }
    }
    float s4[4], q4[4], s2[2], q2[2];
#pragma unroll
    for (int i = 0; i < 4; ++i) {
      const int lo = (i & 1) + 4 * (i >> 1);
      auto a = __builtin_amdgcn_permlane32_swap(__float_as_uint(s[lo]), __float_as_uint(s[lo + 2]), false, false);
      auto b = __builtin_amdgcn_permlane32_swap(__float_as_uint(q[lo]), __float_as_uint(q[lo + 2]), false, false);
      s4[i] = __uint_as_float(a[0]) + __uint_as_float(a[1]); q4[i] = __uint_as_float(b[0]) + __uint_as_float(b[1]);
    }
#pragma unroll
    for (int i = 0; i < 2; ++i) {
      auto a = __builtin_amdgcn_permlane16_swap(__float_as_uint(s4[2 * i]), __float_as_uint(s4[2 * i + 1]), false, false);
      auto b = __builtin_amdgcn_permlane16_swap(__float_as_uint(q4[2 * i]), __float_as_uint(q4[2 * i + 1]), false, false);
      s2[i] = row16_sum(__uint_as_float(a[0]) + __uint_as_float(a[1])); q2[i] = row16_sum(__uint_as_float(b[0]) + __uint_as_float(b[1]));
    }
    float ra[2], rb[2];
#pragma unroll
    for (int i = 0; i < 2; ++i) {
      const float mean = s2[i] * (1.f / D_);
      ra[i] = rsqrtf(fmaxf(q2[i] * (1.f / D_) - mean * mean, 0.f) + LN_EPS);
      rb[i] = -mean * ra[i];
    }
    LDSP(char) xd = (LDSP(char))smem + LXN + (lane >> 3) * 8192 + n * 1024;
#pragma unroll
    for (int i = 0; i < 8; ++i) {
      const float a = __builtin_bit_cast(float, __builtin_amdgcn_readlane(__builtin_bit_cast(int, ra[i >> 2]), (i & 3) * 16));
      const float b = __builtin_bit_cast(float, __builtin_amdgcn_readlane(__builtin_bit_cast(int, rb[i >> 2]), (i & 3) * 16));
      u32x4 o;
#pragma unroll
      for (int e = 0; e < 4; ++e) o[e] = pk2(lo16(v[i][e]) * a + b, hi16(v[i][e]) * a + b);
      *(LDSP(u32x4))(xd + i * 128 + (((lane & 7) ^ ((4 * n + (i >> 1)) & 7)) * 16)) = o;
    }
  }
  WAITBAR_L();
  if (w >= 4) {
    if (w == 4 && idx < KMAIN_KB) {
      bf16x8 t;
      asm volatile("s_getpc_b64 s[20:21]\n\ts_add_u32 s20, s20, k_main@rel32@lo+4\n\ts_addc_u32 s21, s21, k_main@rel32@hi+12\n\t"
                   "global_load_dwordx4 %0, %1, s[20:21]\n\ts_waitcnt vmcnt(0)" : "=v"(t) : "v"(lane * 16 + idx * 1024) : "s20", "s21", "scc", "memory");
    }
    return;
  }
  const int ta = w & 1, hp = w >> 1, q4 = (lane & 15) >> 2, xsw = (r >> 1) & 7;
  LDSP(const char) lw = (LDSP(const char))smem + (8 * hh + q4) * 128 + ((hp ^ (q4 >> 1)) * 64) + ((lane >> 4) & 1) * 32 + (lane & 3) * 8;
  LDSP(const char) lx = (LDSP(const char))smem + LXN + ta * 4096 + r * 128;
  int xo[4];
#pragma unroll
  for (int u = 0; u < 4; ++u) xo[u] = ((2 * u + hh) ^ xsw) * 16;
  QINIT();
#define RX(B, u) xf[B] = ldfrag(sx + xo[u])
#define RW(B, i, u) wf[B][i] = trfrag(sw + (i) * 8192 + (u) * 2048, 512)
#pragma unroll 1
  for (int c = 0; c < 8; ++c) {
    LDSP(const char) sw = lw + (c & 3) * CH;
    LDSP(const char) sx = lx + ((c + rot) & 7) * 8192;
    QCHUNK();
    WAITBAR_L();
  }
  QFINISH();
#undef RX
#undef RW
  const int tt = tb * 2 + ta, nb = nq * 2 + hp;
  float v[16];
#pragma unroll
  for (int e = 0; e < 16; ++e) v[e] = acc[0][e] * QSCALE;
  qk_store(v, ws + OFF_Q + ((size_t)(2 * nb) * NTT + tt) * 1024 + lane * 16);
#pragma unroll
  for (int e = 0; e < 16; ++e) v[e] = acc[1][e];
  qk_store(v, ws + OFF_K + ((size_t)(2 * nb) * NTT + tt) * 1024 + lane * 16);
#pragma unroll
  for (int e = 0; e < 16; ++e) v[e] = acc[2][e];
  v_store(v, ws + OFF_V + ((size_t)(2 * nb + (r >> 4)) * NTT + tt) * 1024 + hh * 256 + (r & 15) * 16);
}

DI void poll(const char* cnt, unsigned voff, unsigned target) {
#pragma unroll 1
  for (unsigned spins = 0; spins < 200000u; ++spins) {
    unsigned v;
    asm volatile("global_load_dword %0, %1, %2 sc1\n\ts_waitcnt vmcnt(0)" : "=v"(v) : "v"(voff), "s"(cnt) : "memory");
    if (__all(v >= target)) break;
  }
}
DI void poll_b(const char* cnt, unsigned target, LDSP(unsigned) go, unsigned seq, int j) {
  if (j == 0) {
    poll(cnt, 0u, target);
    __hip_atomic_store(go, seq, __ATOMIC_RELAXED, __HIP_MEMORY_SCOPE_WORKGROUP);
  } else {
    WAITV(0);
#pragma unroll 1
    for (unsigned spins = 0; spins < 4000000u; ++spins) {
      if (__builtin_amdgcn_readfirstlane(__hip_atomic_load(go, __ATOMIC_RELAXED, __HIP_MEMORY_SCOPE_WORKGROUP)) >= seq) break;
    }
  }
  asm volatile("" ::: "memory");
}
DI void arrive4(LDSP(unsigned) lw, char* cnt, int lane) {
  WAITV(0);
  unsigned old = 0;
  if (lane == 0) old = __hip_atomic_fetch_add(lw, 1u, __ATOMIC_RELAXED, __HIP_MEMORY_SCOPE_WORKGROUP);
  old = __builtin_amdgcn_readfirstlane(old);
  if ((old & 3u) == 3u && lane < 8) asm volatile("global_atomic_add %0, %1, %2" :: "v"(lane * 128), "v"(1u), "s"(cnt) : "memory");
  asm volatile("" ::: "memory");
}
DI void stat_issue(f32x4 (&p)[4], const char* sb, unsigned voff) {
  asm volatile("global_load_dwordx4 %0, %4, %5 sc1\n\tglobal_load_dwordx4 %1, %4, %5 offset:1024 sc1\n\tglobal_load_dwordx4 %2, %4, %5 offset:2048 sc1\n\t"
               "global_load_dwordx4 %3, %4, %5 offset:3072 sc1" : "=&v"(p[0]), "=&v"(p[1]), "=&v"(p[2]), "=&v"(p[3]) : "v"(voff), "s"(sb) : "memory");
}
DI void stat_finish(f32x4 (&p)[4], float& mean, float& var) {
  asm volatile("" : "+v"(p[0]), "+v"(p[1]), "+v"(p[2]), "+v"(p[3]));
  const f32x4 t = (p[0] + p[1]) + (p[2] + p[3]);
  auto a = __builtin_amdgcn_permlane16_swap(__float_as_uint(t[0]), __float_as_uint(t[2]), false, false);
  auto b = __builtin_amdgcn_permlane16_swap(__float_as_uint(t[1]), __float_as_uint(t[3]), false, false);
  mean = xhalf_add(__uint_as_float(a[0]) + __uint_as_float(a[1])) * (1.f / D_);
  var = fmaxf(xhalf_add(__uint_as_float(b[0]) + __uint_as_float(b[1])) * (1.f / D_) - mean * mean, 0.f) + LN_EPS;
}
DI void colsum_part(const char* src, unsigned voff, LDSP(char) dst) {
  f32x4 p[4];
  asm volatile("global_load_dwordx4 %0, %4, %5 sc1\n\tglobal_load_dwordx4 %1, %4, %6 sc1\n\tglobal_load_dwordx4 %2, %4, %7 sc1\n\t"
               "global_load_dwordx4 %3, %4, %8 sc1\n\ts_waitcnt vmcnt(0)" : "=&v"(p[0]), "=&v"(p[1]), "=&v"(p[2]), "=&v"(p[3])
               : "v"(voff), "s"(src), "s"(src + 20480), "s"(src + 40960), "s"(src + 61440) : "memory");
  *(LDSP(f32x4))dst = (p[0] + p[1]) + (p[2] + p[3]);
}

#define RES_FINISH(WR, RD) do { \
    _Pragma("unroll") for (int i = 1; i < 4; ++i) _Pragma("unroll") for (int g = 0; g < 4; ++g) { \
      const f32x4 v_ = {acc[i][4 * g], acc[i][4 * g + 1], acc[i][4 * g + 2], acc[i][4 * g + 3]}; \
      *(LDSP(f32x4))(lb + (WR) + g * 1024) = v_; } \
    asm volatile("s_waitcnt vmcnt(0)" : "+v"(rn[0]), "+v"(rn[1]), "+v"(rn[2]), "+v"(rn[3]) :: "memory"); \
    WAITBAR_L(); \
    _Pragma("unroll") for (int g = 0; g < 4; ++g) { \
      f32x4 t_ = {acc[0][4 * g], acc[0][4 * g + 1], acc[0][4 * g + 2], acc[0][4 * g + 3]}; \
      f32x4 u_[3]; \
      _Pragma("unroll") for (int i = 1; i < 4; ++i) u_[i - 1] = *(LDSP(const f32x4))(lb + (RD) + g * 1024); \
      t_ = (u_[0] + u_[1]) + (u_[2] + t_); \
      res[2 * g] = pk2(t_[0] + lo16(rn[g][0]), t_[1] + hi16(rn[g][0])); \
      res[2 * g + 1] = pk2(t_[2] + lo16(rn[g][1]), t_[3] + hi16(rn[g][1])); } } while (0)
DI void res_load(u32x2 (&rn)[4], unsigned vo, const char* p0, int d1, int d2) {
  asm volatile("global_load_dwordx2 %0, %4, %5 sc1\n\tglobal_load_dwordx2 %1, %4, %6 sc1\n\tglobal_load_dwordx2 %2, %4, %7 sc1\n\tglobal_load_dwordx2 %3, %4, %8 sc1"
               : "=&v"(rn[0]), "=&v"(rn[1]), "=&v"(rn[2]), "=&v"(rn[3]) : "v"(vo), "s"(p0), "s"(p0 + d1), "s"(p0 + d2), "s"(p0 + d2 + d1) : "memory");
}
DI void res_store_tiled(const unsigned (&res)[8], char* o, char* st, int lane) {
  const u32x4 p0 = {res[0], res[1], res[2], res[3]}, p1 = {res[4], res[5], res[6], res[7]};
  st16(o, p0);
  st16(o + 1024, p1);
  float s1 = 0.f, s2 = 0.f, t1 = 0.f, t2 = 0.f;
#pragma unroll
  for (int e = 0; e < 4; ++e) { s1 = dot2(res[e], ONE2, s1); s2 = dot2(res[e], res[e], s2); t1 = dot2(res[4 + e], ONE2, t1); t2 = dot2(res[4 + e], res[4 + e], t2); }
  s1 = xhalf_add(s1 + t1); s2 = xhalf_add(s2 + t2);
  if (lane < 32) { const f32x2 v = {s1, s2}; asm volatile("global_store_dwordx2 %0, %1, off sc1" :: "v"(st), "v"(v) : "memory"); }
}

DI int hsrc(int p) { const int g = p / 28, q = p - g * 28; return g * 28 + (q < 16 ? (q >> 2) * 7 + (q & 3) : ((q - 16) / 3) * 7 + 4 + (q - 16) % 3); }
DI const char* karg(int i) {
  const __attribute__((address_space(4))) unsigned long long* ka = (const __attribute__((address_space(4))) unsigned long long*)__builtin_amdgcn_kernarg_segment_ptr();
  asm volatile("" : "+s"(ka));
  return (const char*)ka[i];
}
extern "C" __global__ __launch_bounds__(512, 2) void k_main(char* __restrict__ ws_, const char* x_, const char* wq_, const char* wo_, const char* wg_,
                                                            const char* wu_, const char* wd_, char* out_) {
  constexpr int L_K = 0, L_V = 65536, L_ONE = 131072, L_CMB = 133120;
  constexpr int GS = 32768, L_RED = 3 * GS;
  constexpr int L_D0 = 3 * GS, L_D1 = 2 * GS, L_D2 = 0;
  constexpr int QW = 12288, L_QX = 49152;
  constexpr int L_CS = 157696, L_ST = L_CS + 4096, L_CNT = L_ST + 1024;
  __shared__ __attribute__((aligned(16))) char smem[163840];
  const int w_ = __builtin_amdgcn_readfirstlane(threadIdx.x >> 6);
  const unsigned lds0 = (unsigned)(uintptr_t)smem;
#define FRESH() \
  int tid; asm volatile("v_mbcnt_lo_u32_b32 %0, -1, 0\n\tv_mbcnt_hi_u32_b32 %0, -1, %0" : "=v"(tid)); int bid = blockIdx.x, w = w_; char* ws = ws_; asm volatile("" : "+s"(bid), "+s"(w), "+s"(ws)); \
  tid += w * 64; const int j = w & 3; \
  const int lane = tid & 63, r = lane & 31, hh = lane >> 5, xcd = bid & 7, idx = bid >> 3, grp = xcd * 2 + (idx >> 4), m = idx & 15; \
  const unsigned voff = lane * 16; LDSP(char) lb = (LDSP(char))smem + lane * 16; char* cnt = ws + OFF_CNT; char* wl = ws + layer * LSTRIDE; \
  LDSP(unsigned) lcnt = (LDSP(unsigned))((LDSP(char))smem + L_CNT) + (w >> 2); \
  const int ont = (m >> 1) * 2 + (j >> 1), ott = grp * 4 + (m & 1) * 2 + (j & 1); \
  const int gtb = (xcd & 1) * 8 + (idx & 7), gnb = (xcd >> 1) * 4 + (idx >> 3), qtb = (xcd & 3) * 4 + (idx & 3), qnb = (xcd >> 2) * 8 + (idx >> 2); \
  (void)r; (void)hh; (void)voff; (void)lb; (void)cnt; (void)wl; (void)lcnt; (void)ont; (void)ott; (void)gtb; (void)gnb; (void)qtb; (void)qnb
  if (threadIdx.x < 8) ((LDSP(unsigned))((LDSP(char))smem + L_CNT))[threadIdx.x] = 0u;
#pragma unroll 1
  for (int layer = 0; layer < 2; ++layer) {
    {
      FRESH();
      const int head = xcd * 4 + (idx & 3), qb = idx >> 2, half = w >> 2, tt0 = qb * 8 + 2 * j;
      if (layer) { if (w == 4) poll(cnt + (C_KV + (head >> 1)) * 1024 + qb * 128, 0u, 16u); WAITBAR_L(); }
      const char* ks = wl + OFF_K + (size_t)head * 65536 + half * 32768;
      const unsigned kd = lds0 + L_K + half * 32768;
      bf16x8 q0, q1;
      gld16<0>(q0, voff, wl + OFF_Q + ((size_t)head * NTT + tt0) * 1024);
      gld16<1024>(q1, voff, wl + OFF_Q + ((size_t)head * NTT + tt0) * 1024);
      glds2(voff, ks + j * 2048, kd + j * 2048);
      glds2(voff, ks + (OFF_V - OFF_K) + j * 2048, kd + L_V + j * 2048);
      *(LDSP(unsigned))((LDSP(char))smem + L_ONE + tid * 4) = ((tid >> 2) & 11) == 0 ? ONE2 : 0u;
      asm volatile("s_waitcnt vmcnt(0) lgkmcnt(0)\n\ts_barrier" : "+v"(q0), "+v"(q1) :: "memory");
      LDSP(const char) kp = lb + L_K;
      LDSP(const char) vp0 = (LDSP(const char))smem + (r < 16 ? L_V : L_ONE) + hh * 256 + (r & 15) * 16;
      const int vstep = r < 16 ? 2048 : 0;
      LDSP(const char) kq = kp + half * 32768; LDSP(const char) vq = vp0 + half * 16 * vstep;
      f32x16 n0, n1, sA, sB = f32x16{}, o0 = f32x16{}, o1 = f32x16{};
      bf16x8 kA, kB, vp = bf16x8{};
      int sidx = (int)0xDDDDDDDDu; PIN(sidx);
      {
        const bf16x8 k0 = ldfrag(kp);
        kA = ldfrag(kq);
        const f32x16 s = MFMA(k0, q0, f32x16{}), t = MFMA(k0, q1, f32x16{});
        float a = fmaxf(fmaxf(s[0], s[1]), s[2]), b = fmaxf(fmaxf(t[0], t[1]), t[2]);
#pragma unroll
        for (int i = 3; i < 15; i += 2) { a = fmaxf(fmaxf(a, s[i]), s[i + 1]); b = fmaxf(fmaxf(b, t[i]), t[i + 1]); }
        a = xhalf_max(fmaxf(a, s[15])); b = xhalf_max(fmaxf(b, t[15]));
#pragma unroll
        for (int i = 0; i < 16; ++i) { n0[i] = -a; n1[i] = -b; }
        PIN(n0); PIN(n1);
        sA = MFMA(kA, q0, n0);
      }
#define EX(S, i) S[i] = __builtin_amdgcn_exp2f(S[i])
#define STEPN(CUR, NXT, KN_, QN, NN, O, VA, OP, VP, READS) do { \
      EX(CUR, 0); EX(CUR, 1); PIN(CUR); SBAR(); OP = smf<1>(VP, NXT, OP, sidx); SBAR(); READS; SBAR(); \
      EX(CUR, 2); EX(CUR, 3); EX(CUR, 4); EX(CUR, 5); PIN(CUR); SBAR(); NXT = MFMA(KN_, QN, NN); SBAR(); \
      EX(CUR, 6); EX(CUR, 7); EX(CUR, 8); EX(CUR, 9); EX(CUR, 10); PIN(CUR); SBAR(); O = smf<0>(VA, CUR, O, sidx); SBAR(); \
      EX(CUR, 11); EX(CUR, 12); EX(CUR, 13); EX(CUR, 14); EX(CUR, 15); PIN(CUR); SBAR(); } while (0)
#define TILE(t, KA, KB) do { bf16x8 va, vb; \
      STEPN(sA, sB, KA, q1, n1, o0, va, o1, vp, (va = ldfrag(vq + (t) * 1024), vb = ldfrag(vq + (t) * 1024 + 512), KB = ldfrag(kq + ((t) + 1) * 1024))); \
      STEPN(sB, sA, KB, q0, n0, o1, va, o0, vb, (vp = vb)); } while (0)
      __builtin_amdgcn_s_setprio(3);
#pragma unroll 1
      for (int ph = -2; ph < 5; ++ph) {
        if (ph == -1) {
          glds3(voff, ks + 8192 + j * 6144, kd + 8192 + j * 6144);
          glds3(voff, ks + (OFF_V - OFF_K) + 8192 + j * 6144, kd + L_V + 8192 + j * 6144);
          glds3(voff, ks + 11264 + j * 6144, kd + 11264 + j * 6144);
          glds3(voff, ks + (OFF_V - OFF_K) + 11264 + j * 6144, kd + L_V + 11264 + j * 6144);
        }
        if (ph == 0) {
          WAITV(0);
          if (lane == 0) __hip_atomic_fetch_add(lcnt + 4, 1u, __ATOMIC_RELAXED, __HIP_MEMORY_SCOPE_WORKGROUP);
#pragma unroll 1
          for (unsigned spins = 0; spins < 4000000u; ++spins)
            if (__builtin_amdgcn_readfirstlane(__hip_atomic_load(lcnt + 4, __ATOMIC_RELAXED, __HIP_MEMORY_SCOPE_WORKGROUP)) >= 4u * (layer + 1)) break;
          asm volatile("" ::: "memory");
        }
        if (ph == 1) __builtin_amdgcn_s_setprio(2);
        if (ph == 3) __builtin_amdgcn_s_setprio(1);
        if (ph == 4) __builtin_amdgcn_s_setprio(0);
        if (layer == 0 && ph >= 0) {
          const int q4 = (lane & 15) >> 2, b16 = (lane >> 4) & 1;
          const int scr = L_CMB + (half ? j * 5120 : 20480 + j * 2048);
          const int jo = ((ph - 1) * 8 + w) * 256 + bid;
          if ((ph == 1 || ph == 2) && jo < 3712) {
            const bool gu = jo < 1792;
            LDSP(const char) sp = (LDSP(const char))smem + scr + (lane & 3) * 8 +
                                  (gu ? (16 * hh + 4 * b16 + q4) * 32 : (4 * hh + q4) * 64 + b16 * 32);
            const int ho = gu ? 256 : 512;
            const char* pd;
            if (gu) { const int ft = jo >> 4, S = jo & 15, nb = ft / 7, ii = ft - nb * 7; pd = ws + OFF_WGU + (size_t)(nb * 224 + (ii < 4 ? S * 8 + 2 * ii : 128 + S * 6 + 2 * (ii - 4))) * 1024; }
            else if (jo < 2048) pd = ws + OFF_WO + (size_t)(jo - 1792) * 2048;
            else if (jo < 2944) pd = ws + OFF_WD + (size_t)(jo - 2048) * 2048;
            else { const int t = (jo - 2944) >> 4, nb = t / 3, ii = t - nb * 3, s0 = (jo & 15) * 2; pd = ws + OFF_WQ + (size_t)(((nb * 8 + (s0 >> 2)) * 3 + ii) * 4 + (s0 & 3)) * 1024; }
            WAITV(0);
            const u32x4 fa = __builtin_bit_cast(u32x4, trfrag(sp, ho)), fb = __builtin_bit_cast(u32x4, trfrag(sp + 1024, ho));
            st16s<0>(voff, pd, fa); st16s<1024>(voff, pd, fb);
            if (gu || jo >= 2944) {
              float ca = (lo16(fa[0]) + hi16(fa[0])) + (lo16(fa[1]) + hi16(fa[1])) + (lo16(fa[2]) + hi16(fa[2])) + (lo16(fa[3]) + hi16(fa[3]));
              float cb = (lo16(fb[0]) + hi16(fb[0])) + (lo16(fb[1]) + hi16(fb[1])) + (lo16(fb[2]) + hi16(fb[2])) + (lo16(fb[3]) + hi16(fb[3]));
              if (gu) { ca = xhalf_add(x16_add(ca)); cb = xhalf_add(x16_add(cb)); ca = lane < 16 ? ca : cb; }
              else ca = xhalf_add(ca + cb);
              const char* cp = ws + OFF_CSP + ((jo & 15) * 5120 + ((gu ? jo : jo - 2944 + 1792) >> 4) * 32) * 4;
              if (lane < 32) asm volatile("global_store_dword %0, %1, %2 sc1" :: "v"(lane * 4), "v"(ca), "s"(cp) : "memory");
            }
          }
          const int jn = jo + 2048;
          if (ph < 2 && jn < 3712) {
            const char *sa, *sb; unsigned gv;
            if (jn < 1792) {
              const int o = 32 * (jn & 15) * (F_ * 2) + (jn >> 4) * 32;
              gv = (lane >> 1) * (F_ * 2) + (lane & 1) * 16; sa = karg(4) + o; sb = karg(5) + o;
            } else {
              int nt, s0, s1, ld = 1024, ai;
              if (jn < 2048) { const int f = (jn - 1792) * 2; nt = f >> 5; s0 = f & 31; s1 = s0 + 1; ai = 3; }
              else if (jn < 2944) { const int f = (jn - 2048) * 2; nt = f / KSF; s0 = hsrc(f - nt * KSF); s1 = hsrc(f - nt * KSF + 1); ai = 6; }
              else { const int t = (jn - 2944) >> 4, nb = t / 3; s0 = (jn & 15) * 2; s1 = s0 + 1; nt = (t - nb * 3) * 16 + nb; ai = 2; ld = 3072; }
              gv = (lane >> 2) * ld + (lane & 3) * 16;
              const char* src = karg(ai) + nt * 64;
              sa = src + 16 * s0 * ld; sb = src + 16 * s1 * ld;
            }
            __builtin_amdgcn_s_waitcnt(0xc07f);
            glds1(gv, sa, lds0 + scr); glds1(gv, sb, lds0 + scr + 1024);
          }
          if (ph == 3) arrive4(lcnt, cnt + (C_PREP + xcd) * 1024, lane);
        }
        int n = ph <= 0 ? ph + 3 : ph == 2 ? 2 : ph == 4 ? 2 : 3;
#pragma unroll 1
        do { TILE(0, kA, kB); TILE(1, kB, kA); kq += 2048; vq += vstep; } while (--n > 0);
      }
      o1 = smf<1>(vp, sB, o1, sidx);
#undef TILE
#undef STEPN
      LDSP(char) xp = lb + L_CMB + j * 5120;
      if (half == 1) {
        const f32x4 a = {o0[0], o0[1], o0[2], o0[3]}, b = {o0[4], o0[5], o0[6], o0[7]}, c = {o1[0], o1[1], o1[2], o1[3]}, d = {o1[4], o1[5], o1[6], o1[7]};
        const f32x2 l = {o0[8], o1[8]};
        *(LDSP(f32x4))xp = a; *(LDSP(f32x4))(xp + 1024) = b; *(LDSP(f32x4))(xp + 2048) = c; *(LDSP(f32x4))(xp + 3072) = d; *(LDSP(f32x2))(xp + 4096) = l;
      }
      if (layer == 0 && w == 0) poll(cnt + C_PREP * 1024 + (idx & 7) * 128, (lane & 7) * 1024, 64u);
      WAITBAR_L();
      if (half == 0) {
        __builtin_amdgcn_s_setprio(3);
        const f32x4 a = *(LDSP(const f32x4))xp, b = *(LDSP(const f32x4))(xp + 1024), c = *(LDSP(const f32x4))(xp + 2048), d = *(LDSP(const f32x4))(xp + 3072);
        const f32x2 l = *(LDSP(const f32x2))(xp + 4096);
#pragma unroll
        for (int i = 0; i < 4; ++i) { o0[i] += a[i]; o0[4 + i] += b[i]; o1[i] += c[i]; o1[4 + i] += d[i]; }
        o0[8] += l[0]; o1[8] += l[1];
        float chk = (o0[8] + o1[8]) * 5.4210109e-20f;
#pragma unroll
        for (int i = 0; i < 8; ++i) chk = __builtin_fmaf(o0[i], 0.f, __builtin_fmaf(o1[i], 0.f, chk));
        const bool bad = !(chk < 1.f);
        if (__builtin_expect(__any(bad), 0)) {
#pragma unroll 1
          for (int z = 0; z < 2; ++z) {
            const bf16x8 q = z ? q1 : q0;
            float mx = -INFINITY;
            f32x16 o = f32x16{};
            kq = kp; vq = vp0;
#pragma unroll 1
            for (int t = 0; t < 64; ++t) {
              const bf16x8 k = ldfrag(kq), v0 = ldfrag(vq), v1 = ldfrag(vq + 512);
              f32x16 s = MFMA(k, q, f32x16{});
              float tm = s[0];
#pragma unroll
              for (int i = 1; i < 16; ++i) tm = fmaxf(tm, s[i]);
              tm = xhalf_max(tm);
              const float mn = fmaxf(mx, tm);
              const float alpha = __builtin_amdgcn_exp2f(mx - mn);
              mx = mn;
#pragma unroll
              for (int i = 0; i < 16; ++i) { o[i] *= alpha; s[i] = __builtin_amdgcn_exp2f(s[i] - mn); }
              o = smf<0>(v0, s, o, sidx);
              o = smf<1>(v1, s, o, sidx);
              kq += 1024; vq += vstep >> 1;
            }
            if (z) o1 = o; else o0 = o;
          }
        }
        const float i0 = __builtin_amdgcn_rcpf(o0[8]), i1 = __builtin_amdgcn_rcpf(o1[8]);
        const u32x4 a0 = {pk2(o0[0] * i0, o0[1] * i0), pk2(o0[2] * i0, o0[3] * i0), pk2(o0[4] * i0, o0[5] * i0), pk2(o0[6] * i0, o0[7] * i0)};
        const u32x4 a1 = {pk2(o1[0] * i1, o1[1] * i1), pk2(o1[2] * i1, o1[3] * i1), pk2(o1[4] * i1, o1[5] * i1), pk2(o1[6] * i1, o1[7] * i1)};
        char* op = wl + OFF_O + ((size_t)tt0 * KSD + head) * 1024 + voff;
        st16(op, a0); st16(op + 32768, a1);
        WAITBAR_L();
        arrive4(lcnt, cnt + (C_O + layer * 8 + qb) * 1024, lane);
      }
    }
    int role = w_ >> 2;
    asm volatile("" : "+s"(role));
    if (role == 0) {
      {
        FRESH();
        const char* wb = ws + OFF_WGU + (size_t)gnb * 229376 + j * 8192;
#pragma unroll 1
        for (int c = 0; c < 4; ++c) glds4(voff, wb + (c >> 1) * GS + (c & 1) * 4096, lds0 + (c >> 1) * GS + (c & 1) * 4096 + j * 8192);
        colsum_part(ws + OFF_CSP + (size_t)j * 81920 + gnb * 896, voff, lb + L_CS + j * 1024);
      }
      WAITBAR_L();
      {
        FRESH();
        const int g4 = lane >> 4, c16 = lane & 15;
        const char* gx = wl + OFF_X2 + (size_t)(gtb * 4 + j) * 32768;
        unsigned xo = (g4 >> 1) * 1024 + ((g4 & 1) * 32 + c16) * 16;
        const char* ob = wl + OFF_H + ((size_t)(gtb * 4 + j) * KSF + (gnb >> 2) * 28) * 1024;
        unsigned oo = (((g4 & 1) * 32) + (g4 >> 1) * 16 + c16) * 16;
        asm volatile("" : "+s"(gx), "+s"(ob)); PIN(xo); PIN(oo);
        bf16x8 xr[16][2], wf[8];
        f32x4 ac[2][8];
#pragma unroll 1
        for (unsigned spins = 0; spins < 4000000u; ++spins)
          if (__builtin_amdgcn_readfirstlane(__hip_atomic_load(lcnt + 2, __ATOMIC_RELAXED, __HIP_MEMORY_SCOPE_WORKGROUP)) >= (unsigned)(layer * 8 + 2)) break; else __builtin_amdgcn_s_sleep(1);
        asm volatile("" ::: "memory");
#define LDXS(S) do { gld16<((S) & 1) * 2048>(xr[S][0], xo, gx + ((S) >> 1) * 4096); gld16<((S) & 1) * 2048 + 256>(xr[S][1], xo, gx + ((S) >> 1) * 4096); } while (0)
        sfor<0, 8>([&](auto ic) { LDXS(decltype(ic)::value); });
        auto pass = [&](auto nt_, auto st0_) {
          constexpr int NT = decltype(nt_)::value, ST0 = decltype(st0_)::value, PD = 6, NF = 16 * NT;
          sfor<0, NF + PD>([&](auto ic) {
            constexpr int f = decltype(ic)::value, g = f - PD;
            if constexpr (g >= 0) {
              constexpr int S = g / NT, i = g % NT;
              if constexpr (ST0 == 0 && i == 0) {
                constexpr int last = 4 * (S / 4) + 7 > 15 ? 15 : 4 * (S / 4) + 7;
                asm volatile("s_waitcnt vmcnt(%2)" : "+v"(xr[S][0]), "+v"(xr[S][1]) : "i"(2 * (last - S)) : "memory");
              }
              SBAR();
              if constexpr (S == 0) ac[0][i] = __builtin_amdgcn_mfma_f32_16x16x32_bf16(wf[g & 7], xr[S][0], f32x4{}, 0, 0, 0);
              else ac[0][i] = __builtin_amdgcn_mfma_f32_16x16x32_bf16(wf[g & 7], xr[S][0], ac[0][i], 0, 0, 0);
              SBAR();
            }
            if constexpr (f < NF) {
              if constexpr (f % (4 * NT) == 0 && (ST0 + f / (4 * NT)) > 0) WAITBAR_L();
              wf[f & 7] = ldfrag(lb + ((ST0 + f / (4 * NT)) % 3) * GS + (f % (4 * NT)) * 1024);
            }
            if constexpr (g >= 0) {
              constexpr int S = g / NT, i = g % NT;
              SBAR();
              if constexpr (S == 0) ac[1][i] = __builtin_amdgcn_mfma_f32_16x16x32_bf16(wf[g & 7], xr[S][1], f32x4{}, 0, 0, 0);
              else ac[1][i] = __builtin_amdgcn_mfma_f32_16x16x32_bf16(wf[g & 7], xr[S][1], ac[1][i], 0, 0, 0);
              SBAR();
              if constexpr (ST0 == 0 && i == NT - 1 && S % 4 == 3 && S < 8) sfor<S + 5, S + 9>([&](auto jc) { LDXS(decltype(jc)::value); });
              if constexpr (ST0 == 4 && g == 4 * NT) arrive4(lcnt, cnt + (C_H + layer * 16 + gtb) * 1024, lane);
            }
          });
        };
        auto epilogue = [&](auto nt_, auto t0_, const char* o) {
          constexpr int NH = decltype(nt_)::value, T0 = decltype(t0_)::value;
          LDSP(const float) sx = (LDSP(const float))((LDSP(char))smem + L_ST) + j * 64 + c16;
          const float rs0 = sx[0], rs1 = sx[16], m0 = -sx[32], m1 = -sx[48];
          f32x4 c4[2 * NH];
#pragma unroll
          for (int i = 0; i < 2 * NH; ++i) c4[i] = *(LDSP(const f32x4))((LDSP(const char))smem + L_CS + (2 * T0 + i) * 64 + g4 * 16);
          const f32x4 vm0 = {m0, m0, m0, m0}, vm1 = {m1, m1, m1, m1};
          sfor<0, NH>([&](auto ic) {
            constexpr int i = decltype(ic)::value;
            const f32x4 ga = __builtin_elementwise_fma(c4[2 * i], vm0, ac[0][2 * i]), ua = __builtin_elementwise_fma(c4[2 * i + 1], vm0, ac[0][2 * i + 1]) * rs0;
            const f32x4 gb = __builtin_elementwise_fma(c4[2 * i], vm1, ac[1][2 * i]), ub = __builtin_elementwise_fma(c4[2 * i + 1], vm1, ac[1][2 * i + 1]) * rs1;
            const f32x4 h0 = __builtin_elementwise_max(ga, f32x4{}) * ua, h1 = __builtin_elementwise_max(gb, f32x4{}) * ub;
            auto e0 = __builtin_amdgcn_permlane32_swap(pk2(h0[0], h0[1]), pk2(h1[0], h1[1]), false, false);
            auto e1 = __builtin_amdgcn_permlane32_swap(pk2(h0[2], h0[3]), pk2(h1[2], h1[3]), false, false);
            const u32x4 a_ = {e0[0], e1[0], e0[1], e1[1]};
            st16s<i * 1024>(oo, o, a_);
          });
        };
        pass(IC<8>{}, IC<0>{});
        epilogue(IC<4>{}, IC<0>{}, ob + (gnb & 3) * 4096);
        pass(IC<6>{}, IC<4>{});
        epilogue(IC<3>{}, IC<4>{}, ob + 16384 + (gnb & 3) * 3072);
#undef LDXS
        arrive4(lcnt, cnt + (C_H2 + layer * 16 + gtb) * 1024, lane);
      }
      WAITBAR_L();
      if (layer == 0) {
        FRESH();
        __builtin_amdgcn_s_barrier();
        { const char* wb = ws + OFF_WQ + (size_t)qnb * 8 * QW + j * 3072;
#pragma unroll 1
          for (int c = 0; c < 4; ++c) glds3(voff, wb + c * QW, lds0 + c * QW + j * 3072); }
        colsum_part(ws + OFF_CSP + (size_t)j * 81920 + (3584 + qnb * 96) * 4, voff, lb + L_CS + j * 1024);
        QINIT();
#define RX(B, u) xf[B] = ldfrag(sxp + (u) * 1024)
#define RW(B, i, u) wf[B][i] = ldfrag(swp + (i) * 4096 + (u) * 1024)
        WAITBAR_L();
#pragma unroll 1
        for (int c = 0; c < 8; ++c) {
          LDSP(const char) swp = lb + (c & 3) * QW;
          LDSP(const char) sxp = lb + L_QX + (c & 3) * 16384 + j * 4096;
          QCHUNK();
          WAITBAR_L();
        }
        LDSP(const float) cx = (LDSP(const float))((LDSP(char))smem + L_CS);
        LDSP(const float) sx = (LDSP(const float))((LDSP(char))smem + L_ST) + j * 64;
        f32x4 cq[8], ra[4], rb[4];
        const float rstd = sx[r], mr = sx[32 + r], cv = cx[64 + r];
#pragma unroll
        for (int g = 0; g < 8; ++g) cq[g] = *(LDSP(const f32x4))(cx + 8 * g + 4 * hh);
#pragma unroll
        for (int g = 0; g < 4; ++g) { ra[g] = *(LDSP(const f32x4))(sx + 8 * g + 4 * hh); rb[g] = *(LDSP(const f32x4))(sx + 32 + 8 * g + 4 * hh); }
        SBAR();
        QFINISH();
        SBAR();
#undef RX
#undef RW
        char* wn = ws + LSTRIDE;
        const int tt = qtb * 4 + j;
        float v[16];
        const float rq = rstd * QSCALE, mq = mr * QSCALE;
#pragma unroll
        for (int e = 0; e < 16; ++e) v[e] = acc[0][e] * rq - mq * cq[e >> 2][e & 3];
        qk_store(v, wn + OFF_Q + ((size_t)(2 * qnb) * NTT + tt) * 1024 + voff);
#pragma unroll
        for (int e = 0; e < 16; ++e) v[e] = acc[1][e] * rstd - mr * cq[4 + (e >> 2)][e & 3];
        qk_store(v, wn + OFF_K + ((size_t)(2 * qnb) * NTT + tt) * 1024 + voff);
#pragma unroll
        for (int e = 0; e < 16; ++e) v[e] = acc[2][e] * ra[e >> 2][e & 3] - rb[e >> 2][e & 3] * cv;
        v_store(v, wn + OFF_V + ((size_t)(2 * qnb + (r >> 4)) * NTT + tt) * 1024 + hh * 256 + (r & 15) * 16);
        arrive4(lcnt, cnt + (C_KV + qnb) * 1024, lane);
      }
    } else {
      {
        FRESH();
        bf16x8 w0[8], w1[8], x0[8], x1[8];
        const char* wa = ws + OFF_WO + ((size_t)ont * KSD + j * 8) * 1024;
        const char* wb = ws + OFF_WO + ((size_t)(ont ^ 1) * KSD + j * 8) * 1024;
#define LD4(D, o, base) do { gld16<0>(D[o], voff, base); gld16<1024>(D[(o) + 1], voff, base); gld16<2048>(D[(o) + 2], voff, base); gld16<3072>(D[(o) + 3], voff, base); } while (0)
        __builtin_amdgcn_s_barrier();
        LD4(w0, 0, wa); LD4(w0, 4, wa + 4096); LD4(w1, 0, wb); LD4(w1, 4, wb + 4096);
        u32x2 rn[4]; unsigned res[8];
        res_load(rn, layer ? voff : r * 1024 + hh * 8, (layer ? ws + OFF_X3 + ont * 2048 : karg(1) + ont * 64) + (size_t)ott * 32768, layer ? 8 : 16, layer ? 1024 : 32);
        const char* xa = wl + OFF_O + ((size_t)ott * KSD + j * 8) * 1024;
        const char* xb = wl + OFF_O + ((size_t)(ott ^ 1) * KSD + j * 8) * 1024;
        asm volatile("" : "+s"(xa), "+s"(xb));
        poll_b(cnt + (C_O + layer * 8 + xcd) * 1024 + (idx & 7) * 128, 32u, lcnt + 1, layer * 8 + 1, j);
        #define LD2(s) do { gld16<((s) & 3) * 1024>(x0[s], voff, xa + ((s) >> 2) * 4096); gld16<((s) & 3) * 1024>(x1[s], voff, xb + ((s) >> 2) * 4096); } while (0)
        LD2(0); LD2(1); LD2(2); LD2(3); LD2(4); LD2(5); LD2(6); LD2(7);
#undef LD2
        f32x16 acc[4];
#pragma unroll
        for (int i = 0; i < 4; ++i) acc[i] = f32x16{};
#define MM4(W0, W1, X0, X1) do { acc[1] = MFMA(W0, X1, acc[1]); acc[2] = MFMA(W1, X0, acc[2]); acc[3] = MFMA(W1, X1, acc[3]); acc[0] = MFMA(W0, X0, acc[0]); } while (0)
#define PIN8(n, X, Y, o) asm volatile("s_waitcnt vmcnt(%8)" : "+v"(X[o]), "+v"(X[(o) + 1]), "+v"(X[(o) + 2]), "+v"(X[(o) + 3]), "+v"(Y[o]), "+v"(Y[(o) + 1]), "+v"(Y[(o) + 2]), "+v"(Y[(o) + 3]) : "i"(n) : "memory")
        PIN8(16, w0, w1, 0); PIN8(16, w0, w1, 4);
#define WX(b, n) asm volatile("s_waitcnt vmcnt(%2)" : "+v"(x0[b]), "+v"(x1[b]) : "i"(n) : "memory")
#define ST(s) do { WX(s, 14 - 2 * (s)); MM4(w0[s], w1[s], x0[s], x1[s]); SBAR(); } while (0)
        ST(0); ST(1); ST(2); ST(3); ST(4); ST(5); ST(6); ST(7);
#undef ST
#undef WX
        RES_FINISH(L_RED + ((j ^ i) * 3 + i - 1) * 4096, L_RED + (j * 3 + i - 1) * 4096);
        res_store_tiled(res, wl + OFF_X2 + ((size_t)ott * KSD + 2 * ont) * 1024 + voff, wl + OFF_ST2 + (size_t)ott * 4096 + ont * 256 + r * 8, lane);
        arrive4(lcnt, cnt + (C_X2 + layer * 16 + grp) * 1024, lane);
      }
      {
        FRESH();
        { LDSP(char) cp = (LDSP(char))smem + L_CS + j * 256 + lane * 4;
          *(LDSP(float))cp = (*(LDSP(float))cp + *(LDSP(float))(cp + 1024)) + (*(LDSP(float))(cp + 2048) + *(LDSP(float))(cp + 3072)); }
        bf16x8 w0[12], w1[12], x0[8], x1[8];
        const char* wa = ws + OFF_WD + ((size_t)ont * KSF + j * 28) * 1024;
        const char* wb = ws + OFF_WD + ((size_t)(ont ^ 1) * KSF + j * 28) * 1024;
#define DISSUE(c, P) do { glds4(voff, wa + 12288 + (c) * 4096, lds0 + (P) + j * 8192); glds4(voff, wb + 12288 + (c) * 4096, lds0 + (P) + j * 8192 + 4096); } while (0)
        {
          const char* gw = ws + OFF_WGU + (size_t)gnb * 229376;
          const char* gs = wl + OFF_ST2 + (size_t)(gtb * 4 + j) * 4096;
          f32x4 sp[4];
          asm volatile("" : "+s"(gw), "+s"(gs));
#define G1(st) do { glds4(voff, gw + (st) * GS + j * 8192, lds0 + ((st) % 3) * GS + j * 8192); glds4(voff, gw + (st) * GS + j * 8192 + 4096, lds0 + ((st) % 3) * GS + j * 8192 + 4096); } while (0)
#define G2(st) do { glds3(voff, gw + 4 * GS + ((st) - 4) * 24576 + j * 6144, lds0 + ((st) % 3) * GS + j * 6144); \
                    glds3(voff, gw + 4 * GS + ((st) - 4) * 24576 + j * 6144 + 3072, lds0 + ((st) % 3) * GS + j * 6144 + 3072); } while (0)
          poll_b(cnt + (C_X2 + layer * 16 + gtb) * 1024 + (gnb & 7) * 128, 16u, lcnt + 1, layer * 8 + 2, j);
          __builtin_amdgcn_s_sleep(8);
          stat_issue(sp, gs, voff);
          G1(2);
          __builtin_amdgcn_s_barrier();
          G1(3); LD4(w0, 0, wa); LD4(w1, 0, wb);
          WAITV(16);
          { float mean, var; stat_finish(sp, mean, var);
            ((LDSP(float))((LDSP(char))smem + L_ST))[j * 64 + hh * 32 + (lane & 15) * 2 + ((lane >> 4) & 1)] = hh ? mean : 1.f / var; }
          WAITBAR_V(16);
          G2(4); LD4(w0, 4, wa + 4096); LD4(w1, 4, wb + 4096);
          WAITBAR_V(22);
          G2(5); LD4(w0, 8, wa + 8192); LD4(w1, 8, wb + 8192);
          WAITBAR_V(22);
          G2(6); DISSUE(0, L_D0);
          WAITBAR_V(22);
          G2(7);
          WAITBAR_V(14);
          DISSUE(1, L_D1);
          WAITBAR_V(8);
          DISSUE(2, L_D2);
#undef G1
#undef G2
        }
        const char* xa = wl + OFF_H + ((size_t)ott * KSF + j * 28) * 1024;
        const char* xb = wl + OFF_H + ((size_t)(ott ^ 1) * KSF + j * 28) * 1024;
        asm volatile("" : "+s"(xa), "+s"(xb));
        poll_b(cnt + (C_H + layer * 16 + grp) * 1024 + (idx & 7) * 128, 16u, lcnt + 1, layer * 8 + 3, j);
        PIN8(0, w0, w1, 0); PIN8(0, w0, w1, 4); PIN8(0, w0, w1, 8);
#define LDX(b, blk) do { gld16<((b) & 3) * 1024>(x0[b], voff, xa + (blk) * 4096); gld16<((b) & 3) * 1024>(x1[b], voff, xb + (blk) * 4096); } while (0)
#define WX(b, n) asm volatile("s_waitcnt vmcnt(%2)" : "+v"(x0[b]), "+v"(x1[b]) : "i"(n) : "memory")
        LDX(0, 0); LDX(1, 0); LDX(2, 0); LDX(3, 0); LDX(4, 1); LDX(5, 1); LDX(6, 1); LDX(7, 1);
        f32x16 acc[4];
#pragma unroll
        for (int i = 0; i < 4; ++i) acc[i] = f32x16{};
        LDSP(const char) lw = lb + j * 8192;
#define BLK(o, h, nb, n0) do { \
          WX((h) + 0, n0); MM4(w0[(o) + 0], w1[(o) + 0], x0[(h) + 0], x1[(h) + 0]); SBAR(); if ((nb) >= 0) LDX((h) + 0, nb); SBAR(); \
          WX((h) + 1, (nb) >= 0 ? n0 : n0 - 2); MM4(w0[(o) + 1], w1[(o) + 1], x0[(h) + 1], x1[(h) + 1]); SBAR(); if ((nb) >= 0) LDX((h) + 1, nb); SBAR(); \
          WX((h) + 2, (nb) >= 0 ? n0 : n0 - 4); MM4(w0[(o) + 2], w1[(o) + 2], x0[(h) + 2], x1[(h) + 2]); SBAR(); if ((nb) >= 0) LDX((h) + 2, nb); SBAR(); \
          WX((h) + 3, (nb) >= 0 ? n0 : n0 - 6); MM4(w0[(o) + 3], w1[(o) + 3], x0[(h) + 3], x1[(h) + 3]); SBAR(); if ((nb) >= 0) LDX((h) + 3, nb); SBAR(); } while (0)
#define RDL(o, c) do { _Pragma("unroll") for (int s = 0; s < 4; ++s) { w0[(o) + s] = ldfrag(lw + (c) + s * 1024); w1[(o) + s] = ldfrag(lw + (c) + 4096 + s * 1024); } SBAR(); } while (0)
        BLK(0, 0, 2, 14); RDL(0, L_D0);
        BLK(4, 4, 3, 14); RDL(4, L_D1);
        BLK(8, 0, -1, 14); RDL(8, L_D2);
        BLK(0, 4, -1, 6);
        LD4(w0, 0, wa + 24576); LD4(w1, 0, wb + 24576);
        poll_b(cnt + (C_H2 + layer * 16 + grp) * 1024 + (idx & 7) * 128, 16u, lcnt + 1, layer * 8 + 4, j);
        PIN8(0, w0, w1, 0);
        LDX(0, 4); LDX(1, 4); LDX(2, 4); LDX(3, 4); LDX(4, 5); LDX(5, 5); LDX(6, 5); LDX(7, 5);
        BLK(4, 0, 6, 14);
        u32x2 rn[4]; unsigned res[8];
        res_load(rn, voff, wl + OFF_X2 + (size_t)ott * 32768 + ont * 2048, 8, 1024);
        BLK(8, 4, -1, 18);
        BLK(0, 0, -1, 10);
#undef DISSUE
#undef RDL
#undef BLK
#undef LDX
#undef WX
        {
        FRESH();
        RES_FINISH((i == 3 ? L_D1 : L_D0 + (i - 1) * 4096) + j * 8192, (i == 3 ? L_D1 : L_D0 + (i - 1) * 4096) + (j ^ i) * 8192);
        if (layer == 0) {
          res_store_tiled(res, ws + OFF_X3 + ((size_t)ott * KSD + 2 * ont) * 1024 + voff, ws + OFF_ST3 + (size_t)ott * 4096 + ont * 256 + r * 8, lane);
          arrive4(lcnt, cnt + (C_X3 + grp) * 1024, lane);
        } else {
          auto s0 = __builtin_amdgcn_permlane32_swap(res[0], res[2], false, false);
          auto s1 = __builtin_amdgcn_permlane32_swap(res[1], res[3], false, false);
          auto s2 = __builtin_amdgcn_permlane32_swap(res[4], res[6], false, false);
          auto s3 = __builtin_amdgcn_permlane32_swap(res[5], res[7], false, false);
          const u32x4 a = {s0[0], s1[0], s0[1], s1[1]}, b = {s2[0], s3[0], s2[1], s3[1]};
          char* o = (char*)karg(7) + ((size_t)(ott * 32 + r) * D_ + ont * 32) * 2 + hh * 16;
          st16(o, a);
          st16(o + 32, b);
        }
        }
      }
#undef LD4
#undef MM4
#undef PIN8
      if (layer == 0) {
        FRESH();
        const int tt = qtb * 4 + j;
        const char* xb = ws + OFF_X3 + (size_t)tt * 32768;
        const char* wb = ws + OFF_WQ + (size_t)qnb * 8 * QW + j * 3072;
        f32x4 sp[4];
        asm volatile("" : "+s"(xb), "+s"(wb));
        __builtin_amdgcn_s_barrier();
        poll_b(cnt + (C_X3 + qtb) * 1024 + (qnb & 7) * 128, 16u, lcnt + 1, 5, j);
#define QISSUE(c) do { glds4(voff, xb + (c) * 4096, lds0 + L_QX + ((c) & 3) * 16384 + j * 4096); glds3(voff, wb + (c) * QW, lds0 + ((c) & 3) * QW + j * 3072); } while (0)
        glds4(voff, xb, lds0 + L_QX + j * 4096);
        glds4(voff, xb + 4096, lds0 + L_QX + 16384 + j * 4096);
        stat_issue(sp, ws + OFF_ST3 + (size_t)tt * 4096, voff);
        glds4(voff, xb + 8192, lds0 + L_QX + 32768 + j * 4096);
        glds4(voff, xb + 12288, lds0 + L_QX + 49152 + j * 4096);
        WAITBAR_V(16);
        { LDSP(char) cp = (LDSP(char))smem + L_CS + j * 256 + lane * 4;
          *(LDSP(float))cp = (*(LDSP(float))cp + *(LDSP(float))(cp + 1024)) + (*(LDSP(float))(cp + 2048) + *(LDSP(float))(cp + 3072)); }
        WAITBAR_V(12);
        QISSUE(4); WAITBAR_V(11);
#pragma unroll 1
        for (int c = 5; c < 8; ++c) { QISSUE(c); WAITBAR_V(14); }
#undef QISSUE
        { float mean, var; stat_finish(sp, mean, var); const float rs = rsqrtf(var);
          ((LDSP(float))((LDSP(char))smem + L_ST))[j * 64 + hh * 32 + (lane & 15) * 2 + ((lane >> 4) & 1)] = hh ? mean * rs : rs; }
        WAITBAR_V(7);
        WAITBAR_V(0);
        __builtin_amdgcn_s_barrier();
      }
    }
  }
}

extern "C" void kernel_launch(void* const* d_in, const int* in_sizes, int n_in, void* d_out, int out_size, void* d_ws, size_t ws_size,
                              hipStream_t stream) {
  const char* x = (const char*)d_in[0];
  const char* wqkv = (const char*)d_in[1];
  const char* wo = (const char*)d_in[2];
  const char* wg = (const char*)d_in[4];
  const char* wu = (const char*)d_in[5];
  const char* wd = (const char*)d_in[6];
  char* ws = (char*)d_ws;
  k_qkv1<<<256, 768, 0, stream>>>(x, wqkv, ws);
  k_main<<<256, 512, 0, stream>>>(ws, x, wqkv, wo, wg, wu, wd, (char*)d_out);
}
```

```cpp
#include <hip/hip_runtime.h>
#include <hip/hip_bf16.h>
#include <stdint.h>

typedef short bf16x8 __attribute__((ext_vector_type(8)));
typedef float f32x16 __attribute__((ext_vector_type(16)));
typedef float f32x4 __attribute__((ext_vector_type(4)));
typedef float f32x2 __attribute__((ext_vector_type(2)));
typedef unsigned u32x4 __attribute__((ext_vector_type(4)));
typedef unsigned u32x2 __attribute__((ext_vector_type(2)));
typedef __bf16 bf16v2 __attribute__((ext_vector_type(2)));
typedef unsigned short u16;

#define STAMP(i)
#define STAMP_DECL
#define PSTAMP(i)
#define STAMP_STORE()
#define DIAG_ARG
#define DIAG_PASS(k)
#define DI __device__ __forceinline__
#define MFMA(a, b, c) __builtin_amdgcn_mfma_f32_32x32x16_bf16((a), (b), (c), 0, 0, 0)
#define LDSP(T) __attribute__((address_space(3))) T*

constexpr int S_ = 2048, D_ = 512, H_ = 32, F_ = 1792;
constexpr int NTT = S_ / 32;
constexpr int KSD = D_ / 16;
constexpr int KSF = F_ / 16;
constexpr float QSCALE = 0.25f * 1.4426950408889634f;
constexpr float LN_EPS = 1e-5f;

constexpr size_t KB = 1024;
constexpr size_t OFF_CNT = 0;
constexpr size_t OFF_WO = 192 * KB;
constexpr size_t OFF_WGU = OFF_WO + 512 * KB;
constexpr size_t OFF_WD = OFF_WGU + 3584 * KB;
constexpr size_t OFF_WQ = OFF_WD + 1792 * KB;
constexpr size_t OFF_CSP = OFF_WQ + 1536 * KB;
constexpr size_t OFF_ST3 = OFF_CSP + 324 * KB;
constexpr size_t OFF_X3 = OFF_ST3 + 256 * KB;
constexpr size_t OFF_L = OFF_X3 + 2048 * KB;
constexpr size_t OFF_Q = OFF_L;
constexpr size_t OFF_K = OFF_Q + 2048 * KB;
constexpr size_t OFF_V = OFF_K + 2048 * KB;
constexpr size_t OFF_O = OFF_V + 2048 * KB;
constexpr size_t OFF_X2 = OFF_O + 2048 * KB;
constexpr size_t OFF_ST2 = OFF_X2 + 2048 * KB;
constexpr size_t OFF_H = OFF_ST2 + 256 * KB;
constexpr size_t LSTRIDE = OFF_H + 7168 * KB - OFF_L;
constexpr int KMAIN_KB = 26;
constexpr int C_O = 0, C_X2 = 16, C_H = 48, C_X3 = 80, C_KV = 96, C_PREP = 112, C_H2 = 128, C_N = 160;

template <int I> struct IC { static constexpr int value = I; };
template <int B, int E, class F> DI void sfor(F&& f) { if constexpr (B < E) { f(IC<B>{}); sfor<B + 1, E>(f); } }
DI unsigned pk2(float lo, float hi) {
  f32x2 v = {lo, hi};
  bf16v2 r = __builtin_convertvector(v, bf16v2);
  return __builtin_bit_cast(unsigned, r);
}
DI float lo16(unsigned u) { return __uint_as_float(u << 16); }
DI float hi16(unsigned u) { return __uint_as_float(u & 0xffff0000u); }
DI float xhalf_add(float v) {
  auto r = __builtin_amdgcn_permlane32_swap(__float_as_uint(v), __float_as_uint(v), false, false);
  return __uint_as_float(r[0]) + __uint_as_float(r[1]);
}
DI float xhalf_max(float v) {
  auto r = __builtin_amdgcn_permlane32_swap(__float_as_uint(v), __float_as_uint(v), false, false);
  return fmaxf(__uint_as_float(r[0]), __uint_as_float(r[1]));
}
DI float x16_add(float v) {
  auto r = __builtin_amdgcn_permlane16_swap(__float_as_uint(v), __float_as_uint(v), false, false);
  return __uint_as_float(r[0]) + __uint_as_float(r[1]);
}
DI void st16(void* p, u32x4 v) { asm volatile("global_store_dwordx4 %0, %1, off sc1\n\ts_nop 1" :: "v"(p), "v"(v) : "memory"); }
template <int IMM>
DI void st16s(unsigned voff, const char* sbase, u32x4 v) {
  asm volatile("global_store_dwordx4 %0, %1, %2 offset:%3 sc1\n\ts_nop 1" :: "v"(voff), "v"(v), "s"(sbase), "i"(IMM) : "memory");
}
template <int IMM>
DI void gld16(bf16x8& d, unsigned voff, const char* sbase) {
  asm volatile("global_load_dwordx4 %0, %1, %2 offset:%3 sc1" : "=v"(d) : "v"(voff), "s"(sbase), "i"(IMM) : "memory");
}
#define GLDS_HEAD "s_mov_b32 %0, m0\n\ts_mov_b32 m0, %3\n\ts_nop 0\n\t"
#define GLDS_OP(o) "global_load_lds_dwordx4 %1, %2 offset:" #o " sc1\n\t"
#define GLDS_TAIL "s_mov_b32 m0, %0" : "=&s"(keep) : "v"(voff), "s"(sbase), "s"(lds) : "memory"
DI void glds1(unsigned voff, const char* sbase, unsigned lds) { unsigned keep; asm volatile(GLDS_HEAD GLDS_OP(0) GLDS_TAIL); }
DI void glds2(unsigned voff, const char* sbase, unsigned lds) { unsigned keep; asm volatile(GLDS_HEAD GLDS_OP(0) GLDS_OP(1024) GLDS_TAIL); }
DI void glds3(unsigned voff, const char* sbase, unsigned lds) { unsigned keep; asm volatile(GLDS_HEAD GLDS_OP(0) GLDS_OP(1024) GLDS_OP(2048) GLDS_TAIL); }
DI void glds4(unsigned voff, const char* sbase, unsigned lds) { unsigned keep; asm volatile(GLDS_HEAD GLDS_OP(0) GLDS_OP(1024) GLDS_OP(2048) GLDS_OP(3072) GLDS_TAIL); }
#define SBAR() __builtin_amdgcn_sched_barrier(0)
#define PIN(x) asm volatile("" : "+v"(x))
#define WAITV(n) asm volatile("s_waitcnt vmcnt(%0)" :: "i"(n) : "memory")
#define WAITBAR_V(n) asm volatile("s_waitcnt vmcnt(%0) lgkmcnt(0)\n\ts_barrier" :: "i"(n) : "memory")
#define WAITBAR_L() do { asm volatile("" ::: "memory"); __builtin_amdgcn_s_waitcnt(0xc07f); __builtin_amdgcn_s_barrier(); asm volatile("" ::: "memory"); } while (0)
typedef short s16x4 __attribute__((ext_vector_type(4)));
DI s16x4 trread(LDSP(const char) p) { return __builtin_amdgcn_ds_read_tr16_b64_v4i16((LDSP(s16x4))p); }
DI bf16x8 trfrag(LDSP(const char) p, int off2) { const s16x4 lo = trread(p), hi = trread(p + off2); return __builtin_shufflevector(lo, hi, 0, 1, 2, 3, 4, 5, 6, 7); }
DI bf16x8 ldfrag(LDSP(const char) p) { return *(LDSP(const bf16x8))p; }
DI float dot2(unsigned a, unsigned b, float c) { return __builtin_amdgcn_fdot2_f32_bf16(__builtin_bit_cast(bf16v2, a), __builtin_bit_cast(bf16v2, b), c, false); }
constexpr unsigned ONE2 = 0x3f803f80u;
#define BC8(x) __builtin_bit_cast(bf16x8, x)

template <int LOOK, int PER> DI void ring_wait(int i) {
  if (i <= 8 - LOOK) WAITBAR_V((LOOK - 1) * PER);
  else if (LOOK > 3 && i == 5) WAITBAR_V(2 * PER);
  else if (LOOK > 2 && i == 6) WAITBAR_V(PER);
  else WAITBAR_V(0);
}

DI void qk_store(const float (&v)[16], char* o) {
  u32x4 a = {pk2(v[0], v[1]), pk2(v[2], v[3]), pk2(v[4], v[5]), pk2(v[6], v[7])};
  u32x4 b = {pk2(v[8], v[9]), pk2(v[10], v[11]), pk2(v[12], v[13]), pk2(v[14], v[15])};
  st16(o, a);
  st16(o + NTT * 1024, b);
}
DI void v_store(const float (&v)[16], char* o) {
  auto s0 = __builtin_amdgcn_permlane32_swap(pk2(v[0], v[1]), pk2(v[4], v[5]), false, false);
  auto s1 = __builtin_amdgcn_permlane32_swap(pk2(v[2], v[3]), pk2(v[6], v[7]), false, false);
  auto s2 = __builtin_amdgcn_permlane32_swap(pk2(v[8], v[9]), pk2(v[12], v[13]), false, false);
  auto s3 = __builtin_amdgcn_permlane32_swap(pk2(v[10], v[11]), pk2(v[14], v[15]), false, false);
  const u32x4 a = {s0[0], s1[0], s0[1], s1[1]}, b = {s2[0], s3[0], s2[1], s3[1]};
  st16(o, a);
  st16(o + 512, b);
}
typedef __bf16 bfv8 __attribute__((ext_vector_type(8)));
typedef __bf16 bfv16 __attribute__((ext_vector_type(16)));
typedef float f32x8 __attribute__((ext_vector_type(8)));
template <int H> DI f32x16 smf(bf16x8 a, const f32x16& s, f32x16 c, int idx) {
  const f32x8 p = __builtin_shufflevector(s, s, 8 * H, 8 * H + 1, 8 * H + 2, 8 * H + 3, 8 * H + 4, 8 * H + 5, 8 * H + 6, 8 * H + 7);
  return __builtin_amdgcn_smfmac_f32_32x32x32_bf16(__builtin_bit_cast(bfv8, a), __builtin_bit_cast(bfv16, p), c, idx, 0, 0);
}
DI float row16_sum(float v) {
  v += __builtin_bit_cast(float, __builtin_amdgcn_update_dpp(0, __builtin_bit_cast(int, v), 0x128, 0xf, 0xf, false));
  v += __builtin_bit_cast(float, __builtin_amdgcn_update_dpp(0, __builtin_bit_cast(int, v), 0x124, 0xf, 0xf, false));
  v += __builtin_bit_cast(float, __builtin_amdgcn_update_dpp(0, __builtin_bit_cast(int, v), 0x122, 0xf, 0xf, false));
  v += __builtin_bit_cast(float, __builtin_amdgcn_update_dpp(0, __builtin_bit_cast(int, v), 0x121, 0xf, 0xf, false));
  return v;
}
#define KSTEP(B, u) do { SBAR(); RX((B) ^ 1, u); RW((B) ^ 1, 0, u); SBAR(); acc[0] = MFMA(wf[B][0], xf[B], acc[0]); SBAR(); RW((B) ^ 1, 1, u); RW((B) ^ 1, 2, u); SBAR(); \
                         acc[1] = MFMA(wf[B][1], xf[B], acc[1]); acc[2] = MFMA(xf[B], wf[B][2], acc[2]); SBAR(); } while (0)
#define QCHUNK() do { KSTEP(1, 0); KSTEP(0, 1); KSTEP(1, 2); KSTEP(0, 3); } while (0)
#define QINIT() \
  f32x16 acc[3]; bf16x8 wf[2][3], xf[2]; \
  _Pragma("unroll") for (int i = 0; i < 3; ++i) { acc[i] = f32x16{}; wf[1][i] = bf16x8{}; } \
  xf[1] = bf16x8{}
#define QFINISH() do { acc[0] = MFMA(wf[1][0], xf[1], acc[0]); acc[1] = MFMA(wf[1][1], xf[1], acc[1]); acc[2] = MFMA(xf[1], wf[1][2], acc[2]); } while (0)

extern "C" __global__ __launch_bounds__(768, 3) void k_qkv1(const char* __restrict__ x, const char* __restrict__ wn, char* __restrict__ ws DIAG_ARG) {
  STAMP_DECL; STAMP(0);
  constexpr int LOOK = 3, CH = 24576, LXN = 4 * CH;
  __shared__ __attribute__((aligned(16))) char smem[LXN + 65536];
  const int tid = threadIdx.x, lane = tid & 63, r = lane & 31, hh = lane >> 5;
  const int w = __builtin_amdgcn_readfirstlane(tid >> 6);
  const int xcd = blockIdx.x & 7, idx = blockIdx.x >> 3;
  const int tb = (xcd & 3) * 8 + (idx & 7), nq = (xcd >> 2) * 4 + (idx >> 3);
  const unsigned lds0 = (unsigned)(uintptr_t)smem;
  const int rot = 2 * (idx & 3);
  if (w >= 8) {
    const int j = w - 8;
    const unsigned wv = (lane >> 3) * 3072 + (((lane & 7) ^ (((lane >> 4) & 1) << 2)) * 16);
    const char* wb = wn + (size_t)j * 16 * 3072 + nq * 128;
    if (blockIdx.x < 5) *(unsigned*)(ws + OFF_CNT + ((blockIdx.x * 4 + j) * 64 + lane) * 128) = 0u;
    asm volatile("s_sleep 8\n\ts_nop 4" ::: "memory");
#pragma unroll 1
    for (int c = 0; c < 8 + LOOK; ++c) {
      if (c >= LOOK) ring_wait<LOOK, 6>(c - LOOK);
      if (c < 8) {
        const char* ws_ = wb + (size_t)((c + rot) & 7) * 64 * 3072;
        const unsigned d = lds0 + (c & 3) * CH + j * 2048;
        glds1(wv, ws_, d); glds1(wv, ws_ + 1024, d + 8192); glds1(wv, ws_ + 2048, d + 16384);
        glds1(wv, ws_ + 8 * 3072, d + 1024); glds1(wv, ws_ + 8 * 3072 + 1024, d + 8192 + 1024); glds1(wv, ws_ + 8 * 3072 + 2048, d + 16384 + 1024);
      }
    }
    return;
  }
  {
    const int n = w;
    const u32x4* xr = (const u32x4*)(x + (size_t)(tb * 64 + n * 8) * 1024) + lane;
    u32x4 v[8];
#pragma unroll
    for (int i = 0; i < 8; ++i) v[i] = xr[i * 64];
    float s[8], q[8];
#pragma unroll
    for (int i = 0; i < 8; ++i) {
      s[i] = 0.f; q[i] = 0.f;
#pragma unroll
      for (int e = 0; e < 4; ++e) { s[i] = dot2(v[i][e], ONE2, s[i]); q[i] = dot2(v[i][e], v[i][e], q[i]); }
    }
    float s4[4], q4[4], s2[2], q2[2];
#pragma unroll
    for (int i = 0; i < 4; ++i) {
      const int lo = (i & 1) + 4 * (i >> 1);
      auto a = __builtin_amdgcn_permlane32_swap(__float_as_uint(s[lo]), __float_as_uint(s[lo + 2]), false, false);
      auto b = __builtin_amdgcn_permlane32_swap(__float_as_uint(q[lo]), __float_as_uint(q[lo + 2]), false, false);
      s4[i] = __uint_as_float(a[0]) + __uint_as_float(a[1]); q4[i] = __uint_as_float(b[0]) + __uint_as_float(b[1]);
    }
#pragma unroll
    for (int i = 0; i < 2; ++i) {
      auto a = __builtin_amdgcn_permlane16_swap(__float_as_uint(s4[2 * i]), __float_as_uint(s4[2 * i + 1]), false, false);
      auto b = __builtin_amdgcn_permlane16_swap(__float_as_uint(q4[2 * i]), __float_as_uint(q4[2 * i + 1]), false, false);
      s2[i] = row16_sum(__uint_as_float(a[0]) + __uint_as_float(a[1])); q2[i] = row16_sum(__uint_as_float(b[0]) + __uint_as_float(b[1]));
    }
    float ra[2], rb[2];
#pragma unroll
    for (int i = 0; i < 2; ++i) {
      const float mean = s2[i] * (1.f / D_);
      ra[i] = rsqrtf(fmaxf(q2[i] * (1.f / D_) - mean * mean, 0.f) + LN_EPS);
      rb[i] = -mean * ra[i];
    }
    LDSP(char) xd = (LDSP(char))smem + LXN + (lane >> 3) * 8192 + n * 1024;
#pragma unroll
    for (int i = 0; i < 8; ++i) {
      const float a = __builtin_bit_cast(float, __builtin_amdgcn_readlane(__builtin_bit_cast(int, ra[i >> 2]), (i & 3) * 16));
      const float b = __builtin_bit_cast(float, __builtin_amdgcn_readlane(__builtin_bit_cast(int, rb[i >> 2]), (i & 3) * 16));
      u32x4 o;
#pragma unroll
      for (int e = 0; e < 4; ++e) o[e] = pk2(lo16(v[i][e]) * a + b, hi16(v[i][e]) * a + b);
      *(LDSP(u32x4))(xd + i * 128 + (((lane & 7) ^ ((4 * n + (i >> 1)) & 7)) * 16)) = o;
    }
  }
  STAMP(1);
  WAITBAR_L();
  if (w >= 4) {
    if (w == 4 && idx < KMAIN_KB) {
      bf16x8 t;
      asm volatile("s_getpc_b64 s[20:21]\n\ts_add_u32 s20, s20, k_main@rel32@lo+4\n\ts_addc_u32 s21, s21, k_main@rel32@hi+12\n\t"
                   "global_load_dwordx4 %0, %1, s[20:21]\n\ts_waitcnt vmcnt(0)" : "=v"(t) : "v"(lane * 16 + idx * 1024) : "s20", "s21", "scc", "memory");
    }
    return;
  }
  STAMP(2);
  const int ta = w & 1, hp = w >> 1, q4 = (lane & 15) >> 2, xsw = (r >> 1) & 7;
  LDSP(const char) lw = (LDSP(const char))smem + (8 * hh + q4) * 128 + ((hp ^ (q4 >> 1)) * 64) + ((lane >> 4) & 1) * 32 + (lane & 3) * 8;
  LDSP(const char) lx = (LDSP(const char))smem + LXN + ta * 4096 + r * 128;
  int xo[4];
#pragma unroll
  for (int u = 0; u < 4; ++u) xo[u] = ((2 * u + hh) ^ xsw) * 16;
  QINIT();
#define RX(B, u) xf[B] = ldfrag(sx + xo[u])
#define RW(B, i, u) wf[B][i] = trfrag(sw + (i) * 8192 + (u) * 2048, 512)
#pragma unroll 1
  for (int c = 0; c < 8; ++c) {
    LDSP(const char) sw = lw + (c & 3) * CH;
    LDSP(const char) sx = lx + ((c + rot) & 7) * 8192;
    QCHUNK();
    WAITBAR_L();
  }
  QFINISH();
#undef RX
#undef RW
  STAMP(4);
  const int tt = tb * 2 + ta, nb = nq * 2 + hp;
  float v[16];
#pragma unroll
  for (int e = 0; e < 16; ++e) v[e] = acc[0][e] * QSCALE;
  qk_store(v, ws + OFF_Q + ((size_t)(2 * nb) * NTT + tt) * 1024 + lane * 16);
#pragma unroll
  for (int e = 0; e < 16; ++e) v[e] = acc[1][e];
  qk_store(v, ws + OFF_K + ((size_t)(2 * nb) * NTT + tt) * 1024 + lane * 16);
#pragma unroll
  for (int e = 0; e < 16; ++e) v[e] = acc[2][e];
  v_store(v, ws + OFF_V + ((size_t)(2 * nb + (r >> 4)) * NTT + tt) * 1024 + hh * 256 + (r & 15) * 16);
  STAMP(5); STAMP_STORE();
}

DI void poll(const char* cnt, unsigned voff, unsigned target) {
#pragma unroll 1
  for (unsigned spins = 0; spins < 200000u; ++spins) {
    unsigned v;
    asm volatile("global_load_dword %0, %1, %2 sc1\n\ts_waitcnt vmcnt(0)" : "=v"(v) : "v"(voff), "s"(cnt) : "memory");
    if (__all(v >= target)) break;
  }
}
DI void poll_b(const char* cnt, unsigned target, LDSP(unsigned) go, unsigned seq, int j) {
  if (j == 0) {
    poll(cnt, 0u, target);
    __hip_atomic_store(go, seq, __ATOMIC_RELAXED, __HIP_MEMORY_SCOPE_WORKGROUP);
  } else {
    WAITV(0);
#pragma unroll 1
    for (unsigned spins = 0; spins < 4000000u; ++spins) {
      if (__builtin_amdgcn_readfirstlane(__hip_atomic_load(go, __ATOMIC_RELAXED, __HIP_MEMORY_SCOPE_WORKGROUP)) >= seq) break;
    }
  }
  asm volatile("" ::: "memory");
}
DI void arrive4(LDSP(unsigned) lw, char* cnt, int lane) {
  WAITV(0);
  unsigned old = 0;
  if (lane == 0) old = __hip_atomic_fetch_add(lw, 1u, __ATOMIC_RELAXED, __HIP_MEMORY_SCOPE_WORKGROUP);
  old = __builtin_amdgcn_readfirstlane(old);
  if ((old & 3u) == 3u && lane < 8) asm volatile("global_atomic_add %0, %1, %2" :: "v"(lane * 128), "v"(1u), "s"(cnt) : "memory");
  asm volatile("" ::: "memory");
}
DI void stat_issue(f32x4 (&p)[4], const char* sb, unsigned voff) {
  asm volatile("global_load_dwordx4 %0, %4, %5 sc1\n\tglobal_load_dwordx4 %1, %4, %5 offset:1024 sc1\n\tglobal_load_dwordx4 %2, %4, %5 offset:2048 sc1\n\t"
               "global_load_dwordx4 %3, %4, %5 offset:3072 sc1" : "=&v"(p[0]), "=&v"(p[1]), "=&v"(p[2]), "=&v"(p[3]) : "v"(voff), "s"(sb) : "memory");
}
DI void stat_finish(f32x4 (&p)[4], float& mean, float& var) {
  asm volatile("" : "+v"(p[0]), "+v"(p[1]), "+v"(p[2]), "+v"(p[3]));
  const f32x4 t = (p[0] + p[1]) + (p[2] + p[3]);
  auto a = __builtin_amdgcn_permlane16_swap(__float_as_uint(t[0]), __float_as_uint(t[2]), false, false);
  auto b = __builtin_amdgcn_permlane16_swap(__float_as_uint(t[1]), __float_as_uint(t[3]), false, false);
  mean = xhalf_add(__uint_as_float(a[0]) + __uint_as_float(a[1])) * (1.f / D_);
  var = fmaxf(xhalf_add(__uint_as_float(b[0]) + __uint_as_float(b[1])) * (1.f / D_) - mean * mean, 0.f) + LN_EPS;
}
DI void colsum_part(const char* src, unsigned voff, LDSP(char) dst) {
  f32x4 p[4];
  asm volatile("global_load_dwordx4 %0, %4, %5 sc1\n\tglobal_load_dwordx4 %1, %4, %6 sc1\n\tglobal_load_dwordx4 %2, %4, %7 sc1\n\t"
               "global_load_dwordx4 %3, %4, %8 sc1\n\ts_waitcnt vmcnt(0)" : "=&v"(p[0]), "=&v"(p[1]), "=&v"(p[2]), "=&v"(p[3])
               : "v"(voff), "s"(src), "s"(src + 20480), "s"(src + 40960), "s"(src + 61440) : "memory");
  *(LDSP(f32x4))dst = (p[0] + p[1]) + (p[2] + p[3]);
}

#define RES_FINISH(WR, RD) do { \
    _Pragma("unroll") for (int i = 1; i < 4; ++i) _Pragma("unroll") for (int g = 0; g < 4; ++g) { \
      const f32x4 v_ = {acc[i][4 * g], acc[i][4 * g + 1], acc[i][4 * g + 2], acc[i][4 * g + 3]}; \
      *(LDSP(f32x4))(lb + (WR) + g * 1024) = v_; } \
    asm volatile("s_waitcnt vmcnt(0)" : "+v"(rn[0]), "+v"(rn[1]), "+v"(rn[2]), "+v"(rn[3]) :: "memory"); \
    WAITBAR_L(); \
    _Pragma("unroll") for (int g = 0; g < 4; ++g) { \
      f32x4 t_ = {acc[0][4 * g], acc[0][4 * g + 1], acc[0][4 * g + 2], acc[0][4 * g + 3]}; \
      f32x4 u_[3]; \
      _Pragma("unroll") for (int i = 1; i < 4; ++i) u_[i - 1] = *(LDSP(const f32x4))(lb + (RD) + g * 1024); \
      t_ = (u_[0] + u_[1]) + (u_[2] + t_); \
      res[2 * g] = pk2(t_[0] + lo16(rn[g][0]), t_[1] + hi16(rn[g][0])); \
      res[2 * g + 1] = pk2(t_[2] + lo16(rn[g][1]), t_[3] + hi16(rn[g][1])); } } while (0)
DI void res_load(u32x2 (&rn)[4], unsigned vo, const char* p0, int d1, int d2) {
  asm volatile("global_load_dwordx2 %0, %4, %5 sc1\n\tglobal_load_dwordx2 %1, %4, %6 sc1\n\tglobal_load_dwordx2 %2, %4, %7 sc1\n\tglobal_load_dwordx2 %3, %4, %8 sc1"
               : "=&v"(rn[0]), "=&v"(rn[1]), "=&v"(rn[2]), "=&v"(rn[3]) : "v"(vo), "s"(p0), "s"(p0 + d1), "s"(p0 + d2), "s"(p0 + d2 + d1) : "memory");
}
DI void res_store_tiled(const unsigned (&res)[8], char* o, char* st, int lane) {
  const u32x4 p0 = {res[0], res[1], res[2], res[3]}, p1 = {res[4], res[5], res[6], res[7]};
  st16(o, p0);
  st16(o + 1024, p1);
  float s1 = 0.f, s2 = 0.f, t1 = 0.f, t2 = 0.f;
#pragma unroll
  for (int e = 0; e < 4; ++e) { s1 = dot2(res[e], ONE2, s1); s2 = dot2(res[e], res[e], s2); t1 = dot2(res[4 + e], ONE2, t1); t2 = dot2(res[4 + e], res[4 + e], t2); }
  s1 = xhalf_add(s1 + t1); s2 = xhalf_add(s2 + t2);
  if (lane < 32) { const f32x2 v = {s1, s2}; asm volatile("global_store_dwordx2 %0, %1, off sc1" :: "v"(st), "v"(v) : "memory"); }
}

DI int hsrc(int p) { const int g = p / 28, q = p - g * 28; return g * 28 + (q < 16 ? (q >> 2) * 7 + (q & 3) : ((q - 16) / 3) * 7 + 4 + (q - 16) % 3); }
DI const char* karg(int i) {
  const __attribute__((address_space(4))) unsigned long long* ka = (const __attribute__((address_space(4))) unsigned long long*)__builtin_amdgcn_kernarg_segment_ptr();
  asm volatile("" : "+s"(ka));
  return (const char*)ka[i];
}
extern "C" __global__ __launch_bounds__(512, 2) void k_main(char* __restrict__ ws_, const char* x_, const char* wq_, const char* wo_, const char* wg_,
                                                            const char* wu_, const char* wd_, char* out_ DIAG_ARG) {
  constexpr int L_K = 0, L_V = 65536, L_ONE = 131072, L_CMB = 133120;
  constexpr int GS = 32768, L_RED = 3 * GS;
  constexpr int L_D0 = 3 * GS, L_D1 = 2 * GS, L_D2 = 0;
  constexpr int QW = 12288, L_QX = 49152;
  constexpr int L_CS = 157696, L_ST = L_CS + 4096, L_CNT = L_ST + 1024, L_DBG = L_CNT + 64;
  __shared__ __attribute__((aligned(16))) char smem[163840];
  const int w_ = __builtin_amdgcn_readfirstlane(threadIdx.x >> 6);
  const unsigned lds0 = (unsigned)(uintptr_t)smem;
#define FRESH() \
  int tid; asm volatile("v_mbcnt_lo_u32_b32 %0, -1, 0\n\tv_mbcnt_hi_u32_b32 %0, -1, %0" : "=v"(tid)); int bid = blockIdx.x, w = w_; char* ws = ws_; asm volatile("" : "+s"(bid), "+s"(w), "+s"(ws)); \
  tid += w * 64; const int j = w & 3; \
  const int lane = tid & 63, r = lane & 31, hh = lane >> 5, xcd = bid & 7, idx = bid >> 3, grp = xcd * 2 + (idx >> 4), m = idx & 15; \
  const unsigned voff = lane * 16; LDSP(char) lb = (LDSP(char))smem + lane * 16; char* cnt = ws + OFF_CNT; char* wl = ws + layer * LSTRIDE; \
  LDSP(unsigned) lcnt = (LDSP(unsigned))((LDSP(char))smem + L_CNT) + (w >> 2); \
  const int ont = (m >> 1) * 2 + (j >> 1), ott = grp * 4 + (m & 1) * 2 + (j & 1); \
  const int gtb = (xcd & 1) * 8 + (idx & 7), gnb = (xcd >> 1) * 4 + (idx >> 3), qtb = (xcd & 3) * 4 + (idx & 3), qnb = (xcd >> 2) * 8 + (idx >> 2); \
  (void)r; (void)hh; (void)voff; (void)lb; (void)cnt; (void)wl; (void)lcnt; (void)ont; (void)ott; (void)gtb; (void)gnb; (void)qtb; (void)qnb
  if (threadIdx.x < 8) ((LDSP(unsigned))((LDSP(char))smem + L_CNT))[threadIdx.x] = 0u;
#pragma unroll 1
  for (int layer = 0; layer < 2; ++layer) {
    {
      FRESH();
      const int head = xcd * 4 + (idx & 3), qb = idx >> 2, half = w >> 2, tt0 = qb * 8 + 2 * j;
      PSTAMP(0);
      if (layer) { if (w == 4) poll(cnt + (C_KV + (head >> 1)) * 1024 + qb * 128, 0u, 16u); WAITBAR_L(); }
      PSTAMP(1);
      const char* ks = wl + OFF_K + (size_t)head * 65536 + half * 32768;
      const unsigned kd = lds0 + L_K + half * 32768;
      bf16x8 q0, q1;
      gld16<0>(q0, voff, wl + OFF_Q + ((size_t)head * NTT + tt0) * 1024);
      gld16<1024>(q1, voff, wl + OFF_Q + ((size_t)head * NTT + tt0) * 1024);
      glds2(voff, ks + j * 2048, kd + j * 2048);
      glds2(voff, ks + (OFF_V - OFF_K) + j * 2048, kd + L_V + j * 2048);
      *(LDSP(unsigned))((LDSP(char))smem + L_ONE + tid * 4) = ((tid >> 2) & 11) == 0 ? ONE2 : 0u;
      asm volatile("s_waitcnt vmcnt(0) lgkmcnt(0)\n\ts_barrier" : "+v"(q0), "+v"(q1) :: "memory");
      PSTAMP(2);
      LDSP(const char) kp = lb + L_K;
      LDSP(const char) vp0 = (LDSP(const char))smem + (r < 16 ? L_V : L_ONE) + hh * 256 + (r & 15) * 16;
      const int vstep = r < 16 ? 2048 : 0;
      LDSP(const char) kq = kp + half * 32768; LDSP(const char) vq = vp0 + half * 16 * vstep;
      f32x16 n0, n1, sA, sB = f32x16{}, o0 = f32x16{}, o1 = f32x16{};
      bf16x8 kA, kB, vp = bf16x8{};
      int sidx = (int)0xDDDDDDDDu; PIN(sidx);
      {
        const bf16x8 k0 = ldfrag(kp);
        kA = ldfrag(kq);
        const f32x16 s = MFMA(k0, q0, f32x16{}), t = MFMA(k0, q1, f32x16{});
        float a = fmaxf(fmaxf(s[0], s[1]), s[2]), b = fmaxf(fmaxf(t[0], t[1]), t[2]);
#pragma unroll
        for (int i = 3; i < 15; i += 2) { a = fmaxf(fmaxf(a, s[i]), s[i + 1]); b = fmaxf(fmaxf(b, t[i]), t[i + 1]); }
        a = xhalf_max(fmaxf(a, s[15])); b = xhalf_max(fmaxf(b, t[15]));
#pragma unroll
        for (int i = 0; i < 16; ++i) { n0[i] = -a; n1[i] = -b; }
        PIN(n0); PIN(n1);
        sA = MFMA(kA, q0, n0);
      }
#define EX(S, i) S[i] = __builtin_amdgcn_exp2f(S[i])
#define STEPN(CUR, NXT, KN_, QN, NN, O, VA, OP, VP, READS) do { \
      EX(CUR, 0); EX(CUR, 1); PIN(CUR); SBAR(); OP = smf<1>(VP, NXT, OP, sidx); SBAR(); READS; SBAR(); \
      EX(CUR, 2); EX(CUR, 3); EX(CUR, 4); EX(CUR, 5); PIN(CUR); SBAR(); NXT = MFMA(KN_, QN, NN); SBAR(); \
      EX(CUR, 6); EX(CUR, 7); EX(CUR, 8); EX(CUR, 9); EX(CUR, 10); PIN(CUR); SBAR(); O = smf<0>(VA, CUR, O, sidx); SBAR(); \
      EX(CUR, 11); EX(CUR, 12); EX(CUR, 13); EX(CUR, 14); EX(CUR, 15); PIN(CUR); SBAR(); } while (0)
#define TILE(t, KA, KB) do { bf16x8 va, vb; \
      STEPN(sA, sB, KA, q1, n1, o0, va, o1, vp, (va = ldfrag(vq + (t) * 1024), vb = ldfrag(vq + (t) * 1024 + 512), KB = ldfrag(kq + ((t) + 1) * 1024))); \
      STEPN(sB, sA, KB, q0, n0, o1, va, o0, vb, (vp = vb)); } while (0)
      __builtin_amdgcn_s_setprio(3);
#pragma unroll 1
      for (int ph = -2; ph < 5; ++ph) {
        if (ph == -1) {
          glds3(voff, ks + 8192 + j * 6144, kd + 8192 + j * 6144);
          glds3(voff, ks + (OFF_V - OFF_K) + 8192 + j * 6144, kd + L_V + 8192 + j * 6144);
          glds3(voff, ks + 11264 + j * 6144, kd + 11264 + j * 6144);
          glds3(voff, ks + (OFF_V - OFF_K) + 11264 + j * 6144, kd + L_V + 11264 + j * 6144);
        }
        if (ph == 0) {
          WAITV(0);
          if (lane == 0) __hip_atomic_fetch_add(lcnt + 4, 1u, __ATOMIC_RELAXED, __HIP_MEMORY_SCOPE_WORKGROUP);
#pragma unroll 1
          for (unsigned spins = 0; spins < 4000000u; ++spins)
            if (__builtin_amdgcn_readfirstlane(__hip_atomic_load(lcnt + 4, __ATOMIC_RELAXED, __HIP_MEMORY_SCOPE_WORKGROUP)) >= 4u * (layer + 1)) break;
          asm volatile("" ::: "memory");
        }
        if (ph == 1) __builtin_amdgcn_s_setprio(2);
        if (ph == 3) __builtin_amdgcn_s_setprio(1);
        if (ph == 4) __builtin_amdgcn_s_setprio(0);
        if (layer == 0 && ph >= 0) {
          const int q4 = (lane & 15) >> 2, b16 = (lane >> 4) & 1;
          const int scr = L_CMB + (half ? j * 5120 : 20480 + j * 2048);
          const int jo = ((ph - 1) * 8 + w) * 256 + bid;
          if ((ph == 1 || ph == 2) && jo < 3712) {
            const bool gu = jo < 1792;
            LDSP(const char) sp = (LDSP(const char))smem + scr + (lane & 3) * 8 +
                                  (gu ? (16 * hh + 4 * b16 + q4) * 32 : (4 * hh + q4) * 64 + b16 * 32);
            const int ho = gu ? 256 : 512;
            const char* pd;
            if (gu) { const int ft = jo >> 4, S = jo & 15, nb = ft / 7, ii = ft - nb * 7; pd = ws + OFF_WGU + (size_t)(nb * 224 + (ii < 4 ? S * 8 + 2 * ii : 128 + S * 6 + 2 * (ii - 4))) * 1024; }
            else if (jo < 2048) pd = ws + OFF_WO + (size_t)(jo - 1792) * 2048;
            else if (jo < 2944) pd = ws + OFF_WD + (size_t)(jo - 2048) * 2048;
            else { const int t = (jo - 2944) >> 4, nb = t / 3, ii = t - nb * 3, s0 = (jo & 15) * 2; pd = ws + OFF_WQ + (size_t)(((nb * 8 + (s0 >> 2)) * 3 + ii) * 4 + (s0 & 3)) * 1024; }
            WAITV(0);
            const u32x4 fa = __builtin_bit_cast(u32x4, trfrag(sp, ho)), fb = __builtin_bit_cast(u32x4, trfrag(sp + 1024, ho));
            st16s<0>(voff, pd, fa); st16s<1024>(voff, pd, fb);
            if (gu || jo >= 2944) {
              float ca = (lo16(fa[0]) + hi16(fa[0])) + (lo16(fa[1]) + hi16(fa[1])) + (lo16(fa[2]) + hi16(fa[2])) + (lo16(fa[3]) + hi16(fa[3]));
              float cb = (lo16(fb[0]) + hi16(fb[0])) + (lo16(fb[1]) + hi16(fb[1])) + (lo16(fb[2]) + hi16(fb[2])) + (lo16(fb[3]) + hi16(fb[3]));
              if (gu) { ca = xhalf_add(x16_add(ca)); cb = xhalf_add(x16_add(cb)); ca = lane < 16 ? ca : cb; }
              else ca = xhalf_add(ca + cb);
              const char* cp = ws + OFF_CSP + ((jo & 15) * 5120 + ((gu ? jo : jo - 2944 + 1792) >> 4) * 32) * 4;
              if (lane < 32) asm volatile("global_store_dword %0, %1, %2 sc1" :: "v"(lane * 4), "v"(ca), "s"(cp) : "memory");
            }
          }
          const int jn = jo + 2048;
          if (ph < 2 && jn < 3712) {
            const char *sa, *sb; unsigned gv;
            if (jn < 1792) {
              const int o = 32 * (jn & 15) * (F_ * 2) + (jn >> 4) * 32;
              gv = (lane >> 1) * (F_ * 2) + (lane & 1) * 16; sa = karg(4) + o; sb = karg(5) + o;
            } else {
              int nt, s0, s1, ld = 1024, ai;
              if (jn < 2048) { const int f = (jn - 1792) * 2; nt = f >> 5; s0 = f & 31; s1 = s0 + 1; ai = 3; }
              else if (jn < 2944) { const int f = (jn - 2048) * 2; nt = f / KSF; s0 = hsrc(f - nt * KSF); s1 = hsrc(f - nt * KSF + 1); ai = 6; }
              else { const int t = (jn - 2944) >> 4, nb = t / 3; s0 = (jn & 15) * 2; s1 = s0 + 1; nt = (t - nb * 3) * 16 + nb; ai = 2; ld = 3072; }
              gv = (lane >> 2) * ld + (lane & 3) * 16;
              const char* src = karg(ai) + nt * 64;
              sa = src + 16 * s0 * ld; sb = src + 16 * s1 * ld;
            }
            __builtin_amdgcn_s_waitcnt(0xc07f);
            glds1(gv, sa, lds0 + scr); glds1(gv, sb, lds0 + scr + 1024);
          }
          if (ph == 3) arrive4(lcnt, cnt + (C_PREP + xcd) * 1024, lane);
        }
        int n = ph <= 0 ? ph + 3 : ph == 2 ? 2 : ph == 4 ? 2 : 3;
#pragma unroll 1
        do { TILE(0, kA, kB); TILE(1, kB, kA); kq += 2048; vq += vstep; } while (--n > 0);
      }
      o1 = smf<1>(vp, sB, o1, sidx);
#undef TILE
#undef STEPN
      PSTAMP(3);
      LDSP(char) xp = lb + L_CMB + j * 5120;
      if (half == 1) {
        const f32x4 a = {o0[0], o0[1], o0[2], o0[3]}, b = {o0[4], o0[5], o0[6], o0[7]}, c = {o1[0], o1[1], o1[2], o1[3]}, d = {o1[4], o1[5], o1[6], o1[7]};
        const f32x2 l = {o0[8], o1[8]};
        *(LDSP(f32x4))xp = a; *(LDSP(f32x4))(xp + 1024) = b; *(LDSP(f32x4))(xp + 2048) = c; *(LDSP(f32x4))(xp + 3072) = d; *(LDSP(f32x2))(xp + 4096) = l;
      }
      if (layer == 0 && w == 0) poll(cnt + C_PREP * 1024 + (idx & 7) * 128, (lane & 7) * 1024, 64u);
      WAITBAR_L();
      if (half == 0) {
        __builtin_amdgcn_s_setprio(3);
        PSTAMP(11);
        const f32x4 a = *(LDSP(const f32x4))xp, b = *(LDSP(const f32x4))(xp + 1024), c = *(LDSP(const f32x4))(xp + 2048), d = *(LDSP(const f32x4))(xp + 3072);
        const f32x2 l = *(LDSP(const f32x2))(xp + 4096);
#pragma unroll
        for (int i = 0; i < 4; ++i) { o0[i] += a[i]; o0[4 + i] += b[i]; o1[i] += c[i]; o1[4 + i] += d[i]; }
        o0[8] += l[0]; o1[8] += l[1];
        float chk = (o0[8] + o1[8]) * 5.4210109e-20f;
#pragma unroll
        for (int i = 0; i < 8; ++i) chk = __builtin_fmaf(o0[i], 0.f, __builtin_fmaf(o1[i], 0.f, chk));
        const bool bad = !(chk < 1.f);
        if (__builtin_expect(__any(bad), 0)) {
#pragma unroll 1
          for (int z = 0; z < 2; ++z) {
            const bf16x8 q = z ? q1 : q0;
            float mx = -INFINITY;
            f32x16 o = f32x16{};
            kq = kp; vq = vp0;
#pragma unroll 1
            for (int t = 0; t < 64; ++t) {
              const bf16x8 k = ldfrag(kq), v0 = ldfrag(vq), v1 = ldfrag(vq + 512);
              f32x16 s = MFMA(k, q, f32x16{});
              float tm = s[0];
#pragma unroll
              for (int i = 1; i < 16; ++i) tm = fmaxf(tm, s[i]);
              tm = xhalf_max(tm);
              const float mn = fmaxf(mx, tm);
              const float alpha = __builtin_amdgcn_exp2f(mx - mn);
              mx = mn;
#pragma unroll
              for (int i = 0; i < 16; ++i) { o[i] *= alpha; s[i] = __builtin_amdgcn_exp2f(s[i] - mn); }
              o = smf<0>(v0, s, o, sidx);
              o = smf<1>(v1, s, o, sidx);
              kq += 1024; vq += vstep >> 1;
            }
            if (z) o1 = o; else o0 = o;
          }
        }
        const float i0 = __builtin_amdgcn_rcpf(o0[8]), i1 = __builtin_amdgcn_rcpf(o1[8]);
        const u32x4 a0 = {pk2(o0[0] * i0, o0[1] * i0), pk2(o0[2] * i0, o0[3] * i0), pk2(o0[4] * i0, o0[5] * i0), pk2(o0[6] * i0, o0[7] * i0)};
        const u32x4 a1 = {pk2(o1[0] * i1, o1[1] * i1), pk2(o1[2] * i1, o1[3] * i1), pk2(o1[4] * i1, o1[5] * i1), pk2(o1[6] * i1, o1[7] * i1)};
        char* op = wl + OFF_O + ((size_t)tt0 * KSD + head) * 1024 + voff;
        st16(op, a0); st16(op + 32768, a1);
        PSTAMP(12);
        WAITBAR_L();
        arrive4(lcnt, cnt + (C_O + layer * 8 + qb) * 1024, lane);
      }
    }
    PSTAMP(4);
    int role = w_ >> 2;
    asm volatile("" : "+s"(role));
    if (role == 0) {
      {
        FRESH();
        const char* wb = ws + OFF_WGU + (size_t)gnb * 229376 + j * 8192;
#pragma unroll 1
        for (int c = 0; c < 4; ++c) glds4(voff, wb + (c >> 1) * GS + (c & 1) * 4096, lds0 + (c >> 1) * GS + (c & 1) * 4096 + j * 8192);
        colsum_part(ws + OFF_CSP + (size_t)j * 81920 + gnb * 896, voff, lb + L_CS + j * 1024);
      }
      PSTAMP(14);
      WAITBAR_L();
      PSTAMP(15);
      {
        FRESH();
        const int g4 = lane >> 4, c16 = lane & 15;
        const char* gx = wl + OFF_X2 + (size_t)(gtb * 4 + j) * 32768;
        unsigned xo = (g4 >> 1) * 1024 + ((g4 & 1) * 32 + c16) * 16;
        const char* ob = wl + OFF_H + ((size_t)(gtb * 4 + j) * KSF + (gnb >> 2) * 28) * 1024;
        unsigned oo = (((g4 & 1) * 32) + (g4 >> 1) * 16 + c16) * 16;
        asm volatile("" : "+s"(gx), "+s"(ob)); PIN(xo); PIN(oo);
        bf16x8 xr[16][2], wf[8];
        f32x4 ac[2][8];
#pragma unroll 1
        for (unsigned spins = 0; spins < 4000000u; ++spins)
          if (__builtin_amdgcn_readfirstlane(__hip_atomic_load(lcnt + 2, __ATOMIC_RELAXED, __HIP_MEMORY_SCOPE_WORKGROUP)) >= (unsigned)(layer * 8 + 2)) break; else __builtin_amdgcn_s_sleep(1);
        asm volatile("" ::: "memory");
#define LDXS(S) do { gld16<((S) & 1) * 2048>(xr[S][0], xo, gx + ((S) >> 1) * 4096); gld16<((S) & 1) * 2048 + 256>(xr[S][1], xo, gx + ((S) >> 1) * 4096); } while (0)
        sfor<0, 8>([&](auto ic) { LDXS(decltype(ic)::value); });
        PSTAMP(5);
        auto pass = [&](auto nt_, auto st0_) {
          constexpr int NT = decltype(nt_)::value, ST0 = decltype(st0_)::value, PD = 6, NF = 16 * NT;
          sfor<0, NF + PD>([&](auto ic) {
            constexpr int f = decltype(ic)::value, g = f - PD;
            if constexpr (g >= 0) {
              constexpr int S = g / NT, i = g % NT;
              if constexpr (ST0 == 0 && i == 0) {
                constexpr int last = 4 * (S / 4) + 7 > 15 ? 15 : 4 * (S / 4) + 7;
                asm volatile("s_waitcnt vmcnt(%2)" : "+v"(xr[S][0]), "+v"(xr[S][1]) : "i"(2 * (last - S)) : "memory");
              }
              SBAR();
              if constexpr (S == 0) ac[0][i] = __builtin_amdgcn_mfma_f32_16x16x32_bf16(wf[g & 7], xr[S][0], f32x4{}, 0, 0, 0);
              else ac[0][i] = __builtin_amdgcn_mfma_f32_16x16x32_bf16(wf[g & 7], xr[S][0], ac[0][i], 0, 0, 0);
              SBAR();
            }
            if constexpr (f < NF) {
              if constexpr (f % (4 * NT) == 0 && (ST0 + f / (4 * NT)) > 0) WAITBAR_L();
              wf[f & 7] = ldfrag(lb + ((ST0 + f / (4 * NT)) % 3) * GS + (f % (4 * NT)) * 1024);
            }
            if constexpr (g >= 0) {
              constexpr int S = g / NT, i = g % NT;
              SBAR();
              if constexpr (S == 0) ac[1][i] = __builtin_amdgcn_mfma_f32_16x16x32_bf16(wf[g & 7], xr[S][1], f32x4{}, 0, 0, 0);
              else ac[1][i] = __builtin_amdgcn_mfma_f32_16x16x32_bf16(wf[g & 7], xr[S][1], ac[1][i], 0, 0, 0);
              SBAR();
              if constexpr (ST0 == 0 && i == NT - 1 && S % 4 == 3 && S < 8) sfor<S + 5, S + 9>([&](auto jc) { LDXS(decltype(jc)::value); });
              if constexpr (ST0 == 4 && g == 4 * NT) arrive4(lcnt, cnt + (C_H + layer * 16 + gtb) * 1024, lane);
            }
          });
        };
        auto epilogue = [&](auto nt_, auto t0_, const char* o) {
          constexpr int NH = decltype(nt_)::value, T0 = decltype(t0_)::value;
          LDSP(const float) sx = (LDSP(const float))((LDSP(char))smem + L_ST) + j * 64 + c16;
          const float rs0 = sx[0], rs1 = sx[16], m0 = -sx[32], m1 = -sx[48];
          f32x4 c4[2 * NH];
#pragma unroll
          for (int i = 0; i < 2 * NH; ++i) c4[i] = *(LDSP(const f32x4))((LDSP(const char))smem + L_CS + (2 * T0 + i) * 64 + g4 * 16);
          const f32x4 vm0 = {m0, m0, m0, m0}, vm1 = {m1, m1, m1, m1};
          sfor<0, NH>([&](auto ic) {
            constexpr int i = decltype(ic)::value;
            const f32x4 ga = __builtin_elementwise_fma(c4[2 * i], vm0, ac[0][2 * i]), ua = __builtin_elementwise_fma(c4[2 * i + 1], vm0, ac[0][2 * i + 1]) * rs0;
            const f32x4 gb = __builtin_elementwise_fma(c4[2 * i], vm1, ac[1][2 * i]), ub = __builtin_elementwise_fma(c4[2 * i + 1], vm1, ac[1][2 * i + 1]) * rs1;
            const f32x4 h0 = __builtin_elementwise_max(ga, f32x4{}) * ua, h1 = __builtin_elementwise_max(gb, f32x4{}) * ub;
            auto e0 = __builtin_amdgcn_permlane32_swap(pk2(h0[0], h0[1]), pk2(h1[0], h1[1]), false, false);
            auto e1 = __builtin_amdgcn_permlane32_swap(pk2(h0[2], h0[3]), pk2(h1[2], h1[3]), false, false);
            const u32x4 a_ = {e0[0], e1[0], e0[1], e1[1]};
            st16s<i * 1024>(oo, o, a_);
          });
        };
        pass(IC<8>{}, IC<0>{});
        epilogue(IC<4>{}, IC<0>{}, ob + (gnb & 3) * 4096);
        pass(IC<6>{}, IC<4>{});
        PSTAMP(6);
        epilogue(IC<3>{}, IC<4>{}, ob + 16384 + (gnb & 3) * 3072);
#undef LDXS
        PSTAMP(13);
        arrive4(lcnt, cnt + (C_H2 + layer * 16 + gtb) * 1024, lane);
        PSTAMP(7);
      }
      WAITBAR_L();
      if (layer == 0) {
        FRESH();
        __builtin_amdgcn_s_barrier();
        { const char* wb = ws + OFF_WQ + (size_t)qnb * 8 * QW + j * 3072;
#pragma unroll 1
          for (int c = 0; c < 4; ++c) glds3(voff, wb + c * QW, lds0 + c * QW + j * 3072); }
        colsum_part(ws + OFF_CSP + (size_t)j * 81920 + (3584 + qnb * 96) * 4, voff, lb + L_CS + j * 1024);
        QINIT();
#define RX(B, u) xf[B] = ldfrag(sxp + (u) * 1024)
#define RW(B, i, u) wf[B][i] = ldfrag(swp + (i) * 4096 + (u) * 1024)
        WAITBAR_L();
        PSTAMP(8);
#pragma unroll 1
        for (int c = 0; c < 8; ++c) {
          LDSP(const char) swp = lb + (c & 3) * QW;
          LDSP(const char) sxp = lb + L_QX + (c & 3) * 16384 + j * 4096;
          QCHUNK();
          WAITBAR_L();
        }
        LDSP(const float) cx = (LDSP(const float))((LDSP(char))smem + L_CS);
        LDSP(const float) sx = (LDSP(const float))((LDSP(char))smem + L_ST) + j * 64;
        f32x4 cq[8], ra[4], rb[4];
        const float rstd = sx[r], mr = sx[32 + r], cv = cx[64 + r];
#pragma unroll
        for (int g = 0; g < 8; ++g) cq[g] = *(LDSP(const f32x4))(cx + 8 * g + 4 * hh);
#pragma unroll
        for (int g = 0; g < 4; ++g) { ra[g] = *(LDSP(const f32x4))(sx + 8 * g + 4 * hh); rb[g] = *(LDSP(const f32x4))(sx + 32 + 8 * g + 4 * hh); }
        SBAR();
        QFINISH();
        SBAR();
#undef RX
#undef RW
        PSTAMP(9);
        char* wn = ws + LSTRIDE;
        const int tt = qtb * 4 + j;
        float v[16];
        const float rq = rstd * QSCALE, mq = mr * QSCALE;
#pragma unroll
        for (int e = 0; e < 16; ++e) v[e] = acc[0][e] * rq - mq * cq[e >> 2][e & 3];
        qk_store(v, wn + OFF_Q + ((size_t)(2 * qnb) * NTT + tt) * 1024 + voff);
#pragma unroll
        for (int e = 0; e < 16; ++e) v[e] = acc[1][e] * rstd - mr * cq[4 + (e >> 2)][e & 3];
        qk_store(v, wn + OFF_K + ((size_t)(2 * qnb) * NTT + tt) * 1024 + voff);
#pragma unroll
        for (int e = 0; e < 16; ++e) v[e] = acc[2][e] * ra[e >> 2][e & 3] - rb[e >> 2][e & 3] * cv;
        v_store(v, wn + OFF_V + ((size_t)(2 * qnb + (r >> 4)) * NTT + tt) * 1024 + hh * 256 + (r & 15) * 16);
        arrive4(lcnt, cnt + (C_KV + qnb) * 1024, lane);
        PSTAMP(10);
      }
    } else {
      {
        FRESH();
        bf16x8 w0[8], w1[8], x0[8], x1[8];
        const char* wa = ws + OFF_WO + ((size_t)ont * KSD + j * 8) * 1024;
        const char* wb = ws + OFF_WO + ((size_t)(ont ^ 1) * KSD + j * 8) * 1024;
#define LD4(D, o, base) do { gld16<0>(D[o], voff, base); gld16<1024>(D[(o) + 1], voff, base); gld16<2048>(D[(o) + 2], voff, base); gld16<3072>(D[(o) + 3], voff, base); } while (0)
        __builtin_amdgcn_s_barrier();
        LD4(w0, 0, wa); LD4(w0, 4, wa + 4096); LD4(w1, 0, wb); LD4(w1, 4, wb + 4096);
        u32x2 rn[4]; unsigned res[8];
        res_load(rn, layer ? voff : r * 1024 + hh * 8, (layer ? ws + OFF_X3 + ont * 2048 : karg(1) + ont * 64) + (size_t)ott * 32768, layer ? 8 : 16, layer ? 1024 : 32);
        const char* xa = wl + OFF_O + ((size_t)ott * KSD + j * 8) * 1024;
        const char* xb = wl + OFF_O + ((size_t)(ott ^ 1) * KSD + j * 8) * 1024;
        asm volatile("" : "+s"(xa), "+s"(xb));
        poll_b(cnt + (C_O + layer * 8 + xcd) * 1024 + (idx & 7) * 128, 32u, lcnt + 1, layer * 8 + 1, j);
        PSTAMP(5);
        #define LD2(s) do { gld16<((s) & 3) * 1024>(x0[s], voff, xa + ((s) >> 2) * 4096); gld16<((s) & 3) * 1024>(x1[s], voff, xb + ((s) >> 2) * 4096); } while (0)
        LD2(0); LD2(1); LD2(2); LD2(3); LD2(4); LD2(5); LD2(6); LD2(7);
#undef LD2
        f32x16 acc[4];
#pragma unroll
        for (int i = 0; i < 4; ++i) acc[i] = f32x16{};
#define MM4(W0, W1, X0, X1) do { acc[1] = MFMA(W0, X1, acc[1]); acc[2] = MFMA(W1, X0, acc[2]); acc[3] = MFMA(W1, X1, acc[3]); acc[0] = MFMA(W0, X0, acc[0]); } while (0)
#define PIN8(n, X, Y, o) asm volatile("s_waitcnt vmcnt(%8)" : "+v"(X[o]), "+v"(X[(o) + 1]), "+v"(X[(o) + 2]), "+v"(X[(o) + 3]), "+v"(Y[o]), "+v"(Y[(o) + 1]), "+v"(Y[(o) + 2]), "+v"(Y[(o) + 3]) : "i"(n) : "memory")
        PIN8(16, w0, w1, 0); PIN8(16, w0, w1, 4);
#define WX(b, n) asm volatile("s_waitcnt vmcnt(%2)" : "+v"(x0[b]), "+v"(x1[b]) : "i"(n) : "memory")
#define ST(s) do { WX(s, 14 - 2 * (s)); MM4(w0[s], w1[s], x0[s], x1[s]); SBAR(); } while (0)
        ST(0); ST(1); ST(2); ST(3); ST(4); ST(5); ST(6); ST(7);
#undef ST
#undef WX
        PSTAMP(6);
        RES_FINISH(L_RED + ((j ^ i) * 3 + i - 1) * 4096, L_RED + (j * 3 + i - 1) * 4096);
        PSTAMP(13);
        res_store_tiled(res, wl + OFF_X2 + ((size_t)ott * KSD + 2 * ont) * 1024 + voff, wl + OFF_ST2 + (size_t)ott * 4096 + ont * 256 + r * 8, lane);
        PSTAMP(14);
        arrive4(lcnt, cnt + (C_X2 + layer * 16 + grp) * 1024, lane);
        PSTAMP(7);
      }
      {
        FRESH();
        { LDSP(char) cp = (LDSP(char))smem + L_CS + j * 256 + lane * 4;
          *(LDSP(float))cp = (*(LDSP(float))cp + *(LDSP(float))(cp + 1024)) + (*(LDSP(float))(cp + 2048) + *(LDSP(float))(cp + 3072)); }
        bf16x8 w0[12], w1[12], x0[8], x1[8];
        const char* wa = ws + OFF_WD + ((size_t)ont * KSF + j * 28) * 1024;
        const char* wb = ws + OFF_WD + ((size_t)(ont ^ 1) * KSF + j * 28) * 1024;
#define DISSUE(c, P) do { glds4(voff, wa + 12288 + (c) * 4096, lds0 + (P) + j * 8192); glds4(voff, wb + 12288 + (c) * 4096, lds0 + (P) + j * 8192 + 4096); } while (0)
        {
          const char* gw = ws + OFF_WGU + (size_t)gnb * 229376;
          const char* gs = wl + OFF_ST2 + (size_t)(gtb * 4 + j) * 4096;
          f32x4 sp[4];
          asm volatile("" : "+s"(gw), "+s"(gs));
#define G1(st) do { glds4(voff, gw + (st) * GS + j * 8192, lds0 + ((st) % 3) * GS + j * 8192); glds4(voff, gw + (st) * GS + j * 8192 + 4096, lds0 + ((st) % 3) * GS + j * 8192 + 4096); } while (0)
#define G2(st) do { glds3(voff, gw + 4 * GS + ((st) - 4) * 24576 + j * 6144, lds0 + ((st) % 3) * GS + j * 6144); \
                    glds3(voff, gw + 4 * GS + ((st) - 4) * 24576 + j * 6144 + 3072, lds0 + ((st) % 3) * GS + j * 6144 + 3072); } while (0)
          poll_b(cnt + (C_X2 + layer * 16 + gtb) * 1024 + (gnb & 7) * 128, 16u, lcnt + 1, layer * 8 + 2, j);
          PSTAMP(8);
          __builtin_amdgcn_s_sleep(8);
          stat_issue(sp, gs, voff);
          G1(2);
          __builtin_amdgcn_s_barrier();
          G1(3); LD4(w0, 0, wa); LD4(w1, 0, wb);
          WAITV(16);
          { float mean, var; stat_finish(sp, mean, var);
            ((LDSP(float))((LDSP(char))smem + L_ST))[j * 64 + hh * 32 + (lane & 15) * 2 + ((lane >> 4) & 1)] = hh ? mean : 1.f / var; }
          WAITBAR_V(16);
          G2(4); LD4(w0, 4, wa + 4096); LD4(w1, 4, wb + 4096);
          WAITBAR_V(22);
          G2(5); LD4(w0, 8, wa + 8192); LD4(w1, 8, wb + 8192);
          WAITBAR_V(22);
          G2(6); DISSUE(0, L_D0);
          WAITBAR_V(22);
          G2(7);
          WAITBAR_V(14);
          DISSUE(1, L_D1);
          WAITBAR_V(8);
          DISSUE(2, L_D2);
#undef G1
#undef G2
        }
        const char* xa = wl + OFF_H + ((size_t)ott * KSF + j * 28) * 1024;
        const char* xb = wl + OFF_H + ((size_t)(ott ^ 1) * KSF + j * 28) * 1024;
        asm volatile("" : "+s"(xa), "+s"(xb));
        poll_b(cnt + (C_H + layer * 16 + grp) * 1024 + (idx & 7) * 128, 16u, lcnt + 1, layer * 8 + 3, j);
        PSTAMP(9);
        PIN8(0, w0, w1, 0); PIN8(0, w0, w1, 4); PIN8(0, w0, w1, 8);
#define LDX(b, blk) do { gld16<((b) & 3) * 1024>(x0[b], voff, xa + (blk) * 4096); gld16<((b) & 3) * 1024>(x1[b], voff, xb + (blk) * 4096); } while (0)
#define WX(b, n) asm volatile("s_waitcnt vmcnt(%2)" : "+v"(x0[b]), "+v"(x1[b]) : "i"(n) : "memory")
        LDX(0, 0); LDX(1, 0); LDX(2, 0); LDX(3, 0); LDX(4, 1); LDX(5, 1); LDX(6, 1); LDX(7, 1);
        f32x16 acc[4];
#pragma unroll
        for (int i = 0; i < 4; ++i) acc[i] = f32x16{};
        LDSP(const char) lw = lb + j * 8192;
#define BLK(o, h, nb, n0) do { \
          WX((h) + 0, n0); MM4(w0[(o) + 0], w1[(o) + 0], x0[(h) + 0], x1[(h) + 0]); SBAR(); if ((nb) >= 0) LDX((h) + 0, nb); SBAR(); \
          WX((h) + 1, (nb) >= 0 ? n0 : n0 - 2); MM4(w0[(o) + 1], w1[(o) + 1], x0[(h) + 1], x1[(h) + 1]); SBAR(); if ((nb) >= 0) LDX((h) + 1, nb); SBAR(); \
          WX((h) + 2, (nb) >= 0 ? n0 : n0 - 4); MM4(w0[(o) + 2], w1[(o) + 2], x0[(h) + 2], x1[(h) + 2]); SBAR(); if ((nb) >= 0) LDX((h) + 2, nb); SBAR(); \
          WX((h) + 3, (nb) >= 0 ? n0 : n0 - 6); MM4(w0[(o) + 3], w1[(o) + 3], x0[(h) + 3], x1[(h) + 3]); SBAR(); if ((nb) >= 0) LDX((h) + 3, nb); SBAR(); } while (0)
#define RDL(o, c) do { _Pragma("unroll") for (int s = 0; s < 4; ++s) { w0[(o) + s] = ldfrag(lw + (c) + s * 1024); w1[(o) + s] = ldfrag(lw + (c) + 4096 + s * 1024); } SBAR(); } while (0)
        BLK(0, 0, 2, 14); RDL(0, L_D0);
        BLK(4, 4, 3, 14); RDL(4, L_D1);
        BLK(8, 0, -1, 14); RDL(8, L_D2);
        BLK(0, 4, -1, 6);
        LD4(w0, 0, wa + 24576); LD4(w1, 0, wb + 24576);
        PSTAMP(15);
        poll_b(cnt + (C_H2 + layer * 16 + grp) * 1024 + (idx & 7) * 128, 16u, lcnt + 1, layer * 8 + 4, j);
        PIN8(0, w0, w1, 0);
        LDX(0, 4); LDX(1, 4); LDX(2, 4); LDX(3, 4); LDX(4, 5); LDX(5, 5); LDX(6, 5); LDX(7, 5);
        BLK(4, 0, 6, 14);
        u32x2 rn[4]; unsigned res[8];
        res_load(rn, voff, wl + OFF_X2 + (size_t)ott * 32768 + ont * 2048, 8, 1024);
        BLK(8, 4, -1, 18);
        BLK(0, 0, -1, 10);
#undef DISSUE
#undef RDL
#undef BLK
#undef LDX
#undef WX
        {
        FRESH();
        PSTAMP(10);
        RES_FINISH((i == 3 ? L_D1 : L_D0 + (i - 1) * 4096) + j * 8192, (i == 3 ? L_D1 : L_D0 + (i - 1) * 4096) + (j ^ i) * 8192);
        if (layer == 0) {
          res_store_tiled(res, ws + OFF_X3 + ((size_t)ott * KSD + 2 * ont) * 1024 + voff, ws + OFF_ST3 + (size_t)ott * 4096 + ont * 256 + r * 8, lane);
          arrive4(lcnt, cnt + (C_X3 + grp) * 1024, lane);
        } else {
          auto s0 = __builtin_amdgcn_permlane32_swap(res[0], res[2], false, false);
          auto s1 = __builtin_amdgcn_permlane32_swap(res[1], res[3], false, false);
          auto s2 = __builtin_amdgcn_permlane32_swap(res[4], res[6], false, false);
          auto s3 = __builtin_amdgcn_permlane32_swap(res[5], res[7], false, false);
          const u32x4 a = {s0[0], s1[0], s0[1], s1[1]}, b = {s2[0], s3[0], s2[1], s3[1]};
          char* o = (char*)karg(7) + ((size_t)(ott * 32 + r) * D_ + ont * 32) * 2 + hh * 16;
          st16(o, a);
          st16(o + 32, b);
        }
        }
        PSTAMP(11);
      }
#undef LD4
#undef MM4
#undef PIN8
      if (layer == 0) {
        FRESH();
        const int tt = qtb * 4 + j;
        const char* xb = ws + OFF_X3 + (size_t)tt * 32768;
        const char* wb = ws + OFF_WQ + (size_t)qnb * 8 * QW + j * 3072;
        f32x4 sp[4];
        asm volatile("" : "+s"(xb), "+s"(wb));
        __builtin_amdgcn_s_barrier();
        poll_b(cnt + (C_X3 + qtb) * 1024 + (qnb & 7) * 128, 16u, lcnt + 1, 5, j);
        PSTAMP(12);
#define QISSUE(c) do { glds4(voff, xb + (c) * 4096, lds0 + L_QX + ((c) & 3) * 16384 + j * 4096); glds3(voff, wb + (c) * QW, lds0 + ((c) & 3) * QW + j * 3072); } while (0)
        glds4(voff, xb, lds0 + L_QX + j * 4096);
        glds4(voff, xb + 4096, lds0 + L_QX + 16384 + j * 4096);
        stat_issue(sp, ws + OFF_ST3 + (size_t)tt * 4096, voff);
        glds4(voff, xb + 8192, lds0 + L_QX + 32768 + j * 4096);
        glds4(voff, xb + 12288, lds0 + L_QX + 49152 + j * 4096);
        WAITBAR_V(16);
        { LDSP(char) cp = (LDSP(char))smem + L_CS + j * 256 + lane * 4;
          *(LDSP(float))cp = (*(LDSP(float))cp + *(LDSP(float))(cp + 1024)) + (*(LDSP(float))(cp + 2048) + *(LDSP(float))(cp + 3072)); }
        WAITBAR_V(12);
        QISSUE(4); WAITBAR_V(11);
#pragma unroll 1
        for (int c = 5; c < 8; ++c) { QISSUE(c); WAITBAR_V(14); }
#undef QISSUE
        { float mean, var; stat_finish(sp, mean, var); const float rs = rsqrtf(var);
          ((LDSP(float))((LDSP(char))smem + L_ST))[j * 64 + hh * 32 + (lane & 15) * 2 + ((lane >> 4) & 1)] = hh ? mean * rs : rs; }
        WAITBAR_V(7);
        WAITBAR_V(0);
        __builtin_amdgcn_s_barrier();
      }
    }
  }
}

extern "C" void kernel_launch(void* const* d_in, const int* in_sizes, int n_in, void* d_out, int out_size, void* d_ws, size_t ws_size,
                              hipStream_t stream) {
  const char* x = (const char*)d_in[0];
  const char* wqkv = (const char*)d_in[1];
  const char* wo = (const char*)d_in[2];
  const char* wg = (const char*)d_in[4];
  const char* wu = (const char*)d_in[5];
  const char* wd = (const char*)d_in[6];
  char* ws = (char*)d_ws;
  k_qkv1<<<256, 768, 0, stream>>>(x, wqkv, ws DIAG_PASS(0));
  k_main<<<256, 512, 0, stream>>>(ws, x, wqkv, wo, wg, wu, wd, (char*)d_out DIAG_PASS(1));
}
```

```cpp
#include <hip/hip_runtime.h>
#include <hip/hip_bf16.h>
#include <stdint.h>

typedef short bf16x8 __attribute__((ext_vector_type(8)));
typedef float f32x16 __attribute__((ext_vector_type(16)));
typedef float f32x4 __attribute__((ext_vector_type(4)));
typedef float f32x2 __attribute__((ext_vector_type(2)));
typedef unsigned u32x4 __attribute__((ext_vector_type(4)));
typedef unsigned u32x2 __attribute__((ext_vector_type(2)));
typedef __bf16 bf16v2 __attribute__((ext_vector_type(2)));
typedef unsigned short u16;

#define STAMP(i)
#define STAMP_DECL
#define PSTAMP(i)
#define STAMP_STORE()
#define DIAG_ARG
#define DIAG_PASS(k)
#define DI __device__ __forceinline__
#define MFMA(a, b, c) __builtin_amdgcn_mfma_f32_32x32x16_bf16((a), (b), (c), 0, 0, 0)
#define LDSP(T) __attribute__((address_space(3))) T*

constexpr int S_ = 2048, D_ = 512, H_ = 32, F_ = 1792;
constexpr int NTT = S_ / 32;
constexpr int KSD = D_ / 16;
constexpr int KSF = F_ / 16;
constexpr float QSCALE = 0.25f * 1.4426950408889634f;
constexpr float LN_EPS = 1e-5f;

constexpr size_t KB = 1024;
constexpr size_t OFF_CNT = 0;
constexpr size_t OFF_WO = 192 * KB;
constexpr size_t OFF_WGU = OFF_WO + 512 * KB;
constexpr size_t OFF_WD = OFF_WGU + 3584 * KB;
constexpr size_t OFF_WQ = OFF_WD + 1792 * KB;
constexpr size_t OFF_CSP = OFF_WQ + 1536 * KB;
constexpr size_t OFF_ST3 = OFF_CSP + 324 * KB;
constexpr size_t OFF_X3 = OFF_ST3 + 256 * KB;
constexpr size_t OFF_L = OFF_X3 + 2048 * KB;
constexpr size_t OFF_Q = OFF_L;
constexpr size_t OFF_K = OFF_Q + 2048 * KB;
constexpr size_t OFF_V = OFF_K + 2048 * KB;
constexpr size_t OFF_O = OFF_V + 2048 * KB;
constexpr size_t OFF_X2 = OFF_O + 2048 * KB;
constexpr size_t OFF_ST2 = OFF_X2 + 2048 * KB;
constexpr size_t OFF_H = OFF_ST2 + 256 * KB;
constexpr size_t LSTRIDE = OFF_H + 7168 * KB - OFF_L;
constexpr int KMAIN_KB = 28;
constexpr int C_O = 0, C_X2 = 16, C_H = 48, C_X3 = 80, C_KV = 96, C_PREP = 112, C_H2 = 128, C_N = 160;

template <int I> struct IC { static constexpr int value = I; };
template <int B, int E, class F> DI void sfor(F&& f) { if constexpr (B < E) { f(IC<B>{}); sfor<B + 1, E>(f); } }
DI unsigned pk2(float lo, float hi) {
  f32x2 v = {lo, hi};
  bf16v2 r = __builtin_convertvector(v, bf16v2);
  return __builtin_bit_cast(unsigned, r);
}
DI float lo16(unsigned u) { return __uint_as_float(u << 16); }
DI float hi16(unsigned u) { return __uint_as_float(u & 0xffff0000u); }
DI float xhalf_add(float v) {
  auto r = __builtin_amdgcn_permlane32_swap(__float_as_uint(v), __float_as_uint(v), false, false);
  return __uint_as_float(r[0]) + __uint_as_float(r[1]);
}
DI float xhalf_max(float v) {
  auto r = __builtin_amdgcn_permlane32_swap(__float_as_uint(v), __float_as_uint(v), false, false);
  return fmaxf(__uint_as_float(r[0]), __uint_as_float(r[1]));
}
DI float x16_add(float v) {
  auto r = __builtin_amdgcn_permlane16_swap(__float_as_uint(v), __float_as_uint(v), false, false);
  return __uint_as_float(r[0]) + __uint_as_float(r[1]);
}
DI void st16(void* p, u32x4 v) { asm volatile("global_store_dwordx4 %0, %1, off sc1\n\ts_nop 1" :: "v"(p), "v"(v) : "memory"); }
template <int IMM>
DI void st16s(unsigned voff, const char* sbase, u32x4 v) {
  asm volatile("global_store_dwordx4 %0, %1, %2 offset:%3 sc1\n\ts_nop 1" :: "v"(voff), "v"(v), "s"(sbase), "i"(IMM) : "memory");
}
template <int IMM>
DI void gld16(bf16x8& d, unsigned voff, const char* sbase) {
  asm volatile("global_load_dwordx4 %0, %1, %2 offset:%3 sc1" : "=v"(d) : "v"(voff), "s"(sbase), "i"(IMM) : "memory");
}
#define GLDS_HEAD "s_mov_b32 %0, m0\n\ts_mov_b32 m0, %3\n\ts_nop 0\n\t"
#define GLDS_OP(o) "global_load_lds_dwordx4 %1, %2 offset:" #o " sc1\n\t"
#define GLDS_TAIL "s_mov_b32 m0, %0" : "=&s"(keep) : "v"(voff), "s"(sbase), "s"(lds) : "memory"
DI void glds1(unsigned voff, const char* sbase, unsigned lds) { unsigned keep; asm volatile(GLDS_HEAD GLDS_OP(0) GLDS_TAIL); }
DI void glds2(unsigned voff, const char* sbase, unsigned lds) { unsigned keep; asm volatile(GLDS_HEAD GLDS_OP(0) GLDS_OP(1024) GLDS_TAIL); }
DI void glds3(unsigned voff, const char* sbase, unsigned lds) { unsigned keep; asm volatile(GLDS_HEAD GLDS_OP(0) GLDS_OP(1024) GLDS_OP(2048) GLDS_TAIL); }
DI void glds4(unsigned voff, const char* sbase, unsigned lds) { unsigned keep; asm volatile(GLDS_HEAD GLDS_OP(0) GLDS_OP(1024) GLDS_OP(2048) GLDS_OP(3072) GLDS_TAIL); }
#define SBAR() __builtin_amdgcn_sched_barrier(0)
#define PIN(x) asm volatile("" : "+v"(x))
#define WAITV(n) asm volatile("s_waitcnt vmcnt(%0)" :: "i"(n) : "memory")
#define WAITBAR_V(n) asm volatile("s_waitcnt vmcnt(%0) lgkmcnt(0)\n\ts_barrier" :: "i"(n) : "memory")
#define WAITBAR_L() do { asm volatile("" ::: "memory"); __builtin_amdgcn_s_waitcnt(0xc07f); __builtin_amdgcn_s_barrier(); asm volatile("" ::: "memory"); } while (0)
typedef short s16x4 __attribute__((ext_vector_type(4)));
DI s16x4 trread(LDSP(const char) p) { return __builtin_amdgcn_ds_read_tr16_b64_v4i16((LDSP(s16x4))p); }
DI bf16x8 trfrag(LDSP(const char) p, int off2) { const s16x4 lo = trread(p), hi = trread(p + off2); return __builtin_shufflevector(lo, hi, 0, 1, 2, 3, 4, 5, 6, 7); }
DI bf16x8 ldfrag(LDSP(const char) p) { return *(LDSP(const bf16x8))p; }
DI float dot2(unsigned a, unsigned b, float c) { return __builtin_amdgcn_fdot2_f32_bf16(__builtin_bit_cast(bf16v2, a), __builtin_bit_cast(bf16v2, b), c, false); }
constexpr unsigned ONE2 = 0x3f803f80u;
#define BC8(x) __builtin_bit_cast(bf16x8, x)

template <int LOOK, int PER> DI void ring_wait(int i) {
  if (i <= 8 - LOOK) WAITBAR_V((LOOK - 1) * PER);
  else if (LOOK > 3 && i == 5) WAITBAR_V(2 * PER);
  else if (LOOK > 2 && i == 6) WAITBAR_V(PER);
  else WAITBAR_V(0);
}

DI void qk_store(const float (&v)[16], char* o) {
  u32x4 a = {pk2(v[0], v[1]), pk2(v[2], v[3]), pk2(v[4], v[5]), pk2(v[6], v[7])};
  u32x4 b = {pk2(v[8], v[9]), pk2(v[10], v[11]), pk2(v[12], v[13]), pk2(v[14], v[15])};
  st16(o, a);
  st16(o + NTT * 1024, b);
}
DI void v_store(const float (&v)[16], char* o) {
  auto s0 = __builtin_amdgcn_permlane32_swap(pk2(v[0], v[1]), pk2(v[4], v[5]), false, false);
  auto s1 = __builtin_amdgcn_permlane32_swap(pk2(v[2], v[3]), pk2(v[6], v[7]), false, false);
  auto s2 = __builtin_amdgcn_permlane32_swap(pk2(v[8], v[9]), pk2(v[12], v[13]), false, false);
  auto s3 = __builtin_amdgcn_permlane32_swap(pk2(v[10], v[11]), pk2(v[14], v[15]), false, false);
  const u32x4 a = {s0[0], s1[0], s0[1], s1[1]}, b = {s2[0], s3[0], s2[1], s3[1]};
  st16(o, a);
  st16(o + 512, b);
}
typedef __bf16 bfv8 __attribute__((ext_vector_type(8)));
typedef __bf16 bfv16 __attribute__((ext_vector_type(16)));
typedef float f32x8 __attribute__((ext_vector_type(8)));
template <int H> DI f32x16 smf(bf16x8 a, const f32x16& s, f32x16 c, int idx) {
  const f32x8 p = __builtin_shufflevector(s, s, 8 * H, 8 * H + 1, 8 * H + 2, 8 * H + 3, 8 * H + 4, 8 * H + 5, 8 * H + 6, 8 * H + 7);
  return __builtin_amdgcn_smfmac_f32_32x32x32_bf16(__builtin_bit_cast(bfv8, a), __builtin_bit_cast(bfv16, p), c, idx, 0, 0);
}
DI float row16_sum(float v) {
  v += __builtin_bit_cast(float, __builtin_amdgcn_update_dpp(0, __builtin_bit_cast(int, v), 0x128, 0xf, 0xf, false));
  v += __builtin_bit_cast(float, __builtin_amdgcn_update_dpp(0, __builtin_bit_cast(int, v), 0x124, 0xf, 0xf, false));
  v += __builtin_bit_cast(float, __builtin_amdgcn_update_dpp(0, __builtin_bit_cast(int, v), 0x122, 0xf, 0xf, false));
  v += __builtin_bit_cast(float, __builtin_amdgcn_update_dpp(0, __builtin_bit_cast(int, v), 0x121, 0xf, 0xf, false));
  return v;
}
#define KSTEP(B, u) do { SBAR(); RX((B) ^ 1, u); RW((B) ^ 1, 0, u); SBAR(); acc[0] = MFMA(wf[B][0], xf[B], acc[0]); SBAR(); RW((B) ^ 1, 1, u); RW((B) ^ 1, 2, u); SBAR(); \
                         acc[1] = MFMA(wf[B][1], xf[B], acc[1]); acc[2] = MFMA(xf[B], wf[B][2], acc[2]); SBAR(); } while (0)
#define QCHUNK() do { KSTEP(1, 0); KSTEP(0, 1); KSTEP(1, 2); KSTEP(0, 3); } while (0)
#define QINIT() \
  f32x16 acc[3]; bf16x8 wf[2][3], xf[2]; \
  _Pragma("unroll") for (int i = 0; i < 3; ++i) { acc[i] = f32x16{}; wf[1][i] = bf16x8{}; } \
  xf[1] = bf16x8{}
#define QFINISH() do { acc[0] = MFMA(wf[1][0], xf[1], acc[0]); acc[1] = MFMA(wf[1][1], xf[1], acc[1]); acc[2] = MFMA(xf[1], wf[1][2], acc[2]); } while (0)

extern "C" __global__ __launch_bounds__(768, 3) void k_qkv1(const char* __restrict__ x, const char* __restrict__ wn, char* __restrict__ ws DIAG_ARG) {
  STAMP_DECL; STAMP(0);
  constexpr int LOOK = 3, CH = 24576, LXN = 4 * CH;
  __shared__ __attribute__((aligned(16))) char smem[LXN + 65536];
  const int tid = threadIdx.x, lane = tid & 63, r = lane & 31, hh = lane >> 5;
  const int w = __builtin_amdgcn_readfirstlane(tid >> 6);
  const int xcd = blockIdx.x & 7, idx = blockIdx.x >> 3;
  const int tb = (xcd & 3) * 8 + (idx & 7), nq = (xcd >> 2) * 4 + (idx >> 3);
  const unsigned lds0 = (unsigned)(uintptr_t)smem;
  const int rot = 2 * (idx & 3);
  if (w >= 8) {
    const int j = w - 8;
    const unsigned wv = (lane >> 3) * 3072 + (((lane & 7) ^ (((lane >> 4) & 1) << 2)) * 16);
    const char* wb = wn + (size_t)j * 16 * 3072 + nq * 128;
    if (blockIdx.x < 5) *(unsigned*)(ws + OFF_CNT + ((blockIdx.x * 4 + j) * 64 + lane) * 128) = 0u;
    asm volatile("s_sleep 8\n\ts_nop 4" ::: "memory");
#pragma unroll 1
    for (int c = 0; c < 8 + LOOK; ++c) {
      if (c >= LOOK) ring_wait<LOOK, 6>(c - LOOK);
      if (c < 8) {
        const char* ws_ = wb + (size_t)((c + rot) & 7) * 64 * 3072;
        const unsigned d = lds0 + (c & 3) * CH + j * 2048;
        glds1(wv, ws_, d); glds1(wv, ws_ + 1024, d + 8192); glds1(wv, ws_ + 2048, d + 16384);
        glds1(wv, ws_ + 8 * 3072, d + 1024); glds1(wv, ws_ + 8 * 3072 + 1024, d + 8192 + 1024); glds1(wv, ws_ + 8 * 3072 + 2048, d + 16384 + 1024);
      }
    }
    return;
  }
  {
    const int n = w;
    const u32x4* xr = (const u32x4*)(x + (size_t)(tb * 64 + n * 8) * 1024) + lane;
    u32x4 v[8];
#pragma unroll
    for (int i = 0; i < 8; ++i) v[i] = xr[i * 64];
    float s[8], q[8];
#pragma unroll
    for (int i = 0; i < 8; ++i) {
      s[i] = 0.f; q[i] = 0.f;
#pragma unroll
      for (int e = 0; e < 4; ++e) { s[i] = dot2(v[i][e], ONE2, s[i]); q[i] = dot2(v[i][e], v[i][e], q[i]); }
    }
    float s4[4], q4[4], s2[2], q2[2];
#pragma unroll
    for (int i = 0; i < 4; ++i) {
      const int lo = (i & 1) + 4 * (i >> 1);
      auto a = __builtin_amdgcn_permlane32_swap(__float_as_uint(s[lo]), __float_as_uint(s[lo + 2]), false, false);
      auto b = __builtin_amdgcn_permlane32_swap(__float_as_uint(q[lo]), __float_as_uint(q[lo + 2]), false, false);
      s4[i] = __uint_as_float(a[0]) + __uint_as_float(a[1]); q4[i] = __uint_as_float(b[0]) + __uint_as_float(b[1]);
    }
#pragma unroll
    for (int i = 0; i < 2; ++i) {
      auto a = __builtin_amdgcn_permlane16_swap(__float_as_uint(s4[2 * i]), __float_as_uint(s4[2 * i + 1]), false, false);
      auto b = __builtin_amdgcn_permlane16_swap(__float_as_uint(q4[2 * i]), __float_as_uint(q4[2 * i + 1]), false, false);
      s2[i] = row16_sum(__uint_as_float(a[0]) + __uint_as_float(a[1])); q2[i] = row16_sum(__uint_as_float(b[0]) + __uint_as_float(b[1]));
    }
    float ra[2], rb[2];
#pragma unroll
    for (int i = 0; i < 2; ++i) {
      const float mean = s2[i] * (1.f / D_);
      ra[i] = rsqrtf(fmaxf(q2[i] * (1.f / D_) - mean * mean, 0.f) + LN_EPS);
      rb[i] = -mean * ra[i];
    }
    LDSP(char) xd = (LDSP(char))smem + LXN + (lane >> 3) * 8192 + n * 1024;
#pragma unroll
    for (int i = 0; i < 8; ++i) {
      const float a = __builtin_bit_cast(float, __builtin_amdgcn_readlane(__builtin_bit_cast(int, ra[i >> 2]), (i & 3) * 16));
      const float b = __builtin_bit_cast(float, __builtin_amdgcn_readlane(__builtin_bit_cast(int, rb[i >> 2]), (i & 3) * 16));
      u32x4 o;
#pragma unroll
      for (int e = 0; e < 4; ++e) o[e] = pk2(lo16(v[i][e]) * a + b, hi16(v[i][e]) * a + b);
      *(LDSP(u32x4))(xd + i * 128 + (((lane & 7) ^ ((4 * n + (i >> 1)) & 7)) * 16)) = o;
    }
  }
  STAMP(1);
  WAITBAR_L();
  if (w >= 4) {
    if (w == 4 && idx < KMAIN_KB) {
      bf16x8 t;
      asm volatile("s_getpc_b64 s[20:21]\n\ts_add_u32 s20, s20, k_main@rel32@lo+4\n\ts_addc_u32 s21, s21, k_main@rel32@hi+12\n\t"
                   "global_load_dwordx4 %0, %1, s[20:21]\n\ts_waitcnt vmcnt(0)" : "=v"(t) : "v"(lane * 16 + idx * 1024) : "s20", "s21", "scc", "memory");
    }
    return;
  }
  STAMP(2);
  const int ta = w & 1, hp = w >> 1, q4 = (lane & 15) >> 2, xsw = (r >> 1) & 7;
  LDSP(const char) lw = (LDSP(const char))smem + (8 * hh + q4) * 128 + ((hp ^ (q4 >> 1)) * 64) + ((lane >> 4) & 1) * 32 + (lane & 3) * 8;
  LDSP(const char) lx = (LDSP(const char))smem + LXN + ta * 4096 + r * 128;
  int xo[4];
#pragma unroll
  for (int u = 0; u < 4; ++u) xo[u] = ((2 * u + hh) ^ xsw) * 16;
  QINIT();
#define RX(B, u) xf[B] = ldfrag(sx + xo[u])
#define RW(B, i, u) wf[B][i] = trfrag(sw + (i) * 8192 + (u) * 2048, 512)
#pragma unroll 1
  for (int c = 0; c < 8; ++c) {
    LDSP(const char) sw = lw + (c & 3) * CH;
    LDSP(const char) sx = lx + ((c + rot) & 7) * 8192;
    QCHUNK();
    WAITBAR_L();
  }
  QFINISH();
#undef RX
#undef RW
  STAMP(4);
  const int tt = tb * 2 + ta, nb = nq * 2 + hp;
  float v[16];
#pragma unroll
  for (int e = 0; e < 16; ++e) v[e] = acc[0][e] * QSCALE;
  qk_store(v, ws + OFF_Q + ((size_t)(2 * nb) * NTT + tt) * 1024 + lane * 16);
#pragma unroll
  for (int e = 0; e < 16; ++e) v[e] = acc[1][e];
  qk_store(v, ws + OFF_K + ((size_t)(2 * nb) * NTT + tt) * 1024 + lane * 16);
#pragma unroll
  for (int e = 0; e < 16; ++e) v[e] = acc[2][e];
  v_store(v, ws + OFF_V + ((size_t)(2 * nb + (r >> 4)) * NTT + tt) * 1024 + hh * 256 + (r & 15) * 16);
  STAMP(5); STAMP_STORE();
}

DI void poll(const char* cnt, unsigned voff, unsigned target) {
#pragma unroll 1
  for (unsigned spins = 0; spins < 200000u; ++spins) {
    unsigned v;
    asm volatile("global_load_dword %0, %1, %2 sc1\n\ts_waitcnt vmcnt(0)" : "=v"(v) : "v"(voff), "s"(cnt) : "memory");
    if (__all(v >= target)) break;
  }
}
DI void poll_b(const char* cnt, unsigned target, LDSP(unsigned) go, unsigned seq, int j) {
  if (j == 0) {
    poll(cnt, 0u, target);
    __hip_atomic_store(go, seq, __ATOMIC_RELAXED, __HIP_MEMORY_SCOPE_WORKGROUP);
  } else {
    WAITV(0);
#pragma unroll 1
    for (unsigned spins = 0; spins < 4000000u; ++spins) {
      if (__builtin_amdgcn_readfirstlane(__hip_atomic_load(go, __ATOMIC_RELAXED, __HIP_MEMORY_SCOPE_WORKGROUP)) >= seq) break;
    }
  }
  asm volatile("" ::: "memory");
}
DI void arrive4(LDSP(unsigned) lw, char* cnt, int lane) {
  WAITV(0);
  unsigned old = 0;
  if (lane == 0) old = __hip_atomic_fetch_add(lw, 1u, __ATOMIC_RELAXED, __HIP_MEMORY_SCOPE_WORKGROUP);
  old = __builtin_amdgcn_readfirstlane(old);
  if ((old & 3u) == 3u && lane < 8) asm volatile("global_atomic_add %0, %1, %2" :: "v"(lane * 128), "v"(1u), "s"(cnt) : "memory");
  asm volatile("" ::: "memory");
}
DI void stat_issue(f32x4 (&p)[4], const char* sb, unsigned voff) {
  asm volatile("global_load_dwordx4 %0, %4, %5 sc1\n\tglobal_load_dwordx4 %1, %4, %5 offset:1024 sc1\n\tglobal_load_dwordx4 %2, %4, %5 offset:2048 sc1\n\t"
               "global_load_dwordx4 %3, %4, %5 offset:3072 sc1" : "=&v"(p[0]), "=&v"(p[1]), "=&v"(p[2]), "=&v"(p[3]) : "v"(voff), "s"(sb) : "memory");
}
DI void stat_finish(f32x4 (&p)[4], float& mean, float& var) {
  asm volatile("" : "+v"(p[0]), "+v"(p[1]), "+v"(p[2]), "+v"(p[3]));
  const f32x4 t = (p[0] + p[1]) + (p[2] + p[3]);
  auto a = __builtin_amdgcn_permlane16_swap(__float_as_uint(t[0]), __float_as_uint(t[2]), false, false);
  auto b = __builtin_amdgcn_permlane16_swap(__float_as_uint(t[1]), __float_as_uint(t[3]), false, false);
  mean = xhalf_add(__uint_as_float(a[0]) + __uint_as_float(a[1])) * (1.f / D_);
  var = fmaxf(xhalf_add(__uint_as_float(b[0]) + __uint_as_float(b[1])) * (1.f / D_) - mean * mean, 0.f) + LN_EPS;
}
DI void colsum_part(const char* src, unsigned voff, LDSP(char) dst) {
  f32x4 p[4];
  asm volatile("global_load_dwordx4 %0, %4, %5 sc1\n\tglobal_load_dwordx4 %1, %4, %6 sc1\n\tglobal_load_dwordx4 %2, %4, %7 sc1\n\t"
               "global_load_dwordx4 %3, %4, %8 sc1\n\ts_waitcnt vmcnt(0)" : "=&v"(p[0]), "=&v"(p[1]), "=&v"(p[2]), "=&v"(p[3])
               : "v"(voff), "s"(src), "s"(src + 20480), "s"(src + 40960), "s"(src + 61440) : "memory");
  *(LDSP(f32x4))dst = (p[0] + p[1]) + (p[2] + p[3]);
}

#define RES_FINISH(WR, RD) do { \
    _Pragma("unroll") for (int i = 1; i < 4; ++i) _Pragma("unroll") for (int g = 0; g < 4; ++g) { \
      const f32x4 v_ = {acc[i][4 * g], acc[i][4 * g + 1], acc[i][4 * g + 2], acc[i][4 * g + 3]}; \
      *(LDSP(f32x4))(lb + (WR) + g * 1024) = v_; } \
    asm volatile("s_waitcnt vmcnt(0)" : "+v"(rn[0]), "+v"(rn[1]), "+v"(rn[2]), "+v"(rn[3]) :: "memory"); \
    WAITBAR_L(); \
    _Pragma("unroll") for (int g = 0; g < 4; ++g) { \
      f32x4 t_ = {acc[0][4 * g], acc[0][4 * g + 1], acc[0][4 * g + 2], acc[0][4 * g + 3]}; \
      f32x4 u_[3]; \
      _Pragma("unroll") for (int i = 1; i < 4; ++i) u_[i - 1] = *(LDSP(const f32x4))(lb + (RD) + g * 1024); \
      t_ = (u_[0] + u_[1]) + (u_[2] + t_); \
      res[2 * g] = pk2(t_[0] + lo16(rn[g][0]), t_[1] + hi16(rn[g][0])); \
      res[2 * g + 1] = pk2(t_[2] + lo16(rn[g][1]), t_[3] + hi16(rn[g][1])); } } while (0)
DI void res_load(u32x2 (&rn)[4], unsigned vo, const char* p0, int d1, int d2) {
  asm volatile("global_load_dwordx2 %0, %4, %5 sc1\n\tglobal_load_dwordx2 %1, %4, %6 sc1\n\tglobal_load_dwordx2 %2, %4, %7 sc1\n\tglobal_load_dwordx2 %3, %4, %8 sc1"
               : "=&v"(rn[0]), "=&v"(rn[1]), "=&v"(rn[2]), "=&v"(rn[3]) : "v"(vo), "s"(p0), "s"(p0 + d1), "s"(p0 + d2), "s"(p0 + d2 + d1) : "memory");
}
DI void res_store_tiled(const unsigned (&res)[8], char* o, char* st, int lane) {
  const u32x4 p0 = {res[0], res[1], res[2], res[3]}, p1 = {res[4], res[5], res[6], res[7]};
  st16(o, p0);
  st16(o + 1024, p1);
  float s1 = 0.f, s2 = 0.f, t1 = 0.f, t2 = 0.f;
#pragma unroll
  for (int e = 0; e < 4; ++e) { s1 = dot2(res[e], ONE2, s1); s2 = dot2(res[e], res[e], s2); t1 = dot2(res[4 + e], ONE2, t1); t2 = dot2(res[4 + e], res[4 + e], t2); }
  s1 = xhalf_add(s1 + t1); s2 = xhalf_add(s2 + t2);
  if (lane < 32) { const f32x2 v = {s1, s2}; asm volatile("global_store_dwordx2 %0, %1, off sc1" :: "v"(st), "v"(v) : "memory"); }
}

DI int hsrc(int p) { const int g = p / 28, q = p - g * 28; return g * 28 + (q < 16 ? (q >> 2) * 7 + (q & 3) : ((q - 16) / 3) * 7 + 4 + (q - 16) % 3); }
DI const char* karg(int i) {
  const __attribute__((address_space(4))) unsigned long long* ka = (const __attribute__((address_space(4))) unsigned long long*)__builtin_amdgcn_kernarg_segment_ptr();
  asm volatile("" : "+s"(ka));
  return (const char*)ka[i];
}
extern "C" __global__ __launch_bounds__(512, 2) void k_main(char* __restrict__ ws_, const char* x_, const char* wq_, const char* wo_, const char* wg_,
                                                            const char* wu_, const char* wd_, char* out_ DIAG_ARG) {
  constexpr int L_K = 0, L_V = 65536, L_ONE = 131072, L_CMB = 133120;
  constexpr int GS = 32768, L_RED = 3 * GS;
  constexpr int L_D0 = 3 * GS, L_D1 = 2 * GS, L_D2 = 0;
  constexpr int QW = 12288, L_QX = 49152;
  constexpr int L_CS = 157696, L_ST = L_CS + 4096, L_CNT = L_ST + 1024, L_DBG = L_CNT + 64;
  __shared__ __attribute__((aligned(16))) char smem[163840];
  const int w_ = __builtin_amdgcn_readfirstlane(threadIdx.x >> 6);
  const unsigned lds0 = (unsigned)(uintptr_t)smem;
#define FRESH() \
  int tid; asm volatile("v_mbcnt_lo_u32_b32 %0, -1, 0\n\tv_mbcnt_hi_u32_b32 %0, -1, %0" : "=v"(tid)); int bid = blockIdx.x, w = w_; char* ws = ws_; asm volatile("" : "+s"(bid), "+s"(w), "+s"(ws)); \
  tid += w * 64; const int j = w & 3; \
  const int lane = tid & 63, r = lane & 31, hh = lane >> 5, xcd = bid & 7, idx = bid >> 3, grp = xcd * 2 + (idx >> 4), m = idx & 15; \
  const unsigned voff = lane * 16; LDSP(char) lb = (LDSP(char))smem + lane * 16; char* cnt = ws + OFF_CNT; char* wl = ws + layer * LSTRIDE; \
  LDSP(unsigned) lcnt = (LDSP(unsigned))((LDSP(char))smem + L_CNT) + (w >> 2); \
  const int ont = (m >> 1) * 2 + (j >> 1), ott = grp * 4 + (m & 1) * 2 + (j & 1); \
  const int gtb = (xcd & 1) * 8 + (idx & 7), gnb = (xcd >> 1) * 4 + (idx >> 3), qtb = (xcd & 3) * 4 + (idx & 3), qnb = (xcd >> 2) * 8 + (idx >> 2); \
  (void)r; (void)hh; (void)voff; (void)lb; (void)cnt; (void)wl; (void)lcnt; (void)ont; (void)ott; (void)gtb; (void)gnb; (void)qtb; (void)qnb
  if (threadIdx.x < 8) ((LDSP(unsigned))((LDSP(char))smem + L_CNT))[threadIdx.x] = 0u;
#pragma unroll 1
  for (int layer = 0; layer < 2; ++layer) {
    {
      FRESH();
      const int head = xcd * 4 + (idx & 3), qb = idx >> 2, half = w >> 2, tt0 = qb * 8 + 2 * j;
      PSTAMP(0);
      if (layer) { if (w == 4) poll(cnt + (C_KV + (head >> 1)) * 1024 + qb * 128, 0u, 16u); WAITBAR_L(); }
      PSTAMP(1);
      const char* ks = wl + OFF_K + (size_t)head * 65536 + half * 32768;
      const unsigned kd = lds0 + L_K + half * 32768;
      bf16x8 q0, q1;
      gld16<0>(q0, voff, wl + OFF_Q + ((size_t)head * NTT + tt0) * 1024);
      gld16<1024>(q1, voff, wl + OFF_Q + ((size_t)head * NTT + tt0) * 1024);
      glds2(voff, ks + j * 2048, kd + j * 2048);
      glds2(voff, ks + (OFF_V - OFF_K) + j * 2048, kd + L_V + j * 2048);
      *(LDSP(unsigned))((LDSP(char))smem + L_ONE + tid * 4) = ((tid >> 2) & 11) == 0 ? ONE2 : 0u;
      asm volatile("s_waitcnt vmcnt(0) lgkmcnt(0)\n\ts_barrier" : "+v"(q0), "+v"(q1) :: "memory");
      PSTAMP(2);
      LDSP(const char) kp = lb + L_K;
      LDSP(const char) vp0 = (LDSP(const char))smem + (r < 16 ? L_V : L_ONE) + hh * 256 + (r & 15) * 16;
      const int vstep = r < 16 ? 2048 : 0;
      LDSP(const char) kq = kp + half * 32768; LDSP(const char) vq = vp0 + half * 16 * vstep;
      f32x16 n0, n1, sA, sB = f32x16{}, o0 = f32x16{}, o1 = f32x16{};
      bf16x8 kA, kB, vp = bf16x8{};
      int sidx = (int)0xDDDDDDDDu; PIN(sidx);
      {
        const bf16x8 k0 = ldfrag(kp);
        kA = ldfrag(kq);
        const f32x16 s = MFMA(k0, q0, f32x16{}), t = MFMA(k0, q1, f32x16{});
        float a = fmaxf(fmaxf(s[0], s[1]), s[2]), b = fmaxf(fmaxf(t[0], t[1]), t[2]);
#pragma unroll
        for (int i = 3; i < 15; i += 2) { a = fmaxf(fmaxf(a, s[i]), s[i + 1]); b = fmaxf(fmaxf(b, t[i]), t[i + 1]); }
        a = xhalf_max(fmaxf(a, s[15])); b = xhalf_max(fmaxf(b, t[15]));
#pragma unroll
        for (int i = 0; i < 16; ++i) { n0[i] = -a; n1[i] = -b; }
        PIN(n0); PIN(n1);
        sA = MFMA(kA, q0, n0);
      }
#define EX(S, i) S[i] = __builtin_amdgcn_exp2f(S[i])
#define STEPN(CUR, NXT, KN_, QN, NN, O, VA, OP, VP, READS) do { \
      EX(CUR, 0); EX(CUR, 1); PIN(CUR); SBAR(); OP = smf<1>(VP, NXT, OP, sidx); SBAR(); READS; SBAR(); \
      EX(CUR, 2); EX(CUR, 3); EX(CUR, 4); EX(CUR, 5); PIN(CUR); SBAR(); NXT = MFMA(KN_, QN, NN); SBAR(); \
      EX(CUR, 6); EX(CUR, 7); EX(CUR, 8); EX(CUR, 9); EX(CUR, 10); PIN(CUR); SBAR(); O = smf<0>(VA, CUR, O, sidx); SBAR(); \
      EX(CUR, 11); EX(CUR, 12); EX(CUR, 13); EX(CUR, 14); EX(CUR, 15); PIN(CUR); SBAR(); } while (0)
#define TILE(t, KA, KB) do { bf16x8 va, vb; \
      STEPN(sA, sB, KA, q1, n1, o0, va, o1, vp, (va = ldfrag(vq + (t) * 1024), vb = ldfrag(vq + (t) * 1024 + 512), KB = ldfrag(kq + ((t) + 1) * 1024))); \
      STEPN(sB, sA, KB, q0, n0, o1, va, o0, vb, (vp = vb)); } while (0)
      __builtin_amdgcn_s_setprio(3);
#pragma unroll 1
      for (int ph = -2; ph < 5; ++ph) {
        if (ph == -1) {
          glds3(voff, ks + 8192 + j * 6144, kd + 8192 + j * 6144);
          glds3(voff, ks + (OFF_V - OFF_K) + 8192 + j * 6144, kd + L_V + 8192 + j * 6144);
          glds3(voff, ks + 11264 + j * 6144, kd + 11264 + j * 6144);
          glds3(voff, ks + (OFF_V - OFF_K) + 11264 + j * 6144, kd + L_V + 11264 + j * 6144);
        }
        if (ph == 0) {
          WAITV(0);
          if (lane == 0) __hip_atomic_fetch_add(lcnt + 4, 1u, __ATOMIC_RELAXED, __HIP_MEMORY_SCOPE_WORKGROUP);
#pragma unroll 1
          for (unsigned spins = 0; spins < 4000000u; ++spins)
            if (__builtin_amdgcn_readfirstlane(__hip_atomic_load(lcnt + 4, __ATOMIC_RELAXED, __HIP_MEMORY_SCOPE_WORKGROUP)) >= 4u * (layer + 1)) break;
          asm volatile("" ::: "memory");
        }
        if (ph == 1) __builtin_amdgcn_s_setprio(2);
        if (ph == 3) __builtin_amdgcn_s_setprio(1);
        if (ph == 4) __builtin_amdgcn_s_setprio(0);
        if (layer == 0 && ph >= 0) {
          const int q4 = (lane & 15) >> 2, b16 = (lane >> 4) & 1;
          const int scr = L_CMB + (half ? j * 5120 : 20480 + j * 2048);
          const int jo = ((ph - 1) * 8 + w) * 256 + bid;
          if ((ph == 1 || ph == 2) && jo < 3712) {
            const bool gu = jo < 1792;
            LDSP(const char) sp = (LDSP(const char))smem + scr + (lane & 3) * 8 +
                                  (gu ? (16 * hh + 4 * b16 + q4) * 32 : (4 * hh + q4) * 64 + b16 * 32);
            const int ho = gu ? 256 : 512;
            const char* pd;
            if (gu) { const int ft = jo >> 4, S = jo & 15, nb = ft / 7, ii = ft - nb * 7; pd = ws + OFF_WGU + (size_t)(nb * 224 + (ii < 4 ? S * 8 + 2 * ii : 128 + S * 6 + 2 * (ii - 4))) * 1024; }
            else if (jo < 2048) pd = ws + OFF_WO + (size_t)(jo - 1792) * 2048;
            else if (jo < 2944) pd = ws + OFF_WD + (size_t)(jo - 2048) * 2048;
            else { const int t = (jo - 2944) >> 4, nb = t / 3, ii = t - nb * 3, s0 = (jo & 15) * 2; pd = ws + OFF_WQ + (size_t)(((nb * 8 + (s0 >> 2)) * 3 + ii) * 4 + (s0 & 3)) * 1024; }
            WAITV(0);
            const u32x4 fa = __builtin_bit_cast(u32x4, trfrag(sp, ho)), fb = __builtin_bit_cast(u32x4, trfrag(sp + 1024, ho));
            st16s<0>(voff, pd, fa); st16s<1024>(voff, pd, fb);
            if (gu || jo >= 2944) {
              float ca = (lo16(fa[0]) + hi16(fa[0])) + (lo16(fa[1]) + hi16(fa[1])) + (lo16(fa[2]) + hi16(fa[2])) + (lo16(fa[3]) + hi16(fa[3]));
              float cb = (lo16(fb[0]) + hi16(fb[0])) + (lo16(fb[1]) + hi16(fb[1])) + (lo16(fb[2]) + hi16(fb[2])) + (lo16(fb[3]) + hi16(fb[3]));
              if (gu) { ca = xhalf_add(x16_add(ca)); cb = xhalf_add(x16_add(cb)); ca = lane < 16 ? ca : cb; }
              else ca = xhalf_add(ca + cb);
              const char* cp = ws + OFF_CSP + ((jo & 15) * 5120 + ((gu ? jo : jo - 2944 + 1792) >> 4) * 32) * 4;
              if (lane < 32) asm volatile("global_store_dword %0, %1, %2 sc1" :: "v"(lane * 4), "v"(ca), "s"(cp) : "memory");
            }
          }
          const int jn = jo + 2048;
          if (ph < 2 && jn < 3712) {
            const char *sa, *sb; unsigned gv;
            if (jn < 1792) {
              const int o = 32 * (jn & 15) * (F_ * 2) + (jn >> 4) * 32;
              gv = (lane >> 1) * (F_ * 2) + (lane & 1) * 16; sa = karg(4) + o; sb = karg(5) + o;
            } else {
              int nt, s0, s1, ld = 1024, ai;
              if (jn < 2048) { const int f = (jn - 1792) * 2; nt = f >> 5; s0 = f & 31; s1 = s0 + 1; ai = 3; }
              else if (jn < 2944) { const int f = (jn - 2048) * 2; nt = f / KSF; s0 = hsrc(f - nt * KSF); s1 = hsrc(f - nt * KSF + 1); ai = 6; }
              else { const int t = (jn - 2944) >> 4, nb = t / 3; s0 = (jn & 15) * 2; s1 = s0 + 1; nt = (t - nb * 3) * 16 + nb; ai = 2; ld = 3072; }
              gv = (lane >> 2) * ld + (lane & 3) * 16;
              const char* src = karg(ai) + nt * 64;
              sa = src + 16 * s0 * ld; sb = src + 16 * s1 * ld;
            }
            __builtin_amdgcn_s_waitcnt(0xc07f);
            glds1(gv, sa, lds0 + scr); glds1(gv, sb, lds0 + scr + 1024);
          }
          if (ph == 3) arrive4(lcnt, cnt + (C_PREP + xcd) * 1024, lane);
          if (ph == 3 && w == 1 && idx < KMAIN_KB) {
            unsigned lo, hi;
            asm volatile("s_getpc_b64 vcc\n\ts_add_u32 %0, vcc_lo, k_main@rel32@lo+4\n\ts_addc_u32 %1, vcc_hi, k_main@rel32@hi+12" : "=s"(lo), "=s"(hi) :: "vcc", "scc");
            glds1(voff, (const char*)(((unsigned long long)hi << 32) | lo) + idx * 1024, lds0 + scr);
          }
        }
        int n = ph <= 0 ? ph + 3 : ph == 2 ? 2 : ph == 4 ? 2 : 3;
#pragma unroll 1
        do { TILE(0, kA, kB); TILE(1, kB, kA); kq += 2048; vq += vstep; } while (--n > 0);
      }
      o1 = smf<1>(vp, sB, o1, sidx);
#undef TILE
#undef STEPN
      PSTAMP(3);
      LDSP(char) xp = lb + L_CMB + j * 5120;
      if (half == 1) {
        const f32x4 a = {o0[0], o0[1], o0[2], o0[3]}, b = {o0[4], o0[5], o0[6], o0[7]}, c = {o1[0], o1[1], o1[2], o1[3]}, d = {o1[4], o1[5], o1[6], o1[7]};
        const f32x2 l = {o0[8], o1[8]};
        *(LDSP(f32x4))xp = a; *(LDSP(f32x4))(xp + 1024) = b; *(LDSP(f32x4))(xp + 2048) = c; *(LDSP(f32x4))(xp + 3072) = d; *(LDSP(f32x2))(xp + 4096) = l;
      }
      if (layer == 0 && w == 0) poll(cnt + C_PREP * 1024 + (idx & 7) * 128, (lane & 7) * 1024, 64u);
      WAITBAR_L();
      if (half == 0) {
        __builtin_amdgcn_s_setprio(3);
        PSTAMP(11);
        const f32x4 a = *(LDSP(const f32x4))xp, b = *(LDSP(const f32x4))(xp + 1024), c = *(LDSP(const f32x4))(xp + 2048), d = *(LDSP(const f32x4))(xp + 3072);
        const f32x2 l = *(LDSP(const f32x2))(xp + 4096);
#pragma unroll
        for (int i = 0; i < 4; ++i) { o0[i] += a[i]; o0[4 + i] += b[i]; o1[i] += c[i]; o1[4 + i] += d[i]; }
        o0[8] += l[0]; o1[8] += l[1];
        float chk = (o0[8] + o1[8]) * 5.4210109e-20f;
#pragma unroll
        for (int i = 0; i < 8; ++i) chk = __builtin_fmaf(o0[i], 0.f, __builtin_fmaf(o1[i], 0.f, chk));
        const bool bad = !(chk < 1.f);
        if (__builtin_expect(__any(bad), 0)) {
#pragma unroll 1
          for (int z = 0; z < 2; ++z) {
            const bf16x8 q = z ? q1 : q0;
            float mx = -INFINITY;
            f32x16 o = f32x16{};
            kq = kp; vq = vp0;
#pragma unroll 1
            for (int t = 0; t < 64; ++t) {
              const bf16x8 k = ldfrag(kq), v0 = ldfrag(vq), v1 = ldfrag(vq + 512);
              f32x16 s = MFMA(k, q, f32x16{});
              float tm = s[0];
#pragma unroll
              for (int i = 1; i < 16; ++i) tm = fmaxf(tm, s[i]);
              tm = xhalf_max(tm);
              const float mn = fmaxf(mx, tm);
              const float alpha = __builtin_amdgcn_exp2f(mx - mn);
              mx = mn;
#pragma unroll
              for (int i = 0; i < 16; ++i) { o[i] *= alpha; s[i] = __builtin_amdgcn_exp2f(s[i] - mn); }
              o = smf<0>(v0, s, o, sidx);
              o = smf<1>(v1, s, o, sidx);
              kq += 1024; vq += vstep >> 1;
            }
            if (z) o1 = o; else o0 = o;
          }
        }
        const float i0 = __builtin_amdgcn_rcpf(o0[8]), i1 = __builtin_amdgcn_rcpf(o1[8]);
        const u32x4 a0 = {pk2(o0[0] * i0, o0[1] * i0), pk2(o0[2] * i0, o0[3] * i0), pk2(o0[4] * i0, o0[5] * i0), pk2(o0[6] * i0, o0[7] * i0)};
        const u32x4 a1 = {pk2(o1[0] * i1, o1[1] * i1), pk2(o1[2] * i1, o1[3] * i1), pk2(o1[4] * i1, o1[5] * i1), pk2(o1[6] * i1, o1[7] * i1)};
        char* op = wl + OFF_O + ((size_t)tt0 * KSD + head) * 1024 + voff;
        st16(op, a0); st16(op + 32768, a1);
        PSTAMP(12);
        WAITBAR_L();
        arrive4(lcnt, cnt + (C_O + layer * 8 + qb) * 1024, lane);
      }
    }
    PSTAMP(4);
    int role = w_ >> 2;
    asm volatile("" : "+s"(role));
    if (role == 0) {
      {
        FRESH();
        const char* wb = ws + OFF_WGU + (size_t)gnb * 229376 + j * 8192;
#pragma unroll 1
        for (int c = 0; c < 4; ++c) glds4(voff, wb + (c >> 1) * GS + (c & 1) * 4096, lds0 + (c >> 1) * GS + (c & 1) * 4096 + j * 8192);
        colsum_part(ws + OFF_CSP + (size_t)j * 81920 + gnb * 896, voff, lb + L_CS + j * 1024);
      }
      PSTAMP(14);
      WAITBAR_L();
      PSTAMP(15);
      {
        FRESH();
        const int g4 = lane >> 4, c16 = lane & 15;
        const char* gx = wl + OFF_X2 + (size_t)(gtb * 4 + j) * 32768;
        unsigned xo = (g4 >> 1) * 1024 + ((g4 & 1) * 32 + c16) * 16;
        const char* ob = wl + OFF_H + ((size_t)(gtb * 4 + j) * KSF + (gnb >> 2) * 28) * 1024;
        unsigned oo = (((g4 & 1) * 32) + (g4 >> 1) * 16 + c16) * 16;
        asm volatile("" : "+s"(gx), "+s"(ob)); PIN(xo); PIN(oo);
        bf16x8 xr[16][2], wf[8];
        f32x4 ac[2][8];
#pragma unroll 1
        for (unsigned spins = 0; spins < 4000000u; ++spins)
          if (__builtin_amdgcn_readfirstlane(__hip_atomic_load(lcnt + 2, __ATOMIC_RELAXED, __HIP_MEMORY_SCOPE_WORKGROUP)) >= (unsigned)(layer * 8 + 2)) break; else __builtin_amdgcn_s_sleep(1);
        asm volatile("" ::: "memory");
#define LDXS(S) do { gld16<((S) & 1) * 2048>(xr[S][0], xo, gx + ((S) >> 1) * 4096); gld16<((S) & 1) * 2048 + 256>(xr[S][1], xo, gx + ((S) >> 1) * 4096); } while (0)
        sfor<0, 8>([&](auto ic) { LDXS(decltype(ic)::value); });
        PSTAMP(5);
        auto pass = [&](auto nt_, auto st0_) {
          constexpr int NT = decltype(nt_)::value, ST0 = decltype(st0_)::value, PD = 6, NF = 16 * NT;
          sfor<0, NF + PD>([&](auto ic) {
            constexpr int f = decltype(ic)::value, g = f - PD;
            if constexpr (g >= 0) {
              constexpr int S = g / NT, i = g % NT;
              if constexpr (ST0 == 0 && i == 0) {
                constexpr int last = 4 * (S / 4) + 7 > 15 ? 15 : 4 * (S / 4) + 7;
                asm volatile("s_waitcnt vmcnt(%2)" : "+v"(xr[S][0]), "+v"(xr[S][1]) : "i"(2 * (last - S)) : "memory");
              }
              SBAR();
              if constexpr (S == 0) ac[0][i] = __builtin_amdgcn_mfma_f32_16x16x32_bf16(wf[g & 7], xr[S][0], f32x4{}, 0, 0, 0);
              else ac[0][i] = __builtin_amdgcn_mfma_f32_16x16x32_bf16(wf[g & 7], xr[S][0], ac[0][i], 0, 0, 0);
              SBAR();
            }
            if constexpr (f < NF) {
              if constexpr (f % (4 * NT) == 0 && (ST0 + f / (4 * NT)) > 0) WAITBAR_L();
              wf[f & 7] = ldfrag(lb + ((ST0 + f / (4 * NT)) % 3) * GS + (f % (4 * NT)) * 1024);
            }
            if constexpr (g >= 0) {
              constexpr int S = g / NT, i = g % NT;
              SBAR();
              if constexpr (S == 0) ac[1][i] = __builtin_amdgcn_mfma_f32_16x16x32_bf16(wf[g & 7], xr[S][1], f32x4{}, 0, 0, 0);
              else ac[1][i] = __builtin_amdgcn_mfma_f32_16x16x32_bf16(wf[g & 7], xr[S][1], ac[1][i], 0, 0, 0);
              SBAR();
              if constexpr (ST0 == 0 && i == NT - 1 && S % 4 == 3 && S < 8) sfor<S + 5, S + 9>([&](auto jc) { LDXS(decltype(jc)::value); });
              if constexpr (ST0 == 4 && g == 4 * NT) arrive4(lcnt, cnt + (C_H + layer * 16 + gtb) * 1024, lane);
            }
          });
        };
        auto epilogue = [&](auto nt_, auto t0_, const char* o) {
          constexpr int NH = decltype(nt_)::value, T0 = decltype(t0_)::value;
          LDSP(const float) sx = (LDSP(const float))((LDSP(char))smem + L_ST) + j * 64 + c16;
          const float rs0 = sx[0], rs1 = sx[16], m0 = -sx[32], m1 = -sx[48];
          f32x4 c4[2 * NH];
#pragma unroll
          for (int i = 0; i < 2 * NH; ++i) c4[i] = *(LDSP(const f32x4))((LDSP(const char))smem + L_CS + (2 * T0 + i) * 64 + g4 * 16);
          const f32x4 vm0 = {m0, m0, m0, m0}, vm1 = {m1, m1, m1, m1};
          sfor<0, NH>([&](auto ic) {
            constexpr int i = decltype(ic)::value;
            const f32x4 ga = __builtin_elementwise_fma(c4[2 * i], vm0, ac[0][2 * i]), ua = __builtin_elementwise_fma(c4[2 * i + 1], vm0, ac[0][2 * i + 1]) * rs0;
            const f32x4 gb = __builtin_elementwise_fma(c4[2 * i], vm1, ac[1][2 * i]), ub = __builtin_elementwise_fma(c4[2 * i + 1], vm1, ac[1][2 * i + 1]) * rs1;
            const f32x4 h0 = __builtin_elementwise_max(ga, f32x4{}) * ua, h1 = __builtin_elementwise_max(gb, f32x4{}) * ub;
            auto e0 = __builtin_amdgcn_permlane32_swap(pk2(h0[0], h0[1]), pk2(h1[0], h1[1]), false, false);
            auto e1 = __builtin_amdgcn_permlane32_swap(pk2(h0[2], h0[3]), pk2(h1[2], h1[3]), false, false);
            const u32x4 a_ = {e0[0], e1[0], e0[1], e1[1]};
            st16s<i * 1024>(oo, o, a_);
          });
        };
        pass(IC<8>{}, IC<0>{});
        epilogue(IC<4>{}, IC<0>{}, ob + (gnb & 3) * 4096);
        pass(IC<6>{}, IC<4>{});
        PSTAMP(6);
        epilogue(IC<3>{}, IC<4>{}, ob + 16384 + (gnb & 3) * 3072);
#undef LDXS
        PSTAMP(13);
        arrive4(lcnt, cnt + (C_H2 + layer * 16 + gtb) * 1024, lane);
        PSTAMP(7);
      }
      WAITBAR_L();
      if (layer == 0) {
        FRESH();
        __builtin_amdgcn_s_barrier();
        { const char* wb = ws + OFF_WQ + (size_t)qnb * 8 * QW + j * 3072;
#pragma unroll 1
          for (int c = 0; c < 4; ++c) glds3(voff, wb + c * QW, lds0 + c * QW + j * 3072); }
        colsum_part(ws + OFF_CSP + (size_t)j * 81920 + (3584 + qnb * 96) * 4, voff, lb + L_CS + j * 1024);
        QINIT();
#define RX(B, u) xf[B] = ldfrag(sxp + (u) * 1024)
#define RW(B, i, u) wf[B][i] = ldfrag(swp + (i) * 4096 + (u) * 1024)
        WAITBAR_L();
        PSTAMP(8);
#pragma unroll 1
        for (int c = 0; c < 8; ++c) {
          LDSP(const char) swp = lb + (c & 3) * QW;
          LDSP(const char) sxp = lb + L_QX + (c & 3) * 16384 + j * 4096;
          QCHUNK();
          WAITBAR_L();
        }
        LDSP(const float) cx = (LDSP(const float))((LDSP(char))smem + L_CS);
        LDSP(const float) sx = (LDSP(const float))((LDSP(char))smem + L_ST) + j * 64;
        f32x4 cq[8], ra[4], rb[4];
        const float rstd = sx[r], mr = sx[32 + r], cv = cx[64 + r];
#pragma unroll
        for (int g = 0; g < 8; ++g) cq[g] = *(LDSP(const f32x4))(cx + 8 * g + 4 * hh);
#pragma unroll
        for (int g = 0; g < 4; ++g) { ra[g] = *(LDSP(const f32x4))(sx + 8 * g + 4 * hh); rb[g] = *(LDSP(const f32x4))(sx + 32 + 8 * g + 4 * hh); }
        SBAR();
        QFINISH();
        SBAR();
#undef RX
#undef RW
        PSTAMP(9);
        char* wn = ws + LSTRIDE;
        const int tt = qtb * 4 + j;
        float v[16];
        const float rq = rstd * QSCALE, mq = mr * QSCALE;
#pragma unroll
        for (int e = 0; e < 16; ++e) v[e] = acc[0][e] * rq - mq * cq[e >> 2][e & 3];
        qk_store(v, wn + OFF_Q + ((size_t)(2 * qnb) * NTT + tt) * 1024 + voff);
#pragma unroll
        for (int e = 0; e < 16; ++e) v[e] = acc[1][e] * rstd - mr * cq[4 + (e >> 2)][e & 3];
        qk_store(v, wn + OFF_K + ((size_t)(2 * qnb) * NTT + tt) * 1024 + voff);
#pragma unroll
        for (int e = 0; e < 16; ++e) v[e] = acc[2][e] * ra[e >> 2][e & 3] - rb[e >> 2][e & 3] * cv;
        v_store(v, wn + OFF_V + ((size_t)(2 * qnb + (r >> 4)) * NTT + tt) * 1024 + hh * 256 + (r & 15) * 16);
        arrive4(lcnt, cnt + (C_KV + qnb) * 1024, lane);
        PSTAMP(10);
      }
    } else {
      {
        FRESH();
        bf16x8 w0[8], w1[8], x0[8], x1[8];
        const char* wa = ws + OFF_WO + ((size_t)ont * KSD + j * 8) * 1024;
        const char* wb = ws + OFF_WO + ((size_t)(ont ^ 1) * KSD + j * 8) * 1024;
#define LD4(D, o, base) do { gld16<0>(D[o], voff, base); gld16<1024>(D[(o) + 1], voff, base); gld16<2048>(D[(o) + 2], voff, base); gld16<3072>(D[(o) + 3], voff, base); } while (0)
        __builtin_amdgcn_s_barrier();
        LD4(w0, 0, wa); LD4(w0, 4, wa + 4096); LD4(w1, 0, wb); LD4(w1, 4, wb + 4096);
        u32x2 rn[4]; unsigned res[8];
        res_load(rn, layer ? voff : r * 1024 + hh * 8, (layer ? ws + OFF_X3 + ont * 2048 : karg(1) + ont * 64) + (size_t)ott * 32768, layer ? 8 : 16, layer ? 1024 : 32);
        const char* xa = wl + OFF_O + ((size_t)ott * KSD + j * 8) * 1024;
        const char* xb = wl + OFF_O + ((size_t)(ott ^ 1) * KSD + j * 8) * 1024;
        asm volatile("" : "+s"(xa), "+s"(xb));
        poll_b(cnt + (C_O + layer * 8 + xcd) * 1024 + (idx & 7) * 128, 32u, lcnt + 1, layer * 8 + 1, j);
        PSTAMP(5);
        #define LD2(s) do { gld16<((s) & 3) * 1024>(x0[s], voff, xa + ((s) >> 2) * 4096); gld16<((s) & 3) * 1024>(x1[s], voff, xb + ((s) >> 2) * 4096); } while (0)
        LD2(0); LD2(1); LD2(2); LD2(3); LD2(4); LD2(5); LD2(6); LD2(7);
#undef LD2
        f32x16 acc[4];
#pragma unroll
        for (int i = 0; i < 4; ++i) acc[i] = f32x16{};
#define MM4(W0, W1, X0, X1) do { acc[1] = MFMA(W0, X1, acc[1]); acc[2] = MFMA(W1, X0, acc[2]); acc[3] = MFMA(W1, X1, acc[3]); acc[0] = MFMA(W0, X0, acc[0]); } while (0)
#define PIN8(n, X, Y, o) asm volatile("s_waitcnt vmcnt(%8)" : "+v"(X[o]), "+v"(X[(o) + 1]), "+v"(X[(o) + 2]), "+v"(X[(o) + 3]), "+v"(Y[o]), "+v"(Y[(o) + 1]), "+v"(Y[(o) + 2]), "+v"(Y[(o) + 3]) : "i"(n) : "memory")
        PIN8(16, w0, w1, 0); PIN8(16, w0, w1, 4);
#define WX(b, n) asm volatile("s_waitcnt vmcnt(%2)" : "+v"(x0[b]), "+v"(x1[b]) : "i"(n) : "memory")
#define ST(s) do { WX(s, 14 - 2 * (s)); MM4(w0[s], w1[s], x0[s], x1[s]); SBAR(); } while (0)
        ST(0); ST(1); ST(2); ST(3); ST(4); ST(5); ST(6); ST(7);
#undef ST
#undef WX
        PSTAMP(6);
        RES_FINISH(L_RED + ((j ^ i) * 3 + i - 1) * 4096, L_RED + (j * 3 + i - 1) * 4096);
        PSTAMP(13);
        res_store_tiled(res, wl + OFF_X2 + ((size_t)ott * KSD + 2 * ont) * 1024 + voff, wl + OFF_ST2 + (size_t)ott * 4096 + ont * 256 + r * 8, lane);
        PSTAMP(14);
        arrive4(lcnt, cnt + (C_X2 + layer * 16 + grp) * 1024, lane);
        PSTAMP(7);
      }
      {
        FRESH();
        { LDSP(char) cp = (LDSP(char))smem + L_CS + j * 256 + lane * 4;
          *(LDSP(float))cp = (*(LDSP(float))cp + *(LDSP(float))(cp + 1024)) + (*(LDSP(float))(cp + 2048) + *(LDSP(float))(cp + 3072)); }
        bf16x8 w0[12], w1[12], x0[8], x1[8];
        const char* wa = ws + OFF_WD + ((size_t)ont * KSF + j * 28) * 1024;
        const char* wb = ws + OFF_WD + ((size_t)(ont ^ 1) * KSF + j * 28) * 1024;
#define DISSUE(c, P) do { glds4(voff, wa + 12288 + (c) * 4096, lds0 + (P) + j * 8192); glds4(voff, wb + 12288 + (c) * 4096, lds0 + (P) + j * 8192 + 4096); } while (0)
        {
          const char* gw = ws + OFF_WGU + (size_t)gnb * 229376;
          const char* gs = wl + OFF_ST2 + (size_t)(gtb * 4 + j) * 4096;
          f32x4 sp[4];
          asm volatile("" : "+s"(gw), "+s"(gs));
#define G1(st) do { glds4(voff, gw + (st) * GS + j * 8192, lds0 + ((st) % 3) * GS + j * 8192); glds4(voff, gw + (st) * GS + j * 8192 + 4096, lds0 + ((st) % 3) * GS + j * 8192 + 4096); } while (0)
#define G2(st) do { glds3(voff, gw + 4 * GS + ((st) - 4) * 24576 + j * 6144, lds0 + ((st) % 3) * GS + j * 6144); \
                    glds3(voff, gw + 4 * GS + ((st) - 4) * 24576 + j * 6144 + 3072, lds0 + ((st) % 3) * GS + j * 6144 + 3072); } while (0)
          poll_b(cnt + (C_X2 + layer * 16 + gtb) * 1024 + (gnb & 7) * 128, 16u, lcnt + 1, layer * 8 + 2, j);
          PSTAMP(8);
          __builtin_amdgcn_s_sleep(8);
          stat_issue(sp, gs, voff);
          G1(2);
          __builtin_amdgcn_s_barrier();
          G1(3); LD4(w0, 0, wa); LD4(w1, 0, wb);
          WAITV(16);
          { float mean, var; stat_finish(sp, mean, var);
            ((LDSP(float))((LDSP(char))smem + L_ST))[j * 64 + hh * 32 + (lane & 15) * 2 + ((lane >> 4) & 1)] = hh ? mean : 1.f / var; }
          WAITBAR_V(16);
          G2(4); LD4(w0, 4, wa + 4096); LD4(w1, 4, wb + 4096);
          WAITBAR_V(22);
          G2(5); LD4(w0, 8, wa + 8192); LD4(w1, 8, wb + 8192);
          WAITBAR_V(22);
          G2(6); DISSUE(0, L_D0);
          WAITBAR_V(22);
          G2(7);
          WAITBAR_V(14);
          DISSUE(1, L_D1);
          WAITBAR_V(8);
          DISSUE(2, L_D2);
#undef G1
#undef G2
        }
        const char* xa = wl + OFF_H + ((size_t)ott * KSF + j * 28) * 1024;
        const char* xb = wl + OFF_H + ((size_t)(ott ^ 1) * KSF + j * 28) * 1024;
        asm volatile("" : "+s"(xa), "+s"(xb));
        poll_b(cnt + (C_H + layer * 16 + grp) * 1024 + (idx & 7) * 128, 16u, lcnt + 1, layer * 8 + 3, j);
        PSTAMP(9);
        PIN8(0, w0, w1, 0); PIN8(0, w0, w1, 4); PIN8(0, w0, w1, 8);
#define LDX(b, blk) do { gld16<((b) & 3) * 1024>(x0[b], voff, xa + (blk) * 4096); gld16<((b) & 3) * 1024>(x1[b], voff, xb + (blk) * 4096); } while (0)
#define WX(b, n) asm volatile("s_waitcnt vmcnt(%2)" : "+v"(x0[b]), "+v"(x1[b]) : "i"(n) : "memory")
        LDX(0, 0); LDX(1, 0); LDX(2, 0); LDX(3, 0); LDX(4, 1); LDX(5, 1); LDX(6, 1); LDX(7, 1);
        f32x16 acc[4];
#pragma unroll
        for (int i = 0; i < 4; ++i) acc[i] = f32x16{};
        LDSP(const char) lw = lb + j * 8192;
#define BLK(o, h, nb, n0) do { \
          WX((h) + 0, n0); MM4(w0[(o) + 0], w1[(o) + 0], x0[(h) + 0], x1[(h) + 0]); SBAR(); if ((nb) >= 0) LDX((h) + 0, nb); SBAR(); \
          WX((h) + 1, (nb) >= 0 ? n0 : n0 - 2); MM4(w0[(o) + 1], w1[(o) + 1], x0[(h) + 1], x1[(h) + 1]); SBAR(); if ((nb) >= 0) LDX((h) + 1, nb); SBAR(); \
          WX((h) + 2, (nb) >= 0 ? n0 : n0 - 4); MM4(w0[(o) + 2], w1[(o) + 2], x0[(h) + 2], x1[(h) + 2]); SBAR(); if ((nb) >= 0) LDX((h) + 2, nb); SBAR(); \
          WX((h) + 3, (nb) >= 0 ? n0 : n0 - 6); MM4(w0[(o) + 3], w1[(o) + 3], x0[(h) + 3], x1[(h) + 3]); SBAR(); if ((nb) >= 0) LDX((h) + 3, nb); SBAR(); } while (0)
#define RDL(o, c) do { _Pragma("unroll") for (int s = 0; s < 4; ++s) { w0[(o) + s] = ldfrag(lw + (c) + s * 1024); w1[(o) + s] = ldfrag(lw + (c) + 4096 + s * 1024); } SBAR(); } while (0)
        BLK(0, 0, 2, 14); RDL(0, L_D0);
        BLK(4, 4, 3, 14); RDL(4, L_D1);
        BLK(8, 0, -1, 14); RDL(8, L_D2);
        BLK(0, 4, -1, 6);
        LD4(w0, 0, wa + 24576); LD4(w1, 0, wb + 24576);
        PSTAMP(15);
        poll_b(cnt + (C_H2 + layer * 16 + grp) * 1024 + (idx & 7) * 128, 16u, lcnt + 1, layer * 8 + 4, j);
        PIN8(0, w0, w1, 0);
        LDX(0, 4); LDX(1, 4); LDX(2, 4); LDX(3, 4); LDX(4, 5); LDX(5, 5); LDX(6, 5); LDX(7, 5);
        BLK(4, 0, 6, 14);
        u32x2 rn[4]; unsigned res[8];
        res_load(rn, voff, wl + OFF_X2 + (size_t)ott * 32768 + ont * 2048, 8, 1024);
        BLK(8, 4, -1, 18);
        BLK(0, 0, -1, 10);
#undef DISSUE
#undef RDL
#undef BLK
#undef LDX
#undef WX
        {
        FRESH();
        PSTAMP(10);
        RES_FINISH((i == 3 ? L_D1 : L_D0 + (i - 1) * 4096) + j * 8192, (i == 3 ? L_D1 : L_D0 + (i - 1) * 4096) + (j ^ i) * 8192);
        if (layer == 0) {
          res_store_tiled(res, ws + OFF_X3 + ((size_t)ott * KSD + 2 * ont) * 1024 + voff, ws + OFF_ST3 + (size_t)ott * 4096 + ont * 256 + r * 8, lane);
          arrive4(lcnt, cnt + (C_X3 + grp) * 1024, lane);
        } else {
          auto s0 = __builtin_amdgcn_permlane32_swap(res[0], res[2], false, false);
          auto s1 = __builtin_amdgcn_permlane32_swap(res[1], res[3], false, false);
          auto s2 = __builtin_amdgcn_permlane32_swap(res[4], res[6], false, false);
          auto s3 = __builtin_amdgcn_permlane32_swap(res[5], res[7], false, false);
          const u32x4 a = {s0[0], s1[0], s0[1], s1[1]}, b = {s2[0], s3[0], s2[1], s3[1]};
          char* o = (char*)karg(7) + ((size_t)(ott * 32 + r) * D_ + ont * 32) * 2 + hh * 16;
          st16(o, a);
          st16(o + 32, b);
        }
        }
        PSTAMP(11);
      }
#undef LD4
#undef MM4
#undef PIN8
      if (layer == 0) {
        FRESH();
        const int tt = qtb * 4 + j;
        const char* xb = ws + OFF_X3 + (size_t)tt * 32768;
        const char* wb = ws + OFF_WQ + (size_t)qnb * 8 * QW + j * 3072;
        f32x4 sp[4];
        asm volatile("" : "+s"(xb), "+s"(wb));
        __builtin_amdgcn_s_barrier();
        poll_b(cnt + (C_X3 + qtb) * 1024 + (qnb & 7) * 128, 16u, lcnt + 1, 5, j);
        PSTAMP(12);
#define QISSUE(c) do { glds4(voff, xb + (c) * 4096, lds0 + L_QX + ((c) & 3) * 16384 + j * 4096); glds3(voff, wb + (c) * QW, lds0 + ((c) & 3) * QW + j * 3072); } while (0)
        glds4(voff, xb, lds0 + L_QX + j * 4096);
        glds4(voff, xb + 4096, lds0 + L_QX + 16384 + j * 4096);
        stat_issue(sp, ws + OFF_ST3 + (size_t)tt * 4096, voff);
        glds4(voff, xb + 8192, lds0 + L_QX + 32768 + j * 4096);
        glds4(voff, xb + 12288, lds0 + L_QX + 49152 + j * 4096);
        WAITBAR_V(16);
        { LDSP(char) cp = (LDSP(char))smem + L_CS + j * 256 + lane * 4;
          *(LDSP(float))cp = (*(LDSP(float))cp + *(LDSP(float))(cp + 1024)) + (*(LDSP(float))(cp + 2048) + *(LDSP(float))(cp + 3072)); }
        WAITBAR_V(12);
        QISSUE(4); WAITBAR_V(11);
#pragma unroll 1
        for (int c = 5; c < 8; ++c) { QISSUE(c); WAITBAR_V(14); }
#undef QISSUE
        { float mean, var; stat_finish(sp, mean, var); const float rs = rsqrtf(var);
          ((LDSP(float))((LDSP(char))smem + L_ST))[j * 64 + hh * 32 + (lane & 15) * 2 + ((lane >> 4) & 1)] = hh ? mean * rs : rs; }
        WAITBAR_V(7);
        WAITBAR_V(0);
        __builtin_amdgcn_s_barrier();
      }
    }
  }
}

extern "C" void kernel_launch(void* const* d_in, const int* in_sizes, int n_in, void* d_out, int out_size, void* d_ws, size_t ws_size,
                              hipStream_t stream) {
  const char* x = (const char*)d_in[0];
  const char* wqkv = (const char*)d_in[1];
  const char* wo = (const char*)d_in[2];
  const char* wg = (const char*)d_in[4];
  const char* wu = (const char*)d_in[5];
  const char* wd = (const char*)d_in[6];
  char* ws = (char*)d_ws;
  k_qkv1<<<256, 768, 0, stream>>>(x, wqkv, ws DIAG_PASS(0));
  k_main<<<256, 512, 0, stream>>>(ws, x, wqkv, wo, wg, wu, wd, (char*)d_out DIAG_PASS(1));
}
```

```cpp
#include <hip/hip_runtime.h>
#include <hip/hip_bf16.h>
#include <stdint.h>

typedef short bf16x8 __attribute__((ext_vector_type(8)));
typedef float f32x16 __attribute__((ext_vector_type(16)));
typedef float f32x4 __attribute__((ext_vector_type(4)));
typedef float f32x2 __attribute__((ext_vector_type(2)));
typedef unsigned u32x4 __attribute__((ext_vector_type(4)));
typedef unsigned u32x2 __attribute__((ext_vector_type(2)));
typedef __bf16 bf16v2 __attribute__((ext_vector_type(2)));
typedef unsigned short u16;

#define STAMP(i)
#define STAMP_DECL
#define PSTAMP(i)
#define STAMP_STORE()
#define DIAG_ARG
#define DIAG_PASS(k)
#define DI __device__ __forceinline__
#define MFMA(a, b, c) __builtin_amdgcn_mfma_f32_32x32x16_bf16((a), (b), (c), 0, 0, 0)
#define LDSP(T) __attribute__((address_space(3))) T*

constexpr int S_ = 2048, D_ = 512, H_ = 32, F_ = 1792;
constexpr int NTT = S_ / 32;
constexpr int KSD = D_ / 16;
constexpr int KSF = F_ / 16;
constexpr float QSCALE = 0.25f * 1.4426950408889634f;
constexpr float LN_EPS = 1e-5f;

constexpr size_t KB = 1024;
constexpr size_t OFF_CNT = 0;
constexpr size_t OFF_WO = 192 * KB;
constexpr size_t OFF_WGU = OFF_WO + 512 * KB;
constexpr size_t OFF_WD = OFF_WGU + 3584 * KB;
constexpr size_t OFF_WQ = OFF_WD + 1792 * KB;
constexpr size_t OFF_CSP = OFF_WQ + 1536 * KB;
constexpr size_t OFF_ST3 = OFF_CSP + 324 * KB;
constexpr size_t OFF_X3 = OFF_ST3 + 256 * KB;
constexpr size_t OFF_L = OFF_X3 + 2048 * KB;
constexpr size_t OFF_Q = OFF_L;
constexpr size_t OFF_K = OFF_Q + 2048 * KB;
constexpr size_t OFF_V = OFF_K + 2048 * KB;
constexpr size_t OFF_O = OFF_V + 2048 * KB;
constexpr size_t OFF_X2 = OFF_O + 2048 * KB;
constexpr size_t OFF_ST2 = OFF_X2 + 2048 * KB;
constexpr size_t OFF_H = OFF_ST2 + 256 * KB;
constexpr size_t LSTRIDE = OFF_H + 7168 * KB - OFF_L;
static_assert(OFF_L + 2 * LSTRIDE <= (size_t)192 * 262144, "page-touch range");
constexpr int KMAIN_KB = 28;
constexpr int C_O = 0, C_X2 = 16, C_H = 48, C_X3 = 80, C_KV = 96, C_PREP = 112, C_H2 = 128, C_N = 160;

template <int I> struct IC { static constexpr int value = I; };
template <int B, int E, class F> DI void sfor(F&& f) { if constexpr (B < E) { f(IC<B>{}); sfor<B + 1, E>(f); } }
DI unsigned pk2(float lo, float hi) {
  f32x2 v = {lo, hi};
  bf16v2 r = __builtin_convertvector(v, bf16v2);
  return __builtin_bit_cast(unsigned, r);
}
DI float lo16(unsigned u) { return __uint_as_float(u << 16); }
DI float hi16(unsigned u) { return __uint_as_float(u & 0xffff0000u); }
DI float xhalf_add(float v) {
  auto r = __builtin_amdgcn_permlane32_swap(__float_as_uint(v), __float_as_uint(v), false, false);
  return __uint_as_float(r[0]) + __uint_as_float(r[1]);
}
DI float xhalf_max(float v) {
  auto r = __builtin_amdgcn_permlane32_swap(__float_as_uint(v), __float_as_uint(v), false, false);
  return fmaxf(__uint_as_float(r[0]), __uint_as_float(r[1]));
}
DI float x16_add(float v) {
  auto r = __builtin_amdgcn_permlane16_swap(__float_as_uint(v), __float_as_uint(v), false, false);
  return __uint_as_float(r[0]) + __uint_as_float(r[1]);
}
DI void st16(void* p, u32x4 v) { asm volatile("global_store_dwordx4 %0, %1, off sc1\n\ts_nop 1" :: "v"(p), "v"(v) : "memory"); }
template <int IMM>
DI void st16s(unsigned voff, const char* sbase, u32x4 v) {
  asm volatile("global_store_dwordx4 %0, %1, %2 offset:%3 sc1\n\ts_nop 1" :: "v"(voff), "v"(v), "s"(sbase), "i"(IMM) : "memory");
}
template <int IMM>
DI void gld16(bf16x8& d, unsigned voff, const char* sbase) {
  asm volatile("global_load_dwordx4 %0, %1, %2 offset:%3 sc1" : "=v"(d) : "v"(voff), "s"(sbase), "i"(IMM) : "memory");
}
#define GLDS_HEAD "s_mov_b32 %0, m0\n\ts_mov_b32 m0, %3\n\ts_nop 0\n\t"
#define GLDS_OP(o) "global_load_lds_dwordx4 %1, %2 offset:" #o " sc1\n\t"
#define GLDS_TAIL "s_mov_b32 m0, %0" : "=&s"(keep) : "v"(voff), "s"(sbase), "s"(lds) : "memory"
DI void glds1(unsigned voff, const char* sbase, unsigned lds) { unsigned keep; asm volatile(GLDS_HEAD GLDS_OP(0) GLDS_TAIL); }
DI void glds2(unsigned voff, const char* sbase, unsigned lds) { unsigned keep; asm volatile(GLDS_HEAD GLDS_OP(0) GLDS_OP(1024) GLDS_TAIL); }
DI void glds3(unsigned voff, const char* sbase, unsigned lds) { unsigned keep; asm volatile(GLDS_HEAD GLDS_OP(0) GLDS_OP(1024) GLDS_OP(2048) GLDS_TAIL); }
DI void glds4(unsigned voff, const char* sbase, unsigned lds) { unsigned keep; asm volatile(GLDS_HEAD GLDS_OP(0) GLDS_OP(1024) GLDS_OP(2048) GLDS_OP(3072) GLDS_TAIL); }
#define SBAR() __builtin_amdgcn_sched_barrier(0)
#define PIN(x) asm volatile("" : "+v"(x))
#define WAITV(n) asm volatile("s_waitcnt vmcnt(%0)" :: "i"(n) : "memory")
#define WAITBAR_V(n) asm volatile("s_waitcnt vmcnt(%0) lgkmcnt(0)\n\ts_barrier" :: "i"(n) : "memory")
#define WAITBAR_L() do { asm volatile("" ::: "memory"); __builtin_amdgcn_s_waitcnt(0xc07f); __builtin_amdgcn_s_barrier(); asm volatile("" ::: "memory"); } while (0)
typedef short s16x4 __attribute__((ext_vector_type(4)));
DI s16x4 trread(LDSP(const char) p) { return __builtin_amdgcn_ds_read_tr16_b64_v4i16((LDSP(s16x4))p); }
DI bf16x8 trfrag(LDSP(const char) p, int off2) { const s16x4 lo = trread(p), hi = trread(p + off2); return __builtin_shufflevector(lo, hi, 0, 1, 2, 3, 4, 5, 6, 7); }
DI bf16x8 ldfrag(LDSP(const char) p) { return *(LDSP(const bf16x8))p; }
DI float dot2(unsigned a, unsigned b, float c) { return __builtin_amdgcn_fdot2_f32_bf16(__builtin_bit_cast(bf16v2, a), __builtin_bit_cast(bf16v2, b), c, false); }
constexpr unsigned ONE2 = 0x3f803f80u;
#define BC8(x) __builtin_bit_cast(bf16x8, x)

template <int LOOK, int PER> DI void ring_wait(int i) {
  if (i <= 8 - LOOK) WAITBAR_V((LOOK - 1) * PER);
  else if (LOOK > 3 && i == 5) WAITBAR_V(2 * PER);
  else if (LOOK > 2 && i == 6) WAITBAR_V(PER);
  else WAITBAR_V(0);
}

DI void qk_store(const float (&v)[16], char* o) {
  u32x4 a = {pk2(v[0], v[1]), pk2(v[2], v[3]), pk2(v[4], v[5]), pk2(v[6], v[7])};
  u32x4 b = {pk2(v[8], v[9]), pk2(v[10], v[11]), pk2(v[12], v[13]), pk2(v[14], v[15])};
  st16(o, a);
  st16(o + NTT * 1024, b);
}
DI void v_store(const float (&v)[16], char* o) {
  auto s0 = __builtin_amdgcn_permlane32_swap(pk2(v[0], v[1]), pk2(v[4], v[5]), false, false);
  auto s1 = __builtin_amdgcn_permlane32_swap(pk2(v[2], v[3]), pk2(v[6], v[7]), false, false);
  auto s2 = __builtin_amdgcn_permlane32_swap(pk2(v[8], v[9]), pk2(v[12], v[13]), false, false);
  auto s3 = __builtin_amdgcn_permlane32_swap(pk2(v[10], v[11]), pk2(v[14], v[15]), false, false);
  const u32x4 a = {s0[0], s1[0], s0[1], s1[1]}, b = {s2[0], s3[0], s2[1], s3[1]};
  st16(o, a);
  st16(o + 512, b);
}
typedef __bf16 bfv8 __attribute__((ext_vector_type(8)));
typedef __bf16 bfv16 __attribute__((ext_vector_type(16)));
typedef float f32x8 __attribute__((ext_vector_type(8)));
template <int H> DI f32x16 smf(bf16x8 a, const f32x16& s, f32x16 c, int idx) {
  const f32x8 p = __builtin_shufflevector(s, s, 8 * H, 8 * H + 1, 8 * H + 2, 8 * H + 3, 8 * H + 4, 8 * H + 5, 8 * H + 6, 8 * H + 7);
  return __builtin_amdgcn_smfmac_f32_32x32x32_bf16(__builtin_bit_cast(bfv8, a), __builtin_bit_cast(bfv16, p), c, idx, 0, 0);
}
DI float row16_sum(float v) {
  v += __builtin_bit_cast(float, __builtin_amdgcn_update_dpp(0, __builtin_bit_cast(int, v), 0x128, 0xf, 0xf, false));
  v += __builtin_bit_cast(float, __builtin_amdgcn_update_dpp(0, __builtin_bit_cast(int, v), 0x124, 0xf, 0xf, false));
  v += __builtin_bit_cast(float, __builtin_amdgcn_update_dpp(0, __builtin_bit_cast(int, v), 0x122, 0xf, 0xf, false));
  v += __builtin_bit_cast(float, __builtin_amdgcn_update_dpp(0, __builtin_bit_cast(int, v), 0x121, 0xf, 0xf, false));
  return v;
}
#define KSTEP(B, u) do { SBAR(); RX((B) ^ 1, u); RW((B) ^ 1, 0, u); SBAR(); acc[0] = MFMA(wf[B][0], xf[B], acc[0]); SBAR(); RW((B) ^ 1, 1, u); RW((B) ^ 1, 2, u); SBAR(); \
                         acc[1] = MFMA(wf[B][1], xf[B], acc[1]); acc[2] = MFMA(xf[B], wf[B][2], acc[2]); SBAR(); } while (0)
#define QCHUNK() do { KSTEP(1, 0); KSTEP(0, 1); KSTEP(1, 2); KSTEP(0, 3); } while (0)
#define QINIT() \
  f32x16 acc[3]; bf16x8 wf[2][3], xf[2]; \
  _Pragma("unroll") for (int i = 0; i < 3; ++i) { acc[i] = f32x16{}; wf[1][i] = bf16x8{}; } \
  xf[1] = bf16x8{}
#define QFINISH() do { acc[0] = MFMA(wf[1][0], xf[1], acc[0]); acc[1] = MFMA(wf[1][1], xf[1], acc[1]); acc[2] = MFMA(xf[1], wf[1][2], acc[2]); } while (0)

extern "C" __global__ __launch_bounds__(768, 3) void k_qkv1(const char* __restrict__ x, const char* __restrict__ wn, char* __restrict__ ws, const char* __restrict__ out DIAG_ARG) {
  STAMP_DECL; STAMP(0);
  constexpr int LOOK = 3, CH = 24576, LXN = 4 * CH;
  __shared__ __attribute__((aligned(16))) char smem[LXN + 65536];
  const int tid = threadIdx.x, lane = tid & 63, r = lane & 31, hh = lane >> 5;
  const int w = __builtin_amdgcn_readfirstlane(tid >> 6);
  const int xcd = blockIdx.x & 7, idx = blockIdx.x >> 3;
  const int tb = (xcd & 3) * 8 + (idx & 7), nq = (xcd >> 2) * 4 + (idx >> 3);
  const unsigned lds0 = (unsigned)(uintptr_t)smem;
  const int rot = 2 * (idx & 3);
  if (w >= 8) {
    const int j = w - 8;
    const unsigned wv = (lane >> 3) * 3072 + (((lane & 7) ^ (((lane >> 4) & 1) << 2)) * 16);
    const char* wb = wn + (size_t)j * 16 * 3072 + nq * 128;
    if (blockIdx.x < 5) *(unsigned*)(ws + OFF_CNT + ((blockIdx.x * 4 + j) * 64 + lane) * 128) = 0u;
    asm volatile("s_sleep 8\n\ts_nop 4" ::: "memory");
#pragma unroll 1
    for (int c = 0; c < 8 + LOOK; ++c) {
      if (c >= LOOK) ring_wait<LOOK, 6>(c - LOOK);
      if (c < 8) {
        const char* ws_ = wb + (size_t)((c + rot) & 7) * 64 * 3072;
        const unsigned d = lds0 + (c & 3) * CH + j * 2048;
        glds1(wv, ws_, d); glds1(wv, ws_ + 1024, d + 8192); glds1(wv, ws_ + 2048, d + 16384);
        glds1(wv, ws_ + 8 * 3072, d + 1024); glds1(wv, ws_ + 8 * 3072 + 1024, d + 8192 + 1024); glds1(wv, ws_ + 8 * 3072 + 2048, d + 16384 + 1024);
      }
    }
    return;
  }
  {
    const int n = w;
    const u32x4* xr = (const u32x4*)(x + (size_t)(tb * 64 + n * 8) * 1024) + lane;
    u32x4 v[8];
#pragma unroll
    for (int i = 0; i < 8; ++i) v[i] = xr[i * 64];
    float s[8], q[8];
#pragma unroll
    for (int i = 0; i < 8; ++i) {
      s[i] = 0.f; q[i] = 0.f;
#pragma unroll
      for (int e = 0; e < 4; ++e) { s[i] = dot2(v[i][e], ONE2, s[i]); q[i] = dot2(v[i][e], v[i][e], q[i]); }
    }
    float s4[4], q4[4], s2[2], q2[2];
#pragma unroll
    for (int i = 0; i < 4; ++i) {
      const int lo = (i & 1) + 4 * (i >> 1);
      auto a = __builtin_amdgcn_permlane32_swap(__float_as_uint(s[lo]), __float_as_uint(s[lo + 2]), false, false);
      auto b = __builtin_amdgcn_permlane32_swap(__float_as_uint(q[lo]), __float_as_uint(q[lo + 2]), false, false);
      s4[i] = __uint_as_float(a[0]) + __uint_as_float(a[1]); q4[i] = __uint_as_float(b[0]) + __uint_as_float(b[1]);
    }
#pragma unroll
    for (int i = 0; i < 2; ++i) {
      auto a = __builtin_amdgcn_permlane16_swap(__float_as_uint(s4[2 * i]), __float_as_uint(s4[2 * i + 1]), false, false);
      auto b = __builtin_amdgcn_permlane16_swap(__float_as_uint(q4[2 * i]), __float_as_uint(q4[2 * i + 1]), false, false);
      s2[i] = row16_sum(__uint_as_float(a[0]) + __uint_as_float(a[1])); q2[i] = row16_sum(__uint_as_float(b[0]) + __uint_as_float(b[1]));
    }
    float ra[2], rb[2];
#pragma unroll
    for (int i = 0; i < 2; ++i) {
      const float mean = s2[i] * (1.f / D_);
      ra[i] = rsqrtf(fmaxf(q2[i] * (1.f / D_) - mean * mean, 0.f) + LN_EPS);
      rb[i] = -mean * ra[i];
    }
    LDSP(char) xd = (LDSP(char))smem + LXN + (lane >> 3) * 8192 + n * 1024;
#pragma unroll
    for (int i = 0; i < 8; ++i) {
      const float a = __builtin_bit_cast(float, __builtin_amdgcn_readlane(__builtin_bit_cast(int, ra[i >> 2]), (i & 3) * 16));
      const float b = __builtin_bit_cast(float, __builtin_amdgcn_readlane(__builtin_bit_cast(int, rb[i >> 2]), (i & 3) * 16));
      u32x4 o;
#pragma unroll
      for (int e = 0; e < 4; ++e) o[e] = pk2(lo16(v[i][e]) * a + b, hi16(v[i][e]) * a + b);
      *(LDSP(u32x4))(xd + i * 128 + (((lane & 7) ^ ((4 * n + (i >> 1)) & 7)) * 16)) = o;
    }
  }
  STAMP(1);
  WAITBAR_L();
  if (w >= 4) {
    if (w == 4 && idx < KMAIN_KB) {
      bf16x8 t;
      asm volatile("s_getpc_b64 s[20:21]\n\ts_add_u32 s20, s20, k_main@rel32@lo+4\n\ts_addc_u32 s21, s21, k_main@rel32@hi+12\n\t"
                   "global_load_dwordx4 %0, %1, s[20:21]\n\ts_waitcnt vmcnt(0)" : "=v"(t) : "v"(lane * 16 + idx * 1024) : "s20", "s21", "scc", "memory");
    }
    if (w == 5 && lane < 7) {
      const char* p = lane < 6 ? ws + (size_t)(idx * 6 + lane) * 262144 : out + idx * 65536;
      unsigned t;
      asm volatile("global_load_dword %0, %1, off\n\ts_waitcnt vmcnt(0)" : "=v"(t) : "v"(p) : "memory");
    }
    return;
  }
  STAMP(2);
  const int ta = w & 1, hp = w >> 1, q4 = (lane & 15) >> 2, xsw = (r >> 1) & 7;
  LDSP(const char) lw = (LDSP(const char))smem + (8 * hh + q4) * 128 + ((hp ^ (q4 >> 1)) * 64) + ((lane >> 4) & 1) * 32 + (lane & 3) * 8;
  LDSP(const char) lx = (LDSP(const char))smem + LXN + ta * 4096 + r * 128;
  int xo[4];
#pragma unroll
  for (int u = 0; u < 4; ++u) xo[u] = ((2 * u + hh) ^ xsw) * 16;
  QINIT();
#define RX(B, u) xf[B] = ldfrag(sx + xo[u])
#define RW(B, i, u) wf[B][i] = trfrag(sw + (i) * 8192 + (u) * 2048, 512)
#pragma unroll 1
  for (int c = 0; c < 8; ++c) {
    LDSP(const char) sw = lw + (c & 3) * CH;
    LDSP(const char) sx = lx + ((c + rot) & 7) * 8192;
    QCHUNK();
    WAITBAR_L();
  }
  QFINISH();
#undef RX
#undef RW
  STAMP(4);
  const int tt = tb * 2 + ta, nb = nq * 2 + hp;
  float v[16];
#pragma unroll
  for (int e = 0; e < 16; ++e) v[e] = acc[0][e] * QSCALE;
  qk_store(v, ws + OFF_Q + ((size_t)(2 * nb) * NTT + tt) * 1024 + lane * 16);
#pragma unroll
  for (int e = 0; e < 16; ++e) v[e] = acc[1][e];
  qk_store(v, ws + OFF_K + ((size_t)(2 * nb) * NTT + tt) * 1024 + lane * 16);
#pragma unroll
  for (int e = 0; e < 16; ++e) v[e] = acc[2][e];
  v_store(v, ws + OFF_V + ((size_t)(2 * nb + (r >> 4)) * NTT + tt) * 1024 + hh * 256 + (r & 15) * 16);
  STAMP(5); STAMP_STORE();
}

DI void poll(const char* cnt, unsigned voff, unsigned target) {
#pragma unroll 1
  for (unsigned spins = 0; spins < 200000u; ++spins) {
    unsigned v;
    asm volatile("global_load_dword %0, %1, %2 sc1\n\ts_waitcnt vmcnt(0)" : "=v"(v) : "v"(voff), "s"(cnt) : "memory");
    if (__all(v >= target)) break;
  }
}
DI void poll_b(const char* cnt, unsigned target, LDSP(unsigned) go, unsigned seq, int j) {
  if (j == 0) {
    poll(cnt, 0u, target);
    __hip_atomic_store(go, seq, __ATOMIC_RELAXED, __HIP_MEMORY_SCOPE_WORKGROUP);
  } else {
    WAITV(0);
#pragma unroll 1
    for (unsigned spins = 0; spins < 4000000u; ++spins) {
      if (__builtin_amdgcn_readfirstlane(__hip_atomic_load(go, __ATOMIC_RELAXED, __HIP_MEMORY_SCOPE_WORKGROUP)) >= seq) break;
    }
  }
  asm volatile("" ::: "memory");
}
DI void arrive4(LDSP(unsigned) lw, char* cnt, int lane) {
  WAITV(0);
  unsigned old = 0;
  if (lane == 0) old = __hip_atomic_fetch_add(lw, 1u, __ATOMIC_RELAXED, __HIP_MEMORY_SCOPE_WORKGROUP);
  old = __builtin_amdgcn_readfirstlane(old);
  if ((old & 3u) == 3u && lane < 8) asm volatile("global_atomic_add %0, %1, %2" :: "v"(lane * 128), "v"(1u), "s"(cnt) : "memory");
  asm volatile("" ::: "memory");
}
DI void stat_issue(f32x4 (&p)[4], const char* sb, unsigned voff) {
  asm volatile("global_load_dwordx4 %0, %4, %5 sc1\n\tglobal_load_dwordx4 %1, %4, %5 offset:1024 sc1\n\tglobal_load_dwordx4 %2, %4, %5 offset:2048 sc1\n\t"
               "global_load_dwordx4 %3, %4, %5 offset:3072 sc1" : "=&v"(p[0]), "=&v"(p[1]), "=&v"(p[2]), "=&v"(p[3]) : "v"(voff), "s"(sb) : "memory");
}
DI void stat_finish(f32x4 (&p)[4], float& mean, float& var) {
  asm volatile("" : "+v"(p[0]), "+v"(p[1]), "+v"(p[2]), "+v"(p[3]));
  const f32x4 t = (p[0] + p[1]) + (p[2] + p[3]);
  auto a = __builtin_amdgcn_permlane16_swap(__float_as_uint(t[0]), __float_as_uint(t[2]), false, false);
  auto b = __builtin_amdgcn_permlane16_swap(__float_as_uint(t[1]), __float_as_uint(t[3]), false, false);
  mean = xhalf_add(__uint_as_float(a[0]) + __uint_as_float(a[1])) * (1.f / D_);
  var = fmaxf(xhalf_add(__uint_as_float(b[0]) + __uint_as_float(b[1])) * (1.f / D_) - mean * mean, 0.f) + LN_EPS;
}
DI void colsum_part(const char* src, unsigned voff, LDSP(char) dst) {
  f32x4 p[4];
  asm volatile("global_load_dwordx4 %0, %4, %5 sc1\n\tglobal_load_dwordx4 %1, %4, %6 sc1\n\tglobal_load_dwordx4 %2, %4, %7 sc1\n\t"
               "global_load_dwordx4 %3, %4, %8 sc1\n\ts_waitcnt vmcnt(0)" : "=&v"(p[0]), "=&v"(p[1]), "=&v"(p[2]), "=&v"(p[3])
               : "v"(voff), "s"(src), "s"(src + 20480), "s"(src + 40960), "s"(src + 61440) : "memory");
  *(LDSP(f32x4))dst = (p[0] + p[1]) + (p[2] + p[3]);
}

#define RES_FINISH(WR, RD) do { \
    _Pragma("unroll") for (int i = 1; i < 4; ++i) _Pragma("unroll") for (int g = 0; g < 4; ++g) { \
      const f32x4 v_ = {acc[i][4 * g], acc[i][4 * g + 1], acc[i][4 * g + 2], acc[i][4 * g + 3]}; \
      *(LDSP(f32x4))(lb + (WR) + g * 1024) = v_; } \
    asm volatile("s_waitcnt vmcnt(0)" : "+v"(rn[0]), "+v"(rn[1]), "+v"(rn[2]), "+v"(rn[3]) :: "memory"); \
    WAITBAR_L(); \
    _Pragma("unroll") for (int g = 0; g < 4; ++g) { \
      f32x4 t_ = {acc[0][4 * g], acc[0][4 * g + 1], acc[0][4 * g + 2], acc[0][4 * g + 3]}; \
      f32x4 u_[3]; \
      _Pragma("unroll") for (int i = 1; i < 4; ++i) u_[i - 1] = *(LDSP(const f32x4))(lb + (RD) + g * 1024); \
      t_ = (u_[0] + u_[1]) + (u_[2] + t_); \
      res[2 * g] = pk2(t_[0] + lo16(rn[g][0]), t_[1] + hi16(rn[g][0])); \
      res[2 * g + 1] = pk2(t_[2] + lo16(rn[g][1]), t_[3] + hi16(rn[g][1])); } } while (0)
DI void res_load(u32x2 (&rn)[4], unsigned vo, const char* p0, int d1, int d2) {
  asm volatile("global_load_dwordx2 %0, %4, %5 sc1\n\tglobal_load_dwordx2 %1, %4, %6 sc1\n\tglobal_load_dwordx2 %2, %4, %7 sc1\n\tglobal_load_dwordx2 %3, %4, %8 sc1"
               : "=&v"(rn[0]), "=&v"(rn[1]), "=&v"(rn[2]), "=&v"(rn[3]) : "v"(vo), "s"(p0), "s"(p0 + d1), "s"(p0 + d2), "s"(p0 + d2 + d1) : "memory");
}
DI void res_store_tiled(const unsigned (&res)[8], char* o, char* st, int lane) {
  const u32x4 p0 = {res[0], res[1], res[2], res[3]}, p1 = {res[4], res[5], res[6], res[7]};
  st16(o, p0);
  st16(o + 1024, p1);
  float s1 = 0.f, s2 = 0.f, t1 = 0.f, t2 = 0.f;
#pragma unroll
  for (int e = 0; e < 4; ++e) { s1 = dot2(res[e], ONE2, s1); s2 = dot2(res[e], res[e], s2); t1 = dot2(res[4 + e], ONE2, t1); t2 = dot2(res[4 + e], res[4 + e], t2); }
  s1 = xhalf_add(s1 + t1); s2 = xhalf_add(s2 + t2);
  if (lane < 32) { const f32x2 v = {s1, s2}; asm volatile("global_store_dwordx2 %0, %1, off sc1" :: "v"(st), "v"(v) : "memory"); }
}

DI int hsrc(int p) { const int g = p / 28, q = p - g * 28; return g * 28 + (q < 16 ? (q >> 2) * 7 + (q & 3) : ((q - 16) / 3) * 7 + 4 + (q - 16) % 3); }
DI const char* karg(int i) {
  const __attribute__((address_space(4))) unsigned long long* ka = (const __attribute__((address_space(4))) unsigned long long*)__builtin_amdgcn_kernarg_segment_ptr();
  asm volatile("" : "+s"(ka));
  return (const char*)ka[i];
}
extern "C" __global__ __launch_bounds__(512, 2) void k_main(char* __restrict__ ws_, const char* x_, const char* wq_, const char* wo_, const char* wg_,
                                                            const char* wu_, const char* wd_, char* out_ DIAG_ARG) {
  constexpr int L_K = 0, L_V = 65536, L_ONE = 131072, L_CMB = 133120;
  constexpr int GS = 32768, L_RED = 3 * GS;
  constexpr int L_D0 = 3 * GS, L_D1 = 2 * GS, L_D2 = 0;
  constexpr int QW = 12288, L_QX = 49152;
  constexpr int L_CS = 157696, L_ST = L_CS + 4096, L_CNT = L_ST + 1024, L_DBG = L_CNT + 64;
  __shared__ __attribute__((aligned(16))) char smem[163840];
  const int w_ = __builtin_amdgcn_readfirstlane(threadIdx.x >> 6);
  const unsigned lds0 = (unsigned)(uintptr_t)smem;
#define FRESH() \
  int tid; asm volatile("v_mbcnt_lo_u32_b32 %0, -1, 0\n\tv_mbcnt_hi_u32_b32 %0, -1, %0" : "=v"(tid)); int bid = blockIdx.x, w = w_; char* ws = ws_; asm volatile("" : "+s"(bid), "+s"(w), "+s"(ws)); \
  tid += w * 64; const int j = w & 3; \
  const int lane = tid & 63, r = lane & 31, hh = lane >> 5, xcd = bid & 7, idx = bid >> 3, grp = xcd * 2 + (idx >> 4), m = idx & 15; \
  const unsigned voff = lane * 16; LDSP(char) lb = (LDSP(char))smem + lane * 16; char* cnt = ws + OFF_CNT; char* wl = ws + layer * LSTRIDE; \
  LDSP(unsigned) lcnt = (LDSP(unsigned))((LDSP(char))smem + L_CNT) + (w >> 2); \
  const int ont = (m >> 1) * 2 + (j >> 1), ott = grp * 4 + (m & 1) * 2 + (j & 1); \
  const int gtb = (xcd & 1) * 8 + (idx & 7), gnb = (xcd >> 1) * 4 + (idx >> 3), qtb = (xcd & 3) * 4 + (idx & 3), qnb = (xcd >> 2) * 8 + (idx >> 2); \
  (void)r; (void)hh; (void)voff; (void)lb; (void)cnt; (void)wl; (void)lcnt; (void)ont; (void)ott; (void)gtb; (void)gnb; (void)qtb; (void)qnb
  if (threadIdx.x < 8) ((LDSP(unsigned))((LDSP(char))smem + L_CNT))[threadIdx.x] = 0u;
#pragma unroll 1
  for (int layer = 0; layer < 2; ++layer) {
    {
      FRESH();
      const int head = xcd * 4 + (idx & 3), qb = idx >> 2, half = w >> 2, tt0 = qb * 8 + 2 * j;
      PSTAMP(0);
      if (layer) { if (w == 4) poll(cnt + (C_KV + (head >> 1)) * 1024 + qb * 128, 0u, 16u); WAITBAR_L(); }
      PSTAMP(1);
      const char* ks = wl + OFF_K + (size_t)head * 65536 + half * 32768;
      const unsigned kd = lds0 + L_K + half * 32768;
      bf16x8 q0, q1;
      gld16<0>(q0, voff, wl + OFF_Q + ((size_t)head * NTT + tt0) * 1024);
      gld16<1024>(q1, voff, wl + OFF_Q + ((size_t)head * NTT + tt0) * 1024);
      glds2(voff, ks + j * 2048, kd + j * 2048);
      glds2(voff, ks + (OFF_V - OFF_K) + j * 2048, kd + L_V + j * 2048);
      *(LDSP(unsigned))((LDSP(char))smem + L_ONE + tid * 4) = ((tid >> 2) & 11) == 0 ? ONE2 : 0u;
      asm volatile("s_waitcnt vmcnt(0) lgkmcnt(0)\n\ts_barrier" : "+v"(q0), "+v"(q1) :: "memory");
      PSTAMP(2);
      LDSP(const char) kp = lb + L_K;
      LDSP(const char) vp0 = (LDSP(const char))smem + (r < 16 ? L_V : L_ONE) + hh * 256 + (r & 15) * 16;
      const int vstep = r < 16 ? 2048 : 0;
      LDSP(const char) kq = kp + half * 32768; LDSP(const char) vq = vp0 + half * 16 * vstep;
      f32x16 n0, n1, sA, sB = f32x16{}, o0 = f32x16{}, o1 = f32x16{};
      bf16x8 kA, kB, vp = bf16x8{};
      int sidx = (int)0xDDDDDDDDu; PIN(sidx);
      {
        const bf16x8 k0 = ldfrag(kp);
        kA = ldfrag(kq);
        const f32x16 s = MFMA(k0, q0, f32x16{}), t = MFMA(k0, q1, f32x16{});
        float a = fmaxf(fmaxf(s[0], s[1]), s[2]), b = fmaxf(fmaxf(t[0], t[1]), t[2]);
#pragma unroll
        for (int i = 3; i < 15; i += 2) { a = fmaxf(fmaxf(a, s[i]), s[i + 1]); b = fmaxf(fmaxf(b, t[i]), t[i + 1]); }
        a = xhalf_max(fmaxf(a, s[15])); b = xhalf_max(fmaxf(b, t[15]));
#pragma unroll
        for (int i = 0; i < 16; ++i) { n0[i] = -a; n1[i] = -b; }
        PIN(n0); PIN(n1);
        sA = MFMA(kA, q0, n0);
      }
#define EX(S, i) S[i] = __builtin_amdgcn_exp2f(S[i])
#define STEPN(CUR, NXT, KN_, QN, NN, O, VA, OP, VP, READS) do { \
      EX(CUR, 0); EX(CUR, 1); PIN(CUR); SBAR(); OP = smf<1>(VP, NXT, OP, sidx); SBAR(); READS; SBAR(); \
      EX(CUR, 2); EX(CUR, 3); EX(CUR, 4); EX(CUR, 5); PIN(CUR); SBAR(); NXT = MFMA(KN_, QN, NN); SBAR(); \
      EX(CUR, 6); EX(CUR, 7); EX(CUR, 8); EX(CUR, 9); EX(CUR, 10); PIN(CUR); SBAR(); O = smf<0>(VA, CUR, O, sidx); SBAR(); \
      EX(CUR, 11); EX(CUR, 12); EX(CUR, 13); EX(CUR, 14); EX(CUR, 15); PIN(CUR); SBAR(); } while (0)
#define TILE(t, KA, KB) do { bf16x8 va, vb; \
      STEPN(sA, sB, KA, q1, n1, o0, va, o1, vp, (va = ldfrag(vq + (t) * 1024), vb = ldfrag(vq + (t) * 1024 + 512), KB = ldfrag(kq + ((t) + 1) * 1024))); \
      STEPN(sB, sA, KB, q0, n0, o1, va, o0, vb, (vp = vb)); } while (0)
      __builtin_amdgcn_s_setprio(3);
#pragma unroll 1
      for (int ph = -2; ph < 5; ++ph) {
        if (ph == -1) {
          glds3(voff, ks + 8192 + j * 6144, kd + 8192 + j * 6144);
          glds3(voff, ks + (OFF_V - OFF_K) + 8192 + j * 6144, kd + L_V + 8192 + j * 6144);
          glds3(voff, ks + 11264 + j * 6144, kd + 11264 + j * 6144);
          glds3(voff, ks + (OFF_V - OFF_K) + 11264 + j * 6144, kd + L_V + 11264 + j * 6144);
        }
        if (ph == 0) {
          WAITV(0);
          if (lane == 0) __hip_atomic_fetch_add(lcnt + 4, 1u, __ATOMIC_RELAXED, __HIP_MEMORY_SCOPE_WORKGROUP);
#pragma unroll 1
          for (unsigned spins = 0; spins < 4000000u; ++spins)
            if (__builtin_amdgcn_readfirstlane(__hip_atomic_load(lcnt + 4, __ATOMIC_RELAXED, __HIP_MEMORY_SCOPE_WORKGROUP)) >= 4u * (layer + 1)) break;
          asm volatile("" ::: "memory");
        }
        if (ph == 1) __builtin_amdgcn_s_setprio(2);
        if (ph == 3) __builtin_amdgcn_s_setprio(1);
        if (ph == 4) __builtin_amdgcn_s_setprio(0);
        if (layer == 0 && ph >= 0) {
          const int q4 = (lane & 15) >> 2, b16 = (lane >> 4) & 1;
          const int scr = L_CMB + (half ? j * 5120 : 20480 + j * 2048);
          const int jo = ((ph - 1) * 8 + w) * 256 + bid;
          if ((ph == 1 || ph == 2) && jo < 3712) {
            const bool gu = jo < 1792;
            LDSP(const char) sp = (LDSP(const char))smem + scr + (lane & 3) * 8 +
                                  (gu ? (16 * hh + 4 * b16 + q4) * 32 : (4 * hh + q4) * 64 + b16 * 32);
            const int ho = gu ? 256 : 512;
            const char* pd;
            if (gu) { const int ft = jo >> 4, S = jo & 15, nb = ft / 7, ii = ft - nb * 7; pd = ws + OFF_WGU + (size_t)(nb * 224 + (ii < 4 ? S * 8 + 2 * ii : 128 + S * 6 + 2 * (ii - 4))) * 1024; }
            else if (jo < 2048) pd = ws + OFF_WO + (size_t)(jo - 1792) * 2048;
            else if (jo < 2944) pd = ws + OFF_WD + (size_t)(jo - 2048) * 2048;
            else { const int t = (jo - 2944) >> 4, nb = t / 3, ii = t - nb * 3, s0 = (jo & 15) * 2; pd = ws + OFF_WQ + (size_t)(((nb * 8 + (s0 >> 2)) * 3 + ii) * 4 + (s0 & 3)) * 1024; }
            WAITV(0);
            const u32x4 fa = __builtin_bit_cast(u32x4, trfrag(sp, ho)), fb = __builtin_bit_cast(u32x4, trfrag(sp + 1024, ho));
            st16s<0>(voff, pd, fa); st16s<1024>(voff, pd, fb);
            if (gu || jo >= 2944) {
              float ca = (lo16(fa[0]) + hi16(fa[0])) + (lo16(fa[1]) + hi16(fa[1])) + (lo16(fa[2]) + hi16(fa[2])) + (lo16(fa[3]) + hi16(fa[3]));
              float cb = (lo16(fb[0]) + hi16(fb[0])) + (lo16(fb[1]) + hi16(fb[1])) + (lo16(fb[2]) + hi16(fb[2])) + (lo16(fb[3]) + hi16(fb[3]));
              if (gu) { ca = xhalf_add(x16_add(ca)); cb = xhalf_add(x16_add(cb)); ca = lane < 16 ? ca : cb; }
              else ca = xhalf_add(ca + cb);
              const char* cp = ws + OFF_CSP + ((jo & 15) * 5120 + ((gu ? jo : jo - 2944 + 1792) >> 4) * 32) * 4;
              if (lane < 32) asm volatile("global_store_dword %0, %1, %2 sc1" :: "v"(lane * 4), "v"(ca), "s"(cp) : "memory");
            }
          }
          const int jn = jo + 2048;
          if (ph < 2 && jn < 3712) {
            const char *sa, *sb; unsigned gv;
            if (jn < 1792) {
              const int o = 32 * (jn & 15) * (F_ * 2) + (jn >> 4) * 32;
              gv = (lane >> 1) * (F_ * 2) + (lane & 1) * 16; sa = karg(4) + o; sb = karg(5) + o;
            } else {
              int nt, s0, s1, ld = 1024, ai;
              if (jn < 2048) { const int f = (jn - 1792) * 2; nt = f >> 5; s0 = f & 31; s1 = s0 + 1; ai = 3; }
              else if (jn < 2944) { const int f = (jn - 2048) * 2; nt = f / KSF; s0 = hsrc(f - nt * KSF); s1 = hsrc(f - nt * KSF + 1); ai = 6; }
              else { const int t = (jn - 2944) >> 4, nb = t / 3; s0 = (jn & 15) * 2; s1 = s0 + 1; nt = (t - nb * 3) * 16 + nb; ai = 2; ld = 3072; }
              gv = (lane >> 2) * ld + (lane & 3) * 16;
              const char* src = karg(ai) + nt * 64;
              sa = src + 16 * s0 * ld; sb = src + 16 * s1 * ld;
            }
            __builtin_amdgcn_s_waitcnt(0xc07f);
            glds1(gv, sa, lds0 + scr); glds1(gv, sb, lds0 + scr + 1024);
          }
          if (ph == 3) arrive4(lcnt, cnt + (C_PREP + xcd) * 1024, lane);
          if (ph == 3 && w == 1 && idx < KMAIN_KB) {
            unsigned lo, hi;
            asm volatile("s_getpc_b64 vcc\n\ts_add_u32 %0, vcc_lo, k_main@rel32@lo+4\n\ts_addc_u32 %1, vcc_hi, k_main@rel32@hi+12" : "=s"(lo), "=s"(hi) :: "vcc", "scc");
            glds1(voff, (const char*)(((unsigned long long)hi << 32) | lo) + idx * 1024, lds0 + scr);
          }
        }
        int n = ph <= 0 ? ph + 3 : ph == 2 ? 2 : ph == 4 ? 2 : 3;
#pragma unroll 1
        do { TILE(0, kA, kB); TILE(1, kB, kA); kq += 2048; vq += vstep; } while (--n > 0);
      }
      o1 = smf<1>(vp, sB, o1, sidx);
#undef TILE
#undef STEPN
      PSTAMP(3);
      LDSP(char) xp = lb + L_CMB + j * 5120;
      if (half == 1) {
        const f32x4 a = {o0[0], o0[1], o0[2], o0[3]}, b = {o0[4], o0[5], o0[6], o0[7]}, c = {o1[0], o1[1], o1[2], o1[3]}, d = {o1[4], o1[5], o1[6], o1[7]};
        const f32x2 l = {o0[8], o1[8]};
        *(LDSP(f32x4))xp = a; *(LDSP(f32x4))(xp + 1024) = b; *(LDSP(f32x4))(xp + 2048) = c; *(LDSP(f32x4))(xp + 3072) = d; *(LDSP(f32x2))(xp + 4096) = l;
      }
      if (layer == 0 && w == 0) poll(cnt + C_PREP * 1024 + (idx & 7) * 128, (lane & 7) * 1024, 64u);
      WAITBAR_L();
      if (half == 0) {
        __builtin_amdgcn_s_setprio(3);
        PSTAMP(11);
        const f32x4 a = *(LDSP(const f32x4))xp, b = *(LDSP(const f32x4))(xp + 1024), c = *(LDSP(const f32x4))(xp + 2048), d = *(LDSP(const f32x4))(xp + 3072);
        const f32x2 l = *(LDSP(const f32x2))(xp + 4096);
#pragma unroll
        for (int i = 0; i < 4; ++i) { o0[i] += a[i]; o0[4 + i] += b[i]; o1[i] += c[i]; o1[4 + i] += d[i]; }
        o0[8] += l[0]; o1[8] += l[1];
        float chk = (o0[8] + o1[8]) * 5.4210109e-20f;
#pragma unroll
        for (int i = 0; i < 8; ++i) chk = __builtin_fmaf(o0[i], 0.f, __builtin_fmaf(o1[i], 0.f, chk));
        const bool bad = !(chk < 1.f);
        if (__builtin_expect(__any(bad), 0)) {
#pragma unroll 1
          for (int z = 0; z < 2; ++z) {
            const bf16x8 q = z ? q1 : q0;
            float mx = -INFINITY;
            f32x16 o = f32x16{};
            kq = kp; vq = vp0;
#pragma unroll 1
            for (int t = 0; t < 64; ++t) {
              const bf16x8 k = ldfrag(kq), v0 = ldfrag(vq), v1 = ldfrag(vq + 512);
              f32x16 s = MFMA(k, q, f32x16{});
              float tm = s[0];
#pragma unroll
              for (int i = 1; i < 16; ++i) tm = fmaxf(tm, s[i]);
              tm = xhalf_max(tm);
              const float mn = fmaxf(mx, tm);
              const float alpha = __builtin_amdgcn_exp2f(mx - mn);
              mx = mn;
#pragma unroll
              for (int i = 0; i < 16; ++i) { o[i] *= alpha; s[i] = __builtin_amdgcn_exp2f(s[i] - mn); }
              o = smf<0>(v0, s, o, sidx);
              o = smf<1>(v1, s, o, sidx);
              kq += 1024; vq += vstep >> 1;
            }
            if (z) o1 = o; else o0 = o;
          }
        }
        const float i0 = __builtin_amdgcn_rcpf(o0[8]), i1 = __builtin_amdgcn_rcpf(o1[8]);
        const u32x4 a0 = {pk2(o0[0] * i0, o0[1] * i0), pk2(o0[2] * i0, o0[3] * i0), pk2(o0[4] * i0, o0[5] * i0), pk2(o0[6] * i0, o0[7] * i0)};
        const u32x4 a1 = {pk2(o1[0] * i1, o1[1] * i1), pk2(o1[2] * i1, o1[3] * i1), pk2(o1[4] * i1, o1[5] * i1), pk2(o1[6] * i1, o1[7] * i1)};
        char* op = wl + OFF_O + ((size_t)tt0 * KSD + head) * 1024 + voff;
        st16(op, a0); st16(op + 32768, a1);
        PSTAMP(12);
        WAITBAR_L();
        arrive4(lcnt, cnt + (C_O + layer * 8 + qb) * 1024, lane);
      }
    }
    PSTAMP(4);
    int role = w_ >> 2;
    asm volatile("" : "+s"(role));
    if (role == 0) {
      {
        FRESH();
        const char* wb = ws + OFF_WGU + (size_t)gnb * 229376 + j * 8192;
#pragma unroll 1
        for (int c = 0; c < 4; ++c) glds4(voff, wb + (c >> 1) * GS + (c & 1) * 4096, lds0 + (c >> 1) * GS + (c & 1) * 4096 + j * 8192);
        colsum_part(ws + OFF_CSP + (size_t)j * 81920 + gnb * 896, voff, lb + L_CS + j * 1024);
      }
      PSTAMP(14);
      WAITBAR_L();
      PSTAMP(15);
      {
        FRESH();
        const int g4 = lane >> 4, c16 = lane & 15;
        const char* gx = wl + OFF_X2 + (size_t)(gtb * 4 + j) * 32768;
        unsigned xo = (g4 >> 1) * 1024 + ((g4 & 1) * 32 + c16) * 16;
        const char* ob = wl + OFF_H + ((size_t)(gtb * 4 + j) * KSF + (gnb >> 2) * 28) * 1024;
        unsigned oo = (((g4 & 1) * 32) + (g4 >> 1) * 16 + c16) * 16;
        asm volatile("" : "+s"(gx), "+s"(ob)); PIN(xo); PIN(oo);
        bf16x8 xr[16][2], wf[8];
        f32x4 ac[2][8];
#pragma unroll 1
        for (unsigned spins = 0; spins < 4000000u; ++spins)
          if (__builtin_amdgcn_readfirstlane(__hip_atomic_load(lcnt + 2, __ATOMIC_RELAXED, __HIP_MEMORY_SCOPE_WORKGROUP)) >= (unsigned)(layer * 8 + 2)) break; else __builtin_amdgcn_s_sleep(1);
        asm volatile("" ::: "memory");
#define LDXS(S) do { gld16<((S) & 1) * 2048>(xr[S][0], xo, gx + ((S) >> 1) * 4096); gld16<((S) & 1) * 2048 + 256>(xr[S][1], xo, gx + ((S) >> 1) * 4096); } while (0)
        sfor<0, 8>([&](auto ic) { LDXS(decltype(ic)::value); });
        PSTAMP(5);
        auto pass = [&](auto nt_, auto st0_) {
          constexpr int NT = decltype(nt_)::value, ST0 = decltype(st0_)::value, PD = 6, NF = 16 * NT;
          sfor<0, NF + PD>([&](auto ic) {
            constexpr int f = decltype(ic)::value, g = f - PD;
            if constexpr (g >= 0) {
              constexpr int S = g / NT, i = g % NT;
              if constexpr (ST0 == 0 && i == 0) {
                constexpr int last = 4 * (S / 4) + 7 > 15 ? 15 : 4 * (S / 4) + 7;
                asm volatile("s_waitcnt vmcnt(%2)" : "+v"(xr[S][0]), "+v"(xr[S][1]) : "i"(2 * (last - S)) : "memory");
              }
              SBAR();
              if constexpr (S == 0) ac[0][i] = __builtin_amdgcn_mfma_f32_16x16x32_bf16(wf[g & 7], xr[S][0], f32x4{}, 0, 0, 0);
              else ac[0][i] = __builtin_amdgcn_mfma_f32_16x16x32_bf16(wf[g & 7], xr[S][0], ac[0][i], 0, 0, 0);
              SBAR();
            }
            if constexpr (f < NF) {
              if constexpr (f % (4 * NT) == 0 && (ST0 + f / (4 * NT)) > 0) WAITBAR_L();
              wf[f & 7] = ldfrag(lb + ((ST0 + f / (4 * NT)) % 3) * GS + (f % (4 * NT)) * 1024);
            }
            if constexpr (g >= 0) {
              constexpr int S = g / NT, i = g % NT;
              SBAR();
              if constexpr (S == 0) ac[1][i] = __builtin_amdgcn_mfma_f32_16x16x32_bf16(wf[g & 7], xr[S][1], f32x4{}, 0, 0, 0);
              else ac[1][i] = __builtin_amdgcn_mfma_f32_16x16x32_bf16(wf[g & 7], xr[S][1], ac[1][i], 0, 0, 0);
              SBAR();
              if constexpr (ST0 == 0 && i == NT - 1 && S % 4 == 3 && S < 8) sfor<S + 5, S + 9>([&](auto jc) { LDXS(decltype(jc)::value); });
              if constexpr (ST0 == 4 && g == 4 * NT) arrive4(lcnt, cnt + (C_H + layer * 16 + gtb) * 1024, lane);
            }
          });
        };
        auto epilogue = [&](auto nt_, auto t0_, const char* o) {
          constexpr int NH = decltype(nt_)::value, T0 = decltype(t0_)::value;
          LDSP(const float) sx = (LDSP(const float))((LDSP(char))smem + L_ST) + j * 64 + c16;
          const float rs0 = sx[0], rs1 = sx[16], m0 = -sx[32], m1 = -sx[48];
          f32x4 c4[2 * NH];
#pragma unroll
          for (int i = 0; i < 2 * NH; ++i) c4[i] = *(LDSP(const f32x4))((LDSP(const char))smem + L_CS + (2 * T0 + i) * 64 + g4 * 16);
          const f32x4 vm0 = {m0, m0, m0, m0}, vm1 = {m1, m1, m1, m1};
          sfor<0, NH>([&](auto ic) {
            constexpr int i = decltype(ic)::value;
            const f32x4 ga = __builtin_elementwise_fma(c4[2 * i], vm0, ac[0][2 * i]), ua = __builtin_elementwise_fma(c4[2 * i + 1], vm0, ac[0][2 * i + 1]) * rs0;
            const f32x4 gb = __builtin_elementwise_fma(c4[2 * i], vm1, ac[1][2 * i]), ub = __builtin_elementwise_fma(c4[2 * i + 1], vm1, ac[1][2 * i + 1]) * rs1;
            const f32x4 h0 = __builtin_elementwise_max(ga, f32x4{}) * ua, h1 = __builtin_elementwise_max(gb, f32x4{}) * ub;
            auto e0 = __builtin_amdgcn_permlane32_swap(pk2(h0[0], h0[1]), pk2(h1[0], h1[1]), false, false);
            auto e1 = __builtin_amdgcn_permlane32_swap(pk2(h0[2], h0[3]), pk2(h1[2], h1[3]), false, false);
            const u32x4 a_ = {e0[0], e1[0], e0[1], e1[1]};
            st16s<i * 1024>(oo, o, a_);
          });
        };
        pass(IC<8>{}, IC<0>{});
        epilogue(IC<4>{}, IC<0>{}, ob + (gnb & 3) * 4096);
        pass(IC<6>{}, IC<4>{});
        PSTAMP(6);
        epilogue(IC<3>{}, IC<4>{}, ob + 16384 + (gnb & 3) * 3072);
#undef LDXS
        PSTAMP(13);
        arrive4(lcnt, cnt + (C_H2 + layer * 16 + gtb) * 1024, lane);
        PSTAMP(7);
      }
      WAITBAR_L();
      if (layer == 0) {
        FRESH();
        __builtin_amdgcn_s_barrier();
        { const char* wb = ws + OFF_WQ + (size_t)qnb * 8 * QW + j * 3072;
#pragma unroll 1
          for (int c = 0; c < 4; ++c) glds3(voff, wb + c * QW, lds0 + c * QW + j * 3072); }
        colsum_part(ws + OFF_CSP + (size_t)j * 81920 + (3584 + qnb * 96) * 4, voff, lb + L_CS + j * 1024);
        QINIT();
#define RX(B, u) xf[B] = ldfrag(sxp + (u) * 1024)
#define RW(B, i, u) wf[B][i] = ldfrag(swp + (i) * 4096 + (u) * 1024)
        WAITBAR_L();
        PSTAMP(8);
#pragma unroll 1
        for (int c = 0; c < 8; ++c) {
          LDSP(const char) swp = lb + (c & 3) * QW;
          LDSP(const char) sxp = lb + L_QX + (c & 3) * 16384 + j * 4096;
          QCHUNK();
          WAITBAR_L();
        }
        LDSP(const float) cx = (LDSP(const float))((LDSP(char))smem + L_CS);
        LDSP(const float) sx = (LDSP(const float))((LDSP(char))smem + L_ST) + j * 64;
        f32x4 cq[8], ra[4], rb[4];
        const float rstd = sx[r], mr = sx[32 + r], cv = cx[64 + r];
#pragma unroll
        for (int g = 0; g < 8; ++g) cq[g] = *(LDSP(const f32x4))(cx + 8 * g + 4 * hh);
#pragma unroll
        for (int g = 0; g < 4; ++g) { ra[g] = *(LDSP(const f32x4))(sx + 8 * g + 4 * hh); rb[g] = *(LDSP(const f32x4))(sx + 32 + 8 * g + 4 * hh); }
        SBAR();
        QFINISH();
        SBAR();
#undef RX
#undef RW
        PSTAMP(9);
        char* wn = ws + LSTRIDE;
        const int tt = qtb * 4 + j;
        float v[16];
        const float rq = rstd * QSCALE, mq = mr * QSCALE;
#pragma unroll
        for (int e = 0; e < 16; ++e) v[e] = acc[0][e] * rq - mq * cq[e >> 2][e & 3];
        qk_store(v, wn + OFF_Q + ((size_t)(2 * qnb) * NTT + tt) * 1024 + voff);
#pragma unroll
        for (int e = 0; e < 16; ++e) v[e] = acc[1][e] * rstd - mr * cq[4 + (e >> 2)][e & 3];
        qk_store(v, wn + OFF_K + ((size_t)(2 * qnb) * NTT + tt) * 1024 + voff);
#pragma unroll
        for (int e = 0; e < 16; ++e) v[e] = acc[2][e] * ra[e >> 2][e & 3] - rb[e >> 2][e & 3] * cv;
        v_store(v, wn + OFF_V + ((size_t)(2 * qnb + (r >> 4)) * NTT + tt) * 1024 + hh * 256 + (r & 15) * 16);
        arrive4(lcnt, cnt + (C_KV + qnb) * 1024, lane);
        PSTAMP(10);
      }
    } else {
      {
        FRESH();
        bf16x8 w0[8], w1[8], x0[8], x1[8];
        const char* wa = ws + OFF_WO + ((size_t)ont * KSD + j * 8) * 1024;
        const char* wb = ws + OFF_WO + ((size_t)(ont ^ 1) * KSD + j * 8) * 1024;
#define LD4(D, o, base) do { gld16<0>(D[o], voff, base); gld16<1024>(D[(o) + 1], voff, base); gld16<2048>(D[(o) + 2], voff, base); gld16<3072>(D[(o) + 3], voff, base); } while (0)
        __builtin_amdgcn_s_barrier();
        LD4(w0, 0, wa); LD4(w0, 4, wa + 4096); LD4(w1, 0, wb); LD4(w1, 4, wb + 4096);
        u32x2 rn[4]; unsigned res[8];
        res_load(rn, layer ? voff : r * 1024 + hh * 8, (layer ? ws + OFF_X3 + ont * 2048 : karg(1) + ont * 64) + (size_t)ott * 32768, layer ? 8 : 16, layer ? 1024 : 32);
        const char* xa = wl + OFF_O + ((size_t)ott * KSD + j * 8) * 1024;
        const char* xb = wl + OFF_O + ((size_t)(ott ^ 1) * KSD + j * 8) * 1024;
        asm volatile("" : "+s"(xa), "+s"(xb));
        poll_b(cnt + (C_O + layer * 8 + xcd) * 1024 + (idx & 7) * 128, 32u, lcnt + 1, layer * 8 + 1, j);
        PSTAMP(5);
        #define LD2(s) do { gld16<((s) & 3) * 1024>(x0[s], voff, xa + ((s) >> 2) * 4096); gld16<((s) & 3) * 1024>(x1[s], voff, xb + ((s) >> 2) * 4096); } while (0)
        LD2(0); LD2(1); LD2(2); LD2(3); LD2(4); LD2(5); LD2(6); LD2(7);
#undef LD2
        f32x16 acc[4];
#pragma unroll
        for (int i = 0; i < 4; ++i) acc[i] = f32x16{};
#define MM4(W0, W1, X0, X1) do { acc[1] = MFMA(W0, X1, acc[1]); acc[2] = MFMA(W1, X0, acc[2]); acc[3] = MFMA(W1, X1, acc[3]); acc[0] = MFMA(W0, X0, acc[0]); } while (0)
#define PIN8(n, X, Y, o) asm volatile("s_waitcnt vmcnt(%8)" : "+v"(X[o]), "+v"(X[(o) + 1]), "+v"(X[(o) + 2]), "+v"(X[(o) + 3]), "+v"(Y[o]), "+v"(Y[(o) + 1]), "+v"(Y[(o) + 2]), "+v"(Y[(o) + 3]) : "i"(n) : "memory")
        PIN8(16, w0, w1, 0); PIN8(16, w0, w1, 4);
#define WX(b, n) asm volatile("s_waitcnt vmcnt(%2)" : "+v"(x0[b]), "+v"(x1[b]) : "i"(n) : "memory")
#define ST(s) do { WX(s, 14 - 2 * (s)); MM4(w0[s], w1[s], x0[s], x1[s]); SBAR(); } while (0)
        ST(0); ST(1); ST(2); ST(3); ST(4); ST(5); ST(6); ST(7);
#undef ST
#undef WX
        PSTAMP(6);
        RES_FINISH(L_RED + ((j ^ i) * 3 + i - 1) * 4096, L_RED + (j * 3 + i - 1) * 4096);
        PSTAMP(13);
        res_store_tiled(res, wl + OFF_X2 + ((size_t)ott * KSD + 2 * ont) * 1024 + voff, wl + OFF_ST2 + (size_t)ott * 4096 + ont * 256 + r * 8, lane);
        PSTAMP(14);
        arrive4(lcnt, cnt + (C_X2 + layer * 16 + grp) * 1024, lane);
        PSTAMP(7);
      }
      {
        FRESH();
        { LDSP(char) cp = (LDSP(char))smem + L_CS + j * 256 + lane * 4;
          *(LDSP(float))cp = (*(LDSP(float))cp + *(LDSP(float))(cp + 1024)) + (*(LDSP(float))(cp + 2048) + *(LDSP(float))(cp + 3072)); }
        bf16x8 w0[12], w1[12], x0[8], x1[8];
        const char* wa = ws + OFF_WD + ((size_t)ont * KSF + j * 28) * 1024;
        const char* wb = ws + OFF_WD + ((size_t)(ont ^ 1) * KSF + j * 28) * 1024;
#define DISSUE(c, P) do { glds4(voff, wa + 12288 + (c) * 4096, lds0 + (P) + j * 8192); glds4(voff, wb + 12288 + (c) * 4096, lds0 + (P) + j * 8192 + 4096); } while (0)
        {
          const char* gw = ws + OFF_WGU + (size_t)gnb * 229376;
          const char* gs = wl + OFF_ST2 + (size_t)(gtb * 4 + j) * 4096;
          f32x4 sp[4];
          asm volatile("" : "+s"(gw), "+s"(gs));
#define G1(st) do { glds4(voff, gw + (st) * GS + j * 8192, lds0 + ((st) % 3) * GS + j * 8192); glds4(voff, gw + (st) * GS + j * 8192 + 4096, lds0 + ((st) % 3) * GS + j * 8192 + 4096); } while (0)
#define G2(st) do { glds3(voff, gw + 4 * GS + ((st) - 4) * 24576 + j * 6144, lds0 + ((st) % 3) * GS + j * 6144); \
                    glds3(voff, gw + 4 * GS + ((st) - 4) * 24576 + j * 6144 + 3072, lds0 + ((st) % 3) * GS + j * 6144 + 3072); } while (0)
          poll_b(cnt + (C_X2 + layer * 16 + gtb) * 1024 + (gnb & 7) * 128, 16u, lcnt + 1, layer * 8 + 2, j);
          PSTAMP(8);
          __builtin_amdgcn_s_sleep(8);
          stat_issue(sp, gs, voff);
          G1(2);
          __builtin_amdgcn_s_barrier();
          G1(3); LD4(w0, 0, wa); LD4(w1, 0, wb);
          WAITV(16);
          { float mean, var; stat_finish(sp, mean, var);
            ((LDSP(float))((LDSP(char))smem + L_ST))[j * 64 + hh * 32 + (lane & 15) * 2 + ((lane >> 4) & 1)] = hh ? mean : 1.f / var; }
          WAITBAR_V(16);
          G2(4); LD4(w0, 4, wa + 4096); LD4(w1, 4, wb + 4096);
          WAITBAR_V(22);
          G2(5); LD4(w0, 8, wa + 8192); LD4(w1, 8, wb + 8192);
          WAITBAR_V(22);
          G2(6); DISSUE(0, L_D0);
          WAITBAR_V(22);
          G2(7);
          WAITBAR_V(14);
          DISSUE(1, L_D1);
          WAITBAR_V(8);
          DISSUE(2, L_D2);
#undef G1
#undef G2
        }
        const char* xa = wl + OFF_H + ((size_t)ott * KSF + j * 28) * 1024;
        const char* xb = wl + OFF_H + ((size_t)(ott ^ 1) * KSF + j * 28) * 1024;
        asm volatile("" : "+s"(xa), "+s"(xb));
        poll_b(cnt + (C_H + layer * 16 + grp) * 1024 + (idx & 7) * 128, 16u, lcnt + 1, layer * 8 + 3, j);
        PSTAMP(9);
        PIN8(0, w0, w1, 0); PIN8(0, w0, w1, 4); PIN8(0, w0, w1, 8);
#define LDX(b, blk) do { gld16<((b) & 3) * 1024>(x0[b], voff, xa + (blk) * 4096); gld16<((b) & 3) * 1024>(x1[b], voff, xb + (blk) * 4096); } while (0)
#define WX(b, n) asm volatile("s_waitcnt vmcnt(%2)" : "+v"(x0[b]), "+v"(x1[b]) : "i"(n) : "memory")
        LDX(0, 0); LDX(1, 0); LDX(2, 0); LDX(3, 0); LDX(4, 1); LDX(5, 1); LDX(6, 1); LDX(7, 1);
        f32x16 acc[4];
#pragma unroll
        for (int i = 0; i < 4; ++i) acc[i] = f32x16{};
        LDSP(const char) lw = lb + j * 8192;
#define BLK(o, h, nb, n0) do { \
          WX((h) + 0, n0); MM4(w0[(o) + 0], w1[(o) + 0], x0[(h) + 0], x1[(h) + 0]); SBAR(); if ((nb) >= 0) LDX((h) + 0, nb); SBAR(); \
          WX((h) + 1, (nb) >= 0 ? n0 : n0 - 2); MM4(w0[(o) + 1], w1[(o) + 1], x0[(h) + 1], x1[(h) + 1]); SBAR(); if ((nb) >= 0) LDX((h) + 1, nb); SBAR(); \
          WX((h) + 2, (nb) >= 0 ? n0 : n0 - 4); MM4(w0[(o) + 2], w1[(o) + 2], x0[(h) + 2], x1[(h) + 2]); SBAR(); if ((nb) >= 0) LDX((h) + 2, nb); SBAR(); \
          WX((h) + 3, (nb) >= 0 ? n0 : n0 - 6); MM4(w0[(o) + 3], w1[(o) + 3], x0[(h) + 3], x1[(h) + 3]); SBAR(); if ((nb) >= 0) LDX((h) + 3, nb); SBAR(); } while (0)
#define RDL(o, c) do { _Pragma("unroll") for (int s = 0; s < 4; ++s) { w0[(o) + s] = ldfrag(lw + (c) + s * 1024); w1[(o) + s] = ldfrag(lw + (c) + 4096 + s * 1024); } SBAR(); } while (0)
        BLK(0, 0, 2, 14); RDL(0, L_D0);
        BLK(4, 4, 3, 14); RDL(4, L_D1);
        BLK(8, 0, -1, 14); RDL(8, L_D2);
        BLK(0, 4, -1, 6);
        LD4(w0, 0, wa + 24576); LD4(w1, 0, wb + 24576);
        PSTAMP(15);
        poll_b(cnt + (C_H2 + layer * 16 + grp) * 1024 + (idx & 7) * 128, 16u, lcnt + 1, layer * 8 + 4, j);
        PIN8(0, w0, w1, 0);
        LDX(0, 4); LDX(1, 4); LDX(2, 4); LDX(3, 4); LDX(4, 5); LDX(5, 5); LDX(6, 5); LDX(7, 5);
        BLK(4, 0, 6, 14);
        u32x2 rn[4]; unsigned res[8];
        res_load(rn, voff, wl + OFF_X2 + (size_t)ott * 32768 + ont * 2048, 8, 1024);
        BLK(8, 4, -1, 18);
        BLK(0, 0, -1, 10);
#undef DISSUE
#undef RDL
#undef BLK
#undef LDX
#undef WX
        {
        FRESH();
        PSTAMP(10);
        RES_FINISH((i == 3 ? L_D1 : L_D0 + (i - 1) * 4096) + j * 8192, (i == 3 ? L_D1 : L_D0 + (i - 1) * 4096) + (j ^ i) * 8192);
        if (layer == 0) {
          res_store_tiled(res, ws + OFF_X3 + ((size_t)ott * KSD + 2 * ont) * 1024 + voff, ws + OFF_ST3 + (size_t)ott * 4096 + ont * 256 + r * 8, lane);
          arrive4(lcnt, cnt + (C_X3 + grp) * 1024, lane);
        } else {
          auto s0 = __builtin_amdgcn_permlane32_swap(res[0], res[2], false, false);
          auto s1 = __builtin_amdgcn_permlane32_swap(res[1], res[3], false, false);
          auto s2 = __builtin_amdgcn_permlane32_swap(res[4], res[6], false, false);
          auto s3 = __builtin_amdgcn_permlane32_swap(res[5], res[7], false, false);
          const u32x4 a = {s0[0], s1[0], s0[1], s1[1]}, b = {s2[0], s3[0], s2[1], s3[1]};
          char* o = (char*)karg(7) + ((size_t)(ott * 32 + r) * D_ + ont * 32) * 2 + hh * 16;
          st16(o, a);
          st16(o + 32, b);
        }
        }
        PSTAMP(11);
      }
#undef LD4
#undef MM4
#undef PIN8
      if (layer == 0) {
        FRESH();
        const int tt = qtb * 4 + j;
        const char* xb = ws + OFF_X3 + (size_t)tt * 32768;
        const char* wb = ws + OFF_WQ + (size_t)qnb * 8 * QW + j * 3072;
        f32x4 sp[4];
        asm volatile("" : "+s"(xb), "+s"(wb));
        __builtin_amdgcn_s_barrier();
        poll_b(cnt + (C_X3 + qtb) * 1024 + (qnb & 7) * 128, 16u, lcnt + 1, 5, j);
        PSTAMP(12);
#define QISSUE(c) do { glds4(voff, xb + (c) * 4096, lds0 + L_QX + ((c) & 3) * 16384 + j * 4096); glds3(voff, wb + (c) * QW, lds0 + ((c) & 3) * QW + j * 3072); } while (0)
        glds4(voff, xb, lds0 + L_QX + j * 4096);
        glds4(voff, xb + 4096, lds0 + L_QX + 16384 + j * 4096);
        stat_issue(sp, ws + OFF_ST3 + (size_t)tt * 4096, voff);
        glds4(voff, xb + 8192, lds0 + L_QX + 32768 + j * 4096);
        glds4(voff, xb + 12288, lds0 + L_QX + 49152 + j * 4096);
        WAITBAR_V(16);
        { LDSP(char) cp = (LDSP(char))smem + L_CS + j * 256 + lane * 4;
          *(LDSP(float))cp = (*(LDSP(float))cp + *(LDSP(float))(cp + 1024)) + (*(LDSP(float))(cp + 2048) + *(LDSP(float))(cp + 3072)); }
        WAITBAR_V(12);
        QISSUE(4); WAITBAR_V(11);
#pragma unroll 1
        for (int c = 5; c < 8; ++c) { QISSUE(c); WAITBAR_V(14); }
#undef QISSUE
        { float mean, var; stat_finish(sp, mean, var); const float rs = rsqrtf(var);
          ((LDSP(float))((LDSP(char))smem + L_ST))[j * 64 + hh * 32 + (lane & 15) * 2 + ((lane >> 4) & 1)] = hh ? mean * rs : rs; }
        WAITBAR_V(7);
        WAITBAR_V(0);
        __builtin_amdgcn_s_barrier();
      }
    }
  }
}

extern "C" void kernel_launch(void* const* d_in, const int* in_sizes, int n_in, void* d_out, int out_size, void* d_ws, size_t ws_size,
                              hipStream_t stream) {
  const char* x = (const char*)d_in[0];
  const char* wqkv = (const char*)d_in[1];
  const char* wo = (const char*)d_in[2];
  const char* wg = (const char*)d_in[4];
  const char* wu = (const char*)d_in[5];
  const char* wd = (const char*)d_in[6];
  char* ws = (char*)d_ws;
  k_qkv1<<<256, 768, 0, stream>>>(x, wqkv, ws, (const char*)d_out DIAG_PASS(0));
  k_main<<<256, 512, 0, stream>>>(ws, x, wqkv, wo, wg, wu, wd, (char*)d_out DIAG_PASS(1));
}
```

```cpp
#include <hip/hip_runtime.h>
#include <hip/hip_bf16.h>
#include <stdint.h>

typedef short bf16x8 __attribute__((ext_vector_type(8)));
typedef float f32x16 __attribute__((ext_vector_type(16)));
typedef float f32x4 __attribute__((ext_vector_type(4)));
typedef float f32x2 __attribute__((ext_vector_type(2)));
typedef unsigned u32x4 __attribute__((ext_vector_type(4)));
typedef unsigned u32x2 __attribute__((ext_vector_type(2)));
typedef __bf16 bf16v2 __attribute__((ext_vector_type(2)));
typedef unsigned short u16;

#define DI __device__ __forceinline__
#define MFMA(a, b, c) __builtin_amdgcn_mfma_f32_32x32x16_bf16((a), (b), (c), 0, 0, 0)
#define LDSP(T) __attribute__((address_space(3))) T*

constexpr int S_ = 2048, D_ = 512, H_ = 32, F_ = 1792;
constexpr int NTT = S_ / 32;
constexpr int KSD = D_ / 16;
constexpr int KSF = F_ / 16;
constexpr float QSCALE = 0.25f * 1.4426950408889634f;
constexpr float LN_EPS = 1e-5f;

constexpr size_t KB = 1024;
constexpr size_t OFF_CNT = 0;
constexpr size_t OFF_WO = 192 * KB;
constexpr size_t OFF_WGU = OFF_WO + 512 * KB;
constexpr size_t OFF_WD = OFF_WGU + 3584 * KB;
constexpr size_t OFF_WQ = OFF_WD + 1792 * KB;
constexpr size_t OFF_CSP = OFF_WQ + 1536 * KB;
constexpr size_t OFF_ST3 = OFF_CSP + 324 * KB;
constexpr size_t OFF_X3 = OFF_ST3 + 256 * KB;
constexpr size_t OFF_L = OFF_X3 + 2048 * KB;
constexpr size_t OFF_Q = OFF_L;
constexpr size_t OFF_K = OFF_Q + 2048 * KB;
constexpr size_t OFF_V = OFF_K + 2048 * KB;
constexpr size_t OFF_O = OFF_V + 2048 * KB;
constexpr size_t OFF_X2 = OFF_O + 2048 * KB;
constexpr size_t OFF_ST2 = OFF_X2 + 2048 * KB;
constexpr size_t OFF_H = OFF_ST2 + 256 * KB;
constexpr size_t LSTRIDE = OFF_H + 7168 * KB - OFF_L;
constexpr int KMAIN_KB = 28;
constexpr int C_O = 0, C_X2 = 16, C_H = 48, C_X3 = 80, C_KV = 96, C_PREP = 112, C_H2 = 128, C_N = 160;

template <int I> struct IC { static constexpr int value = I; };
template <int B, int E, class F> DI void sfor(F&& f) { if constexpr (B < E) { f(IC<B>{}); sfor<B + 1, E>(f); } }
DI unsigned pk2(float lo, float hi) {
  f32x2 v = {lo, hi};
  bf16v2 r = __builtin_convertvector(v, bf16v2);
  return __builtin_bit_cast(unsigned, r);
}
DI float lo16(unsigned u) { return __uint_as_float(u << 16); }
DI float hi16(unsigned u) { return __uint_as_float(u & 0xffff0000u); }
DI float xhalf_add(float v) {
  auto r = __builtin_amdgcn_permlane32_swap(__float_as_uint(v), __float_as_uint(v), false, false);
  return __uint_as_float(r[0]) + __uint_as_float(r[1]);
}
DI float xhalf_max(float v) {
  auto r = __builtin_amdgcn_permlane32_swap(__float_as_uint(v), __float_as_uint(v), false, false);
  return fmaxf(__uint_as_float(r[0]), __uint_as_float(r[1]));
}
DI float x16_add(float v) {
  auto r = __builtin_amdgcn_permlane16_swap(__float_as_uint(v), __float_as_uint(v), false, false);
  return __uint_as_float(r[0]) + __uint_as_float(r[1]);
}
DI void st16(void* p, u32x4 v) { asm volatile("global_store_dwordx4 %0, %1, off sc1\n\ts_nop 1" :: "v"(p), "v"(v) : "memory"); }
template <int IMM>
DI void st16s(unsigned voff, const char* sbase, u32x4 v) {
  asm volatile("global_store_dwordx4 %0, %1, %2 offset:%3 sc1\n\ts_nop 1" :: "v"(voff), "v"(v), "s"(sbase), "i"(IMM) : "memory");
}
template <int IMM>
DI void gld16(bf16x8& d, unsigned voff, const char* sbase) {
  asm volatile("global_load_dwordx4 %0, %1, %2 offset:%3 sc1" : "=v"(d) : "v"(voff), "s"(sbase), "i"(IMM) : "memory");
}
#define GLDS_HEAD "s_mov_b32 %0, m0\n\ts_mov_b32 m0, %3\n\ts_nop 0\n\t"
#define GLDS_OP(o) "global_load_lds_dwordx4 %1, %2 offset:" #o " sc1\n\t"
#define GLDS_TAIL "s_mov_b32 m0, %0" : "=&s"(keep) : "v"(voff), "s"(sbase), "s"(lds) : "memory"
DI void glds1(unsigned voff, const char* sbase, unsigned lds) { unsigned keep; asm volatile(GLDS_HEAD GLDS_OP(0) GLDS_TAIL); }
DI void glds2(unsigned voff, const char* sbase, unsigned lds) { unsigned keep; asm volatile(GLDS_HEAD GLDS_OP(0) GLDS_OP(1024) GLDS_TAIL); }
DI void glds3(unsigned voff, const char* sbase, unsigned lds) { unsigned keep; asm volatile(GLDS_HEAD GLDS_OP(0) GLDS_OP(1024) GLDS_OP(2048) GLDS_TAIL); }
DI void glds4(unsigned voff, const char* sbase, unsigned lds) { unsigned keep; asm volatile(GLDS_HEAD GLDS_OP(0) GLDS_OP(1024) GLDS_OP(2048) GLDS_OP(3072) GLDS_TAIL); }
#define SBAR() __builtin_amdgcn_sched_barrier(0)
#define PIN(x) asm volatile("" : "+v"(x))
#define WAITV(n) asm volatile("s_waitcnt vmcnt(%0)" :: "i"(n) : "memory")
#define WAITBAR_V(n) asm volatile("s_waitcnt vmcnt(%0) lgkmcnt(0)\n\ts_barrier" :: "i"(n) : "memory")
#define WAITBAR_L() do { asm volatile("" ::: "memory"); __builtin_amdgcn_s_waitcnt(0xc07f); __builtin_amdgcn_s_barrier(); asm volatile("" ::: "memory"); } while (0)
typedef short s16x4 __attribute__((ext_vector_type(4)));
DI s16x4 trread(LDSP(const char) p) { return __builtin_amdgcn_ds_read_tr16_b64_v4i16((LDSP(s16x4))p); }
DI bf16x8 trfrag(LDSP(const char) p, int off2) { const s16x4 lo = trread(p), hi = trread(p + off2); return __builtin_shufflevector(lo, hi, 0, 1, 2, 3, 4, 5, 6, 7); }
DI bf16x8 ldfrag(LDSP(const char) p) { return *(LDSP(const bf16x8))p; }
DI float dot2(unsigned a, unsigned b, float c) { return __builtin_amdgcn_fdot2_f32_bf16(__builtin_bit_cast(bf16v2, a), __builtin_bit_cast(bf16v2, b), c, false); }
constexpr unsigned ONE2 = 0x3f803f80u;
#define BC8(x) __builtin_bit_cast(bf16x8, x)

template <int LOOK, int PER> DI void ring_wait(int i) {
  if (i <= 8 - LOOK) WAITBAR_V((LOOK - 1) * PER);
  else if (LOOK > 3 && i == 5) WAITBAR_V(2 * PER);
  else if (LOOK > 2 && i == 6) WAITBAR_V(PER);
  else WAITBAR_V(0);
}

DI void qk_store(const float (&v)[16], char* o) {
  u32x4 a = {pk2(v[0], v[1]), pk2(v[2], v[3]), pk2(v[4], v[5]), pk2(v[6], v[7])};
  u32x4 b = {pk2(v[8], v[9]), pk2(v[10], v[11]), pk2(v[12], v[13]), pk2(v[14], v[15])};
  st16(o, a);
  st16(o + NTT * 1024, b);
}
DI void v_store(const float (&v)[16], char* o) {
  auto s0 = __builtin_amdgcn_permlane32_swap(pk2(v[0], v[1]), pk2(v[4], v[5]), false, false);
  auto s1 = __builtin_amdgcn_permlane32_swap(pk2(v[2], v[3]), pk2(v[6], v[7]), false, false);
  auto s2 = __builtin_amdgcn_permlane32_swap(pk2(v[8], v[9]), pk2(v[12], v[13]), false, false);
  auto s3 = __builtin_amdgcn_permlane32_swap(pk2(v[10], v[11]), pk2(v[14], v[15]), false, false);
  const u32x4 a = {s0[0], s1[0], s0[1], s1[1]}, b = {s2[0], s3[0], s2[1], s3[1]};
  st16(o, a);
  st16(o + 512, b);
}
typedef __bf16 bfv8 __attribute__((ext_vector_type(8)));
typedef __bf16 bfv16 __attribute__((ext_vector_type(16)));
typedef float f32x8 __attribute__((ext_vector_type(8)));
template <int H> DI f32x16 smf(bf16x8 a, const f32x16& s, f32x16 c, int idx) {
  const f32x8 p = __builtin_shufflevector(s, s, 8 * H, 8 * H + 1, 8 * H + 2, 8 * H + 3, 8 * H + 4, 8 * H + 5, 8 * H + 6, 8 * H + 7);
  return __builtin_amdgcn_smfmac_f32_32x32x32_bf16(__builtin_bit_cast(bfv8, a), __builtin_bit_cast(bfv16, p), c, idx, 0, 0);
}
DI float row16_sum(float v) {
  v += __builtin_bit_cast(float, __builtin_amdgcn_update_dpp(0, __builtin_bit_cast(int, v), 0x128, 0xf, 0xf, false));
  v += __builtin_bit_cast(float, __builtin_amdgcn_update_dpp(0, __builtin_bit_cast(int, v), 0x124, 0xf, 0xf, false));
  v += __builtin_bit_cast(float, __builtin_amdgcn_update_dpp(0, __builtin_bit_cast(int, v), 0x122, 0xf, 0xf, false));
  v += __builtin_bit_cast(float, __builtin_amdgcn_update_dpp(0, __builtin_bit_cast(int, v), 0x121, 0xf, 0xf, false));
  return v;
}
#define KSTEP(B, u) do { SBAR(); RX((B) ^ 1, u); RW((B) ^ 1, 0, u); SBAR(); acc[0] = MFMA(wf[B][0], xf[B], acc[0]); SBAR(); RW((B) ^ 1, 1, u); RW((B) ^ 1, 2, u); SBAR(); \
                         acc[1] = MFMA(wf[B][1], xf[B], acc[1]); acc[2] = MFMA(xf[B], wf[B][2], acc[2]); SBAR(); } while (0)
#define QCHUNK() do { KSTEP(1, 0); KSTEP(0, 1); KSTEP(1, 2); KSTEP(0, 3); } while (0)
#define QINIT() \
  f32x16 acc[3]; bf16x8 wf[2][3], xf[2]; \
  _Pragma("unroll") for (int i = 0; i < 3; ++i) { acc[i] = f32x16{}; wf[1][i] = bf16x8{}; } \
  xf[1] = bf16x8{}
#define QFINISH() do { acc[0] = MFMA(wf[1][0], xf[1], acc[0]); acc[1] = MFMA(wf[1][1], xf[1], acc[1]); acc[2] = MFMA(xf[1], wf[1][2], acc[2]); } while (0)

extern "C" __global__ __launch_bounds__(768, 3) void k_qkv1(const char* __restrict__ x, const char* __restrict__ wn, char* __restrict__ ws) {
  constexpr int LOOK = 3, CH = 24576, LXN = 4 * CH;
  __shared__ __attribute__((aligned(16))) char smem[LXN + 65536];
  const int tid = threadIdx.x, lane = tid & 63, r = lane & 31, hh = lane >> 5;
  const int w = __builtin_amdgcn_readfirstlane(tid >> 6);
  const int xcd = blockIdx.x & 7, idx = blockIdx.x >> 3;
  const int tb = (xcd & 3) * 8 + (idx & 7), nq = (xcd >> 2) * 4 + (idx >> 3);
  const unsigned lds0 = (unsigned)(uintptr_t)smem;
  const int rot = 2 * (idx & 3);
  if (w >= 8) {
    const int j = w - 8;
    const unsigned wv = (lane >> 3) * 3072 + (((lane & 7) ^ (((lane >> 4) & 1) << 2)) * 16);
    const char* wb = wn + (size_t)j * 16 * 3072 + nq * 128;
    if (blockIdx.x < 5) *(unsigned*)(ws + OFF_CNT + ((blockIdx.x * 4 + j) * 64 + lane) * 128) = 0u;
    asm volatile("s_sleep 8\n\ts_nop 4" ::: "memory");
#pragma unroll 1
    for (int c = 0; c < 8 + LOOK; ++c) {
      if (c >= LOOK) ring_wait<LOOK, 6>(c - LOOK);
      if (c < 8) {
        const char* ws_ = wb + (size_t)((c + rot) & 7) * 64 * 3072;
        const unsigned d = lds0 + (c & 3) * CH + j * 2048;
        glds1(wv, ws_, d); glds1(wv, ws_ + 1024, d + 8192); glds1(wv, ws_ + 2048, d + 16384);
        glds1(wv, ws_ + 8 * 3072, d + 1024); glds1(wv, ws_ + 8 * 3072 + 1024, d + 8192 + 1024); glds1(wv, ws_ + 8 * 3072 + 2048, d + 16384 + 1024);
      }
    }
    return;
  }
  {
    const int n = w;
    const u32x4* xr = (const u32x4*)(x + (size_t)(tb * 64 + n * 8) * 1024) + lane;
    u32x4 v[8];
#pragma unroll
    for (int i = 0; i < 8; ++i) v[i] = xr[i * 64];
    float s[8], q[8];
#pragma unroll
    for (int i = 0; i < 8; ++i) {
      s[i] = 0.f; q[i] = 0.f;
#pragma unroll
      for (int e = 0; e < 4; ++e) { s[i] = dot2(v[i][e], ONE2, s[i]); q[i] = dot2(v[i][e], v[i][e], q[i]); }
    }
    float s4[4], q4[4], s2[2], q2[2];
#pragma unroll
    for (int i = 0; i < 4; ++i) {
      const int lo = (i & 1) + 4 * (i >> 1);
      auto a = __builtin_amdgcn_permlane32_swap(__float_as_uint(s[lo]), __float_as_uint(s[lo + 2]), false, false);
      auto b = __builtin_amdgcn_permlane32_swap(__float_as_uint(q[lo]), __float_as_uint(q[lo + 2]), false, false);
      s4[i] = __uint_as_float(a[0]) + __uint_as_float(a[1]); q4[i] = __uint_as_float(b[0]) + __uint_as_float(b[1]);
    }
#pragma unroll
    for (int i = 0; i < 2; ++i) {
      auto a = __builtin_amdgcn_permlane16_swap(__float_as_uint(s4[2 * i]), __float_as_uint(s4[2 * i + 1]), false, false);
      auto b = __builtin_amdgcn_permlane16_swap(__float_as_uint(q4[2 * i]), __float_as_uint(q4[2 * i + 1]), false, false);
      s2[i] = row16_sum(__uint_as_float(a[0]) + __uint_as_float(a[1])); q2[i] = row16_sum(__uint_as_float(b[0]) + __uint_as_float(b[1]));
    }
    float ra[2], rb[2];
#pragma unroll
    for (int i = 0; i < 2; ++i) {
      const float mean = s2[i] * (1.f / D_);
      ra[i] = rsqrtf(fmaxf(q2[i] * (1.f / D_) - mean * mean, 0.f) + LN_EPS);
      rb[i] = -mean * ra[i];
    }
    LDSP(char) xd = (LDSP(char))smem + LXN + (lane >> 3) * 8192 + n * 1024;
#pragma unroll
    for (int i = 0; i < 8; ++i) {
      const float a = __builtin_bit_cast(float, __builtin_amdgcn_readlane(__builtin_bit_cast(int, ra[i >> 2]), (i & 3) * 16));
      const float b = __builtin_bit_cast(float, __builtin_amdgcn_readlane(__builtin_bit_cast(int, rb[i >> 2]), (i & 3) * 16));
      u32x4 o;
#pragma unroll
      for (int e = 0; e < 4; ++e) o[e] = pk2(lo16(v[i][e]) * a + b, hi16(v[i][e]) * a + b);
      *(LDSP(u32x4))(xd + i * 128 + (((lane & 7) ^ ((4 * n + (i >> 1)) & 7)) * 16)) = o;
    }
  }
  WAITBAR_L();
  if (w >= 4) {
    if (w == 4 && idx < KMAIN_KB) {
      bf16x8 t;
      asm volatile("s_getpc_b64 s[20:21]\n\ts_add_u32 s20, s20, k_main@rel32@lo+4\n\ts_addc_u32 s21, s21, k_main@rel32@hi+12\n\t"
                   "global_load_dwordx4 %0, %1, s[20:21]\n\ts_waitcnt vmcnt(0)" : "=v"(t) : "v"(lane * 16 + idx * 1024) : "s20", "s21", "scc", "memory");
    }
    return;
  }
  const int ta = w & 1, hp = w >> 1, q4 = (lane & 15) >> 2, xsw = (r >> 1) & 7;
  LDSP(const char) lw = (LDSP(const char))smem + (8 * hh + q4) * 128 + ((hp ^ (q4 >> 1)) * 64) + ((lane >> 4) & 1) * 32 + (lane & 3) * 8;
  LDSP(const char) lx = (LDSP(const char))smem + LXN + ta * 4096 + r * 128;
  int xo[4];
#pragma unroll
  for (int u = 0; u < 4; ++u) xo[u] = ((2 * u + hh) ^ xsw) * 16;
  QINIT();
#define RX(B, u) xf[B] = ldfrag(sx + xo[u])
#define RW(B, i, u) wf[B][i] = trfrag(sw + (i) * 8192 + (u) * 2048, 512)
#pragma unroll 1
  for (int c = 0; c < 8; ++c) {
    LDSP(const char) sw = lw + (c & 3) * CH;
    LDSP(const char) sx = lx + ((c + rot) & 7) * 8192;
    QCHUNK();
    WAITBAR_L();
  }
  QFINISH();
#undef RX
#undef RW
  const int tt = tb * 2 + ta, nb = nq * 2 + hp;
  float v[16];
#pragma unroll
  for (int e = 0; e < 16; ++e) v[e] = acc[0][e] * QSCALE;
  qk_store(v, ws + OFF_Q + ((size_t)(2 * nb) * NTT + tt) * 1024 + lane * 16);
#pragma unroll
  for (int e = 0; e < 16; ++e) v[e] = acc[1][e];
  qk_store(v, ws + OFF_K + ((size_t)(2 * nb) * NTT + tt) * 1024 + lane * 16);
#pragma unroll
  for (int e = 0; e < 16; ++e) v[e] = acc[2][e];
  v_store(v, ws + OFF_V + ((size_t)(2 * nb + (r >> 4)) * NTT + tt) * 1024 + hh * 256 + (r & 15) * 16);
}

DI void poll(const char* cnt, unsigned voff, unsigned target) {
#pragma unroll 1
  for (unsigned spins = 0; spins < 200000u; ++spins) {
    unsigned v;
    asm volatile("global_load_dword %0, %1, %2 sc1\n\ts_waitcnt vmcnt(0)" : "=v"(v) : "v"(voff), "s"(cnt) : "memory");
    if (__all(v >= target)) break;
  }
}
DI void poll_b(const char* cnt, unsigned target, LDSP(unsigned) go, unsigned seq, int j) {
  if (j == 0) {
    poll(cnt, 0u, target);
    __hip_atomic_store(go, seq, __ATOMIC_RELAXED, __HIP_MEMORY_SCOPE_WORKGROUP);
  } else {
    WAITV(0);
#pragma unroll 1
    for (unsigned spins = 0; spins < 4000000u; ++spins) {
      if (__builtin_amdgcn_readfirstlane(__hip_atomic_load(go, __ATOMIC_RELAXED, __HIP_MEMORY_SCOPE_WORKGROUP)) >= seq) break;
    }
  }
  asm volatile("" ::: "memory");
}
DI void arrive4(LDSP(unsigned) lw, char* cnt, int lane) {
  WAITV(0);
  unsigned old = 0;
  if (lane == 0) old = __hip_atomic_fetch_add(lw, 1u, __ATOMIC_RELAXED, __HIP_MEMORY_SCOPE_WORKGROUP);
  old = __builtin_amdgcn_readfirstlane(old);
  if ((old & 3u) == 3u && lane < 8) asm volatile("global_atomic_add %0, %1, %2" :: "v"(lane * 128), "v"(1u), "s"(cnt) : "memory");
  asm volatile("" ::: "memory");
}
DI void stat_issue(f32x4 (&p)[4], const char* sb, unsigned voff) {
  asm volatile("global_load_dwordx4 %0, %4, %5 sc1\n\tglobal_load_dwordx4 %1, %4, %5 offset:1024 sc1\n\tglobal_load_dwordx4 %2, %4, %5 offset:2048 sc1\n\t"
               "global_load_dwordx4 %3, %4, %5 offset:3072 sc1" : "=&v"(p[0]), "=&v"(p[1]), "=&v"(p[2]), "=&v"(p[3]) : "v"(voff), "s"(sb) : "memory");
}
DI void stat_finish(f32x4 (&p)[4], float& mean, float& var) {
  asm volatile("" : "+v"(p[0]), "+v"(p[1]), "+v"(p[2]), "+v"(p[3]));
  const f32x4 t = (p[0] + p[1]) + (p[2] + p[3]);
  auto a = __builtin_amdgcn_permlane16_swap(__float_as_uint(t[0]), __float_as_uint(t[2]), false, false);
  auto b = __builtin_amdgcn_permlane16_swap(__float_as_uint(t[1]), __float_as_uint(t[3]), false, false);
  mean = xhalf_add(__uint_as_float(a[0]) + __uint_as_float(a[1])) * (1.f / D_);
  var = fmaxf(xhalf_add(__uint_as_float(b[0]) + __uint_as_float(b[1])) * (1.f / D_) - mean * mean, 0.f) + LN_EPS;
}
DI void colsum_part(const char* src, unsigned voff, LDSP(char) dst) {
  f32x4 p[4];
  asm volatile("global_load_dwordx4 %0, %4, %5 sc1\n\tglobal_load_dwordx4 %1, %4, %6 sc1\n\tglobal_load_dwordx4 %2, %4, %7 sc1\n\t"
               "global_load_dwordx4 %3, %4, %8 sc1\n\ts_waitcnt vmcnt(0)" : "=&v"(p[0]), "=&v"(p[1]), "=&v"(p[2]), "=&v"(p[3])
               : "v"(voff), "s"(src), "s"(src + 20480), "s"(src + 40960), "s"(src + 61440) : "memory");
  *(LDSP(f32x4))dst = (p[0] + p[1]) + (p[2] + p[3]);
}

#define RES_FINISH(WR, RD) do { \
    _Pragma("unroll") for (int i = 1; i < 4; ++i) _Pragma("unroll") for (int g = 0; g < 4; ++g) { \
      const f32x4 v_ = {acc[i][4 * g], acc[i][4 * g + 1], acc[i][4 * g + 2], acc[i][4 * g + 3]}; \
      *(LDSP(f32x4))(lb + (WR) + g * 1024) = v_; } \
    asm volatile("s_waitcnt vmcnt(0)" : "+v"(rn[0]), "+v"(rn[1]), "+v"(rn[2]), "+v"(rn[3]) :: "memory"); \
    WAITBAR_L(); \
    _Pragma("unroll") for (int g = 0; g < 4; ++g) { \
      f32x4 t_ = {acc[0][4 * g], acc[0][4 * g + 1], acc[0][4 * g + 2], acc[0][4 * g + 3]}; \
      f32x4 u_[3]; \
      _Pragma("unroll") for (int i = 1; i < 4; ++i) u_[i - 1] = *(LDSP(const f32x4))(lb + (RD) + g * 1024); \
      t_ = (u_[0] + u_[1]) + (u_[2] + t_); \
      res[2 * g] = pk2(t_[0] + lo16(rn[g][0]), t_[1] + hi16(rn[g][0])); \
      res[2 * g + 1] = pk2(t_[2] + lo16(rn[g][1]), t_[3] + hi16(rn[g][1])); } } while (0)
DI void res_load(u32x2 (&rn)[4], unsigned vo, const char* p0, int d1, int d2) {
  asm volatile("global_load_dwordx2 %0, %4, %5 sc1\n\tglobal_load_dwordx2 %1, %4, %6 sc1\n\tglobal_load_dwordx2 %2, %4, %7 sc1\n\tglobal_load_dwordx2 %3, %4, %8 sc1"
               : "=&v"(rn[0]), "=&v"(rn[1]), "=&v"(rn[2]), "=&v"(rn[3]) : "v"(vo), "s"(p0), "s"(p0 + d1), "s"(p0 + d2), "s"(p0 + d2 + d1) : "memory");
}
DI void res_store_tiled(const unsigned (&res)[8], char* o, char* st, int lane) {
  const u32x4 p0 = {res[0], res[1], res[2], res[3]}, p1 = {res[4], res[5], res[6], res[7]};
  st16(o, p0);
  st16(o + 1024, p1);
  float s1 = 0.f, s2 = 0.f, t1 = 0.f, t2 = 0.f;
#pragma unroll
  for (int e = 0; e < 4; ++e) { s1 = dot2(res[e], ONE2, s1); s2 = dot2(res[e], res[e], s2); t1 = dot2(res[4 + e], ONE2, t1); t2 = dot2(res[4 + e], res[4 + e], t2); }
  s1 = xhalf_add(s1 + t1); s2 = xhalf_add(s2 + t2);
  if (lane < 32) { const f32x2 v = {s1, s2}; asm volatile("global_store_dwordx2 %0, %1, off sc1" :: "v"(st), "v"(v) : "memory"); }
}

DI int hsrc(int p) { const int g = p / 28, q = p - g * 28; return g * 28 + (q < 16 ? (q >> 2) * 7 + (q & 3) : ((q - 16) / 3) * 7 + 4 + (q - 16) % 3); }
DI const char* karg(int i) {
  const __attribute__((address_space(4))) unsigned long long* ka = (const __attribute__((address_space(4))) unsigned long long*)__builtin_amdgcn_kernarg_segment_ptr();
  asm volatile("" : "+s"(ka));
  return (const char*)ka[i];
}
extern "C" __global__ __launch_bounds__(512, 2) void k_main(char* __restrict__ ws_, const char* x_, const char* wq_, const char* wo_, const char* wg_,
                                                            const char* wu_, const char* wd_, char* out_) {
  constexpr int L_K = 0, L_V = 65536, L_ONE = 131072, L_CMB = 133120;
  constexpr int GS = 32768, L_RED = 3 * GS;
  constexpr int L_D0 = 3 * GS, L_D1 = 2 * GS, L_D2 = 0;
  constexpr int QW = 12288, L_QX = 49152;
  constexpr int L_CS = 157696, L_ST = L_CS + 4096, L_CNT = L_ST + 1024;
  __shared__ __attribute__((aligned(16))) char smem[163840];
  const int w_ = __builtin_amdgcn_readfirstlane(threadIdx.x >> 6);
  const unsigned lds0 = (unsigned)(uintptr_t)smem;
#define FRESH() \
  int tid; asm volatile("v_mbcnt_lo_u32_b32 %0, -1, 0\n\tv_mbcnt_hi_u32_b32 %0, -1, %0" : "=v"(tid)); int bid = blockIdx.x, w = w_; char* ws = ws_; asm volatile("" : "+s"(bid), "+s"(w), "+s"(ws)); \
  tid += w * 64; const int j = w & 3; \
  const int lane = tid & 63, r = lane & 31, hh = lane >> 5, xcd = bid & 7, idx = bid >> 3, grp = xcd * 2 + (idx >> 4), m = idx & 15; \
  const unsigned voff = lane * 16; LDSP(char) lb = (LDSP(char))smem + lane * 16; char* cnt = ws + OFF_CNT; char* wl = ws + layer * LSTRIDE; \
  LDSP(unsigned) lcnt = (LDSP(unsigned))((LDSP(char))smem + L_CNT) + (w >> 2); \
  const int ont = (m >> 1) * 2 + (j >> 1), ott = grp * 4 + (m & 1) * 2 + (j & 1); \
  const int gtb = (xcd & 1) * 8 + (idx & 7), gnb = (xcd >> 1) * 4 + (idx >> 3), qtb = (xcd & 3) * 4 + (idx & 3), qnb = (xcd >> 2) * 8 + (idx >> 2); \
  (void)r; (void)hh; (void)voff; (void)lb; (void)cnt; (void)wl; (void)lcnt; (void)ont; (void)ott; (void)gtb; (void)gnb; (void)qtb; (void)qnb
  if (threadIdx.x < 8) ((LDSP(unsigned))((LDSP(char))smem + L_CNT))[threadIdx.x] = 0u;
#pragma unroll 1
  for (int layer = 0; layer < 2; ++layer) {
    {
      FRESH();
      const int head = xcd * 4 + (idx & 3), qb = idx >> 2, half = w >> 2, tt0 = qb * 8 + 2 * j;
      if (layer) { if (w == 4) poll(cnt + (C_KV + (head >> 1)) * 1024 + qb * 128, 0u, 16u); WAITBAR_L(); }
      const char* ks = wl + OFF_K + (size_t)head * 65536 + half * 32768;
      const unsigned kd = lds0 + L_K + half * 32768;
      bf16x8 q0, q1;
      gld16<0>(q0, voff, wl + OFF_Q + ((size_t)head * NTT + tt0) * 1024);
      gld16<1024>(q1, voff, wl + OFF_Q + ((size_t)head * NTT + tt0) * 1024);
      glds2(voff, ks + j * 2048, kd + j * 2048);
      glds2(voff, ks + (OFF_V - OFF_K) + j * 2048, kd + L_V + j * 2048);
      *(LDSP(unsigned))((LDSP(char))smem + L_ONE + tid * 4) = ((tid >> 2) & 11) == 0 ? ONE2 : 0u;
      asm volatile("s_waitcnt vmcnt(0) lgkmcnt(0)\n\ts_barrier" : "+v"(q0), "+v"(q1) :: "memory");
      LDSP(const char) kp = lb + L_K;
      LDSP(const char) vp0 = (LDSP(const char))smem + (r < 16 ? L_V : L_ONE) + hh * 256 + (r & 15) * 16;
      const int vstep = r < 16 ? 2048 : 0;
      LDSP(const char) kq = kp + half * 32768; LDSP(const char) vq = vp0 + half * 16 * vstep;
      f32x16 n0, n1, sA, sB = f32x16{}, o0 = f32x16{}, o1 = f32x16{};
      bf16x8 kA, kB, vp = bf16x8{};
      int sidx = (int)0xDDDDDDDDu; PIN(sidx);
      {
        const bf16x8 k0 = ldfrag(kp);
        kA = ldfrag(kq);
        const f32x16 s = MFMA(k0, q0, f32x16{}), t = MFMA(k0, q1, f32x16{});
        float a = fmaxf(fmaxf(s[0], s[1]), s[2]), b = fmaxf(fmaxf(t[0], t[1]), t[2]);
#pragma unroll
        for (int i = 3; i < 15; i += 2) { a = fmaxf(fmaxf(a, s[i]), s[i + 1]); b = fmaxf(fmaxf(b, t[i]), t[i + 1]); }
        a = xhalf_max(fmaxf(a, s[15])); b = xhalf_max(fmaxf(b, t[15]));
#pragma unroll
        for (int i = 0; i < 16; ++i) { n0[i] = -a; n1[i] = -b; }
        PIN(n0); PIN(n1);
        sA = MFMA(kA, q0, n0);
      }
#define EX(S, i) S[i] = __builtin_amdgcn_exp2f(S[i])
#define STEPN(CUR, NXT, KN_, QN, NN, O, VA, OP, VP, READS) do { \
      EX(CUR, 0); EX(CUR, 1); PIN(CUR); SBAR(); OP = smf<1>(VP, NXT, OP, sidx); SBAR(); READS; SBAR(); \
      EX(CUR, 2); EX(CUR, 3); EX(CUR, 4); EX(CUR, 5); PIN(CUR); SBAR(); NXT = MFMA(KN_, QN, NN); SBAR(); \
      EX(CUR, 6); EX(CUR, 7); EX(CUR, 8); EX(CUR, 9); EX(CUR, 10); PIN(CUR); SBAR(); O = smf<0>(VA, CUR, O, sidx); SBAR(); \
      EX(CUR, 11); EX(CUR, 12); EX(CUR, 13); EX(CUR, 14); EX(CUR, 15); PIN(CUR); SBAR(); } while (0)
#define TILE(t, KA, KB) do { bf16x8 va, vb; \
      STEPN(sA, sB, KA, q1, n1, o0, va, o1, vp, (va = ldfrag(vq + (t) * 1024), vb = ldfrag(vq + (t) * 1024 + 512), KB = ldfrag(kq + ((t) + 1) * 1024))); \
      STEPN(sB, sA, KB, q0, n0, o1, va, o0, vb, (vp = vb)); } while (0)
      __builtin_amdgcn_s_setprio(3);
#pragma unroll 1
      for (int ph = -2; ph < 5; ++ph) {
        if (ph == -1) {
          glds3(voff, ks + 8192 + j * 6144, kd + 8192 + j * 6144);
          glds3(voff, ks + (OFF_V - OFF_K) + 8192 + j * 6144, kd + L_V + 8192 + j * 6144);
          glds3(voff, ks + 11264 + j * 6144, kd + 11264 + j * 6144);
          glds3(voff, ks + (OFF_V - OFF_K) + 11264 + j * 6144, kd + L_V + 11264 + j * 6144);
        }
        if (ph == 0) {
          WAITV(0);
          if (lane == 0) __hip_atomic_fetch_add(lcnt + 4, 1u, __ATOMIC_RELAXED, __HIP_MEMORY_SCOPE_WORKGROUP);
#pragma unroll 1
          for (unsigned spins = 0; spins < 4000000u; ++spins)
            if (__builtin_amdgcn_readfirstlane(__hip_atomic_load(lcnt + 4, __ATOMIC_RELAXED, __HIP_MEMORY_SCOPE_WORKGROUP)) >= 4u * (layer + 1)) break;
          asm volatile("" ::: "memory");
        }
        if (ph == 1) __builtin_amdgcn_s_setprio(2);
        if (ph == 3) __builtin_amdgcn_s_setprio(1);
        if (ph == 4) __builtin_amdgcn_s_setprio(0);
        if (layer == 0 && ph >= 0) {
          const int q4 = (lane & 15) >> 2, b16 = (lane >> 4) & 1;
          const int scr = L_CMB + (half ? j * 5120 : 20480 + j * 2048);
          const int jo = ((ph - 1) * 8 + w) * 256 + bid;
          if ((ph == 1 || ph == 2) && jo < 3712) {
            const bool gu = jo < 1792;
            LDSP(const char) sp = (LDSP(const char))smem + scr + (lane & 3) * 8 +
                                  (gu ? (16 * hh + 4 * b16 + q4) * 32 : (4 * hh + q4) * 64 + b16 * 32);
            const int ho = gu ? 256 : 512;
            const char* pd;
            if (gu) { const int ft = jo >> 4, S = jo & 15, nb = ft / 7, ii = ft - nb * 7; pd = ws + OFF_WGU + (size_t)(nb * 224 + (ii < 4 ? S * 8 + 2 * ii : 128 + S * 6 + 2 * (ii - 4))) * 1024; }
            else if (jo < 2048) pd = ws + OFF_WO + (size_t)(jo - 1792) * 2048;
            else if (jo < 2944) pd = ws + OFF_WD + (size_t)(jo - 2048) * 2048;
            else { const int t = (jo - 2944) >> 4, nb = t / 3, ii = t - nb * 3, s0 = (jo & 15) * 2; pd = ws + OFF_WQ + (size_t)(((nb * 8 + (s0 >> 2)) * 3 + ii) * 4 + (s0 & 3)) * 1024; }
            WAITV(0);
            const u32x4 fa = __builtin_bit_cast(u32x4, trfrag(sp, ho)), fb = __builtin_bit_cast(u32x4, trfrag(sp + 1024, ho));
            st16s<0>(voff, pd, fa); st16s<1024>(voff, pd, fb);
            if (gu || jo >= 2944) {
              float ca = (lo16(fa[0]) + hi16(fa[0])) + (lo16(fa[1]) + hi16(fa[1])) + (lo16(fa[2]) + hi16(fa[2])) + (lo16(fa[3]) + hi16(fa[3]));
              float cb = (lo16(fb[0]) + hi16(fb[0])) + (lo16(fb[1]) + hi16(fb[1])) + (lo16(fb[2]) + hi16(fb[2])) + (lo16(fb[3]) + hi16(fb[3]));
              if (gu) { ca = xhalf_add(x16_add(ca)); cb = xhalf_add(x16_add(cb)); ca = lane < 16 ? ca : cb; }
              else ca = xhalf_add(ca + cb);
              const char* cp = ws + OFF_CSP + ((jo & 15) * 5120 + ((gu ? jo : jo - 2944 + 1792) >> 4) * 32) * 4;
              if (lane < 32) asm volatile("global_store_dword %0, %1, %2 sc1" :: "v"(lane * 4), "v"(ca), "s"(cp) : "memory");
            }
          }
          const int jn = jo + 2048;
          if (ph < 2 && jn < 3712) {
            const char *sa, *sb; unsigned gv;
            if (jn < 1792) {
              const int o = 32 * (jn & 15) * (F_ * 2) + (jn >> 4) * 32;
              gv = (lane >> 1) * (F_ * 2) + (lane & 1) * 16; sa = karg(4) + o; sb = karg(5) + o;
            } else {
              int nt, s0, s1, ld = 1024, ai;
              if (jn < 2048) { const int f = (jn - 1792) * 2; nt = f >> 5; s0 = f & 31; s1 = s0 + 1; ai = 3; }
              else if (jn < 2944) { const int f = (jn - 2048) * 2; nt = f / KSF; s0 = hsrc(f - nt * KSF); s1 = hsrc(f - nt * KSF + 1); ai = 6; }
              else { const int t = (jn - 2944) >> 4, nb = t / 3; s0 = (jn & 15) * 2; s1 = s0 + 1; nt = (t - nb * 3) * 16 + nb; ai = 2; ld = 3072; }
              gv = (lane >> 2) * ld + (lane & 3) * 16;
              const char* src = karg(ai) + nt * 64;
              sa = src + 16 * s0 * ld; sb = src + 16 * s1 * ld;
            }
            __builtin_amdgcn_s_waitcnt(0xc07f);
            glds1(gv, sa, lds0 + scr); glds1(gv, sb, lds0 + scr + 1024);
          }
          if (ph == 3) arrive4(lcnt, cnt + (C_PREP + xcd) * 1024, lane);
        }
        int n = ph <= 0 ? ph + 3 : ph == 2 ? 2 : ph == 4 ? 2 : 3;
#pragma unroll 1
        do { TILE(0, kA, kB); TILE(1, kB, kA); kq += 2048; vq += vstep; } while (--n > 0);
      }
      o1 = smf<1>(vp, sB, o1, sidx);
#undef TILE
#undef STEPN
      LDSP(char) xp = lb + L_CMB + j * 5120;
      if (half == 1) {
        const f32x4 a = {o0[0], o0[1], o0[2], o0[3]}, b = {o0[4], o0[5], o0[6], o0[7]}, c = {o1[0], o1[1], o1[2], o1[3]}, d = {o1[4], o1[5], o1[6], o1[7]};
        const f32x2 l = {o0[8], o1[8]};
        *(LDSP(f32x4))xp = a; *(LDSP(f32x4))(xp + 1024) = b; *(LDSP(f32x4))(xp + 2048) = c; *(LDSP(f32x4))(xp + 3072) = d; *(LDSP(f32x2))(xp + 4096) = l;
      }
      if (layer == 0 && w == 0) poll(cnt + C_PREP * 1024 + (idx & 7) * 128, (lane & 7) * 1024, 64u);
      WAITBAR_L();
      if (half == 0) {
        __builtin_amdgcn_s_setprio(3);
        const f32x4 a = *(LDSP(const f32x4))xp, b = *(LDSP(const f32x4))(xp + 1024), c = *(LDSP(const f32x4))(xp + 2048), d = *(LDSP(const f32x4))(xp + 3072);
        const f32x2 l = *(LDSP(const f32x2))(xp + 4096);
#pragma unroll
        for (int i = 0; i < 4; ++i) { o0[i] += a[i]; o0[4 + i] += b[i]; o1[i] += c[i]; o1[4 + i] += d[i]; }
        o0[8] += l[0]; o1[8] += l[1];
        float chk = (o0[8] + o1[8]) * 5.4210109e-20f;
#pragma unroll
        for (int i = 0; i < 8; ++i) chk = __builtin_fmaf(o0[i], 0.f, __builtin_fmaf(o1[i], 0.f, chk));
        const bool bad = !(chk < 1.f);
        if (__builtin_expect(__any(bad), 0)) {
#pragma unroll 1
          for (int z = 0; z < 2; ++z) {
            const bf16x8 q = z ? q1 : q0;
            float mx = -INFINITY;
            f32x16 o = f32x16{};
            kq = kp; vq = vp0;
#pragma unroll 1
            for (int t = 0; t < 64; ++t) {
              const bf16x8 k = ldfrag(kq), v0 = ldfrag(vq), v1 = ldfrag(vq + 512);
              f32x16 s = MFMA(k, q, f32x16{});
              float tm = s[0];
#pragma unroll
              for (int i = 1; i < 16; ++i) tm = fmaxf(tm, s[i]);
              tm = xhalf_max(tm);
              const float mn = fmaxf(mx, tm);
              const float alpha = __builtin_amdgcn_exp2f(mx - mn);
              mx = mn;
#pragma unroll
              for (int i = 0; i < 16; ++i) { o[i] *= alpha; s[i] = __builtin_amdgcn_exp2f(s[i] - mn); }
              o = smf<0>(v0, s, o, sidx);
              o = smf<1>(v1, s, o, sidx);
              kq += 1024; vq += vstep >> 1;
            }
            if (z) o1 = o; else o0 = o;
          }
        }
        const float i0 = __builtin_amdgcn_rcpf(o0[8]), i1 = __builtin_amdgcn_rcpf(o1[8]);
        const u32x4 a0 = {pk2(o0[0] * i0, o0[1] * i0), pk2(o0[2] * i0, o0[3] * i0), pk2(o0[4] * i0, o0[5] * i0), pk2(o0[6] * i0, o0[7] * i0)};
        const u32x4 a1 = {pk2(o1[0] * i1, o1[1] * i1), pk2(o1[2] * i1, o1[3] * i1), pk2(o1[4] * i1, o1[5] * i1), pk2(o1[6] * i1, o1[7] * i1)};
        char* op = wl + OFF_O + ((size_t)tt0 * KSD + head) * 1024 + voff;
        st16(op, a0); st16(op + 32768, a1);
        WAITBAR_L();
        arrive4(lcnt, cnt + (C_O + layer * 8 + qb) * 1024, lane);
      }
    }
    int role = w_ >> 2;
    asm volatile("" : "+s"(role));
    if (role == 0) {
      {
        FRESH();
        const char* wb = ws + OFF_WGU + (size_t)gnb * 229376 + j * 8192;
#pragma unroll 1
        for (int c = 0; c < 4; ++c) glds4(voff, wb + (c >> 1) * GS + (c & 1) * 4096, lds0 + (c >> 1) * GS + (c & 1) * 4096 + j * 8192);
        colsum_part(ws + OFF_CSP + (size_t)j * 81920 + gnb * 896, voff, lb + L_CS + j * 1024);
      }
      WAITBAR_L();
      {
        FRESH();
        const int g4 = lane >> 4, c16 = lane & 15;
        const char* gx = wl + OFF_X2 + (size_t)(gtb * 4 + j) * 32768;
        unsigned xo = (g4 >> 1) * 1024 + ((g4 & 1) * 32 + c16) * 16;
        const char* ob = wl + OFF_H + ((size_t)(gtb * 4 + j) * KSF + (gnb >> 2) * 28) * 1024;
        unsigned oo = (((g4 & 1) * 32) + (g4 >> 1) * 16 + c16) * 16;
        asm volatile("" : "+s"(gx), "+s"(ob)); PIN(xo); PIN(oo);
        bf16x8 xr[16][2], wf[8];
        f32x4 ac[2][8];
#pragma unroll 1
        for (unsigned spins = 0; spins < 4000000u; ++spins)
          if (__builtin_amdgcn_readfirstlane(__hip_atomic_load(lcnt + 2, __ATOMIC_RELAXED, __HIP_MEMORY_SCOPE_WORKGROUP)) >= (unsigned)(layer * 8 + 2)) break; else __builtin_amdgcn_s_sleep(1);
        asm volatile("" ::: "memory");
#define LDXS(S) do { gld16<((S) & 1) * 2048>(xr[S][0], xo, gx + ((S) >> 1) * 4096); gld16<((S) & 1) * 2048 + 256>(xr[S][1], xo, gx + ((S) >> 1) * 4096); } while (0)
        sfor<0, 8>([&](auto ic) { LDXS(decltype(ic)::value); });
        auto pass = [&](auto nt_, auto st0_) {
          constexpr int NT = decltype(nt_)::value, ST0 = decltype(st0_)::value, PD = 6, NF = 16 * NT;
          sfor<0, NF + PD>([&](auto ic) {
            constexpr int f = decltype(ic)::value, g = f - PD;
            if constexpr (g >= 0) {
              constexpr int S = g / NT, i = g % NT;
              if constexpr (ST0 == 0 && i == 0) {
                constexpr int last = 4 * (S / 4) + 7 > 15 ? 15 : 4 * (S / 4) + 7;
                asm volatile("s_waitcnt vmcnt(%2)" : "+v"(xr[S][0]), "+v"(xr[S][1]) : "i"(2 * (last - S)) : "memory");
              }
              SBAR();
              if constexpr (S == 0) ac[0][i] = __builtin_amdgcn_mfma_f32_16x16x32_bf16(wf[g & 7], xr[S][0], f32x4{}, 0, 0, 0);
              else ac[0][i] = __builtin_amdgcn_mfma_f32_16x16x32_bf16(wf[g & 7], xr[S][0], ac[0][i], 0, 0, 0);
              SBAR();
            }
            if constexpr (f < NF) {
              if constexpr (f % (4 * NT) == 0 && (ST0 + f / (4 * NT)) > 0) WAITBAR_L();
              wf[f & 7] = ldfrag(lb + ((ST0 + f / (4 * NT)) % 3) * GS + (f % (4 * NT)) * 1024);
            }
            if constexpr (g >= 0) {
              constexpr int S = g / NT, i = g % NT;
              SBAR();
              if constexpr (S == 0) ac[1][i] = __builtin_amdgcn_mfma_f32_16x16x32_bf16(wf[g & 7], xr[S][1], f32x4{}, 0, 0, 0);
              else ac[1][i] = __builtin_amdgcn_mfma_f32_16x16x32_bf16(wf[g & 7], xr[S][1], ac[1][i], 0, 0, 0);
              SBAR();
              if constexpr (ST0 == 0 && i == NT - 1 && S % 4 == 3 && S < 8) sfor<S + 5, S + 9>([&](auto jc) { LDXS(decltype(jc)::value); });
              if constexpr (ST0 == 4 && g == 4 * NT) arrive4(lcnt, cnt + (C_H + layer * 16 + gtb) * 1024, lane);
            }
          });
        };
        auto epilogue = [&](auto nt_, auto t0_, const char* o) {
          constexpr int NH = decltype(nt_)::value, T0 = decltype(t0_)::value;
          LDSP(const float) sx = (LDSP(const float))((LDSP(char))smem + L_ST) + j * 64 + c16;
          const float rs0 = sx[0], rs1 = sx[16], m0 = -sx[32], m1 = -sx[48];
          f32x4 c4[2 * NH];
#pragma unroll
          for (int i = 0; i < 2 * NH; ++i) c4[i] = *(LDSP(const f32x4))((LDSP(const char))smem + L_CS + (2 * T0 + i) * 64 + g4 * 16);
          const f32x4 vm0 = {m0, m0, m0, m0}, vm1 = {m1, m1, m1, m1};
          sfor<0, NH>([&](auto ic) {
            constexpr int i = decltype(ic)::value;
            const f32x4 ga = __builtin_elementwise_fma(c4[2 * i], vm0, ac[0][2 * i]), ua = __builtin_elementwise_fma(c4[2 * i + 1], vm0, ac[0][2 * i + 1]) * rs0;
            const f32x4 gb = __builtin_elementwise_fma(c4[2 * i], vm1, ac[1][2 * i]), ub = __builtin_elementwise_fma(c4[2 * i + 1], vm1, ac[1][2 * i + 1]) * rs1;
            const f32x4 h0 = __builtin_elementwise_max(ga, f32x4{}) * ua, h1 = __builtin_elementwise_max(gb, f32x4{}) * ub;
            auto e0 = __builtin_amdgcn_permlane32_swap(pk2(h0[0], h0[1]), pk2(h1[0], h1[1]), false, false);
            auto e1 = __builtin_amdgcn_permlane32_swap(pk2(h0[2], h0[3]), pk2(h1[2], h1[3]), false, false);
            const u32x4 a_ = {e0[0], e1[0], e0[1], e1[1]};
            st16s<i * 1024>(oo, o, a_);
          });
        };
        pass(IC<8>{}, IC<0>{});
        epilogue(IC<4>{}, IC<0>{}, ob + (gnb & 3) * 4096);
        pass(IC<6>{}, IC<4>{});
        epilogue(IC<3>{}, IC<4>{}, ob + 16384 + (gnb & 3) * 3072);
#undef LDXS
        arrive4(lcnt, cnt + (C_H2 + layer * 16 + gtb) * 1024, lane);
      }
      WAITBAR_L();
      if (layer == 0) {
        FRESH();
        __builtin_amdgcn_s_barrier();
        { const char* wb = ws + OFF_WQ + (size_t)qnb * 8 * QW + j * 3072;
#pragma unroll 1
          for (int c = 0; c < 4; ++c) glds3(voff, wb + c * QW, lds0 + c * QW + j * 3072); }
        colsum_part(ws + OFF_CSP + (size_t)j * 81920 + (3584 + qnb * 96) * 4, voff, lb + L_CS + j * 1024);
        QINIT();
#define RX(B, u) xf[B] = ldfrag(sxp + (u) * 1024)
#define RW(B, i, u) wf[B][i] = ldfrag(swp + (i) * 4096 + (u) * 1024)
        WAITBAR_L();
#pragma unroll 1
        for (int c = 0; c < 8; ++c) {
          LDSP(const char) swp = lb + (c & 3) * QW;
          LDSP(const char) sxp = lb + L_QX + (c & 3) * 16384 + j * 4096;
          QCHUNK();
          WAITBAR_L();
        }
        LDSP(const float) cx = (LDSP(const float))((LDSP(char))smem + L_CS);
        LDSP(const float) sx = (LDSP(const float))((LDSP(char))smem + L_ST) + j * 64;
        f32x4 cq[8], ra[4], rb[4];
        const float rstd = sx[r], mr = sx[32 + r], cv = cx[64 + r];
#pragma unroll
        for (int g = 0; g < 8; ++g) cq[g] = *(LDSP(const f32x4))(cx + 8 * g + 4 * hh);
#pragma unroll
        for (int g = 0; g < 4; ++g) { ra[g] = *(LDSP(const f32x4))(sx + 8 * g + 4 * hh); rb[g] = *(LDSP(const f32x4))(sx + 32 + 8 * g + 4 * hh); }
        SBAR();
        QFINISH();
        SBAR();
#undef RX
#undef RW
        char* wn = ws + LSTRIDE;
        const int tt = qtb * 4 + j;
        float v[16];
        const float rq = rstd * QSCALE, mq = mr * QSCALE;
#pragma unroll
        for (int e = 0; e < 16; ++e) v[e] = acc[0][e] * rq - mq * cq[e >> 2][e & 3];
        qk_store(v, wn + OFF_Q + ((size_t)(2 * qnb) * NTT + tt) * 1024 + voff);
#pragma unroll
        for (int e = 0; e < 16; ++e) v[e] = acc[1][e] * rstd - mr * cq[4 + (e >> 2)][e & 3];
        qk_store(v, wn + OFF_K + ((size_t)(2 * qnb) * NTT + tt) * 1024 + voff);
#pragma unroll
        for (int e = 0; e < 16; ++e) v[e] = acc[2][e] * ra[e >> 2][e & 3] - rb[e >> 2][e & 3] * cv;
        v_store(v, wn + OFF_V + ((size_t)(2 * qnb + (r >> 4)) * NTT + tt) * 1024 + hh * 256 + (r & 15) * 16);
        arrive4(lcnt, cnt + (C_KV + qnb) * 1024, lane);
      }
    } else {
      {
        FRESH();
        bf16x8 w0[8], w1[8], x0[8], x1[8];
        const char* wa = ws + OFF_WO + ((size_t)ont * KSD + j * 8) * 1024;
        const char* wb = ws + OFF_WO + ((size_t)(ont ^ 1) * KSD + j * 8) * 1024;
#define LD4(D, o, base) do { gld16<0>(D[o], voff, base); gld16<1024>(D[(o) + 1], voff, base); gld16<2048>(D[(o) + 2], voff, base); gld16<3072>(D[(o) + 3], voff, base); } while (0)
        __builtin_amdgcn_s_barrier();
        LD4(w0, 0, wa); LD4(w0, 4, wa + 4096); LD4(w1, 0, wb); LD4(w1, 4, wb + 4096);
        u32x2 rn[4]; unsigned res[8];
        res_load(rn, layer ? voff : r * 1024 + hh * 8, (layer ? ws + OFF_X3 + ont * 2048 : karg(1) + ont * 64) + (size_t)ott * 32768, layer ? 8 : 16, layer ? 1024 : 32);
        const char* xa = wl + OFF_O + ((size_t)ott * KSD + j * 8) * 1024;
        const char* xb = wl + OFF_O + ((size_t)(ott ^ 1) * KSD + j * 8) * 1024;
        asm volatile("" : "+s"(xa), "+s"(xb));
        poll_b(cnt + (C_O + layer * 8 + xcd) * 1024 + (idx & 7) * 128, 32u, lcnt + 1, layer * 8 + 1, j);
#define LD2(s) do { gld16<((s) & 3) * 1024>(x0[s], voff, xa + ((s) >> 2) * 4096); gld16<((s) & 3) * 1024>(x1[s], voff, xb + ((s) >> 2) * 4096); } while (0)
        LD2(0); LD2(1); LD2(2); LD2(3); LD2(4); LD2(5); LD2(6); LD2(7);
#undef LD2
        f32x16 acc[4];
#pragma unroll
        for (int i = 0; i < 4; ++i) acc[i] = f32x16{};
#define MM4(W0, W1, X0, X1) do { acc[1] = MFMA(W0, X1, acc[1]); acc[2] = MFMA(W1, X0, acc[2]); acc[3] = MFMA(W1, X1, acc[3]); acc[0] = MFMA(W0, X0, acc[0]); } while (0)
#define PIN8(n, X, Y, o) asm volatile("s_waitcnt vmcnt(%8)" : "+v"(X[o]), "+v"(X[(o) + 1]), "+v"(X[(o) + 2]), "+v"(X[(o) + 3]), "+v"(Y[o]), "+v"(Y[(o) + 1]), "+v"(Y[(o) + 2]), "+v"(Y[(o) + 3]) : "i"(n) : "memory")
        PIN8(16, w0, w1, 0); PIN8(16, w0, w1, 4);
#define WX(b, n) asm volatile("s_waitcnt vmcnt(%2)" : "+v"(x0[b]), "+v"(x1[b]) : "i"(n) : "memory")
#define ST(s) do { WX(s, 14 - 2 * (s)); MM4(w0[s], w1[s], x0[s], x1[s]); SBAR(); } while (0)
        ST(0); ST(1); ST(2); ST(3); ST(4); ST(5); ST(6); ST(7);
#undef ST
#undef WX
        RES_FINISH(L_RED + ((j ^ i) * 3 + i - 1) * 4096, L_RED + (j * 3 + i - 1) * 4096);
        res_store_tiled(res, wl + OFF_X2 + ((size_t)ott * KSD + 2 * ont) * 1024 + voff, wl + OFF_ST2 + (size_t)ott * 4096 + ont * 256 + r * 8, lane);
        arrive4(lcnt, cnt + (C_X2 + layer * 16 + grp) * 1024, lane);
      }
      {
        FRESH();
        { LDSP(char) cp = (LDSP(char))smem + L_CS + j * 256 + lane * 4;
          *(LDSP(float))cp = (*(LDSP(float))cp + *(LDSP(float))(cp + 1024)) + (*(LDSP(float))(cp + 2048) + *(LDSP(float))(cp + 3072)); }
        bf16x8 w0[12], w1[12], x0[8], x1[8];
        const char* wa = ws + OFF_WD + ((size_t)ont * KSF + j * 28) * 1024;
        const char* wb = ws + OFF_WD + ((size_t)(ont ^ 1) * KSF + j * 28) * 1024;
#define DISSUE(c, P) do { glds4(voff, wa + 12288 + (c) * 4096, lds0 + (P) + j * 8192); glds4(voff, wb + 12288 + (c) * 4096, lds0 + (P) + j * 8192 + 4096); } while (0)
        {
          const char* gw = ws + OFF_WGU + (size_t)gnb * 229376;
          const char* gs = wl + OFF_ST2 + (size_t)(gtb * 4 + j) * 4096;
          f32x4 sp[4];
          asm volatile("" : "+s"(gw), "+s"(gs));
#define G1(st) do { glds4(voff, gw + (st) * GS + j * 8192, lds0 + ((st) % 3) * GS + j * 8192); glds4(voff, gw + (st) * GS + j * 8192 + 4096, lds0 + ((st) % 3) * GS + j * 8192 + 4096); } while (0)
#define G2(st) do { glds3(voff, gw + 4 * GS + ((st) - 4) * 24576 + j * 6144, lds0 + ((st) % 3) * GS + j * 6144); \
                    glds3(voff, gw + 4 * GS + ((st) - 4) * 24576 + j * 6144 + 3072, lds0 + ((st) % 3) * GS + j * 6144 + 3072); } while (0)
          poll_b(cnt + (C_X2 + layer * 16 + gtb) * 1024 + (gnb & 7) * 128, 16u, lcnt + 1, layer * 8 + 2, j);
          __builtin_amdgcn_s_sleep(8);
          stat_issue(sp, gs, voff);
          G1(2);
          __builtin_amdgcn_s_barrier();
          G1(3); LD4(w0, 0, wa); LD4(w1, 0, wb);
          WAITV(16);
          { float mean, var; stat_finish(sp, mean, var);
            ((LDSP(float))((LDSP(char))smem + L_ST))[j * 64 + hh * 32 + (lane & 15) * 2 + ((lane >> 4) & 1)] = hh ? mean : 1.f / var; }
          WAITBAR_V(16);
          G2(4); LD4(w0, 4, wa + 4096); LD4(w1, 4, wb + 4096);
          WAITBAR_V(22);
          G2(5); LD4(w0, 8, wa + 8192); LD4(w1, 8, wb + 8192);
          WAITBAR_V(22);
          G2(6); DISSUE(0, L_D0);
          WAITBAR_V(22);
          G2(7);
          WAITBAR_V(14);
          DISSUE(1, L_D1);
          WAITBAR_V(8);
          DISSUE(2, L_D2);
#undef G1
#undef G2
        }
        const char* xa = wl + OFF_H + ((size_t)ott * KSF + j * 28) * 1024;
        const char* xb = wl + OFF_H + ((size_t)(ott ^ 1) * KSF + j * 28) * 1024;
        asm volatile("" : "+s"(xa), "+s"(xb));
        poll_b(cnt + (C_H + layer * 16 + grp) * 1024 + (idx & 7) * 128, 16u, lcnt + 1, layer * 8 + 3, j);
        PIN8(0, w0, w1, 0); PIN8(0, w0, w1, 4); PIN8(0, w0, w1, 8);
#define LDX(b, blk) do { gld16<((b) & 3) * 1024>(x0[b], voff, xa + (blk) * 4096); gld16<((b) & 3) * 1024>(x1[b], voff, xb + (blk) * 4096); } while (0)
#define WX(b, n) asm volatile("s_waitcnt vmcnt(%2)" : "+v"(x0[b]), "+v"(x1[b]) : "i"(n) : "memory")
        LDX(0, 0); LDX(1, 0); LDX(2, 0); LDX(3, 0); LDX(4, 1); LDX(5, 1); LDX(6, 1); LDX(7, 1);
        f32x16 acc[4];
#pragma unroll
        for (int i = 0; i < 4; ++i) acc[i] = f32x16{};
        LDSP(const char) lw = lb + j * 8192;
#define BLK(o, h, nb, n0) do { \
          WX((h) + 0, n0); MM4(w0[(o) + 0], w1[(o) + 0], x0[(h) + 0], x1[(h) + 0]); SBAR(); if ((nb) >= 0) LDX((h) + 0, nb); SBAR(); \
          WX((h) + 1, (nb) >= 0 ? n0 : n0 - 2); MM4(w0[(o) + 1], w1[(o) + 1], x0[(h) + 1], x1[(h) + 1]); SBAR(); if ((nb) >= 0) LDX((h) + 1, nb); SBAR(); \
          WX((h) + 2, (nb) >= 0 ? n0 : n0 - 4); MM4(w0[(o) + 2], w1[(o) + 2], x0[(h) + 2], x1[(h) + 2]); SBAR(); if ((nb) >= 0) LDX((h) + 2, nb); SBAR(); \
          WX((h) + 3, (nb) >= 0 ? n0 : n0 - 6); MM4(w0[(o) + 3], w1[(o) + 3], x0[(h) + 3], x1[(h) + 3]); SBAR(); if ((nb) >= 0) LDX((h) + 3, nb); SBAR(); } while (0)
#define RDL(o, c) do { _Pragma("unroll") for (int s = 0; s < 4; ++s) { w0[(o) + s] = ldfrag(lw + (c) + s * 1024); w1[(o) + s] = ldfrag(lw + (c) + 4096 + s * 1024); } SBAR(); } while (0)
        BLK(0, 0, 2, 14); RDL(0, L_D0);
        BLK(4, 4, 3, 14); RDL(4, L_D1);
        BLK(8, 0, -1, 14); RDL(8, L_D2);
        BLK(0, 4, -1, 6);
        LD4(w0, 0, wa + 24576); LD4(w1, 0, wb + 24576);
        poll_b(cnt + (C_H2 + layer * 16 + grp) * 1024 + (idx & 7) * 128, 16u, lcnt + 1, layer * 8 + 4, j);
        PIN8(0, w0, w1, 0);
        LDX(0, 4); LDX(1, 4); LDX(2, 4); LDX(3, 4); LDX(4, 5); LDX(5, 5); LDX(6, 5); LDX(7, 5);
        BLK(4, 0, 6, 14);
        u32x2 rn[4]; unsigned res[8];
        res_load(rn, voff, wl + OFF_X2 + (size_t)ott * 32768 + ont * 2048, 8, 1024);
        BLK(8, 4, -1, 18);
        BLK(0, 0, -1, 10);
#undef DISSUE
#undef RDL
#undef BLK
#undef LDX
#undef WX
        {
        FRESH();
        RES_FINISH((i == 3 ? L_D1 : L_D0 + (i - 1) * 4096) + j * 8192, (i == 3 ? L_D1 : L_D0 + (i - 1) * 4096) + (j ^ i) * 8192);
        if (layer == 0) {
          res_store_tiled(res, ws + OFF_X3 + ((size_t)ott * KSD + 2 * ont) * 1024 + voff, ws + OFF_ST3 + (size_t)ott * 4096 + ont * 256 + r * 8, lane);
          arrive4(lcnt, cnt + (C_X3 + grp) * 1024, lane);
        } else {
          auto s0 = __builtin_amdgcn_permlane32_swap(res[0], res[2], false, false);
          auto s1 = __builtin_amdgcn_permlane32_swap(res[1], res[3], false, false);
          auto s2 = __builtin_amdgcn_permlane32_swap(res[4], res[6], false, false);
          auto s3 = __builtin_amdgcn_permlane32_swap(res[5], res[7], false, false);
          const u32x4 a = {s0[0], s1[0], s0[1], s1[1]}, b = {s2[0], s3[0], s2[1], s3[1]};
          char* o = (char*)karg(7) + ((size_t)(ott * 32 + r) * D_ + ont * 32) * 2 + hh * 16;
          st16(o, a);
          st16(o + 32, b);
        }
        }
      }
#undef LD4
#undef MM4
#undef PIN8
      if (layer == 0) {
        FRESH();
        const int tt = qtb * 4 + j;
        const char* xb = ws + OFF_X3 + (size_t)tt * 32768;
        const char* wb = ws + OFF_WQ + (size_t)qnb * 8 * QW + j * 3072;
        f32x4 sp[4];
        asm volatile("" : "+s"(xb), "+s"(wb));
        __builtin_amdgcn_s_barrier();
        poll_b(cnt + (C_X3 + qtb) * 1024 + (qnb & 7) * 128, 16u, lcnt + 1, 5, j);
#define QISSUE(c) do { glds4(voff, xb + (c) * 4096, lds0 + L_QX + ((c) & 3) * 16384 + j * 4096); glds3(voff, wb + (c) * QW, lds0 + ((c) & 3) * QW + j * 3072); } while (0)
        glds4(voff, xb, lds0 + L_QX + j * 4096);
        glds4(voff, xb + 4096, lds0 + L_QX + 16384 + j * 4096);
        stat_issue(sp, ws + OFF_ST3 + (size_t)tt * 4096, voff);
        glds4(voff, xb + 8192, lds0 + L_QX + 32768 + j * 4096);
        glds4(voff, xb + 12288, lds0 + L_QX + 49152 + j * 4096);
        WAITBAR_V(16);
        { LDSP(char) cp = (LDSP(char))smem + L_CS + j * 256 + lane * 4;
          *(LDSP(float))cp = (*(LDSP(float))cp + *(LDSP(float))(cp + 1024)) + (*(LDSP(float))(cp + 2048) + *(LDSP(float))(cp + 3072)); }
        WAITBAR_V(12);
        QISSUE(4); WAITBAR_V(11);
#pragma unroll 1
        for (int c = 5; c < 8; ++c) { QISSUE(c); WAITBAR_V(14); }
#undef QISSUE
        { float mean, var; stat_finish(sp, mean, var); const float rs = rsqrtf(var);
          ((LDSP(float))((LDSP(char))smem + L_ST))[j * 64 + hh * 32 + (lane & 15) * 2 + ((lane >> 4) & 1)] = hh ? mean * rs : rs; }
        WAITBAR_V(7);
        WAITBAR_V(0);
        __builtin_amdgcn_s_barrier();
      }
    }
  }
}

extern "C" void kernel_launch(void* const* d_in, const int* in_sizes, int n_in, void* d_out, int out_size, void* d_ws, size_t ws_size,
                              hipStream_t stream) {
  const char* x = (const char*)d_in[0];
  const char* wqkv = (const char*)d_in[1];
  const char* wo = (const char*)d_in[2];
  const char* wg = (const char*)d_in[4];
  const char* wu = (const char*)d_in[5];
  const char* wd = (const char*)d_in[6];
  char* ws = (char*)d_ws;
  k_qkv1<<<256, 768, 0, stream>>>(x, wqkv, ws);
  k_main<<<256, 512, 0, stream>>>(ws, x, wqkv, wo, wg, wu, wd, (char*)d_out);
}
```
